```python
import torch

def _softmax(x, axis=-1):
    return torch.softmax(x, dim=axis)
def _gelu(x):
    return torch.nn.functional.gelu(x, approximate="tanh")
def _silu(x):
    return torch.nn.functional.silu(x)
def _sigmoid(x):
    return torch.sigmoid(x)
def _layernorm(x, axis=-1, eps=1e-5):
    m = x.mean(dim=axis, keepdim=True)
    v = x.var(dim=axis, keepdim=True, unbiased=False)
    return (x - m) / torch.sqrt(v + eps)
def _scatter_add(tgt, idx, src, axis=0):
    return tgt.clone().index_add_(axis, idx, src)
def _segment_sum(src, seg, n):
    out = torch.zeros((n,) + src.shape[1:], dtype=src.dtype)
    return out.index_add_(0, seg, src)
def _pad(x, pw):
    flat = tuple(v for pair in reversed(pw) for v in pair)
    return torch.nn.functional.pad(x, flat)

kernel_name = 'gated_mix__grouped_kv_mqa__ssm_conv_h3__prompt'

def setup_inputs():

    _w = (lambda a: (lambda t: t.to(torch.bfloat16) if t.is_floating_point() else t)(torch.as_tensor(a)))
    def _A_setup():
        B, S, D, H = 1, 2048, 576, 32
        Dh = D // H
        return {
            "x": _w((lambda *s: torch.randn(*s, dtype=torch.bfloat16) * 0.05)(B, S, D)),
            "wq": _w((lambda *s: torch.randn(*s, dtype=torch.bfloat16) * 0.8)(D, H * Dh)),
            "wk": _w((lambda *s: torch.randn(*s, dtype=torch.bfloat16) * 0.8)(D, Dh)),
            "wv": _w((lambda *s: torch.randn(*s, dtype=torch.bfloat16) * 0.05)(D, Dh)),
            "wo": _w((lambda *s: torch.randn(*s, dtype=torch.bfloat16) * 0.05)(H * Dh, D)),
        }
    _A_in = _A_setup()

    def _B_setup():
        B, S, D = 1, 2048, 576
        ks = 4
        return {
            "q": _w((lambda *s: torch.randn(*s, dtype=torch.bfloat16) * 0.05)(B, S, D)),
            "k": _w((lambda *s: torch.randn(*s, dtype=torch.bfloat16) * 0.4)(B, S, D)),
            "v": _w((lambda *s: torch.randn(*s, dtype=torch.bfloat16) * 0.2)(B, S, D)),
            "shift_k": _w((lambda *s: torch.randn(*s, dtype=torch.bfloat16) * 0.4)(D, ks) * 0.5),
            "ssm_k": _w((lambda *s: torch.randn(*s, dtype=torch.bfloat16) * 0.4)(D, S) * (1.0 / S ** 0.5)),
            "ssm_D": _w((lambda *s: torch.randn(*s, dtype=torch.bfloat16) * 0.2)(D)),
        }
    _B_in = _B_setup()

    return {
        "A_x": _A_in["x"],
        "A_wq": _A_in["wq"],
        "A_wk": _A_in["wk"],
        "A_wv": _A_in["wv"],
        "A_wo": _A_in["wo"],
        "B_q": _B_in["q"],
        "B_k": _B_in["k"],
        "B_v": _B_in["v"],
        "B_shift_k": _B_in["shift_k"],
        "B_ssm_k": _B_in["ssm_k"],
        "B_ssm_D": _B_in["ssm_D"],
        "gate_w": _w((lambda *s: torch.randn(*s, dtype=torch.bfloat16) * 0.05)(576) * 16.0),
    }

def reference(A_x, A_wq, A_wk, A_wv, A_wo, B_q, B_k, B_v, B_shift_k, B_ssm_k, B_ssm_D, gate_w):
    def _A_ref(x, wq, wk, wv, wo):
        B, S, D = x.shape
        H = 32
        Dh = D // H
        q = (lambda x, *d: x.permute(*d))((x @ wq).reshape(B, S, H, Dh), 0, 2, 1, 3)
        k = x @ wk
        v = x @ wv
        scores = torch.einsum("bhqd,bkd->bhqk", q, k) * (Dh ** -0.5)
        p = _softmax(scores, axis=-1)
        o = torch.einsum("bhqk,bkd->bhqd", p, v)
        return (lambda x, *d: x.permute(*d))(o, 0, 2, 1, 3).reshape(B, S, H * Dh) @ wo

    def _B_ref(q, k, v, shift_k, ssm_k, ssm_D):
        B, S, D = q.shape
        ks = shift_k.shape[-1]
        k_pad = _pad(k, ((0, 0), (ks - 1, 0), (0, 0)))
        k_shift = torch.zeros_like(k)
        for t in range(ks):
            k_shift = k_shift + k_pad[:, t:t + S, :] * shift_k[:, ks - 1 - t][None, None, :]
        kv = k_shift * v
        L = 2 * S
        kv_pad = _pad(kv, ((0, 0), (0, S), (0, 0)))
        kf_pad = _pad(ssm_k, ((0, 0), (0, S)))
        kv_f = (lambda x, axes=(-2,-1): torch.fft.rfft2(x.float(), dim=axes))(kv_pad[:, None, :, :], axes=(1, 2))
        kf_f = (lambda x, axes=(-2,-1): torch.fft.rfft2(x.float(), dim=axes))(kf_pad[:, None, :], axes=(1, 2))
        kf_f = (lambda x, *d: x.permute(*d))(kf_f, 1, 2, 0)
        y_f = kv_f * kf_f[None, :, :, :]
        y = (lambda x, s, axes=(-2,-1): torch.fft.irfft2(x, s=s, dim=axes).to(torch.bfloat16))(y_f, (1, L), axes=(1, 2))[:, 0, :S, :]
        y = y + kv * ssm_D[None, None, :]
        return q * y

    x = A_x
    g = _sigmoid((x * gate_w).sum(-1, keepdims=True))
    a = _A_ref(x, A_wq, A_wk, A_wv, A_wo)
    b = _B_ref(x, B_k, B_v, B_shift_k, B_ssm_k, B_ssm_D)
    return g * a + (1 - g) * b
```

```cpp
#include <hip/hip_runtime.h>
#include <hip/hip_bf16.h>
#include <stdint.h>

typedef __attribute__((ext_vector_type(2))) __bf16 bf16x2;
typedef __attribute__((ext_vector_type(4))) __bf16 bf16x4;
typedef __attribute__((ext_vector_type(8))) __bf16 bf16x8;
typedef __attribute__((ext_vector_type(16))) __bf16 bf16x16;
typedef __attribute__((ext_vector_type(4))) short s16x4;
typedef __attribute__((ext_vector_type(4))) float f32x4;
typedef __attribute__((ext_vector_type(8))) float f32x8;
typedef __attribute__((ext_vector_type(16))) float f32x16;
typedef __attribute__((ext_vector_type(32))) float f32x32;
typedef __attribute__((ext_vector_type(2))) unsigned u32x2;
typedef __attribute__((ext_vector_type(4))) unsigned u32x4;
typedef __attribute__((address_space(3))) char* lds_ptr;
typedef __attribute__((address_space(3))) bf16x4* lds_b4;

#define DEVI __device__ __forceinline__

constexpr int S_ = 2048, D_ = 576, H_ = 32, DH = 18;

constexpr size_t KV_GROUP = 9984;
constexpr size_t WS_KV = 0;
constexpr size_t WS_KNORM = 160 * 1024;
constexpr size_t WS_G = WS_KNORM + 1024;
constexpr size_t WS_QREM = 256 * 1024;
constexpr size_t WS_QMAIN = 512 * 1024;
constexpr size_t WS_UT = WS_QMAIN + 2 * 1024 * 1024;
constexpr size_t WS_Z = WS_UT + (size_t)D_ * S_ * 2;
constexpr size_t WS_O = WS_Z + (size_t)D_ * S_ * 2;

DEVI unsigned pk2(float a, float b) {
  bf16x2 v = {(__bf16)a, (__bf16)b};
  return __builtin_bit_cast(unsigned, v);
}
DEVI float bf_lo(unsigned u) { return __builtin_bit_cast(float, u << 16); }
DEVI float bf_hi(unsigned u) { return __builtin_bit_cast(float, u & 0xffff0000u); }
DEVI float rbf(float a) { return (float)(__bf16)a; }
DEVI int swap23(int r) { return (r & ~12) | ((r & 4) << 1) | ((r & 8) >> 1); }

DEVI void glds16(const void* gsrc, unsigned lds_dst) {
  unsigned keep;
  asm volatile("s_mov_b32 %0, m0\n\ts_mov_b32 m0, %2\n\ts_nop 0\n\tglobal_load_lds_dwordx4 %1, off\n\ts_mov_b32 m0, %0"
               : "=&s"(keep) : "v"(gsrc), "s"(lds_dst) : "memory");
}
#define WAIT_BAR(N) asm volatile("s_waitcnt vmcnt(" #N ") lgkmcnt(0)\n\ts_barrier" ::: "memory")

DEVI bf16x8 tr_frag(lds_ptr p0, lds_ptr p1) {
  bf16x4 a = __builtin_amdgcn_ds_read_tr16_b64_v4bf16((lds_b4)p0);
  bf16x4 b = __builtin_amdgcn_ds_read_tr16_b64_v4bf16((lds_b4)p1);
  return __builtin_shufflevector(a, b, 0, 1, 2, 3, 4, 5, 6, 7);
}
DEVI bf16x8 lds_frag(lds_ptr p) { return *(__attribute__((address_space(3))) bf16x8*)p; }

constexpr int K1_SLOT = 24 * 1024, K1_NSLOT = 6;

__global__ void __launch_bounds__(512, 2)
k1_proj(const __bf16* __restrict__ x, const __bf16* __restrict__ wq, const __bf16* __restrict__ wk,
        const __bf16* __restrict__ wv, const __bf16* __restrict__ bk, const __bf16* __restrict__ bv,
        const __bf16* __restrict__ shk, const __bf16* __restrict__ gw, char* __restrict__ ws) {
  __shared__ __attribute__((aligned(16))) char smem[K1_SLOT * K1_NSLOT];
  const int tid = threadIdx.x, lane = tid & 63;
  const int w = __builtin_amdgcn_readfirstlane(tid >> 6);
  const int bid = blockIdx.x, jj = bid >> 3;
  const int qt = (bid & 7) * 4 + (jj & 3), hg = jj >> 2;
  const unsigned sbase = (unsigned)(size_t)smem;
  lds_ptr sm = (lds_ptr)smem;

  const int xrow = 8 * w + (lane >> 3);
  const char* xsrc = (const char*)(x + (size_t)(64 * qt + xrow) * D_ + 8 * ((lane & 7) ^ ((xrow >> 1) & 7)));
  const int wt = w >> 1, piece = lane & 3, krow = 32 * (w & 1) + (lane >> 2);
  const char* wsrc;
  int wld;
  if (wt < 2) { wld = D_; wsrc = (const char*)(wq + (size_t)krow * D_ + DH * (4 * hg + 2 * wt + (piece >> 1)) + 8 * (piece & 1)); }
  else if (wt == 2) { wld = D_; wsrc = (const char*)(wq + (size_t)krow * D_ + DH * (4 * hg + piece) + 10); }
  else { wld = DH; wsrc = (const char*)(((hg & 2) ? wv : wk) + krow * DH + (piece == 0 ? 0 : (piece == 1 ? 8 : 10))); }
  const int wstep16 = wld * 32, wstep64 = wld * 128;

  auto issue = [&](int c) {
    const unsigned sb = sbase + (c % K1_NSLOT) * K1_SLOT;
    glds16(xsrc + c * 128, sb + 1024 * w);
    glds16(wsrc + c * wstep64, sb + 8192 + 2048 * w);
    glds16(wsrc + c * wstep64 + wstep16, sb + 8192 + 2048 * w + 1024);
  };
#pragma unroll
  for (int c = 0; c < K1_NSLOT; ++c) issue(c);

  const int h = lane >> 5, l31 = lane & 31;
  const int gt = (w < 6) ? (w >> 1) : 3;
  const int tt = (w < 6) ? (w & 1) : (hg & 1);
  const bool active = (w < 6) || (w == 6 && hg < 4);
  const bool vmode = (w == 6) && (hg & 2);
  const int arow = 32 * tt + l31;
  const int xoff = arow * 128, xsw = (arow >> 1) & 7;
  const int woff = 8192 + 4096 * gt + (8 * h + ((lane & 15) >> 2)) * 64 + ((lane >> 4) & 1) * 32 + (lane & 3) * 8;
  f32x16 acc = {0};
#pragma unroll
  for (int c = 0; c < 9; ++c) {
    if (c == 0) WAIT_BAR(15);
    if (c >= 1 && c <= 4) WAIT_BAR(12);
    if (c == 5) WAIT_BAR(9);
    if (c == 6) WAIT_BAR(6);
    if (c == 7) WAIT_BAR(3);
    if (c == 8) WAIT_BAR(0);
    if (c >= 1 && c <= 3) issue(c + 5);
    if (active) {
      lds_ptr sb = sm + (c % K1_NSLOT) * K1_SLOT;
#pragma unroll
      for (int ks = 0; ks < 4; ++ks) {
        bf16x8 xf = lds_frag(sb + xoff + (((2 * ks + h) ^ xsw) << 4));
        bf16x8 wf = tr_frag(sb + woff + ks * 1024, sb + woff + ks * 1024 + 256);
        if (vmode) acc = __builtin_amdgcn_mfma_f32_32x32x16_bf16(xf, wf, acc, 0, 0, 0);
        else       acc = __builtin_amdgcn_mfma_f32_32x32x16_bf16(wf, xf, acc, 0, 0, 0);
      }
    }
  }

  const float QS = 0.23570226039551584f * 1.4426950408889634f;
  const int T = 2 * qt + tt;
  if (w < 4) {
    u32x4 a, b;
#pragma unroll
    for (int i = 0; i < 4; ++i) {
      a[i] = pk2(acc[2 * i] * QS, acc[2 * i + 1] * QS);
      b[i] = pk2(acc[8 + 2 * i] * QS, acc[9 + 2 * i] * QS);
    }
    const int hd = 4 * hg + 2 * gt;
    *(u32x4*)(ws + WS_QMAIN + ((size_t)(hd * 64 + T) << 10) + lane * 16) = a;
    *(u32x4*)(ws + WS_QMAIN + ((size_t)((hd + 1) * 64 + T) << 10) + lane * 16) = b;
  } else if (w < 6) {
    if (h) {
#pragma unroll
      for (int g = 0; g < 4; ++g)
        *(unsigned*)(ws + WS_QREM + ((size_t)(4 * hg + g) * S_ + 32 * T + l31) * 4) = pk2(acc[4 * g + 2] * QS, acc[4 * g + 3] * QS);
    }
  } else if (w == 6 && hg < 2) {
    char* gb = ws + WS_KV + (size_t)(T >> 2) * KV_GROUP;
    const int i4 = T & 3, rp = swap23(l31);
    u32x4 a;
    float n2 = 0.f;
#pragma unroll
    for (int i = 0; i < 4; ++i) {
      a[i] = pk2(acc[2 * i], acc[2 * i + 1]);
      n2 += bf_lo(a[i]) * bf_lo(a[i]) + bf_hi(a[i]) * bf_hi(a[i]);
    }
    *(u32x4*)(gb + i4 * 1024 + (rp + 32 * h) * 16) = a;
    if (h) {
      u32x2 r = {pk2(acc[10], acc[11]), 0x3f803f80u};
      n2 += bf_lo(r[0]) * bf_lo(r[0]) + bf_hi(r[0]) * bf_hi(r[0]);
      *(u32x2*)(gb + 8960 + (rp + 32 * (i4 & 1)) * 16 + (i4 >> 1) * 8) = r;
    }
    n2 += __shfl_xor(n2, 32);
#pragma unroll
    for (int o = 16; o >= 1; o >>= 1) n2 = fmaxf(n2, __shfl_xor(n2, o));
    if (lane == 0) *(float*)(ws + WS_KNORM + T * 4) = n2;
  } else if (w == 6 && hg < 4) {
    char* gb = ws + WS_KV + (size_t)(T >> 2) * KV_GROUP + 4096 + (T & 3) * 1216 + h * 304;
    u32x4 a, b;
#pragma unroll
    for (int i = 0; i < 4; ++i) {
      a[i] = pk2(acc[2 * i], acc[2 * i + 1]);
      b[i] = pk2(acc[8 + 2 * i], acc[9 + 2 * i]);
    }
    if (l31 == 24) { a = (u32x4){0x3f803f80u, 0x3f803f80u, 0x3f803f80u, 0x3f803f80u}; b = a; }
    if (l31 < 16 || (l31 >= 22 && l31 <= 24)) {
      const int d = l31 < 16 ? l31 : l31 - 6;
      *(u32x4*)(gb + d * 16) = a;
      *(u32x4*)(gb + 608 + d * 16) = b;
    }
  }

  if (tid < 288) {
    const int cp = tid % 36, tg = tid / 36;
    const int c0 = 72 * hg + 2 * cp, t0 = 64 * qt + 8 * tg;
    const u32x4 sw = *(const u32x4*)(shk + c0 * 4);
    unsigned kr[11], vr[8];
#pragma unroll
    for (int i = 0; i < 11; ++i) {
      const int t = t0 - 3 + i;
      kr[i] = t >= 0 ? *(const unsigned*)(bk + (size_t)(t < 0 ? 0 : t) * D_ + c0) : 0u;
    }
#pragma unroll
    for (int i = 0; i < 8; ++i) vr[i] = *(const unsigned*)(bv + (size_t)(t0 + i) * D_ + c0);
    float r0[8], r1[8];
#pragma unroll
    for (int i = 0; i < 8; ++i) {
      float a = bf_lo(kr[i]) * bf_hi(sw[1]);
      a = fmaf(bf_lo(kr[i + 1]), bf_lo(sw[1]), a);
      a = fmaf(bf_lo(kr[i + 2]), bf_hi(sw[0]), a);
      a = fmaf(bf_lo(kr[i + 3]), bf_lo(sw[0]), a);
      float b = bf_hi(kr[i]) * bf_hi(sw[3]);
      b = fmaf(bf_hi(kr[i + 1]), bf_lo(sw[3]), b);
      b = fmaf(bf_hi(kr[i + 2]), bf_hi(sw[2]), b);
      b = fmaf(bf_hi(kr[i + 3]), bf_lo(sw[2]), b);
      const unsigned ks = pk2(a, b);
      r0[i] = bf_lo(ks) * bf_lo(vr[i]);
      r1[i] = bf_hi(ks) * bf_hi(vr[i]);
    }
    u32x4 o0, o1;
#pragma unroll
    for (int i = 0; i < 4; ++i) {
      o0[i] = pk2(r0[7 - 2 * i], r0[6 - 2 * i]);
      o1[i] = pk2(r1[7 - 2 * i], r1[6 - 2 * i]);
    }
    *(u32x4*)(ws + WS_UT + ((size_t)c0 * S_ + t0) * 2) = o0;
    *(u32x4*)(ws + WS_UT + ((size_t)(c0 + 1) * S_ + t0) * 2) = o1;
  }
  {
    const int t = 64 * qt + 8 * hg + w;
    float s = 0.f;
#pragma unroll
    for (int i = 0; i < 5; ++i) {
      const int c = 2 * (lane + 64 * i);
      if (c < D_) {
        const unsigned xv = *(const unsigned*)(x + (size_t)t * D_ + c), gv = *(const unsigned*)(gw + c);
        const unsigned p = pk2(bf_lo(xv) * bf_lo(gv), bf_hi(xv) * bf_hi(gv));
        s += bf_lo(p) + bf_hi(p);
      }
    }
#pragma unroll
    for (int o = 32; o >= 1; o >>= 1) s += __shfl_xor(s, o);
    s = rbf(s);
    if (lane == 0) *(float*)(ws + WS_G + t * 4) = rbf(1.f / (1.f + __expf(-s)));
  }
}

constexpr int CV_COPY = 4160;
constexpr int CV_U = 8 * CV_COPY;
constexpr int CV_LDS = CV_U + 6144;

__global__ void __launch_bounds__(256, 2)
k_conv(const __bf16* __restrict__ ssmk, const __bf16* __restrict__ ssmd, char* __restrict__ ws) {
  __shared__ __attribute__((aligned(16))) char smem[CV_LDS];
  const int tid = threadIdx.x, lane = tid & 63;
  const int w = __builtin_amdgcn_readfirstlane(tid >> 6);
  const int c = blockIdx.x;
  lds_ptr sm = (lds_ptr)smem;
  typedef __attribute__((address_space(3))) u32x4* lds_u4;
  const u32x4* hsrc = (const u32x4*)(ssmk + (size_t)c * S_);
  const u32x4 zero4 = {0, 0, 0, 0};
  for (int wv = tid; wv < 259; wv += 256) {
    const u32x4 a = (wv >= 4) ? hsrc[wv - 4] : zero4;
    const u32x4 b = (wv >= 3) ? hsrc[wv - 3] : zero4;
    unsigned d[8] = {a[0], a[1], a[2], a[3], b[0], b[1], b[2], b[3]}, o[7];
#pragma unroll
    for (int i = 0; i < 7; ++i) o[i] = __builtin_amdgcn_alignbit(d[i + 1], d[i], 16);
#pragma unroll
    for (int r = 0; r < 8; ++r) {
      u32x4 v;
#pragma unroll
      for (int i = 0; i < 4; ++i) v[i] = (r & 1) ? d[(r + 1) / 2 + i] : o[r / 2 + i];
      *(lds_u4)(sm + r * CV_COPY + wv * 16) = v;
    }
  }
  if (tid < 128) *(lds_u4)(sm + CV_U + tid * 16) = zero4;
  *(lds_u4)(sm + CV_U + 2048 + tid * 16) = *(const u32x4*)(ws + WS_UT + (size_t)c * S_ * 2 + tid * 16);
  __syncthreads();

  const int hh = lane >> 5, l31 = lane & 31;
  lds_ptr ab = sm + (l31 & 7) * CV_COPY + (2 + hh + (l31 >> 3)) * 16;
  lds_ptr bb = sm + CV_U + 2048 + l31 * 64 + (1 - hh) * 16;
  f32x16 y0 = {0}, y1 = {0};
#pragma unroll
  for (int i = 0; i < 8; ++i) {
    const int dl = 8 * w + i;
#pragma unroll
    for (int s = 0; s < 2; ++s) {
      const bf16x8 a0 = lds_frag(ab + 32 * (2 * dl - s));
      const bf16x8 a1 = lds_frag(ab + 32 * (2 * dl + 64 - s));
      const bf16x8 b0 = lds_frag(bb - 64 * dl + 32 * s);
      const bf16x8 b1 = lds_frag(bb + 64 * (32 - dl) + 32 * s);
      y0 = __builtin_amdgcn_mfma_f32_32x32x16_bf16(a0, b0, y0, 0, 0, 0);
      y1 = __builtin_amdgcn_mfma_f32_32x32x16_bf16(a0, b1, y1, 0, 0, 0);
      y1 = __builtin_amdgcn_mfma_f32_32x32x16_bf16(a1, b0, y1, 0, 0, 0);
    }
  }
  __syncthreads();
  typedef __attribute__((address_space(3))) f32x4* lds_f4;
#pragma unroll
  for (int g = 0; g < 4; ++g) {
    *(lds_f4)(sm + ((w * 2 + 0) * 4 + g) * 1024 + lane * 16) = (f32x4){y0[4 * g], y0[4 * g + 1], y0[4 * g + 2], y0[4 * g + 3]};
    *(lds_f4)(sm + ((w * 2 + 1) * 4 + g) * 1024 + lane * 16) = (f32x4){y1[4 * g], y1[4 * g + 1], y1[4 * g + 2], y1[4 * g + 3]};
  }
  __syncthreads();
  const float dv = (float)ssmd[c];
  const int tile = w >> 1, a = 32 * tile + l31;
  u32x4 outv;
#pragma unroll
  for (int q = 0; q < 2; ++q) {
    const int g = 2 * (w & 1) + q;
    f32x4 s = *(lds_f4)(sm + ((0 * 2 + tile) * 4 + g) * 1024 + lane * 16);
#pragma unroll
    for (int ww = 1; ww < 4; ++ww) s += *(lds_f4)(sm + ((ww * 2 + tile) * 4 + g) * 1024 + lane * 16);
    const u32x2 uu = *(__attribute__((address_space(3))) u32x2*)(sm + CV_U + 2048 + a * 64 + g * 16 + (1 - hh) * 8);
    const float u0 = bf_hi(uu[1]), u1 = bf_lo(uu[1]), u2 = bf_hi(uu[0]), u3 = bf_lo(uu[0]);
    outv[2 * q] = pk2(rbf(s[0]) + rbf(u0 * dv), rbf(s[1]) + rbf(u1 * dv));
    outv[2 * q + 1] = pk2(rbf(s[2]) + rbf(u2 * dv), rbf(s[3]) + rbf(u3 * dv));
  }
  *(u32x4*)(ws + WS_Z + ((size_t)(a * D_ + c) * 2 + hh) * 32 + (w & 1) * 16) = outv;
}

DEVI f32x16 lo16(f32x32 v) { return __builtin_shufflevector(v, v, 0, 1, 2, 3, 4, 5, 6, 7, 8, 9, 10, 11, 12, 13, 14, 15); }
DEVI f32x16 hi16(f32x32 v) { return __builtin_shufflevector(v, v, 16, 17, 18, 19, 20, 21, 22, 23, 24, 25, 26, 27, 28, 29, 30, 31); }
DEVI bf16x16 raw_lo(f32x16 v) { return __builtin_bit_cast(bf16x16, __builtin_shufflevector(v, v, 0, 1, 2, 3, 4, 5, 6, 7)); }
DEVI bf16x16 raw_hi(f32x16 v) { return __builtin_bit_cast(bf16x16, __builtin_shufflevector(v, v, 8, 9, 10, 11, 12, 13, 14, 15)); }
DEVI f32x16 exp2v(f32x16 v) {
#pragma unroll
  for (int i = 0; i < 16; ++i) v[i] = __builtin_amdgcn_exp2f(v[i]);
  return v;
}
constexpr int SPIDX = (int)0xddddddddu;

template <int AB>
DEVI void qk_tile(s16x4 krem, s16x4 qrem, bf16x8 km, bf16x8 q0, bf16x8 q1, f32x16& s0, f32x16& s1) {
  const f32x32 z = {0};
  const f32x32 r = __builtin_amdgcn_mfma_f32_32x32x4bf16_1k(krem, qrem, z, 1, AB, 0);
  s0 = __builtin_amdgcn_mfma_f32_32x32x16_bf16(km, q0, lo16(r), 0, 0, 0);
  s1 = __builtin_amdgcn_mfma_f32_32x32x16_bf16(km, q1, hi16(r), 0, 0, 0);
}
DEVI void pv_tile(bf16x8 va, bf16x8 vb, f32x16 p, f32x16& o) {
  o = __builtin_amdgcn_smfmac_f32_32x32x32_bf16(va, raw_lo(p), o, SPIDX, 0, 0);
  o = __builtin_amdgcn_smfmac_f32_32x32x32_bf16(vb, raw_hi(p), o, SPIDX, 0, 0);
}

__global__ void __launch_bounds__(512, 2)
k2_attn(char* __restrict__ ws) {
  __shared__ __attribute__((aligned(16))) char smem[160 * 1024];
  const int tid = threadIdx.x, lane = tid & 63;
  const int w = __builtin_amdgcn_readfirstlane(tid >> 6);
  const int bid = blockIdx.x, jj = bid >> 3;
  const int qt = (bid & 7) * 4 + (jj & 3), hg = jj >> 2;
  const int head = 4 * hg + (w & 3), kh = w >> 2;
  const unsigned sbase = (unsigned)(size_t)smem;
  lds_ptr sm = (lds_ptr)smem;
  const int h = lane >> 5, l31 = lane & 31;

  u32x4 q0u, q1u;
  unsigned qr;
  float kn;
  {
    const char* p0 = ws + WS_QMAIN + ((size_t)(head * 64 + 2 * qt) << 10) + lane * 16;
    const char* p1 = ws + WS_QREM + ((size_t)head * S_ + 64 * qt + lane) * 4;
    const char* p2 = ws + WS_KNORM + lane * 4;
    asm volatile("global_load_dwordx4 %0, %4, off\n\tglobal_load_dwordx4 %1, %4, off offset:1024\n\t"
                 "global_load_dword %2, %5, off\n\tglobal_load_dword %3, %6, off"
                 : "=&v"(q0u), "=&v"(q1u), "=&v"(qr), "=&v"(kn) : "v"(p0), "v"(p1), "v"(p2) : "memory");
  }
#pragma unroll
  for (int st = 0; st < 4; ++st) {
#pragma unroll
    for (int i = 0; i < 5; ++i) {
      int p = 8 * i + w;
      p = (p > 38 ? 38 : p) + 39 * st;
      glds16(ws + WS_KV + p * 1024 + lane * 16, sbase + p * 1024);
    }
  }
  asm volatile("s_waitcnt vmcnt(20)" : "+v"(q0u), "+v"(q1u), "+v"(qr), "+v"(kn)::"memory");

  float n0 = 0.f, n1 = 0.f;
#pragma unroll
  for (int i = 0; i < 4; ++i) {
    n0 += bf_lo(q0u[i]) * bf_lo(q0u[i]) + bf_hi(q0u[i]) * bf_hi(q0u[i]);
    n1 += bf_lo(q1u[i]) * bf_lo(q1u[i]) + bf_hi(q1u[i]) * bf_hi(q1u[i]);
  }
  n0 += __shfl_xor(n0, 32);
  n1 += __shfl_xor(n1, 32);
  const float nq = (h ? n1 : n0) + bf_lo(qr) * bf_lo(qr) + bf_hi(qr) * bf_hi(qr);
#pragma unroll
  for (int o = 32; o >= 1; o >>= 1) kn = fmaxf(kn, __shfl_xor(kn, o));
  const float bound = sqrtf(nq * kn) * 1.01f;
  const bool safe = __all(bound < 48.f);
  const bf16x8 q0 = __builtin_bit_cast(bf16x8, q0u), q1 = __builtin_bit_cast(bf16x8, q1u);
  const u32x2 qrem_u = {qr, safe ? pk2(-bound, 0.f) : 0u};
  const s16x4 qrem = __builtin_bit_cast(s16x4, qrem_u);

  const int vrow = l31 < 19 ? l31 : ((l31 >= 20 && l31 < 28) ? 0 : 4);
  lds_ptr kb = sm + kh * KV_GROUP + lane * 16;
  lds_ptr vb = sm + kh * KV_GROUP + 4096 + h * 304 + vrow * 16;
  f32x16 o0 = {0}, o1 = {0};

  if (safe) {
#pragma unroll 1
    for (int it = 0; it < 8; ++it) {
      if (it == 0) WAIT_BAR(15);
      if (it == 2) WAIT_BAR(10);
      if (it == 4) WAIT_BAR(5);
      if (it == 6) WAIT_BAR(0);
      lds_ptr kg = kb + it * 2 * KV_GROUP;
      lds_ptr vg = vb + it * 2 * KV_GROUP;
      const u32x4 kr = *(__attribute__((address_space(3))) u32x4*)(kg + 8960);
      const s16x4 krA = __builtin_bit_cast(s16x4, (u32x2){kr[0], kr[1]});
      const s16x4 krB = __builtin_bit_cast(s16x4, (u32x2){kr[2], kr[3]});
      f32x16 s0, s1;
#define TILE(I, KR, AB)                                                     \
      qk_tile<AB>(KR, qrem, lds_frag(kg + I * 1024), q0, q1, s0, s1);       \
      {                                                                     \
        const bf16x8 va = lds_frag(vg + I * 1216), vv = lds_frag(vg + I * 1216 + 608); \
        pv_tile(va, vv, exp2v(s0), o0);                                     \
        pv_tile(va, vv, exp2v(s1), o1);                                     \
      }
      TILE(0, krA, 0)
      TILE(1, krA, 1)
      TILE(2, krB, 0)
      TILE(3, krB, 1)
#undef TILE
    }
  } else {
    WAIT_BAR(15);
    WAIT_BAR(10);
    WAIT_BAR(5);
    WAIT_BAR(0);
    float m0 = -INFINITY, m1 = -INFINITY;
#pragma unroll 1
    for (int t = 0; t < 64; ++t) {
      lds_ptr g = sm + (t >> 2) * KV_GROUP;
      const u32x2 kr = *(__attribute__((address_space(3))) u32x2*)(g + 8960 + (l31 + 32 * (t & 1)) * 16 + ((t >> 1) & 1) * 8);
      f32x16 s0, s1;
      qk_tile<0>(__builtin_bit_cast(s16x4, kr), qrem, lds_frag(g + (t & 3) * 1024 + lane * 16), q0, q1, s0, s1);
#pragma unroll
      for (int i = 0; i < 16; ++i) { m0 = fmaxf(m0, s0[i]); m1 = fmaxf(m1, s1[i]); }
    }
    m0 = fmaxf(m0, __shfl_xor(m0, 32));
    m1 = fmaxf(m1, __shfl_xor(m1, 32));
#pragma unroll 1
    for (int t = 0; t < 32; ++t) {
      lds_ptr g = sm + (2 * (t >> 2) + kh) * KV_GROUP;
      const u32x2 kr = *(__attribute__((address_space(3))) u32x2*)(g + 8960 + (l31 + 32 * (t & 1)) * 16 + ((t >> 1) & 1) * 8);
      f32x16 s0, s1;
      qk_tile<0>(__builtin_bit_cast(s16x4, kr), qrem, lds_frag(g + (t & 3) * 1024 + lane * 16), q0, q1, s0, s1);
      lds_ptr vg = g + 4096 + h * 304 + vrow * 16 + (t & 3) * 1216;
      const bf16x8 va = lds_frag(vg), vv = lds_frag(vg + 608);
      pv_tile(va, vv, exp2v(s0 - m0), o0);
      pv_tile(va, vv, exp2v(s1 - m1), o1);
    }
  }

  typedef __attribute__((address_space(3))) f32x4* lds_f4;
  __syncthreads();
  {
    const f32x16 give = kh ? o0 : o1;
    lds_ptr p = sm + w * 3072 + lane * 16;
#pragma unroll
    for (int g = 0; g < 3; ++g) *(lds_f4)(p + g * 1024) = (f32x4){give[4 * g], give[4 * g + 1], give[4 * g + 2], give[4 * g + 3]};
  }
  __syncthreads();
  {
    const f32x16 keep = kh ? o1 : o0;
    lds_ptr p = sm + (w ^ 4) * 3072 + lane * 16;
    float o[12];
#pragma unroll
    for (int g = 0; g < 3; ++g) {
      const f32x4 t = *(lds_f4)(p + g * 1024);
#pragma unroll
      for (int i = 0; i < 4; ++i) o[4 * g + i] = keep[4 * g + i] + t[i];
    }
    const float l = __shfl(o[10], l31);
    const float inv = 1.f / l;
    u32x4 a;
#pragma unroll
    for (int i = 0; i < 4; ++i) a[i] = pk2(o[2 * i] * inv, o[2 * i + 1] * inv);
    char* ob = ws + WS_O + (size_t)(2 * qt + kh) * 36 * 1024;
    *(u32x4*)(ob + head * 1024 + lane * 16) = a;
    if (!h) *(unsigned*)(ob + (32 + (hg >> 1)) * 1024 + (l31 + 32 * (hg & 1)) * 16 + (w & 3) * 4) = pk2(o[8] * inv, o[9] * inv);
  }
}

constexpr int K3_W = 576 * 144;
constexpr int K3_LDS = K3_W + 1024 + 64 * 72 * 4;

__global__ void __launch_bounds__(384, 2)
k3_out(const __bf16* __restrict__ x, const __bf16* __restrict__ wo, const char* __restrict__ ws, __bf16* __restrict__ out) {
  __shared__ __attribute__((aligned(16))) char smem[K3_LDS];
  const int tid = threadIdx.x, lane = tid & 63;
  const int w = __builtin_amdgcn_readfirstlane(tid >> 6);
  const int bid = blockIdx.x, jj = bid >> 3;
  const int qt = (bid & 7) * 4 + (jj & 3), cg = jj >> 2;
  const unsigned sbase = (unsigned)(size_t)smem;
  lds_ptr sm = (lds_ptr)smem;
  const int h = lane >> 5, l31 = lane & 31;
#pragma unroll
  for (int i = 0; i < 14; ++i) {
    const int p = 6 * i + w;
    if (p < 81) {
      const int n = p * 64 + lane, row = n / 9, pc = n - row * 9;
      glds16(wo + (size_t)row * D_ + 72 * cg + 8 * pc, sbase + p * 1024);
    }
  }
  const int ct = w >> 1, tt = w & 1, T = 2 * qt + tt;
  const char* ob = ws + WS_O + (size_t)T * 36 * 1024 + lane * 16;
  bf16x8 of[36];
#pragma unroll
  for (int k = 0; k < 36; ++k) of[k] = *(const bf16x8*)(ob + k * 1024);
  const int col = 32 * ct + l31;
  const int colc = col < 72 ? col : 64 + (col & 7);
  const u32x4* zp = (const u32x4*)(ws + WS_Z + ((size_t)(T * D_ + 72 * cg + colc) * 2 + h) * 32);
  const u32x4 z0 = zp[0], z1 = zp[1];
  f32x4 gq[4];
#pragma unroll
  for (int g = 0; g < 4; ++g) gq[g] = *(const f32x4*)(ws + WS_G + (32 * T + 8 * g + 4 * h) * 4);
  WAIT_BAR(0);
  const int q = (lane & 15) >> 2;
  int cb = 32 * ct + ((lane >> 4) & 1) * 16 + (lane & 3) * 4;
  if (cb >= 72) cb = 64 + (cb & 4);
  lds_ptr wm = sm + (4 * h + q) * 144 + cb * 2;
  lds_ptr wr = sm + (DH * (4 * h + (q >> 1)) + 16 + (q & 1)) * 144 + cb * 2;
  f32x16 acc = {0};
#pragma unroll
  for (int k = 0; k < 32; ++k)
    acc = __builtin_amdgcn_mfma_f32_32x32x16_bf16(of[k], tr_frag(wm + k * DH * 144, wm + (k * DH + 8) * 144), acc, 0, 0, 0);
#pragma unroll
  for (int k = 0; k < 4; ++k)
    acc = __builtin_amdgcn_mfma_f32_32x32x16_bf16(of[32 + k], tr_frag(wr + k * 8 * DH * 144, wr + (k * 8 + 2) * DH * 144), acc, 0, 0, 0);
  lds_ptr st = sm + K3_W + 1024;
  if (col < 72) {
#pragma unroll
    for (int r = 0; r < 16; ++r) {
      const int tok = 32 * tt + (r & 3) + 8 * (r >> 2) + 4 * h;
      const unsigned zz = r < 8 ? z0[r >> 1] : z1[(r - 8) >> 1];
      const unsigned zb = (r & 1) ? (zz & 0xffff0000u) : (zz << 16);
      const unsigned ga = pk2(gq[r >> 2][r & 3] * rbf(acc[r]), 0.f);
      *(__attribute__((address_space(3))) unsigned*)(st + (tok * 72 + col) * 4) = zb | ga;
    }
  }
  __syncthreads();
  for (int n = tid; n < 64 * 9; n += 384) {
    const int tok = n / 9, pc = n - tok * 9;
    const int t = 64 * qt + tok;
    const u32x4 xv = *(const u32x4*)(x + (size_t)t * D_ + 72 * cg + 8 * pc);
    const float omg = rbf(1.f - *(const float*)(ws + WS_G + t * 4));
    const u32x4 s0 = *(__attribute__((address_space(3))) u32x4*)(st + (tok * 72 + 8 * pc) * 4);
    const u32x4 s1 = *(__attribute__((address_space(3))) u32x4*)(st + (tok * 72 + 8 * pc) * 4 + 16);
    u32x4 o;
#pragma unroll
    for (int i = 0; i < 4; ++i) {
      const unsigned e0 = i < 2 ? s0[2 * i] : s1[2 * i - 4], e1 = i < 2 ? s0[2 * i + 1] : s1[2 * i - 3];
      const float b0 = rbf(bf_lo(xv[i]) * bf_hi(e0)), b1 = rbf(bf_hi(xv[i]) * bf_hi(e1));
      o[i] = pk2(bf_lo(e0) + rbf(omg * b0), bf_lo(e1) + rbf(omg * b1));
    }
    *(u32x4*)(out + (size_t)t * D_ + 72 * cg + 8 * pc) = o;
  }
}

extern "C" void kernel_launch(void* const* d_in, const int* in_sizes, int n_in, void* d_out, int out_size,
                              void* d_ws, size_t ws_size, hipStream_t stream) {
  const __bf16* x = (const __bf16*)d_in[0];
  const __bf16* wq = (const __bf16*)d_in[1];
  const __bf16* wk = (const __bf16*)d_in[2];
  const __bf16* wv = (const __bf16*)d_in[3];
  const __bf16* wo = (const __bf16*)d_in[4];
  const __bf16* bk = (const __bf16*)d_in[6];
  const __bf16* bv = (const __bf16*)d_in[7];
  const __bf16* shk = (const __bf16*)d_in[8];
  const __bf16* ssmk = (const __bf16*)d_in[9];
  const __bf16* ssmd = (const __bf16*)d_in[10];
  const __bf16* gw = (const __bf16*)d_in[11];
  char* ws = (char*)d_ws;
  k1_proj<<<256, 512, 0, stream>>>(x, wq, wk, wv, bk, bv, shk, gw, ws);
  k_conv<<<576, 256, 0, stream>>>(ssmk, ssmd, ws);
  k2_attn<<<256, 512, 0, stream>>>(ws);
  k3_out<<<256, 384, 0, stream>>>(x, wo, ws, (__bf16*)d_out);
}
```

```cpp
#include <hip/hip_runtime.h>
#include <hip/hip_bf16.h>
#include <stdint.h>

typedef __attribute__((ext_vector_type(2))) __bf16 bf16x2;
typedef __attribute__((ext_vector_type(4))) __bf16 bf16x4;
typedef __attribute__((ext_vector_type(8))) __bf16 bf16x8;
typedef __attribute__((ext_vector_type(16))) __bf16 bf16x16;
typedef __attribute__((ext_vector_type(4))) short s16x4;
typedef __attribute__((ext_vector_type(2))) float f32x2;
typedef __attribute__((ext_vector_type(4))) float f32x4;
typedef __attribute__((ext_vector_type(16))) float f32x16;
typedef __attribute__((ext_vector_type(32))) float f32x32;
typedef __attribute__((ext_vector_type(2))) unsigned u32x2;
typedef __attribute__((ext_vector_type(4))) unsigned u32x4;
#define LAS __attribute__((address_space(3)))
#define GAS __attribute__((address_space(1)))
typedef LAS char* lds_ptr;
typedef GAS unsigned gu32;
typedef GAS unsigned long long gu64;
#define DEVI __device__ __forceinline__
#define RLX_AGENT __ATOMIC_RELAXED, __HIP_MEMORY_SCOPE_AGENT
#define RLX_WG __ATOMIC_RELAXED, __HIP_MEMORY_SCOPE_WORKGROUP

constexpr int S_ = 2048, D_ = 576, DH = 18;

constexpr int KV_GROUP = 9984;
constexpr int WS_KV = 0;
constexpr int WS_EPOCH = 192 * 1024;
constexpr int WS_FKV = WS_EPOCH + 1024;
constexpr int WS_FU = WS_FKV + 8192;
constexpr int WS_FZ = WS_FU + 32768;
constexpr int WS_FO = WS_FZ + 32768;
constexpr int WS_UT = 384 * 1024;
constexpr int WS_Z = WS_UT + D_ * S_ * 2;
constexpr int WS_O = WS_Z + D_ * S_ * 2;
constexpr int WS_ZP = WS_O + D_ * S_ * 2;
constexpr int WS_END = WS_ZP + 64 * 4 * 8192;

constexpr int RSLOT = 2 * KV_GROUP;
constexpr int L_X = 0;
constexpr int L_GW = 78848;
constexpr int L_B = 80000;
constexpr int L_M = 162944;
constexpr int CV_COPY = 4160;
constexpr int CV_H = L_B, CV_HB = 8 * CV_COPY, CV_U = L_B + 2 * CV_HB, CV_UB = 7680;
constexpr int L_Q = L_B + 32768;
constexpr int M_DONE = 0, M_C1 = 32, M_C2 = 36, M_C3 = 40, M_C3B = 44, M_KVC = 48, M_KVR = 52, M_UR = 56, M_OR = 60, M_RDY = 64,
              M_QD = 100, M_UD = 104, M_HS = 108, M_ZR = 112, M_BT = 116, M_WO = 120, M_OD = 124, M_PAIR = 128, M_TAGR = 144,
              M_TAG = 148, M_KN = 152, M_EXD = 156, M_KVP = 160, M_KN0 = 164, M_KD = 168, M_QR = 172,
              M_N2 = 176, M_G = 256;
constexpr int L_EX = 0, L_BT = 28672;
#define CV_PRIO 3
#define CV_SLEEP 0
#define POLL_AT M_C2
#define POLL_N 3
#define BUBBLE 0
#define POLL_SLEEP 4
#define BT_AT 28
constexpr unsigned SPIN_CAP = 1u << 18;

#define STAMP_DECL do {} while (0)
#define STAMP(I) do {} while (0)
#define STAMP_FLUSH(KID) do {} while (0)
#define RSTAMP(I) do {} while (0)

#define PSTAMP(I) do {} while (0)
#define NSTAMP(I) STAMP(I)
#define CSTAMP(I) do {} while (0)
#define HSTAMP(I) NSTAMP(I)

DEVI unsigned pk2(float a, float b) {
  bf16x2 v = {(__bf16)a, (__bf16)b};
  return __builtin_bit_cast(unsigned, v);
}
DEVI float u2f(unsigned u) { return __builtin_bit_cast(float, u); }
DEVI float bf_lo(unsigned u) { return __builtin_bit_cast(float, u << 16); }
DEVI float bf_hi(unsigned u) { return __builtin_bit_cast(float, u & 0xffff0000u); }
DEVI float dot2(unsigned u, float c) { const bf16x2 v = __builtin_bit_cast(bf16x2, u); return __builtin_amdgcn_fdot2_f32_bf16(v, v, c, false); }
DEVI float rbf(float a) { return (float)(__bf16)a; }
DEVI int swap23(int r) { return (r & ~12) | ((r & 4) << 1) | ((r & 8) >> 1); }
DEVI float xhalf(float v) {
  const unsigned u = __builtin_bit_cast(unsigned, v);
  const auto r = __builtin_amdgcn_permlane32_swap(u, u, false, false);
  return __builtin_bit_cast(float, (threadIdx.x & 32) ? r[0] : r[1]);
}

template <int CTRL>
DEVI float dpp_max(float v) {
  const int u = __builtin_bit_cast(int, v);
  return fmaxf(v, __builtin_bit_cast(float, __builtin_amdgcn_update_dpp(u, u, CTRL, 0xf, 0xf, false)));
}
DEVI float max16(float v) {
  return dpp_max<0x140>(dpp_max<0x141>(dpp_max<0x4e>(dpp_max<0xb1>(v))));
}
DEVI float wave_max(float v) {
  v = max16(v);
  const unsigned u = __builtin_bit_cast(unsigned, v);
  return fmaxf(fmaxf(u2f(__builtin_amdgcn_readlane(u, 0)), u2f(__builtin_amdgcn_readlane(u, 16))),
               fmaxf(u2f(__builtin_amdgcn_readlane(u, 32)), u2f(__builtin_amdgcn_readlane(u, 48))));
}
DEVI void glds(const void* sbase, unsigned voff, unsigned lds_dst) {
  asm volatile("s_mov_b32 m0, %2\n\ts_nop 0\n\tglobal_load_lds_dwordx4 %0, %1" :: "v"(voff), "s"(sbase), "s"(lds_dst) : "memory");
}
DEVI void glds_sc1(const void* sbase, unsigned voff, unsigned lds_dst) {
  asm volatile("s_mov_b32 m0, %2\n\ts_nop 0\n\tglobal_load_lds_dwordx4 %0, %1 sc1" :: "v"(voff), "s"(sbase), "s"(lds_dst) : "memory");
}
#define VM_WAIT(N) asm volatile("s_waitcnt vmcnt(" #N ")" ::: "memory")
#define BAR() asm volatile("s_waitcnt lgkmcnt(0)\n\ts_barrier" ::: "memory")

DEVI bf16x8 tr_frag(lds_ptr p0, lds_ptr p1) {
  bf16x4 a = __builtin_amdgcn_ds_read_tr16_b64_v4bf16((LAS bf16x4*)p0);
  bf16x4 b = __builtin_amdgcn_ds_read_tr16_b64_v4bf16((LAS bf16x4*)p1);
  return __builtin_shufflevector(a, b, 0, 1, 2, 3, 4, 5, 6, 7);
}
DEVI bf16x8 lds_frag(lds_ptr p) { return *(LAS bf16x8*)p; }
DEVI void lds_signal(lds_ptr p, unsigned n = 1u) {
  asm volatile("s_waitcnt lgkmcnt(0)\n\ts_mov_b64 exec, 1\n\tds_add_u32 %0, %1\n\ts_mov_b64 exec, -1" :: "v"(p), "v"(n) : "memory");
}
DEVI unsigned lds_ticket(lds_ptr p) {
  unsigned old;
  asm volatile("s_waitcnt lgkmcnt(0)\n\ts_mov_b64 exec, 1\n\tds_add_rtn_u32 %0, %1, %2\n\ts_mov_b64 exec, -1\n\ts_waitcnt lgkmcnt(0)"
               : "=&v"(old) : "v"(p), "v"(1u) : "memory");
  return __builtin_amdgcn_readfirstlane(old);
}
template <int SLEEP = 0>
DEVI void lds_wait(lds_ptr p, unsigned n) {
  unsigned v, t;
  asm volatile("1:\n\tds_read_b32 %0, %2\n\ts_waitcnt lgkmcnt(0)\n\tv_readfirstlane_b32 %1, %0\n\ts_nop 0\n\ts_cmp_ge_u32 %1, %3\n\t"
               "s_cbranch_scc1 2f\n\ts_sleep %4\n\ts_branch 1b\n2:"
               : "=&v"(v), "=&s"(t) : "v"(p), "s"(n), "i"(SLEEP) : "scc", "memory");
}
DEVI unsigned lds_peek(lds_ptr p) {
  unsigned v;
  asm volatile("ds_read_b32 %0, %1\n\ts_waitcnt lgkmcnt(0)" : "=&v"(v) : "v"(p) : "memory");
  return __builtin_amdgcn_readfirstlane(v);
}
#define LD_SC1_16(off) __builtin_bit_cast(u32x4, __builtin_amdgcn_raw_buffer_load_b128(rs, (off), 0, 16))
#define ST_SC1_16(v, off) __builtin_amdgcn_raw_buffer_store_b128((v), rs, (off), 0, 16)
#define ST_SC1_8(v, off) __builtin_amdgcn_raw_buffer_store_b64((v), rs, (off), 0, 16)
#define ST_SC1_4(v, off) __builtin_amdgcn_raw_buffer_store_b32((v), rs, (off), 0, 16)

DEVI f32x16 lo16(f32x32 v) { return __builtin_shufflevector(v, v, 0, 1, 2, 3, 4, 5, 6, 7, 8, 9, 10, 11, 12, 13, 14, 15); }
DEVI f32x16 hi16(f32x32 v) { return __builtin_shufflevector(v, v, 16, 17, 18, 19, 20, 21, 22, 23, 24, 25, 26, 27, 28, 29, 30, 31); }
DEVI bf16x16 raw_lo(f32x16 v) { return __builtin_bit_cast(bf16x16, __builtin_shufflevector(v, v, 0, 1, 2, 3, 4, 5, 6, 7)); }
DEVI bf16x16 raw_hi(f32x16 v) { return __builtin_bit_cast(bf16x16, __builtin_shufflevector(v, v, 8, 9, 10, 11, 12, 13, 14, 15)); }
constexpr int SPIDX = (int)0xddddddddu;
#define MFMA32(a, b, c) __builtin_amdgcn_mfma_f32_32x32x16_bf16((a), (b), (c), 0, 0, 0)
#define MFMA16(a, b, c) __builtin_amdgcn_mfma_f32_16x16x32_bf16((a), (b), (c), 0, 0, 0)

DEVI void online_step(f32x16 s, float& m, f32x16& o, bf16x8 va, bf16x8 vb) {
  float t = s[0];
#pragma unroll
  for (int i = 1; i < 16; ++i) t = fmaxf(t, s[i]);
  t = fmaxf(t, xhalf(t));
  const float mn = fmaxf(m, t);
  const float al = __builtin_amdgcn_exp2f(m - mn);
  m = mn;
#pragma unroll
  for (int i = 0; i < 16; ++i) { o[i] *= al; s[i] = __builtin_amdgcn_exp2f(s[i] - mn); }
  o = __builtin_amdgcn_smfmac_f32_32x32x32_bf16(va, raw_lo(s), o, SPIDX, 0, 0);
  o = __builtin_amdgcn_smfmac_f32_32x32x32_bf16(vb, raw_hi(s), o, SPIDX, 0, 0);
}

#define UNIT_LOAD(CP, TG)                                                                                        \
  const int uc0 = 72 * hg + 2 * (CP), ut0 = 64 * qt + 8 * (TG);                                                  \
  const u32x4 usw = *(const u32x4*)(shk + uc0 * 4);                                                              \
  unsigned ukr[11], uvr[8];                                                                                      \
  {                                                                                                              \
    const unsigned bo = (ut0 * D_ + uc0) * 2, bo3 = ut0 ? bo - 3 * 1152 : bo;                                    \
    _Pragma("unroll") for (int i = 0; i < 3; ++i) ukr[i] = *(const unsigned*)((const char*)bk + bo3 + i * 1152); \
    _Pragma("unroll") for (int i = 0; i < 8; ++i) {                                                              \
      ukr[3 + i] = *(const unsigned*)((const char*)bk + bo + i * 1152);                                          \
      uvr[i] = *(const unsigned*)((const char*)bv + bo + i * 1152);                                              \
    }                                                                                                            \
  }
#define UNIT_DO()                                                                                                \
  u32x4 o0, o1;                                                                                                  \
  {                                                                                                              \
    const f32x2 w3 = {bf_hi(usw[1]), bf_hi(usw[3])}, w2 = {bf_lo(usw[1]), bf_lo(usw[3])};                        \
    const f32x2 w1 = {bf_hi(usw[0]), bf_hi(usw[2])}, w0 = {bf_lo(usw[0]), bf_lo(usw[2])};                        \
    f32x2 kf[11], r[8];                                                                                          \
    _Pragma("unroll") for (int i = 0; i < 11; ++i) {                                                             \
      const unsigned kk = (i < 3 && !ut0) ? 0u : ukr[i];                                                         \
      kf[i] = (f32x2){bf_lo(kk), bf_hi(kk)};                                                                     \
    }                                                                                                            \
    _Pragma("unroll") for (int i = 0; i < 8; ++i)                                                                \
      r[i] = __builtin_elementwise_fma(kf[i + 3], w0, __builtin_elementwise_fma(kf[i + 2], w1,                   \
             __builtin_elementwise_fma(kf[i + 1], w2, kf[i] * w3))) * (f32x2){bf_lo(uvr[i]), bf_hi(uvr[i])};     \
    _Pragma("unroll") for (int i = 0; i < 4; ++i) {                                                              \
      o0[i] = pk2(r[7 - 2 * i][0], r[6 - 2 * i][0]);                                                             \
      o1[i] = pk2(r[7 - 2 * i][1], r[6 - 2 * i][1]);                                                             \
    }                                                                                                            \
  }

__global__ void __launch_bounds__(768, 3)
fused_kernel(const __bf16* __restrict__ x, const __bf16* __restrict__ wq, const __bf16* __restrict__ wk,
             const __bf16* __restrict__ wv, const __bf16* __restrict__ wo, const __bf16* __restrict__ bk,
             const __bf16* __restrict__ bv, const __bf16* __restrict__ shk, const __bf16* __restrict__ ssmk,
             const __bf16* __restrict__ ssmd, const __bf16* __restrict__ gw, __bf16* __restrict__ out, char* ws) {
  __shared__ __attribute__((aligned(16))) char smem[160 * 1024];
  asm volatile("fk_beg:");
  STAMP_DECL; STAMP(0);
  const int tid = threadIdx.x, lane = tid & 63;
  const int w = __builtin_amdgcn_readfirstlane(tid >> 6);
  const int bid = blockIdx.x, jj = bid >> 3;
  const int qt = (bid & 7) * 4 + (jj & 3), hg = jj >> 2;
  const unsigned sbase = (unsigned)(size_t)smem;
  lds_ptr sm = (lds_ptr)smem;
  lds_ptr mi = sm + L_M;
  const int h = lane >> 5, l31 = lane & 31, l15 = lane & 15, g4 = lane >> 4;
  const int tq = (lane & 15) >> 2, tp = lane & 3;
  const auto rs = __builtin_amdgcn_make_buffer_rsrc(ws, 0, WS_END, 0x00020000);
  GAS char* gws = (GAS char*)ws;
  const int rot = (hg + qt) % 3;
  const int s0 = bid & 7;
  const int pt = (hg >> 1) & 1, sel = hg & 1;
  const char* xt = (const char*)x + (size_t)qt * 73728;
  const char* wqs = (const char*)wq + 144 * hg;
  const unsigned vxe = (lane >> 3) * 1152 + (((lane & 7) ^ g4) << 4), vxo = 9216 + (lane >> 3) * 1152 + (((lane & 7) ^ (4 + g4)) << 4);
  const int otok = tid / 9, opc = tid - 9 * otok;

  if (w >= 8) {
    const int j = w - 8, ht = tid - 512;
    __builtin_amdgcn_s_setprio(3);
    if (j < 3) {
      const int th = (j + rot) % 3;
      const unsigned stg = L_B + 2 * 27648 + j * 9216;
#pragma unroll
      for (int c = 0; c < 6; ++c) {
        const char* src = xt + (2 * pt + (c & 1)) * 18432 + (3 * th + (c >> 1)) * 128;
        const unsigned dst = sbase + L_X + (3 * j + (c >> 1)) * 8192 + (2 * pt + (c & 1)) * 2048;
        glds(src, vxe, dst);
        glds(src, vxo, dst + 1024);
      }
      if (hg < 4) {
        const char* wsrc = (const char*)(sel ? wv : wk) + th * 6912;
#pragma unroll
        for (int i = 0; i < 3; ++i) {
          const int f = 64 * i + lane, row = f / 3, pc = f - 3 * row;
          const unsigned vo = row * 36 + (pc == 2 ? 20 : 16 * pc);
#pragma unroll
          for (int p = 0; p < 3; ++p) glds(wsrc + p * 2304, vo, sbase + stg + p * 3072 + i * 1024);
        }
      }
      BAR();
      VM_WAIT(0);
      STAMP(1);
      lds_signal(mi + M_KD);
      lds_signal(mi + M_C1);
      if (hg < 4) lds_wait<1>(mi + M_KVC, 4);
      STAMP(2);
      {
        const char* src = wqs + (size_t)(192 * ((2 + rot) % 3) + 64 * j) * 1152;
        int pc = lane % 9;
        unsigned vo = (lane / 9) * 1152 + pc * 16;
#pragma unroll
        for (int i = 0; i < 9; ++i) {
          glds(src, vo, sbase + stg + i * 1024);
          vo += pc == 8 ? 9088 : 8080;
          pc = pc == 8 ? 0 : pc + 1;
        }
      }
      VM_WAIT(0);
      lds_signal(mi + M_C3);
      lds_wait<1>(mi + M_KVR, 1);
    } else {
      const unsigned epoch = __hip_atomic_load((gu32*)(gws + WS_EPOCH), RLX_AGENT);
      unsigned pf = 0;
      {
        unsigned long long pc0;
        unsigned back, len;
        asm volatile("s_getpc_b64 %0\n\tfk_pc:\n\ts_mov_b32 %1, fk_pc-fk_beg\n\ts_mov_b32 %2, fk_end-fk_beg" : "=s"(pc0), "=s"(back), "=s"(len));
        const unsigned o = min((unsigned)(lane * 8 + (jj & 7)) * 128u, len);
        asm volatile("global_load_dword %0, %1, %2" : "=v"(pf) : "v"(o), "s"(pc0 - back) : "memory");
      }
      BAR();
      asm volatile("s_waitcnt vmcnt(0)" : "+v"(pf) :: "memory");
      const unsigned tg = __builtin_amdgcn_readfirstlane(epoch) + 1u;
      if (lane == 0) *(LAS unsigned*)(mi + M_TAG) = tg;
      lds_signal(mi + M_TAGR);
      lds_wait<1>(mi + POLL_AT, POLL_N);
      PSTAMP(5);
      float kn = 0.f;
#pragma unroll 1
      for (unsigned spins = 0; spins < SPIN_CAP; ++spins) {
        u32x4 v0 = {0, tg, 0, tg};
        if (lane < 8) v0 = LD_SC1_16(WS_FKV + (8 * s0 + lane) * 128);
        const bool ok = v0[1] == tg && v0[3] == tg;
        kn = u2f(v0[0]);
        asm volatile("" ::: "memory");
        if (spins == 0) PSTAMP(6);
        if (spins == 1) PSTAMP(7);
        if (spins == 2) PSTAMP(8);
        if (spins == 3) PSTAMP(9);
        if (__all(ok)) break;
        __builtin_amdgcn_s_sleep(POLL_SLEEP);
      }
      lds_signal(mi + M_KVR);
      STAMP(2); RSTAMP(2);
      kn = max16(kn);
      if (lane == 0) *(LAS float*)(mi + M_KN0) = kn;
    }
    const unsigned tag = *(LAS unsigned*)(mi + M_TAG);
    const int kvo = j * 1024 + lane * 16;
    const bool kvlast = kvo + 4 * 4096 < RSLOT;
    const int kvo4 = kvlast ? kvo + 16384 : kvo;
    auto kv_dma = [&](int st, int p0, int step) {
      const char* src = ws + WS_KV + ((s0 + st) & 7) * RSLOT;
      const unsigned dst = sbase + (st & 3) * RSLOT;
#pragma unroll 1
      for (int p = p0; p < 19; p += step) glds_sc1(src + p * 1024, lane * 16, dst + p * 1024);
      if (j == 3 && lane < 32) glds_sc1(src + 19456, lane * 16, dst + 19456);
    };
    {
      u32x4 ra[5];
#pragma unroll
      for (int i = 0; i < 5; ++i) ra[i] = LD_SC1_16(WS_KV + s0 * RSLOT + (i < 4 ? kvo + i * 4096 : kvo4));
      UNIT_LOAD(l31, 2 * j + h);
      lds_wait(mi + M_C3B, 8);
#pragma unroll
      for (int i = 0; i < 5; ++i) if (i < 4 || kvlast) *(LAS u32x4*)(sm + kvo + i * 4096) = ra[i];
      lds_signal(mi + M_QD);
      STAMP(3);
      if (j == 3) {
        float kn = 0.f;
#pragma unroll 1
        for (unsigned spins = 0; spins < SPIN_CAP; ++spins) {
          const u32x4 v0 = LD_SC1_16(WS_FKV + lane * 128);
          const bool ok = v0[1] == tag && v0[3] == tag;
          kn = u2f(v0[0]);
          asm volatile("" ::: "memory");
          if (__all(ok)) break;
          __builtin_amdgcn_s_sleep(POLL_SLEEP);
        }
        kv_dma(1, 0, 1);
        kn = wave_max(kn);
        if (lane == 0) *(LAS float*)(mi + M_KN) = kn;
      }
      UNIT_DO();
      if (j == 3) { VM_WAIT(0); lds_signal(mi + M_RDY + 4, 4); }
      ST_SC1_16(o0, WS_UT + (qt * 8 + hg) * 9216 + j * 2048 + lane * 16);
      ST_SC1_16(o1, WS_UT + (qt * 8 + hg) * 9216 + j * 2048 + 1024 + lane * 16);
      STAMP(4);
      lds_wait<1>(mi + M_QR, 8);
      VM_WAIT(0);
      if (lds_ticket(mi + M_UD) == 3u && lane == 0) __hip_atomic_store((gu32*)(gws + WS_FU) + (hg * 32 + qt) * 32, tag, RLX_AGENT);
      if (j == 3) {
        kv_dma(2, 0, 1);
        kv_dma(3, 0, 1);
        VM_WAIT(0);
        lds_signal(mi + M_RDY + 8, 4);
        lds_signal(mi + M_RDY + 12, 4);
      }
    }
    int np = 4, pend = 0;
    auto service = [&]() {
      if (pend) { VM_WAIT(0); lds_signal(mi + M_RDY + 4 * pend); pend = 0; }
      if (np < 8 && lds_peek(mi + M_DONE + 4 * (np - 4)) == 8u) { kv_dma(np, j, 4); pend = np++; }
    };
#define NPH 2
    const u32x4 zero4 = {0, 0, 0, 0};
    const unsigned vh = (ht == 255 ? 254 : ht) * 16;
    auto hload = [&](int cl, int b) {
      const char* hsrc = (const char*)(ssmk + (size_t)(72 * hg + cl) * S_);
#pragma unroll
      for (int r = 0; r < 8; ++r) glds(hsrc + 2 * r + 2, vh, sbase + CV_H + b * CV_HB + r * CV_COPY + 64 + j * 1024);
    };
    auto uload = [&](int cl) {
      const int tgq = ht & 7;
      const int o = cl < 64 ? (tgq >> 1) * 2048 + (cl & 1) * 1024 + ((tgq & 1) * 32 + (cl >> 1)) * 16
                            : 8192 + (cl & 1) * 512 + (tgq * 4 + ((cl - 64) >> 1)) * 16;
      return LD_SC1_16(WS_UT + ((ht >> 3) * 8 + hg) * 9216 + o);
    };
    if (j == 0) {
      const float lg = *(LAS float*)(mi + M_G + lane * 4);
      *(LAS float*)(mi + M_G + lane * 4) = rbf(__builtin_amdgcn_rcpf(1.f + __expf(-rbf(lg))));
    }
    hload(2 * qt, 0);
    hload(2 * qt + 1, 1);
    *(LAS u32x4*)(sm + CV_U + (ht >> 7) * CV_UB + ((ht & 127) >> 2) * 80 + (ht & 3) * 16) = zero4;
    if (j == 3) {
      lds_wait<2>(mi + M_UD, 4);
#pragma unroll 1
      for (unsigned spins = 0; spins < SPIN_CAP; ++spins) {
        bool ok = h || __hip_atomic_load((gu32*)(gws + WS_FU) + (hg * 32 + lane) * 32, RLX_AGENT) == tag;
        if (bid == 0) {
#pragma unroll
          for (int i = 0; i < 4; ++i) ok &= __hip_atomic_load((gu32*)(gws + WS_FU) + (64 * i + lane) * 32, RLX_AGENT) == tag;
        }
        if (__all(ok)) break;
        __builtin_amdgcn_s_sleep(4);
      }
      if (bid == 0 && lane == 0) __hip_atomic_store((gu32*)(gws + WS_EPOCH), tag, RLX_AGENT);
      lds_signal(mi + M_UR);
    } else {
      lds_wait<2>(mi + M_UR, 1);
    }
    unsigned hs = 0;
#define HSYNC0() do { lds_signal(mi + M_HS); hs += 4; lds_wait<1>(mi + M_HS, hs); } while (0)
#define HSYNC() do { lds_signal(mi + M_HS); service(); hs += 4; lds_wait<1>(mi + M_HS, hs); } while (0)
    NSTAMP(5);
#pragma unroll 1
    for (int ph = 0; ph < NPH; ++ph) {
      const int b = ph ? 0 : j >> 1, cl = ph ? 64 + (qt >> 2) : 2 * qt + b, d0 = ph ? 8 * (qt & 3) + 2 * j : 16 * (j & 1);
      const float dv = (ph && (qt & 3)) ? 0.f : (float)ssmd[72 * hg + cl];
      if (ph == 0) CSTAMP(1);
      {
        const unsigned short h8 = *(const unsigned short*)(ssmk + (size_t)(72 * hg + cl) * S_ + (lane & 7));
        const u32x4 ua = uload(ph ? cl : 2 * qt), ub = uload(2 * qt + 1);
        if (!(j & 1)) {
          lds_ptr hz = sm + CV_H + b * CV_HB + (lane >> 3) * CV_COPY;
          *(LAS u32x2*)(hz + (lane & 7) * 8) = (u32x2){0, 0};
          if ((lane & 7) <= (lane >> 3)) *(LAS unsigned short*)(hz + 48 + (7 - (lane >> 3) + (lane & 7)) * 2) = h8;
        }
        *(LAS u32x4*)(sm + CV_U + 2560 + (ht >> 2) * 80 + (ht & 3) * 16) = ua;
        *(LAS u32x4*)(sm + CV_U + CV_UB + 2560 + (ht >> 2) * 80 + (ht & 3) * 16) = ub;
      }
      VM_WAIT(0);
      if (ph == 0) CSTAMP(2);
      HSYNC();
      if (ph == 0) CSTAMP(3);
      lds_ptr ub = sm + CV_U + b * CV_UB + 2560;
      lds_ptr ab = sm + CV_H + b * CV_HB + (l31 & 7) * CV_COPY + (h + (l31 >> 3)) * 16 + 64 * d0;
      lds_ptr bb = ub + (l31 - d0 - 2) * 80 + (1 - h) * 16;
      f32x16 y0 = {0}, y1 = {0};
      bf16x8 fx[8], fy[8];
#define CV_LD(F, D) _Pragma("unroll") for (int s = 0; s < 2; ++s) {                                     \
        F[4 * s] = lds_frag(ab + 32 * (2 * (D) + 1 - s)); F[4 * s + 1] = lds_frag(bb + 80 * (2 - (D)) + 32 * s);  \
        F[4 * s + 2] = lds_frag(bb + 80 * (34 - (D)) + 32 * s); F[4 * s + 3] = lds_frag(ab + 32 * (2 * (D) + 65 - s)); } \
        __builtin_amdgcn_sched_barrier(0)
#define CV_MM(F) _Pragma("unroll") for (int s = 0; s < 2; ++s) {                                        \
        y0 = MFMA32(F[4 * s], F[4 * s + 1], y0); y1 = MFMA32(F[4 * s], F[4 * s + 2], y1); y1 = MFMA32(F[4 * s + 3], F[4 * s + 1], y1); } \
        __builtin_amdgcn_sched_barrier(0)
      __builtin_amdgcn_s_setprio(CV_PRIO);
      CV_LD(fx, 0);
#pragma unroll 1
      for (int it = ph ? 7 : 0; it < 8; ++it) {
        CV_LD(fy, 1);
        CV_MM(fx);
        CV_LD(fx, 2);
        CV_MM(fy);
        ab += 128;
        bb -= 160;
        if (it == 3) service();
      }
      __builtin_amdgcn_s_setprio(3);
      if (ph == 0) CSTAMP(4);
      HSYNC();
      if (ph == 0 && NPH == 2) hload(64 + (qt >> 2), 0);
      lds_ptr px = sm + CV_H + CV_HB + lane * 16;
      {
        const f32x16 give = (j & 1) ? y0 : y1;
#pragma unroll
        for (int g = 0; g < 4; ++g) *(LAS f32x4*)(px + j * 8192 + g * 1024) = (f32x4){give[4 * g], give[4 * g + 1], give[4 * g + 2], give[4 * g + 3]};
        if (j & 1) y0 = y1;
        if (ph && j >= 2) {
#pragma unroll
          for (int g = 0; g < 4; ++g) *(LAS f32x4*)(px + j * 8192 + 4096 + g * 1024) = (f32x4){y0[4 * g], y0[4 * g + 1], y0[4 * g + 2], y0[4 * g + 3]};
        }
      }
      if (ph == 0) CSTAMP(9);
      HSYNC0();
      if (ph == 0) CSTAMP(10);
      if (!ph || j < 2) {
        u32x4 outv[2];
#pragma unroll
        for (int g = 0; g < 4; ++g) {
          f32x4 s = *(LAS f32x4*)(px + (j ^ 1) * 8192 + g * 1024);
          if (ph) s += *(LAS f32x4*)(px + (3 - j) * 8192 + g * 1024) + *(LAS f32x4*)(px + (j + 2) * 8192 + 4096 + g * 1024);
          const u32x2 uu = *(LAS u32x2*)(ub + (32 * (j & 1) + l31) * 80 + g * 16 + (1 - h) * 8);
          s = (f32x4){fmaf(bf_hi(uu[1]), dv, s[0] + y0[4 * g]), fmaf(bf_lo(uu[1]), dv, s[1] + y0[4 * g + 1]),
                      fmaf(bf_hi(uu[0]), dv, s[2] + y0[4 * g + 2]), fmaf(bf_lo(uu[0]), dv, s[3] + y0[4 * g + 3])};
          if (ph) ST_SC1_16(__builtin_bit_cast(u32x4, s), WS_ZP + (hg * 32 + qt) * 8192 + j * 4096 + g * 1024 + lane * 16);
          outv[g >> 1][2 * (g & 1)] = pk2(s[0], s[1]);
          outv[g >> 1][2 * (g & 1) + 1] = pk2(s[2], s[3]);
        }
        if (!ph) {
          const int zo = WS_Z + (72 * hg + cl) * 4096 + (j & 1) * 2048 + lane * 16;
          ST_SC1_16(outv[0], zo);
          ST_SC1_16(outv[1], zo + 1024);
        }
      }
      if (ph == 0) CSTAMP(7);
      if (ph == 0 && NPH == 2) HSYNC0();
    }
    NSTAMP(6);
    VM_WAIT(0);
    HSYNC();
    if (ht == 192) __hip_atomic_store((gu32*)(gws + WS_FZ) + (hg * 32 + qt) * 32, tag, RLX_AGENT);
    if (lane < 54) {
      const unsigned vo = (lane / 9) * 1152 + (lane % 9) * 16;
#pragma unroll 1
      for (int p = j; p < 96; p += 4) glds((const char*)wo + 144 * hg + p * 6912, vo, sbase + L_B + p * 864);
    }
    while (np < 8 || pend) { service(); __builtin_amdgcn_s_sleep(8); }
    VM_WAIT(0);
    lds_signal(mi + M_WO);
    HSTAMP(7);
    if (j == 3) {
#pragma unroll 1
      for (unsigned spins = 0; spins < SPIN_CAP; ++spins) {
        if (__all(h || __hip_atomic_load((gu32*)(gws + WS_FZ) + (hg * 32 + lane) * 32, RLX_AGENT) == tag)) break;
        __builtin_amdgcn_s_sleep(16);
      }
      lds_signal(mi + M_ZR);
    } else {
      lds_wait<8>(mi + M_ZR, 1);
    }
    {
      u32x4 zv[3], xv[3];
#pragma unroll
      for (int i = 0; i < 3; ++i) {
        const int n = (i < 2 || ht < 64) ? ht + 256 * i : ht;
        const int zl = (2 * (qt & 15) + (n & 1) + 32 * ((n >> 1) & 1)) * 16;
        if (i < 2) {
          zv[i] = LD_SC1_16(WS_Z + (72 * hg + (n >> 3)) * 4096 + (qt >> 4) * 2048 + ((n >> 2) & 1) * 1024 + zl);
        } else if (j == 0) {
          f32x4 sa = {0}, sb = {0};
#pragma unroll
          for (int p = 0; p < 4; ++p) {
            const int zo = WS_ZP + (hg * 32 + (lane >> 3) * 4 + p) * 8192 + (qt >> 4) * 4096 + ((n >> 2) & 1) * 2048 + zl;
            sa += __builtin_bit_cast(f32x4, LD_SC1_16(zo));
            sb += __builtin_bit_cast(f32x4, LD_SC1_16(zo + 1024));
          }
          zv[i] = (u32x4){pk2(sa[0], sa[1]), pk2(sa[2], sa[3]), pk2(sb[0], sb[1]), pk2(sb[2], sb[3])};
        } else {
          zv[i] = zv[0];
        }
        xv[i] = *(const u32x4*)(xt + (n / 9) * 1152 + 144 * hg + (n % 9) * 16);
      }
      lds_wait<4>(mi + M_DONE + BT_AT, 8);
#pragma unroll
      for (int i = 0; i < 3; ++i) {
        const int n = (i < 2 || ht < 64) ? ht + 256 * i : ht;
        lds_ptr zp = sm + L_BT + ((32 * (n & 1) + 16 * ((n >> 2) & 1) + 4 * ((n >> 1) & 1)) * 72 + (n >> 3)) * 2;
#pragma unroll
        for (int e = 0; e < 8; ++e)
          *(LAS unsigned short*)(zp + ((e & 3) + 8 * (e >> 2)) * 144) = (unsigned short)((e & 1) ? zv[i][e >> 1] >> 16 : zv[i][e >> 1]);
      }
      lds_signal(mi + M_HS);
      lds_wait(mi + M_HS, hs + 4);
#pragma unroll
      for (int i = 0; i < 3; ++i) {
        if (i < 2 || ht < 64) {
          const int n = ht + 256 * i;
          const u32x4 z = *(LAS u32x4*)(sm + L_BT + n * 16);
          const float omg = rbf(1.f - *(LAS float*)(mi + M_G + (n / 9) * 4));
          u32x4 o;
#pragma unroll
          for (int e = 0; e < 4; ++e)
            o[e] = pk2(omg * bf_lo(xv[i][e]) * bf_lo(z[e]), omg * bf_hi(xv[i][e]) * bf_hi(z[e]));
          *(LAS u32x4*)(sm + L_BT + n * 16) = o;
        }
      }
      lds_signal(mi + M_BT);
    }
    HSTAMP(8);
    __builtin_amdgcn_s_setprio(0);
  } else {
    if (w == 0) *(LAS unsigned*)(mi + lane * 4) = lane == (M_RDY + 32) / 4 ? 4u : 0u;
    BAR();
    const float QS = 0.23570226039551584f * 1.4426950408889634f;
    if (w >= 5) {
      const int wi = w - 5;
      unsigned vw[3];
#pragma unroll
      for (int m = 0; m < 3; ++m) {
        const int f = 64 * (wi + 3 * m) + lane, row = f / 9;
        vw[m] = row * 1152 + (f - 9 * row) * 16;
      }
#pragma unroll
      for (int p = 0; p < 3; ++p) {
        const int th = (p + rot) % 3;
#pragma unroll
        for (int pr = 0; pr < 2; ++pr) {
          const int g2 = 2 * (1 - pt) + pr;
          const char* src = xt + g2 * 18432 + (3 * th + wi) * 128;
          glds(src, vxe, sbase + L_X + (3 * p + wi) * 8192 + g2 * 2048);
          glds(src, vxo, sbase + L_X + (3 * p + wi) * 8192 + g2 * 2048 + 1024);
        }
        if (p < 2) {
#pragma unroll
          for (int per = 0; per < 3; ++per) {
#pragma unroll
            for (int m = 0; m < 3; ++m)
              glds(wqs + (size_t)(192 * th + 64 * per) * 1152, vw[m], sbase + L_B + p * 27648 + per * 9216 + (wi + 3 * m) * 1024);
            if (p == 1 && per == 0) { VM_WAIT(7); lds_signal(mi + M_C1); }
          }
        }
      }
      STAMP(1);
      VM_WAIT(4);
      lds_signal(mi + M_C2);
    } else if (w < 4) {
      if (hg < 4) {
        const int mt = w & 1, nt = w >> 1, row = 32 * pt + 16 * mt + l15;
        lds_ptr wb = sm + L_B + 2 * 27648 + (8 * g4 + tq) * 48 + (nt ? 32 + (tp & 1) * 8 : tp * 8);
        lds_ptr xb = sm + L_X + row * 128;
        const int T = 2 * qt + pt, i4 = T & 3, gb = WS_KV + (T >> 2) * KV_GROUP, rp = swap23(16 * mt + l15);
        int so;
        bool st;
        if (sel) {
          const int d = nt ? (l15 < 8 ? 10 + l15 + 18 * (g4 & 1) : 36 + (g4 & 1)) : l15 + 18 * (g4 & 1);
          so = gb + 4096 + i4 * 1216 + mt * 608 + d * 16 + (g4 >> 1) * 8;
          st = !nt || (l15 >= 6 && l15 <= 8);
        } else {
          so = nt ? gb + 8960 + i4 * 256 + rp * 8 : gb + i4 * 1024 + (rp + 32 * (g4 & 1)) * 16 + (g4 >> 1) * 8;
          st = !nt || g4 == 1;
        }
        lds_wait(mi + M_KD, 3);
        PSTAMP(5);
        bf16x8 fa[18], fb[18];
#pragma unroll
        for (int k = 0; k < 18; ++k) {
          fa[k] = tr_frag(wb + k * 1536, wb + k * 1536 + 192);
          fb[k] = lds_frag(xb + (k >> 1) * 8192 + (((4 * (k & 1) + g4) ^ (l15 >> 1)) << 4));
        }
        f32x4 acc = {0}, acc1 = {0};
        if (sel) {
#pragma unroll
          for (int k = 0; k < 18; k += 2) { acc = MFMA16(fb[k], fa[k], acc); acc1 = MFMA16(fb[k + 1], fa[k + 1], acc1); }
        } else {
#pragma unroll
          for (int k = 0; k < 18; k += 2) { acc = MFMA16(fa[k], fb[k], acc); acc1 = MFMA16(fa[k + 1], fb[k + 1], acc1); }
        }
        acc += acc1;
        u32x2 a = {pk2(acc[0], acc[1]), pk2(acc[2], acc[3])};
        if (nt) {
          if (!sel) a = (u32x2){a[1], 0x3f803f80u};
          else if (l15 == 8) a[0] = a[1] = (g4 & 1) ? 0u : 0x3f803f80u;
        }
        if (st) ST_SC1_8(a, so);
        PSTAMP(6);
        lds_signal(mi + M_KVC);
        float n2 = bf_lo(a[0]) * bf_lo(a[0]) + bf_hi(a[0]) * bf_hi(a[0]);
        if (!nt) n2 += bf_lo(a[1]) * bf_lo(a[1]) + bf_hi(a[1]) * bf_hi(a[1]);
        if (!st) n2 = 0.f;
        n2 += xhalf(n2);
        n2 += u2f(__builtin_amdgcn_ds_swizzle(__builtin_bit_cast(int, n2), 0x401f));
        n2 = max16(n2);
        if (lane == 0) *(LAS float*)(mi + M_N2 + 4 * w) = n2;
        lds_wait(mi + M_TAGR, 1);
        PSTAMP(7);
        VM_WAIT(0);
        PSTAMP(8);
        if (lds_ticket(mi + M_KVP) == 3u && lane == 0) {
          const f32x4 n = *(LAS f32x4*)(mi + M_N2);
          const float nn = sel ? 0.f : fmaxf(n[0], n[1]) + fmaxf(n[2], n[3]);
          __hip_atomic_store((gu64*)(gws + WS_FKV + T * 128 + sel * 8),
                             ((unsigned long long)*(LAS unsigned*)(mi + M_TAG) << 32) | __builtin_bit_cast(unsigned, nn), RLX_AGENT);
        }
        PSTAMP(9);
      }
    } else {
      {
      *(LAS u32x4*)(sm + L_GW + lane * 16) = *(const u32x4*)(gw + 8 * lane);
      if (lane < 8) *(LAS u32x4*)(sm + L_GW + 1024 + lane * 16) = *(const u32x4*)(gw + 512 + 8 * lane);
      lds_signal(mi + M_C1);
      UNIT_LOAD(32 + (lane & 3), l31 >> 2);
      UNIT_DO();
      ST_SC1_16(h ? o1 : o0, WS_UT + (qt * 8 + hg) * 9216 + 8192 + lane * 16);
      }
    }
    if (w < 4) {
      const int gt = w >> 1, tt = w & 1, arow = 32 * tt + l31, xsw = (arow >> 1) & 7;
      lds_ptr xb = sm + L_X + arow * 128;
      lds_ptr wb = sm + L_B + (8 * h + tq) * 144 + (36 * gt + 20 * (g4 & 1) + 4 * tp) * 2;
      f32x16 acc = {0};
#pragma unroll 1
      for (int p = 0; p < 3; ++p) {
        if (p == 1) CSTAMP(6);
        if (p == 2) CSTAMP(8);
        lds_wait(mi + M_C1 + 4 * p, p == 0 ? 7 : 8 * p - 5);
        if (p == 0) CSTAMP(5);
        if (p == 1) CSTAMP(7);
        if (p == 2) CSTAMP(9);
        bf16x8 fa[12], fb[12];
#pragma unroll
        for (int k = 0; k < 12; ++k) {
          fa[k] = tr_frag(wb + k * 2304, wb + k * 2304 + 576);
          fb[k] = lds_frag(xb + (k >> 2) * 8192 + (((2 * (k & 3) + h) ^ xsw) << 4));
        }
        __builtin_amdgcn_sched_barrier(0);
#pragma unroll
        for (int k = 0; k < 12; ++k) acc = MFMA32(fa[k], fb[k], acc);
        if (p == 1) { lds_signal(mi + M_C3B); lds_signal(mi + M_C3); }
        xb += 24576;
        wb += 27648;
      }
      STAMP(2);
      u32x4 a;
#pragma unroll
      for (int i = 0; i < 4; ++i) a[i] = pk2(acc[2 * i] * QS, acc[2 * i + 1] * QS);
      lds_ptr qe = sm + L_Q + (4 * gt + tt) * 1024;
      *(LAS u32x4*)(qe + lane * 16) = a;
      lds_ptr qo = qe + 2048 + l31 * 16;
      *(LAS unsigned*)(qo + h * 512 + 4) = pk2(acc[8] * QS, acc[9] * QS);
      *(LAS unsigned*)(qo + (h ? 8 : 512)) = pk2(acc[10] * QS, acc[11] * QS);
      *(LAS unsigned*)(qo + h * 512 + 12) = pk2(acc[12] * QS, acc[13] * QS);
      *(LAS unsigned*)(h ? sm + L_Q + 8192 + ((2 * gt + 1) * 64 + arow) * 4 : qo + 520) = pk2(acc[14] * QS, acc[15] * QS);
    } else {
      const int tok = 16 * (w - 4) + l15;
      lds_ptr xb = sm + L_X + tok * 128;
      lds_ptr wb = sm + L_B + (8 * g4 + tq) * 144 + (tp == 1 ? 104 : 32);
      f32x4 acc = {0};
#pragma unroll 1
      for (int p = 0; p < 3; ++p) {
        lds_wait(mi + M_C1 + 4 * p, p == 0 ? 7 : 8 * p - 5);
        lds_ptr gp = sm + L_GW + ((p + rot) % 3) * 384 + g4 * 16;
        bf16x8 fa[6], fg[6], fb[6];
#pragma unroll
        for (int k = 0; k < 6; ++k) {
          fa[k] = tr_frag(wb + k * 4608, wb + k * 4608 + 576);
          fg[k] = lds_frag(gp + k * 64);
          fb[k] = lds_frag(xb + (k >> 1) * 8192 + (((4 * (k & 1) + g4) ^ (l15 >> 1)) << 4));
        }
        __builtin_amdgcn_sched_barrier(0);
#pragma unroll
        for (int k = 0; k < 6; ++k) acc = MFMA16(l15 == 8 ? fg[k] : fa[k], fb[k], acc);
        if (p == 1) { lds_signal(mi + M_C3B); VM_WAIT(0); lds_signal(mi + M_C3); }
        xb += 24576;
        wb += 27648;
      }
      STAMP(2);
      if (g4 < 2) {
        *(LAS unsigned*)(sm + L_Q + 8192 + (2 * g4 * 64 + tok) * 4) = pk2(acc[0] * QS, acc[1] * QS);
        *(LAS unsigned*)(sm + L_Q + ((2 * g4 + 1) * 2 + (tok >> 5)) * 1024 + (tok & 31) * 16) = pk2(acc[2] * QS, acc[3] * QS);
      } else if (g4 == 2) {
        *(LAS float*)(mi + M_G + tok * 4) = acc[0];
      }
    }
    lds_signal(mi + M_QD);
    const int hl = w & 3, kh = w >> 2;
    const int voff = 4096 + (l31 < 18 ? h * 288 + l31 * 16 : (l31 == 18 ? 576 : 592));
    lds_wait(mi + M_QD, 12);
    STAMP(3);
    const u32x4 q0u = *(LAS u32x4*)(sm + L_Q + (2 * hl) * 1024 + lane * 16);
    const u32x4 q1u = *(LAS u32x4*)(sm + L_Q + (2 * hl + 1) * 1024 + lane * 16);
    const unsigned qr = *(LAS unsigned*)(sm + L_Q + 8192 + (hl * 64 + lane) * 4);
    const float kn0 = *(LAS float*)(mi + M_KN0);
    lds_signal(mi + M_QR);
    float n0 = 0.f, n1 = 0.f;
#pragma unroll
    for (int i = 0; i < 4; ++i) { n0 = dot2(q0u[i], n0); n1 = dot2(q1u[i], n1); }
    const float nq = dot2(qr, (h ? n1 : n0) + xhalf(h ? n0 : n1));
    const u32x2 qrem_u = {qr, pk2(-__builtin_amdgcn_sqrtf(nq * kn0), 0.f)};
    f32x16 o0, o1;
    float m0 = 0.f, m1 = 0.f;
    STAMP(4); RSTAMP(2);
    {
      int cnt = 8, off = 0, tmp;
      unsigned ca = sbase + L_M + M_DONE;
      asm volatile(
      "v_mov_b32 v107, v126\n"
      "v_mov_b32 v108, v127\n"
      "v_mov_b32 v109, v128\n"
      "v_mov_b32 v130, 1\n"
      "ds_read2st64_b64 v[110:113], v109 offset0:0 offset1:1\n"
      "ds_read_b128 v[114:117], v107 offset:0\n"
      "ds_read_b128 v[118:121], v108 offset:0\n"
      "s_mov_b32 vcc_lo, %[first]\n"
      "s_mov_b32 vcc_hi, 0\n"
      "s_setprio 1\n"
      "v_mov_b32 v56, 0\n"
      "v_mov_b32 v57, 0\n"
      "v_mov_b32 v58, 0\n"
      "v_mov_b32 v59, 0\n"
      "v_mov_b32 v60, 0\n"
      "v_mov_b32 v61, 0\n"
      "v_mov_b32 v62, 0\n"
      "v_mov_b32 v63, 0\n"
      "v_mov_b32 v122, 0\n"
      "v_mov_b32 v123, 0\n"
      "v_mov_b32 v124, 0\n"
      "v_mov_b32 v125, 0\n"
      "v_mov_b32 v64, 0\n"
      "v_mov_b32 v65, 0\n"
      "v_mov_b32 v66, 0\n"
      "v_mov_b32 v67, 0\n"
      "v_mov_b32 v68, 0\n"
      "v_mov_b32 v69, 0\n"
      "v_mov_b32 v70, 0\n"
      "v_mov_b32 v71, 0\n"
      "v_mov_b32 v72, 0\n"
      "v_mov_b32 v73, 0\n"
      "v_mov_b32 v74, 0\n"
      "v_mov_b32 v75, 0\n"
      "v_mov_b32 v76, 0\n"
      "v_mov_b32 v77, 0\n"
      "v_mov_b32 v78, 0\n"
      "v_mov_b32 v79, 0\n"
      "s_waitcnt lgkmcnt(1)\n"
      "v_mfma_f32_32x32x4_2b_bf16 v[0:31], v[110:111], v[104:105], 0 cbsz:1 abid:0\n"
      "v_mov_b32 v80, 0\n"
      "v_mov_b32 v81, 0\n"
      "v_mov_b32 v82, 0\n"
      "v_mov_b32 v83, 0\n"
      "v_mov_b32 v84, 0\n"
      "v_mov_b32 v85, 0\n"
      "v_mov_b32 v86, 0\n"
      "v_mov_b32 v87, 0\n"
      "v_mov_b32 v88, 0\n"
      "v_mov_b32 v89, 0\n"
      "v_mov_b32 v90, 0\n"
      "v_mov_b32 v91, 0\n"
      "v_mov_b32 v92, 0\n"
      "v_mov_b32 v93, 0\n"
      "v_mov_b32 v94, 0\n"
      "v_mov_b32 v95, 0\n"
      "s_nop 2\n"
      "v_mfma_f32_32x32x16_bf16 v[0:15], v[114:117], v[96:99], v[0:15]\n"
      "v_mfma_f32_32x32x16_bf16 v[16:31], v[114:117], v[100:103], v[16:31]\n"
      "s_nop 11\n"
      "1:\n"
      "s_cbranch_vccz 4f\n"
      "s_setprio 2\n"
      "4:\n"
      "v_exp_f32 v0, v0\n"
      "v_exp_f32 v1, v1\n"
      "v_smfmac_f32_32x32x32_bf16 v[80:95], v[122:125], v[56:63], v106\n"
      "ds_read_b128 v[114:117], v107 offset:1024\n"
      "ds_read_b128 v[122:125], v108 offset:608\n"
      "v_exp_f32 v2, v2\n"
      "v_exp_f32 v3, v3\n"
      "v_exp_f32 v4, v4\n"
      "v_exp_f32 v5, v5\n"
      "v_mfma_f32_32x32x4_2b_bf16 v[32:63], v[110:111], v[104:105], 0 cbsz:1 abid:1\n"
      "v_exp_f32 v6, v6\n"
      "v_exp_f32 v7, v7\n"
      "v_exp_f32 v8, v8\n"
      "v_exp_f32 v9, v9\n"
      "v_exp_f32 v10, v10\n"
      "v_exp_f32 v11, v11\n"
      "v_exp_f32 v12, v12\n"
      "s_waitcnt lgkmcnt(2)\n"
      "v_exp_f32 v13, v13\n"
      "v_smfmac_f32_32x32x32_bf16 v[64:79], v[118:121], v[0:7], v106\n"
      "v_exp_f32 v14, v14\n"
      "v_exp_f32 v15, v15\n"
      "v_exp_f32 v16, v16\n"
      "s_waitcnt lgkmcnt(0)\n"
      "v_exp_f32 v17, v17\n"
      "v_smfmac_f32_32x32x32_bf16 v[64:79], v[122:125], v[8:15], v106\n"
      "v_exp_f32 v18, v18\n"
      "v_exp_f32 v19, v19\n"
      "v_exp_f32 v20, v20\n"
      "v_exp_f32 v21, v21\n"
      "v_mfma_f32_32x32x16_bf16 v[32:47], v[114:117], v[96:99], v[32:47]\n"
      "v_exp_f32 v22, v22\n"
      "v_exp_f32 v23, v23\n"
      "v_exp_f32 v24, v24\n"
      "v_exp_f32 v25, v25\n"
      "v_smfmac_f32_32x32x32_bf16 v[80:95], v[118:121], v[16:23], v106\n"
      "v_exp_f32 v26, v26\n"
      "ds_read_b128 v[118:121], v108 offset:1216\n"
      "v_exp_f32 v27, v27\n"
      "v_exp_f32 v28, v28\n"
      "v_exp_f32 v29, v29\n"
      "v_mfma_f32_32x32x16_bf16 v[48:63], v[114:117], v[100:103], v[48:63]\n"
      "v_exp_f32 v30, v30\n"
      "v_exp_f32 v31, v31\n"
      "v_exp_f32 v32, v32\n"
      "v_exp_f32 v33, v33\n"
      "v_smfmac_f32_32x32x32_bf16 v[80:95], v[122:125], v[24:31], v106\n"
      "ds_read_b128 v[114:117], v107 offset:2048\n"
      "ds_read_b128 v[122:125], v108 offset:1824\n"
      "v_exp_f32 v34, v34\n"
      "v_exp_f32 v35, v35\n"
      "v_exp_f32 v36, v36\n"
      "v_exp_f32 v37, v37\n"
      "v_mfma_f32_32x32x4_2b_bf16 v[0:31], v[112:113], v[104:105], 0 cbsz:1 abid:0\n"
      "v_exp_f32 v38, v38\n"
      "v_exp_f32 v39, v39\n"
      "v_exp_f32 v40, v40\n"
      "v_exp_f32 v41, v41\n"
      "v_exp_f32 v42, v42\n"
      "v_exp_f32 v43, v43\n"
      "v_exp_f32 v44, v44\n"
      "s_waitcnt lgkmcnt(2)\n"
      "v_exp_f32 v45, v45\n"
      "v_smfmac_f32_32x32x32_bf16 v[64:79], v[118:121], v[32:39], v106\n"
      "v_exp_f32 v46, v46\n"
      "v_exp_f32 v47, v47\n"
      "v_exp_f32 v48, v48\n"
      "s_waitcnt lgkmcnt(0)\n"
      "v_exp_f32 v49, v49\n"
      "v_smfmac_f32_32x32x32_bf16 v[64:79], v[122:125], v[40:47], v106\n"
      "v_exp_f32 v50, v50\n"
      "v_exp_f32 v51, v51\n"
      "v_exp_f32 v52, v52\n"
      "v_exp_f32 v53, v53\n"
      "v_mfma_f32_32x32x16_bf16 v[0:15], v[114:117], v[96:99], v[0:15]\n"
      "v_exp_f32 v54, v54\n"
      "v_exp_f32 v55, v55\n"
      "v_exp_f32 v56, v56\n"
      "v_exp_f32 v57, v57\n"
      "v_smfmac_f32_32x32x32_bf16 v[80:95], v[118:121], v[48:55], v106\n"
      "v_exp_f32 v58, v58\n"
      "ds_read_b128 v[118:121], v108 offset:2432\n"
      "v_exp_f32 v59, v59\n"
      "v_exp_f32 v60, v60\n"
      "v_exp_f32 v61, v61\n"
      "v_mfma_f32_32x32x16_bf16 v[16:31], v[114:117], v[100:103], v[16:31]\n"
      "v_exp_f32 v62, v62\n"
      "v_exp_f32 v63, v63\n"
      "v_exp_f32 v0, v0\n"
      "v_exp_f32 v1, v1\n"
      "v_smfmac_f32_32x32x32_bf16 v[80:95], v[122:125], v[56:63], v106\n"
      "ds_read_b128 v[114:117], v107 offset:3072\n"
      "ds_read_b128 v[122:125], v108 offset:3040\n"
      "ds_read_b32 v131, v129 offset:68\n"
      "v_exp_f32 v2, v2\n"
      "v_exp_f32 v3, v3\n"
      "v_exp_f32 v4, v4\n"
      "v_exp_f32 v5, v5\n"
      "v_mfma_f32_32x32x4_2b_bf16 v[32:63], v[112:113], v[104:105], 0 cbsz:1 abid:1\n"
      "v_exp_f32 v6, v6\n"
      "v_exp_f32 v7, v7\n"
      "v_exp_f32 v8, v8\n"
      "v_exp_f32 v9, v9\n"
      "v_exp_f32 v10, v10\n"
      "v_exp_f32 v11, v11\n"
      "v_exp_f32 v12, v12\n"
      "s_waitcnt lgkmcnt(3)\n"
      "v_exp_f32 v13, v13\n"
      "v_smfmac_f32_32x32x32_bf16 v[64:79], v[118:121], v[0:7], v106\n"
      "v_exp_f32 v14, v14\n"
      "v_exp_f32 v15, v15\n"
      "v_exp_f32 v16, v16\n"
      "s_waitcnt lgkmcnt(0)\n"
      "v_exp_f32 v17, v17\n"
      "v_smfmac_f32_32x32x32_bf16 v[64:79], v[122:125], v[8:15], v106\n"
      "v_exp_f32 v18, v18\n"
      "v_exp_f32 v19, v19\n"
      "v_exp_f32 v20, v20\n"
      "v_exp_f32 v21, v21\n"
      "v_mfma_f32_32x32x16_bf16 v[32:47], v[114:117], v[96:99], v[32:47]\n"
      "v_exp_f32 v22, v22\n"
      "v_exp_f32 v23, v23\n"
      "v_exp_f32 v24, v24\n"
      "v_exp_f32 v25, v25\n"
      "v_smfmac_f32_32x32x32_bf16 v[80:95], v[118:121], v[16:23], v106\n"
      "v_exp_f32 v26, v26\n"
      "ds_read_b128 v[118:121], v108 offset:3648\n"
      "v_exp_f32 v27, v27\n"
      "v_exp_f32 v28, v28\n"
      "v_exp_f32 v29, v29\n"
      "v_mfma_f32_32x32x16_bf16 v[48:63], v[114:117], v[100:103], v[48:63]\n"
      "v_exp_f32 v30, v30\n"
      "v_exp_f32 v31, v31\n"
      "s_cbranch_vccz 5f\n"
      "s_setprio 0\n"
      "5:\n"
      "v_readfirstlane_b32 %[tmp], v131\n"
      "s_add_u32 %[off], %[off], 19968\n"
      "s_cmp_eq_u32 %[off], 79872\n"
      "s_cselect_b32 %[off], 0, %[off]\n"
      "s_cmp_lt_u32 %[tmp], 4\n"
      "s_cbranch_scc1 3f\n"
      "2:\n"
      "v_add_u32 v109, %[off], v128\n"
      "ds_read2st64_b64 v[110:113], v109 offset0:0 offset1:1\n"
      "v_add_u32 v107, %[off], v126\n"
      "v_exp_f32 v32, v32\n"
      "v_exp_f32 v33, v33\n"
      "v_smfmac_f32_32x32x32_bf16 v[80:95], v[122:125], v[24:31], v106\n"
      "ds_read_b128 v[114:117], v107 offset:0\n"
      "ds_read_b128 v[122:125], v108 offset:4256\n"
      "v_exp_f32 v34, v34\n"
      "v_exp_f32 v35, v35\n"
      "v_exp_f32 v36, v36\n"
      "s_waitcnt lgkmcnt(2)\n"
      "v_exp_f32 v37, v37\n"
      "v_mfma_f32_32x32x4_2b_bf16 v[0:31], v[110:111], v[104:105], 0 cbsz:1 abid:0\n"
      "v_exp_f32 v38, v38\n"
      "v_exp_f32 v39, v39\n"
      "v_exp_f32 v40, v40\n"
      "v_exp_f32 v41, v41\n"
      "v_exp_f32 v42, v42\n"
      "v_exp_f32 v43, v43\n"
      "v_exp_f32 v44, v44\n"
      "v_exp_f32 v45, v45\n"
      "v_smfmac_f32_32x32x32_bf16 v[64:79], v[118:121], v[32:39], v106\n"
      "v_exp_f32 v46, v46\n"
      "v_exp_f32 v47, v47\n"
      "v_exp_f32 v48, v48\n"
      "s_waitcnt lgkmcnt(0)\n"
      "s_mov_b64 exec, 1\n"
      "ds_add_u32 v129, v130\n"
      "s_mov_b64 exec, -1\n"
      "v_exp_f32 v49, v49\n"
      "v_smfmac_f32_32x32x32_bf16 v[64:79], v[122:125], v[40:47], v106\n"
      "v_exp_f32 v50, v50\n"
      "v_exp_f32 v51, v51\n"
      "v_exp_f32 v52, v52\n"
      "v_exp_f32 v53, v53\n"
      "v_mfma_f32_32x32x16_bf16 v[0:15], v[114:117], v[96:99], v[0:15]\n"
      "v_exp_f32 v54, v54\n"
      "v_exp_f32 v55, v55\n"
      "v_exp_f32 v56, v56\n"
      "v_exp_f32 v57, v57\n"
      "v_smfmac_f32_32x32x32_bf16 v[80:95], v[118:121], v[48:55], v106\n"
      "v_add_u32 v108, %[off], v127\n"
      "v_add_u32 v129, 4, v129\n"
      "v_exp_f32 v58, v58\n"
      "ds_read_b128 v[118:121], v108 offset:0\n"
      "v_exp_f32 v59, v59\n"
      "v_exp_f32 v60, v60\n"
      "v_exp_f32 v61, v61\n"
      "v_mfma_f32_32x32x16_bf16 v[16:31], v[114:117], v[100:103], v[16:31]\n"
      "v_exp_f32 v62, v62\n"
      "v_exp_f32 v63, v63\n"
      "s_sub_u32 %[cnt], %[cnt], 1\n"
      "s_cmp_lg_u32 %[cnt], 0\n"
      "s_cbranch_scc1 1b\n"
      "s_branch 6f\n"
      "3:\n"
      "s_sleep 1\n"
      "ds_read_b32 v131, v129 offset:68\n"
      "s_waitcnt lgkmcnt(0)\n"
      "v_readfirstlane_b32 %[tmp], v131\n"
      "s_nop 0\n"
      "s_cmp_lt_u32 %[tmp], 4\n"
      "s_cbranch_scc1 3b\n"
      "s_branch 2b\n"
      "6:\n"
      "v_smfmac_f32_32x32x32_bf16 v[80:95], v[122:125], v[56:63], v106\n"
      "s_setprio 0\n"
      "s_waitcnt lgkmcnt(0)\n"
      "s_nop 15\n"
      "s_nop 7\n"
      : "=&{v[64:79]}"(o0), "=&{v[80:95]}"(o1), "+{v129}"(ca), [cnt] "+s"(cnt), [off] "+s"(off), [tmp] "=&s"(tmp)
      : "{v[96:99]}"(q0u), "{v[100:103]}"(q1u), "{v[104:105]}"(qrem_u), "{v106}"(SPIDX),
        "{v126}"(sbase + kh * KV_GROUP + lane * 16), "{v127}"(sbase + kh * KV_GROUP + voff),
        "{v128}"(sbase + kh * KV_GROUP + 8960 + lane * 8), [first] "s"(1 - kh)
      : "v0", "v1", "v2", "v3", "v4", "v5", "v6", "v7", "v8", "v9", "v10", "v11", "v12", "v13", "v14", "v15",
        "v16", "v17", "v18", "v19", "v20", "v21", "v22", "v23", "v24", "v25", "v26", "v27", "v28", "v29", "v30", "v31",
        "v32", "v33", "v34", "v35", "v36", "v37", "v38", "v39", "v40", "v41", "v42", "v43", "v44", "v45", "v46", "v47",
        "v48", "v49", "v50", "v51", "v52", "v53", "v54", "v55", "v56", "v57", "v58", "v59", "v60", "v61", "v62", "v63",
        "v107", "v108", "v109", "v110", "v111", "v112", "v113", "v114", "v115", "v116", "v117", "v118", "v119", "v120", "v121", "v122",
        "v123", "v124", "v125", "v130", "v131", "vcc", "scc", "memory");
    }
    if (!__all(nq * *(LAS float*)(mi + M_KN) < 47.f * 47.f)) {
      m0 = m1 = -INFINITY;
      o0 = o1 = (f32x16){0};
      const bf16x8 q0 = __builtin_bit_cast(bf16x8, q0u), q1 = __builtin_bit_cast(bf16x8, q1u);
      const s16x4 qrem = __builtin_bit_cast(s16x4, (u32x2){qr, 0u});
#pragma unroll 1
      for (int t = 0; t < 32; ++t) {
        const int g = WS_KV + (t >> 2) * RSLOT + kh * KV_GROUP;
        const u32x2 kr = __builtin_bit_cast(u32x2, __builtin_amdgcn_raw_buffer_load_b64(rs, g + 8960 + (t & 3) * 256 + l31 * 8, 0, 16));
        const bf16x8 km = __builtin_bit_cast(bf16x8, LD_SC1_16(g + (t & 3) * 1024 + lane * 16));
        const bf16x8 va = __builtin_bit_cast(bf16x8, LD_SC1_16(g + voff + (t & 3) * 1216));
        const bf16x8 vb = __builtin_bit_cast(bf16x8, LD_SC1_16(g + voff + (t & 3) * 1216 + 608));
        const f32x32 z = {0};
        const f32x32 r = __builtin_amdgcn_mfma_f32_32x32x4bf16_1k(__builtin_bit_cast(s16x4, kr), qrem, z, 0, 0, 0);
        online_step(MFMA32(km, q0, lo16(r)), m0, o0, va, vb);
        online_step(MFMA32(km, q1, hi16(r)), m1, o1, va, vb);
      }
    }
    HSTAMP(9);
    lds_wait(mi + M_DONE + 20, 8);
    {
      const f32x16 give = kh ? o0 : o1;
      lds_ptr p = sm + L_EX + w * 3072 + lane * 16;
      *(LAS f32x4*)(p) = (f32x4){give[0], give[1], give[2], give[3]};
      *(LAS f32x4*)(p + 1024) = (f32x4){give[4], give[5], give[6], give[7]};
      *(LAS f32x4*)(p + 2048) = (f32x4){give[8], give[9], give[10], kh ? m0 : m1};
    }
    lds_signal(mi + M_PAIR + 4 * hl);
    lds_wait(mi + M_PAIR + 4 * hl, 2);
    STAMP(10); RSTAMP(3);
    {
      const f32x16 keep = kh ? o1 : o0;
      lds_ptr p = sm + L_EX + (w ^ 4) * 3072 + lane * 16;
      const f32x4 t0 = *(LAS f32x4*)(p), t1 = *(LAS f32x4*)(p + 1024), t2 = *(LAS f32x4*)(p + 2048);
      const float mk = kh ? m1 : m0, mm = fmaxf(mk, t2[3]);
      const float fa = __builtin_amdgcn_exp2f(mk - mm), fb = __builtin_amdgcn_exp2f(t2[3] - mm);
      float o[11];
#pragma unroll
      for (int i = 0; i < 11; ++i) o[i] = keep[i] * fa + (i < 4 ? t0[i & 3] : (i < 8 ? t1[i & 3] : t2[i & 3])) * fb;
      const float ox = xhalf(o[10]);
      const float inv = __builtin_amdgcn_rcpf(h ? ox : o[10]);
      u32x4 a;
#pragma unroll
      for (int i = 0; i < 4; ++i) a[i] = pk2(o[2 * i] * inv, o[2 * i + 1] * inv);
      const int ob = WS_O + (2 * qt + kh) * 36 * 1024;
      ST_SC1_16(a, ob + (4 * hg + hl) * 1024 + lane * 16);
      if (!h) ST_SC1_4(pk2(o[8] * inv, o[9] * inv), ob + 32768 + (4 * hg + hl) * 128 + l31 * 4);
    }
  }

  const unsigned tag = *(LAS unsigned*)(mi + M_TAG);
  if (w < 8) {
    VM_WAIT(0);
    if (lds_ticket(mi + M_OD) == 7u && lane == 0)
      __hip_atomic_store((gu32*)(gws + WS_FO) + (qt * 8 + hg) * 32, tag, RLX_AGENT);
  }
  bf16x8 wf[18];
  u32x4 bt = {0, 0, 0, 0};
  float gq = 0.f;
  lds_wait(mi + M_WO, 4);
  {
    const int ka = w < 8 ? (w >> 2) : (g4 >> 1), kb = w < 8 ? h : (g4 & 1);
    const int cb = w < 8 ? (32 * ((w >> 1) & 1) + 16 * (g4 & 1) + 4 * tp) * 2 : (64 + 4 * (tp & 1)) * 2;
    lds_ptr wm = sm + L_B + (DH * ka + 4 * kb + tq) * 144 + cb;
    lds_ptr wr = sm + L_B + (DH * (8 * ka + 4 * kb + (tq >> 1)) + 16 + (tq & 1)) * 144 + cb;
#pragma unroll
    for (int k = 0; k < 12; ++k) wf[k] = tr_frag(wm + k * 5184, wm + k * 5184 + 1152);
    wm += 12 * 5184;
#pragma unroll
    for (int k = 0; k < 4; ++k) wf[12 + k] = tr_frag(wm + k * 5184, wm + k * 5184 + 1152);
#pragma unroll
    for (int k = 0; k < 2; ++k) wf[16 + k] = tr_frag(wr + k * 41472, wr + k * 41472 + 5184);
  }
  if (w < 9) {
    lds_wait(mi + M_BT, 4);
    bt = *(LAS u32x4*)(sm + L_BT + tid * 16);
    gq = *(LAS float*)(mi + M_G + otok * 4);
    lds_signal(mi + M_EXD);
  }
  if (w == 11) {
    lds_wait<1>(mi + M_OD, 8);
#pragma unroll 1
    for (unsigned spins = 0; spins < SPIN_CAP; ++spins) {
      if (__all(lane >= 8 || __hip_atomic_load((gu32*)(gws + WS_FO) + (qt * 8 + lane) * 32, RLX_AGENT) == tag)) break;
      __builtin_amdgcn_s_sleep(1);
    }
    lds_signal(mi + M_OR);
  } else {
    lds_wait<1>(mi + M_OR, 1);
  }
  if (w < 8) HSTAMP(5);
  {
    u32x4 of[6];
#pragma unroll
    for (int i = 0; i < 6; ++i) of[i] = LD_SC1_16(WS_O + 2 * qt * 36 * 1024 + (12 * i + w) * 1024 + lane * 16);
    lds_wait(mi + M_EXD, 9);
#pragma unroll
    for (int i = 0; i < 6; ++i) {
      if ((i == 2 || i == 5) && w >= 8) {
        lds_ptr p = sm + (12 * i + w) * 1024 + (4 * (lane & 7) + 32 * h) * 16 + ((lane >> 3) & 3) * 4;
#pragma unroll
        for (int e = 0; e < 4; ++e) *(LAS unsigned*)(p + e * 16) = of[i][e];
      } else {
        *(LAS u32x4*)(sm + (12 * i + w) * 1024 + lane * 16) = of[i];
      }
    }
  }
  BAR();
  if (w < 8) HSTAMP(6);
  if (w < 8) {
    lds_ptr ob = sm + ((w & 1) * 36 + (w >> 2)) * 1024 + lane * 16;
    f32x16 acc = {0};
#pragma unroll
    for (int k = 0; k < 18; ++k) acc = MFMA32(lds_frag(ob + k * 2048), wf[k], acc);
    lds_ptr ap = sm + L_B + (((w >> 2) * 64 + 32 * (w & 1) + 4 * h) * 72 + 32 * ((w >> 1) & 1) + l31) * 4;
#pragma unroll
    for (int r = 0; r < 16; ++r) *(LAS float*)(ap + ((r & 3) + 8 * (r >> 2)) * 288) = acc[r];
  } else {
    const int j = w - 8;
    lds_ptr ob = sm + ((j >> 1) * 36 + (g4 >> 1)) * 1024 + (16 * (j & 1) + l15 + 32 * (g4 & 1)) * 16;
    f32x4 acc = {0};
#pragma unroll
    for (int k = 0; k < 18; ++k) acc = MFMA16(lds_frag(ob + k * 2048), wf[k], acc);
    lds_ptr ap = sm + L_B + (((l15 >> 3) * 64 + 16 * j + 4 * g4) * 72 + 64 + (l15 & 7)) * 4;
#pragma unroll
    for (int r = 0; r < 4; ++r) *(LAS float*)(ap + r * 288) = (l15 < 8) ? acc[r] : 0.f;
  }
  if (w < 8) HSTAMP(7);
  BAR();
  if (w < 8) HSTAMP(8);
  if (tid < 576) {
    lds_ptr ap = sm + L_B + tid * 32;
    const f32x4 a0 = *(LAS f32x4*)(ap) + *(LAS f32x4*)(ap + 64 * 72 * 4);
    const f32x4 a1 = *(LAS f32x4*)(ap + 16) + *(LAS f32x4*)(ap + 64 * 72 * 4 + 16);
    u32x4 o;
#pragma unroll
    for (int i = 0; i < 4; ++i) {
      const float e0 = i < 2 ? a0[2 * i] : a1[2 * i - 4], e1 = i < 2 ? a0[2 * i + 1] : a1[2 * i - 3];
      o[i] = pk2(fmaf(gq, e0, bf_lo(bt[i])), fmaf(gq, e1, bf_hi(bt[i])));
    }
    *(u32x4*)(out + (size_t)(64 * qt + otok) * D_ + 72 * hg + 8 * opc) = o;
  }
  STAMP_FLUSH(w < 4 ? 0 : (w < 8 ? 1 : (w < 11 ? 2 : 3)));
  asm volatile("fk_end:");
}

extern "C" void kernel_launch(void* const* d_in, const int* in_sizes, int n_in, void* d_out, int out_size,
                              void* d_ws, size_t ws_size, hipStream_t stream) {
  fused_kernel<<<256, 768, 0, stream>>>((const __bf16*)d_in[0], (const __bf16*)d_in[1], (const __bf16*)d_in[2],
                                        (const __bf16*)d_in[3], (const __bf16*)d_in[4], (const __bf16*)d_in[6],
                                        (const __bf16*)d_in[7], (const __bf16*)d_in[8], (const __bf16*)d_in[9],
                                        (const __bf16*)d_in[10], (const __bf16*)d_in[11], (__bf16*)d_out, (char*)d_ws);
}
```

```cpp
#include <hip/hip_runtime.h>
#include <hip/hip_bf16.h>
#include <stdint.h>
#define NO_TOUCH

typedef __attribute__((ext_vector_type(2))) __bf16 bf16x2;
typedef __attribute__((ext_vector_type(4))) __bf16 bf16x4;
typedef __attribute__((ext_vector_type(8))) __bf16 bf16x8;
typedef __attribute__((ext_vector_type(16))) __bf16 bf16x16;
typedef __attribute__((ext_vector_type(4))) short s16x4;
typedef __attribute__((ext_vector_type(2))) float f32x2;
typedef __attribute__((ext_vector_type(4))) float f32x4;
typedef __attribute__((ext_vector_type(16))) float f32x16;
typedef __attribute__((ext_vector_type(32))) float f32x32;
typedef __attribute__((ext_vector_type(2))) unsigned u32x2;
typedef __attribute__((ext_vector_type(4))) unsigned u32x4;
#define LAS __attribute__((address_space(3)))
#define GAS __attribute__((address_space(1)))
typedef LAS char* lds_ptr;
typedef GAS unsigned gu32;
typedef GAS unsigned long long gu64;
#define DEVI __device__ __forceinline__
#define RLX_AGENT __ATOMIC_RELAXED, __HIP_MEMORY_SCOPE_AGENT
#define RLX_WG __ATOMIC_RELAXED, __HIP_MEMORY_SCOPE_WORKGROUP

constexpr int S_ = 2048, D_ = 576, DH = 18;

constexpr int KV_GROUP = 9984;
constexpr int WS_KV = 0;
constexpr int WS_EPOCH = 192 * 1024;
constexpr int WS_FKV = WS_EPOCH + 1024;
constexpr int WS_FU = WS_FKV + 8192;
constexpr int WS_FZ = WS_FU + 32768;
constexpr int WS_FO = WS_FZ + 32768;
constexpr int WS_UT = 384 * 1024;
constexpr int WS_Z = WS_UT + D_ * S_ * 2;
constexpr int WS_O = WS_Z + D_ * S_ * 2;
constexpr int WS_ZP = WS_O + D_ * S_ * 2;
constexpr int WS_END = WS_ZP + 64 * 4 * 8192;

constexpr int RSLOT = 2 * KV_GROUP;
constexpr int L_X = 0;
constexpr int L_GW = 78848;
constexpr int L_B = 80000;
constexpr int L_M = 162944;
constexpr int CV_COPY = 4160;
constexpr int CV_H = L_B, CV_HB = 8 * CV_COPY, CV_U = L_B + 2 * CV_HB, CV_UB = 7680;
constexpr int L_Q = L_B + 32768;
constexpr int M_DONE = 0, M_C1 = 32, M_C2 = 36, M_C3 = 40, M_C3B = 44, M_KVC = 48, M_KVR = 52, M_UR = 56, M_OR = 60, M_RDY = 64,
              M_QD = 192, M_UD = 104, M_HS = 108, M_ZR = 112, M_BT = 116, M_WO = 120, M_OD = 124, M_PAIR = 128, M_TAGR = 144,
              M_TAG = 148, M_KN = 152, M_EXD = 156, M_KVP = 160, M_KN0 = 164, M_KD = 168, M_QR = 172,
              M_N2 = 176, M_G = 256;
constexpr int L_EX = 0, L_BT = 28672;
#define CV_PRIO 3
#define CV_SLEEP 0
#define POLL_AT M_C2
#define POLL_N 3
#define BUBBLE 0
#define POLL_SLEEP 4
#define BT_AT 28
constexpr unsigned SPIN_CAP = 1u << 18;

#define STAMP_DECL do {} while (0)
#define STAMP(I) do {} while (0)
#define STAMP_FLUSH(KID) do {} while (0)
#define RSTAMP(I) do {} while (0)

#define PSTAMP(I) do {} while (0)
#define NSTAMP(I) STAMP(I)
#define CSTAMP(I) do {} while (0)
#define HSTAMP(I) NSTAMP(I)

DEVI unsigned pk2(float a, float b) {
  bf16x2 v = {(__bf16)a, (__bf16)b};
  return __builtin_bit_cast(unsigned, v);
}
DEVI float u2f(unsigned u) { return __builtin_bit_cast(float, u); }
DEVI float bf_lo(unsigned u) { return __builtin_bit_cast(float, u << 16); }
DEVI float bf_hi(unsigned u) { return __builtin_bit_cast(float, u & 0xffff0000u); }
DEVI float dot2(unsigned u, float c) { const bf16x2 v = __builtin_bit_cast(bf16x2, u); return __builtin_amdgcn_fdot2_f32_bf16(v, v, c, false); }
DEVI float rbf(float a) { return (float)(__bf16)a; }
DEVI int swap23(int r) { return (r & ~12) | ((r & 4) << 1) | ((r & 8) >> 1); }
DEVI float xhalf(float v) {
  const unsigned u = __builtin_bit_cast(unsigned, v);
  const auto r = __builtin_amdgcn_permlane32_swap(u, u, false, false);
  return __builtin_bit_cast(float, (threadIdx.x & 32) ? r[0] : r[1]);
}

template <int CTRL>
DEVI float dpp_max(float v) {
  const int u = __builtin_bit_cast(int, v);
  return fmaxf(v, __builtin_bit_cast(float, __builtin_amdgcn_update_dpp(u, u, CTRL, 0xf, 0xf, false)));
}
DEVI float max16(float v) {
  return dpp_max<0x140>(dpp_max<0x141>(dpp_max<0x4e>(dpp_max<0xb1>(v))));
}
DEVI float wave_max(float v) {
  v = max16(v);
  const unsigned u = __builtin_bit_cast(unsigned, v);
  return fmaxf(fmaxf(u2f(__builtin_amdgcn_readlane(u, 0)), u2f(__builtin_amdgcn_readlane(u, 16))),
               fmaxf(u2f(__builtin_amdgcn_readlane(u, 32)), u2f(__builtin_amdgcn_readlane(u, 48))));
}
DEVI void glds(const void* sbase, unsigned voff, unsigned lds_dst) {
  asm volatile("s_mov_b32 m0, %2\n\ts_nop 0\n\tglobal_load_lds_dwordx4 %0, %1" :: "v"(voff), "s"(sbase), "s"(lds_dst) : "memory");
}
DEVI void glds_sc1(const void* sbase, unsigned voff, unsigned lds_dst) {
  asm volatile("s_mov_b32 m0, %2\n\ts_nop 0\n\tglobal_load_lds_dwordx4 %0, %1 sc1" :: "v"(voff), "s"(sbase), "s"(lds_dst) : "memory");
}
#define GLDS4(SC, sbase_, v0_, v3_, dst_)                                                                                  \
  asm volatile("s_mov_b32 m0, %3\n\ts_nop 0\n\tglobal_load_lds_dwordx4 %0, %2" SC "\n\tglobal_load_lds_dwordx4 %0, %2 offset:1024" SC   \
               "\n\tglobal_load_lds_dwordx4 %0, %2 offset:2048" SC "\n\tglobal_load_lds_dwordx4 %1, %2 offset:3072" SC           \
               :: "v"(v0_), "v"(v3_), "s"(sbase_), "s"(dst_) : "memory")
#define VM_WAIT(N) asm volatile("s_waitcnt vmcnt(" #N ")" ::: "memory")
#define BAR() asm volatile("s_waitcnt lgkmcnt(0)\n\ts_barrier" ::: "memory")

DEVI bf16x8 tr_frag(lds_ptr p0, lds_ptr p1) {
  bf16x4 a = __builtin_amdgcn_ds_read_tr16_b64_v4bf16((LAS bf16x4*)p0);
  bf16x4 b = __builtin_amdgcn_ds_read_tr16_b64_v4bf16((LAS bf16x4*)p1);
  return __builtin_shufflevector(a, b, 0, 1, 2, 3, 4, 5, 6, 7);
}
DEVI bf16x8 lds_frag(lds_ptr p) { return *(LAS bf16x8*)p; }
DEVI void lds_signal(lds_ptr p, unsigned n = 1u) {
  asm volatile("s_waitcnt lgkmcnt(0)\n\ts_mov_b64 exec, 1\n\tds_add_u32 %0, %1\n\ts_mov_b64 exec, -1" :: "v"(p), "v"(n) : "memory");
}
DEVI unsigned lds_ticket(lds_ptr p) {
  unsigned old;
  asm volatile("s_waitcnt lgkmcnt(0)\n\ts_mov_b64 exec, 1\n\tds_add_rtn_u32 %0, %1, %2\n\ts_mov_b64 exec, -1\n\ts_waitcnt lgkmcnt(0)"
               : "=&v"(old) : "v"(p), "v"(1u) : "memory");
  return __builtin_amdgcn_readfirstlane(old);
}
template <int SLEEP = 0>
DEVI void lds_wait(lds_ptr p, unsigned n) {
  unsigned v, t;
  asm volatile("1:\n\tds_read_b32 %0, %2\n\ts_waitcnt lgkmcnt(0)\n\tv_readfirstlane_b32 %1, %0\n\ts_nop 0\n\ts_cmp_ge_u32 %1, %3\n\t"
               "s_cbranch_scc1 2f\n\ts_sleep %4\n\ts_branch 1b\n2:"
               : "=&v"(v), "=&s"(t) : "v"(p), "s"(n), "i"(SLEEP) : "scc", "memory");
}
DEVI unsigned lds_peek(lds_ptr p) {
  unsigned v;
  asm volatile("ds_read_b32 %0, %1\n\ts_waitcnt lgkmcnt(0)" : "=&v"(v) : "v"(p) : "memory");
  return __builtin_amdgcn_readfirstlane(v);
}
#define LD_SC1_16(off) __builtin_bit_cast(u32x4, __builtin_amdgcn_raw_buffer_load_b128(rs, (off), 0, 16))
#define ST_SC1_16(v, off) __builtin_amdgcn_raw_buffer_store_b128((v), rs, (off), 0, 16)
#define ST_SC1_8(v, off) __builtin_amdgcn_raw_buffer_store_b64((v), rs, (off), 0, 16)
#define ST_SC1_4(v, off) __builtin_amdgcn_raw_buffer_store_b32((v), rs, (off), 0, 16)

DEVI f32x16 lo16(f32x32 v) { return __builtin_shufflevector(v, v, 0, 1, 2, 3, 4, 5, 6, 7, 8, 9, 10, 11, 12, 13, 14, 15); }
DEVI f32x16 hi16(f32x32 v) { return __builtin_shufflevector(v, v, 16, 17, 18, 19, 20, 21, 22, 23, 24, 25, 26, 27, 28, 29, 30, 31); }
DEVI bf16x16 raw_lo(f32x16 v) { return __builtin_bit_cast(bf16x16, __builtin_shufflevector(v, v, 0, 1, 2, 3, 4, 5, 6, 7)); }
DEVI bf16x16 raw_hi(f32x16 v) { return __builtin_bit_cast(bf16x16, __builtin_shufflevector(v, v, 8, 9, 10, 11, 12, 13, 14, 15)); }
constexpr int SPIDX = (int)0xddddddddu;
#define MFMA32(a, b, c) __builtin_amdgcn_mfma_f32_32x32x16_bf16((a), (b), (c), 0, 0, 0)
#define MFMA16(a, b, c) __builtin_amdgcn_mfma_f32_16x16x32_bf16((a), (b), (c), 0, 0, 0)

DEVI void online_step(f32x16 s, float& m, f32x16& o, bf16x8 va, bf16x8 vb) {
  float t = s[0];
#pragma unroll
  for (int i = 1; i < 16; ++i) t = fmaxf(t, s[i]);
  t = fmaxf(t, xhalf(t));
  const float mn = fmaxf(m, t);
  const float al = __builtin_amdgcn_exp2f(m - mn);
  m = mn;
#pragma unroll
  for (int i = 0; i < 16; ++i) { o[i] *= al; s[i] = __builtin_amdgcn_exp2f(s[i] - mn); }
  o = __builtin_amdgcn_smfmac_f32_32x32x32_bf16(va, raw_lo(s), o, SPIDX, 0, 0);
  o = __builtin_amdgcn_smfmac_f32_32x32x32_bf16(vb, raw_hi(s), o, SPIDX, 0, 0);
}

#define UNIT_LOAD(CP, TG)                                                                                        \
  const int uc0 = 72 * hg + 2 * (CP), ut0 = 64 * qt + 8 * (TG);                                                  \
  const u32x4 usw = *(const u32x4*)(shk + uc0 * 4);                                                              \
  unsigned ukr[11], uvr[8];                                                                                      \
  {                                                                                                              \
    const unsigned bo = (ut0 * D_ + uc0) * 2, bo3 = ut0 ? bo - 3 * 1152 : bo;                                    \
    _Pragma("unroll") for (int i = 0; i < 3; ++i) ukr[i] = *(const unsigned*)((const char*)bk + bo3 + i * 1152); \
    _Pragma("unroll") for (int i = 0; i < 8; ++i) {                                                              \
      ukr[3 + i] = *(const unsigned*)((const char*)bk + bo + i * 1152);                                          \
      uvr[i] = *(const unsigned*)((const char*)bv + bo + i * 1152);                                              \
    }                                                                                                            \
  }
#define UNIT_DO()                                                                                                \
  u32x4 o0, o1;                                                                                                  \
  {                                                                                                              \
    const f32x2 w3 = {bf_hi(usw[1]), bf_hi(usw[3])}, w2 = {bf_lo(usw[1]), bf_lo(usw[3])};                        \
    const f32x2 w1 = {bf_hi(usw[0]), bf_hi(usw[2])}, w0 = {bf_lo(usw[0]), bf_lo(usw[2])};                        \
    f32x2 kf[11], r[8];                                                                                          \
    _Pragma("unroll") for (int i = 0; i < 11; ++i) {                                                             \
      const unsigned kk = (i < 3 && !ut0) ? 0u : ukr[i];                                                         \
      kf[i] = (f32x2){bf_lo(kk), bf_hi(kk)};                                                                     \
    }                                                                                                            \
    _Pragma("unroll") for (int i = 0; i < 8; ++i)                                                                \
      r[i] = __builtin_elementwise_fma(kf[i + 3], w0, __builtin_elementwise_fma(kf[i + 2], w1,                   \
             __builtin_elementwise_fma(kf[i + 1], w2, kf[i] * w3))) * (f32x2){bf_lo(uvr[i]), bf_hi(uvr[i])};     \
    _Pragma("unroll") for (int i = 0; i < 4; ++i) {                                                              \
      o0[i] = pk2(r[7 - 2 * i][0], r[6 - 2 * i][0]);                                                             \
      o1[i] = pk2(r[7 - 2 * i][1], r[6 - 2 * i][1]);                                                             \
    }                                                                                                            \
  }

__global__ void __launch_bounds__(768, 3)
fused_kernel(const __bf16* __restrict__ x, const __bf16* __restrict__ wq, const __bf16* __restrict__ wk,
             const __bf16* __restrict__ wv, const __bf16* __restrict__ wo, const __bf16* __restrict__ bk,
             const __bf16* __restrict__ bv, const __bf16* __restrict__ shk, const __bf16* __restrict__ ssmk,
             const __bf16* __restrict__ ssmd, const __bf16* __restrict__ gw, __bf16* __restrict__ out, char* ws) {
  __shared__ __attribute__((aligned(16))) char smem[160 * 1024];
  asm volatile("fk_beg:");
  STAMP_DECL; STAMP(0);
  const int tid = threadIdx.x, lane = tid & 63;
  const int w = __builtin_amdgcn_readfirstlane(tid >> 6);
  const int bid = blockIdx.x, jj = bid >> 3;
  const int qt = (bid & 7) * 4 + (jj & 3), hg = jj >> 2;
  const unsigned sbase = (unsigned)(size_t)smem;
  lds_ptr sm = (lds_ptr)smem;
  lds_ptr mi = sm + L_M;
  const int h = lane >> 5, l31 = lane & 31, l15 = lane & 15, g4 = lane >> 4;
  const int tq = (lane & 15) >> 2, tp = lane & 3;
  const auto rs = __builtin_amdgcn_make_buffer_rsrc(ws, 0, WS_END, 0x00020000);
  GAS char* gws = (GAS char*)ws;
  const int rot = (hg + qt) % 3;
  const int s0 = bid & 7;
  const int pt = (hg >> 1) & 1, sel = hg & 1;
  const char* xt = (const char*)x + (size_t)qt * 73728;
  const char* wqs = (const char*)wq + 144 * hg;
  const unsigned vxe = (lane >> 3) * 1152 + (((lane & 7) ^ g4) << 4), vxo = 9216 + (lane >> 3) * 1152 + (((lane & 7) ^ (4 + g4)) << 4);
  const int otok = tid / 9, opc = tid - 9 * otok;

  if (w >= 8) {
    const int j = w - 8, ht = tid - 512;
    __builtin_amdgcn_s_setprio(3);
    if (j < 3) {
      const int th = (j + rot) % 3;
      const unsigned stg = L_B + 2 * 27648 + j * 9216;
#pragma unroll
      for (int c = 0; c < 6; ++c) {
        const char* src = xt + (2 * pt + (c & 1)) * 18432 + (3 * th + (c >> 1)) * 128;
        const unsigned dst = sbase + L_X + (3 * j + (c >> 1)) * 8192 + (2 * pt + (c & 1)) * 2048;
        glds(src, vxe, dst);
        glds(src, vxo, dst + 1024);
      }
      if (hg < 4) {
        const char* wsrc = (const char*)(sel ? wv : wk) + th * 6912;
#pragma unroll
        for (int i = 0; i < 3; ++i) {
          const int f = 64 * i + lane, row = f / 3, pc = f - 3 * row;
          const unsigned vo = row * 36 + (pc == 2 ? 20 : 16 * pc);
#pragma unroll
          for (int p = 0; p < 3; ++p) glds(wsrc + p * 2304, vo, sbase + stg + p * 3072 + i * 1024);
        }
      }
      BAR();
      VM_WAIT(0);
      STAMP(1);
      lds_signal(mi + M_KD);
      lds_signal(mi + M_C1);
      if (hg < 4) lds_wait<1>(mi + M_KVC, 4);
      STAMP(2);
      {
        const char* src = wqs + (size_t)(192 * ((2 + rot) % 3) + 64 * j) * 1152;
        int pc = lane % 9;
        unsigned vo = (lane / 9) * 1152 + pc * 16;
#pragma unroll
        for (int i = 0; i < 9; ++i) {
          glds(src, vo, sbase + stg + i * 1024);
          vo += pc == 8 ? 9088 : 8080;
          pc = pc == 8 ? 0 : pc + 1;
        }
      }
      VM_WAIT(0);
      lds_signal(mi + M_C3);
      lds_wait<1>(mi + M_KVR, 1);
    } else {
      const unsigned epoch = __hip_atomic_load((gu32*)(gws + WS_EPOCH), RLX_AGENT);
      unsigned pf = 0;
      {
        unsigned long long pc0;
        unsigned back, len;
        asm volatile("s_getpc_b64 %0\n\tfk_pc:\n\ts_mov_b32 %1, fk_pc-fk_beg\n\ts_mov_b32 %2, fk_end-fk_beg" : "=s"(pc0), "=s"(back), "=s"(len));
        const unsigned o = min((unsigned)(lane * 8 + (jj & 7)) * 128u, len);
        asm volatile("global_load_dword %0, %1, %2" : "=v"(pf) : "v"(o), "s"(pc0 - back) : "memory");
      }
      BAR();
      asm volatile("s_waitcnt vmcnt(0)" : "+v"(pf) :: "memory");
      const unsigned tg = __builtin_amdgcn_readfirstlane(epoch) + 1u;
      if (lane == 0) *(LAS unsigned*)(mi + M_TAG) = tg;
      lds_signal(mi + M_TAGR);
      lds_wait<1>(mi + POLL_AT, POLL_N);
      PSTAMP(5);
      float kn = 0.f;
#pragma unroll 1
      for (unsigned spins = 0; spins < SPIN_CAP; ++spins) {
        u32x4 v0 = {0, tg, 0, tg};
        if (lane < 8) v0 = LD_SC1_16(WS_FKV + (8 * s0 + lane) * 128);
        const bool ok = v0[1] == tg && v0[3] == tg;
        kn = u2f(v0[0]);
        asm volatile("" ::: "memory");
        if (spins == 0) PSTAMP(6);
        if (spins == 1) PSTAMP(7);
        if (spins == 2) PSTAMP(8);
        if (spins == 3) PSTAMP(9);
        if (__all(ok)) break;
        __builtin_amdgcn_s_sleep(POLL_SLEEP);
      }
      lds_signal(mi + M_KVR);
      STAMP(2); RSTAMP(2);
      kn = max16(kn);
      if (lane == 0) *(LAS float*)(mi + M_KN0) = kn;
    }
    const unsigned tag = *(LAS unsigned*)(mi + M_TAG);
    const int kvo = j * 1024 + lane * 16;
    const bool kvlast = kvo + 4 * 4096 < RSLOT;
    const int kvo4 = kvlast ? kvo + 16384 : kvo;
    auto kv_dma = [&](int st) {
      const char* src = ws + WS_KV + ((s0 + st) & 7) * RSLOT + j * 4992;
      const unsigned dst = sbase + st * RSLOT + j * 4992;
      GLDS4(" sc1", src, lane * 16, lane * 16, dst);
      if (lane < 56) glds_sc1(src + 4096, lane * 16, dst + 4096);
    };
    const unsigned vh3 = (lane == 63 ? 62 : lane) * 16;
    auto hload = [&](int cl, int b) {
      const char* hsrc = (const char*)(ssmk + (size_t)(72 * hg + cl) * S_) + 4 * j + 2;
      const unsigned dst = sbase + CV_H + b * CV_HB + 2 * j * CV_COPY + 64;
      GLDS4("", hsrc, lane * 16, vh3, dst);
      GLDS4("", hsrc + 2, lane * 16, vh3, dst + CV_COPY);
    };
    auto uload = [&](int cl) {
      const int tgq = ht & 7;
      const int o = cl < 64 ? (tgq >> 1) * 2048 + (cl & 1) * 1024 + ((tgq & 1) * 32 + (cl >> 1)) * 16
                            : 8192 + (cl & 1) * 512 + (tgq * 4 + ((cl - 64) >> 1)) * 16;
      return LD_SC1_16(WS_UT + ((ht >> 3) * 8 + hg) * 9216 + o);
    };
    const int ce = 64 + (qt >> 2);
    const unsigned short h8a = *(const unsigned short*)(ssmk + (size_t)(72 * hg + 2 * qt + (j >> 1)) * S_ + (lane & 7));
    const unsigned short h8e = *(const unsigned short*)(ssmk + (size_t)(72 * hg + ce) * S_ + (lane & 7));
    const float dva = (float)ssmd[72 * hg + 2 * qt + (j >> 1)], dve = (qt & 3) ? 0.f : (float)ssmd[72 * hg + ce];
    {
      u32x4 ra[5];
#pragma unroll
      for (int i = 0; i < 5; ++i) ra[i] = LD_SC1_16(WS_KV + s0 * RSLOT + (i < 4 ? kvo + i * 4096 : kvo4));
      UNIT_LOAD(l31, 2 * j + h);
      lds_wait(mi + M_C3B, 8);
#pragma unroll
      for (int i = 0; i < 5; ++i) if (i < 4 || kvlast) *(LAS u32x4*)(sm + kvo + i * 4096) = ra[i];
      lds_signal(mi + M_QD);
      STAMP(3);
      if (j == 3) {
        float kn = 0.f;
#pragma unroll 1
        for (unsigned spins = 0; spins < SPIN_CAP; ++spins) {
          const u32x4 v0 = LD_SC1_16(WS_FKV + lane * 128);
          const bool ok = v0[1] == tag && v0[3] == tag;
          kn = u2f(v0[0]);
          asm volatile("" ::: "memory");
          if (__all(ok)) break;
          __builtin_amdgcn_s_sleep(POLL_SLEEP);
        }
        lds_signal(mi + M_KVR);
        kv_dma(1);
        kn = wave_max(kn);
        if (lane == 0) *(LAS float*)(mi + M_KN) = kn;
      }
      UNIT_DO();
      ST_SC1_16(o0, WS_UT + (qt * 8 + hg) * 9216 + j * 2048 + lane * 16);
      ST_SC1_16(o1, WS_UT + (qt * 8 + hg) * 9216 + j * 2048 + 1024 + lane * 16);
      if (j < 3) { lds_wait(mi + M_KVR, 2); kv_dma(1); }
      STAMP(4);
      VM_WAIT(0);
      lds_signal(mi + M_RDY + 4, 2);
      lds_wait<1>(mi + M_QR, 8);
      if (lds_ticket(mi + M_UD) == 3u && lane == 0) __hip_atomic_store((gu32*)(gws + WS_FU) + (hg * 32 + qt) * 32, tag, RLX_AGENT);
      kv_dma(2);
      kv_dma(3);
    }
    hload(2 * qt, 0);
    hload(2 * qt + 1, 1);
    {
      *(LAS u32x4*)(sm + CV_U + (ht >> 7) * CV_UB + ((ht & 127) >> 2) * 80 + (ht & 3) * 16) = (u32x4){0, 0, 0, 0};
      lds_ptr hz = sm + CV_H + (j >> 1) * CV_HB + (lane >> 3) * CV_COPY;
      *(LAS u32x2*)(hz + (lane & 7) * 8) = (u32x2){0, 0};
      if ((lane & 7) <= (lane >> 3)) *(LAS unsigned short*)(hz + 48 + (7 - (lane >> 3) + (lane & 7)) * 2) = h8a;
      if (j == 0) {
        const float lg = *(LAS float*)(mi + M_G + lane * 4);
        *(LAS float*)(mi + M_G + lane * 4) = rbf(__builtin_amdgcn_rcpf(1.f + __expf(-rbf(lg))));
      }
    }
    VM_WAIT(16);
    lds_signal(mi + M_RDY + 8, 2);
    lds_signal(mi + M_RDY + 12, 2);
    if (j == 3) {
#pragma unroll 1
      for (unsigned spins = 0; spins < SPIN_CAP; ++spins) {
        bool ok = h || __hip_atomic_load((gu32*)(gws + WS_FU) + (hg * 32 + lane) * 32, RLX_AGENT) == tag;
        if (bid == 0) {
#pragma unroll
          for (int i = 0; i < 4; ++i) ok &= __hip_atomic_load((gu32*)(gws + WS_FU) + (64 * i + lane) * 32, RLX_AGENT) == tag;
        }
        if (__all(ok)) break;
        __builtin_amdgcn_s_sleep(4);
      }
      if (bid == 0 && lane == 0) __hip_atomic_store((gu32*)(gws + WS_EPOCH), tag, RLX_AGENT);
      lds_signal(mi + M_UR);
    } else {
      lds_wait<2>(mi + M_UR, 1);
    }
    unsigned hs = 0;
#define HSYNC() do { lds_signal(mi + M_HS); hs += 4; lds_wait<1>(mi + M_HS, hs); } while (0)
    NSTAMP(5);
    CSTAMP(1);
    const u32x4 ue = uload(ce);
    {
      const u32x4 ua = uload(2 * qt), ub = uload(2 * qt + 1);
      *(LAS u32x4*)(sm + CV_U + 2560 + (ht >> 2) * 80 + (ht & 3) * 16) = ua;
      *(LAS u32x4*)(sm + CV_U + CV_UB + 2560 + (ht >> 2) * 80 + (ht & 3) * 16) = ub;
    }
    constexpr int CV_UE = CV_H + CV_HB + 24576;
    lds_ptr px = sm + CV_H + CV_HB + lane * 16;
#pragma unroll 1
    for (int ph = 0; ph < 2; ++ph) {
      const int d0 = ph ? 8 * (qt & 3) + 2 * j : 16 * (j & 1);
      VM_WAIT(0);
      if (ph == 0) CSTAMP(2);
      HSYNC();
      if (ph == 0) CSTAMP(3);
      lds_ptr ub = sm + (ph ? CV_UE : CV_U + (j >> 1) * CV_UB) + 2560;
      lds_ptr ab = sm + CV_H + (ph ? 0 : j >> 1) * CV_HB + (l31 & 7) * CV_COPY + (h + (l31 >> 3)) * 16 + 64 * d0;
      lds_ptr bb = ub + (l31 - d0 - 2) * 80 + (1 - h) * 16;
      f32x16 y0 = {0}, y1 = {0};
      bf16x8 fx[8], fy[8];
#define PACE() do { if (CV_SLEEP) { __builtin_amdgcn_sched_barrier(0); __builtin_amdgcn_s_sleep(CV_SLEEP); __builtin_amdgcn_sched_barrier(0); } } while (0)
#define CV_LD(F, D) _Pragma("unroll") for (int s = 0; s < 2; ++s) {                                     \
        F[4 * s] = lds_frag(ab + 32 * (2 * (D) + 1 - s)); F[4 * s + 1] = lds_frag(bb + 80 * (2 - (D)) + 32 * s);  \
        F[4 * s + 2] = lds_frag(bb + 80 * (34 - (D)) + 32 * s); F[4 * s + 3] = lds_frag(ab + 32 * (2 * (D) + 65 - s)); } \
        __builtin_amdgcn_sched_barrier(0)
#define CV_MM(F) _Pragma("unroll") for (int s = 0; s < 2; ++s) {                                        \
        y0 = MFMA32(F[4 * s], F[4 * s + 1], y0); PACE(); y1 = MFMA32(F[4 * s], F[4 * s + 2], y1); PACE(); y1 = MFMA32(F[4 * s + 3], F[4 * s + 1], y1); PACE(); } \
        __builtin_amdgcn_sched_barrier(0)
      __builtin_amdgcn_s_setprio(CV_PRIO);
      CV_LD(fx, 0);
#pragma unroll 1
      for (int it = ph ? 7 : 0; it < 8; ++it) {
        CV_LD(fy, 1);
        CV_MM(fx);
        CV_LD(fx, 2);
        CV_MM(fy);
        ab += 128;
        bb -= 160;
      }
      __builtin_amdgcn_s_setprio(3);
      if (ph == 0) {
        CSTAMP(4);
        HSYNC();
        hload(ce, 0);
        if (j == 0 && (lane & 7) <= (lane >> 3))
          *(LAS unsigned short*)(sm + CV_H + (lane >> 3) * CV_COPY + 48 + (7 - (lane >> 3) + (lane & 7)) * 2) = h8e;
        if (ht < 160) *(LAS u32x4*)(sm + CV_UE + ht * 16) = (u32x4){0, 0, 0, 0};
        *(LAS u32x4*)(sm + CV_UE + 2560 + (ht >> 2) * 80 + (ht & 3) * 16) = ue;
      }
      {
        const f32x16 give = (j & 1) ? y0 : y1;
#pragma unroll
        for (int g = 0; g < 4; ++g) *(LAS f32x4*)(px + j * 4096 + g * 1024) = (f32x4){give[4 * g], give[4 * g + 1], give[4 * g + 2], give[4 * g + 3]};
        if (j & 1) y0 = y1;
        if (ph && j >= 2) {
#pragma unroll
          for (int g = 0; g < 4; ++g) *(LAS f32x4*)(px + (j + 2) * 4096 + g * 1024) = (f32x4){y0[4 * g], y0[4 * g + 1], y0[4 * g + 2], y0[4 * g + 3]};
        }
      }
      if (ph == 0) CSTAMP(9);
      HSYNC();
      if (ph == 0) CSTAMP(10);
      if (!ph || j < 2) {
        const float dv = ph ? dve : dva;
        u32x4 outv[2];
#pragma unroll
        for (int g = 0; g < 4; ++g) {
          f32x4 s = *(LAS f32x4*)(px + (j ^ 1) * 4096 + g * 1024);
          if (ph) s += *(LAS f32x4*)(px + (3 - j) * 4096 + g * 1024) + *(LAS f32x4*)(px + (j + 4) * 4096 + g * 1024);
          const u32x2 uu = *(LAS u32x2*)(ub + (32 * (j & 1) + l31) * 80 + g * 16 + (1 - h) * 8);
          s = (f32x4){fmaf(bf_hi(uu[1]), dv, s[0] + y0[4 * g]), fmaf(bf_lo(uu[1]), dv, s[1] + y0[4 * g + 1]),
                      fmaf(bf_hi(uu[0]), dv, s[2] + y0[4 * g + 2]), fmaf(bf_lo(uu[0]), dv, s[3] + y0[4 * g + 3])};
          if (ph) ST_SC1_16(__builtin_bit_cast(u32x4, s), WS_ZP + (hg * 32 + qt) * 8192 + j * 4096 + g * 1024 + lane * 16);
          outv[g >> 1][2 * (g & 1)] = pk2(s[0], s[1]);
          outv[g >> 1][2 * (g & 1) + 1] = pk2(s[2], s[3]);
        }
        if (!ph) {
          const int zo = WS_Z + (72 * hg + 2 * qt + (j >> 1)) * 4096 + (j & 1) * 2048 + lane * 16;
          ST_SC1_16(outv[0], zo);
          ST_SC1_16(outv[1], zo + 1024);
        }
      }
      if (ph == 0) CSTAMP(7);
    }
    NSTAMP(6);
    VM_WAIT(0);
    HSYNC();
    if (ht == 192) __hip_atomic_store((gu32*)(gws + WS_FZ) + (hg * 32 + qt) * 32, tag, RLX_AGENT);
    if (lane < 54) {
      const unsigned vo = (lane / 9) * 1152 + (lane % 9) * 16;
#pragma unroll 1
      for (int p = 24 * j; p < 24 * j + 24; p += 4)
        asm volatile("s_mov_b32 m0, %5\n\ts_nop 0\n\tglobal_load_lds_dwordx4 %0, %4\n\tglobal_load_lds_dwordx4 %1, %4 offset:864\n\t"
                     "global_load_lds_dwordx4 %2, %4 offset:1728\n\tglobal_load_lds_dwordx4 %3, %4 offset:2592"
                     :: "v"(vo), "v"(vo + 6048), "v"(vo + 12096), "v"(vo + 18144), "s"((const char*)wo + 144 * hg + p * 6912),
                        "s"(sbase + L_B + p * 864) : "memory");
    }
    VM_WAIT(0);
    lds_signal(mi + M_WO);
    HSTAMP(7);
    if (j == 3) {
#pragma unroll 1
      for (unsigned spins = 0; spins < SPIN_CAP; ++spins) {
        if (__all(h || __hip_atomic_load((gu32*)(gws + WS_FZ) + (hg * 32 + lane) * 32, RLX_AGENT) == tag)) break;
        __builtin_amdgcn_s_sleep(16);
      }
      lds_signal(mi + M_ZR);
    } else {
      lds_wait<8>(mi + M_ZR, 1);
    }
    {
      u32x4 zv[3], xv[3];
#pragma unroll
      for (int i = 0; i < 3; ++i) {
        const int n = (i < 2 || ht < 64) ? ht + 256 * i : ht;
        const int zl = (2 * (qt & 15) + (n & 1) + 32 * ((n >> 1) & 1)) * 16;
        if (i < 2) {
          zv[i] = LD_SC1_16(WS_Z + (72 * hg + (n >> 3)) * 4096 + (qt >> 4) * 2048 + ((n >> 2) & 1) * 1024 + zl);
        } else if (j == 0) {
          f32x4 sa = {0}, sb = {0};
#pragma unroll
          for (int p = 0; p < 4; ++p) {
            const int zo = WS_ZP + (hg * 32 + (lane >> 3) * 4 + p) * 8192 + (qt >> 4) * 4096 + ((n >> 2) & 1) * 2048 + zl;
            sa += __builtin_bit_cast(f32x4, LD_SC1_16(zo));
            sb += __builtin_bit_cast(f32x4, LD_SC1_16(zo + 1024));
          }
          zv[i] = (u32x4){pk2(sa[0], sa[1]), pk2(sa[2], sa[3]), pk2(sb[0], sb[1]), pk2(sb[2], sb[3])};
        } else {
          zv[i] = zv[0];
        }
        xv[i] = *(const u32x4*)(xt + (n / 9) * 1152 + 144 * hg + (n % 9) * 16);
      }
      lds_wait<4>(mi + M_DONE + BT_AT, 8);
#pragma unroll
      for (int i = 0; i < 3; ++i) {
        const int n = (i < 2 || ht < 64) ? ht + 256 * i : ht;
        lds_ptr zp = sm + L_BT + ((32 * (n & 1) + 16 * ((n >> 2) & 1) + 4 * ((n >> 1) & 1)) * 72 + (n >> 3)) * 2;
#pragma unroll
        for (int e = 0; e < 8; ++e)
          *(LAS unsigned short*)(zp + ((e & 3) + 8 * (e >> 2)) * 144) = (unsigned short)((e & 1) ? zv[i][e >> 1] >> 16 : zv[i][e >> 1]);
      }
      lds_signal(mi + M_HS);
      lds_wait(mi + M_HS, hs + 4);
#pragma unroll
      for (int i = 0; i < 3; ++i) {
        if (i < 2 || ht < 64) {
          const int n = ht + 256 * i;
          const u32x4 z = *(LAS u32x4*)(sm + L_BT + n * 16);
          const float omg = rbf(1.f - *(LAS float*)(mi + M_G + (n / 9) * 4));
          u32x4 o;
#pragma unroll
          for (int e = 0; e < 4; ++e)
            o[e] = pk2(omg * bf_lo(xv[i][e]) * bf_lo(z[e]), omg * bf_hi(xv[i][e]) * bf_hi(z[e]));
          *(LAS u32x4*)(sm + L_BT + n * 16) = o;
        }
      }
      lds_signal(mi + M_BT);
    }
    HSTAMP(8);
    __builtin_amdgcn_s_setprio(0);
  } else {
    if (w == 0) *(LAS int*)(mi + lane * 4) = (lane == M_RDY / 4 + 2 || lane == M_RDY / 4 + 3) ? -8 : 0;
    BAR();
    const float QS = 0.23570226039551584f * 1.4426950408889634f;
    if (w >= 5) {
      const int wi = w - 5;
      unsigned vw[3];
#pragma unroll
      for (int m = 0; m < 3; ++m) {
        const int f = 64 * (wi + 3 * m) + lane, row = f / 9;
        vw[m] = row * 1152 + (f - 9 * row) * 16;
      }
#pragma unroll
      for (int p = 0; p < 3; ++p) {
        const int th = (p + rot) % 3;
#pragma unroll
        for (int pr = 0; pr < 2; ++pr) {
          const int g2 = 2 * (1 - pt) + pr;
          const char* src = xt + g2 * 18432 + (3 * th + wi) * 128;
          glds(src, vxe, sbase + L_X + (3 * p + wi) * 8192 + g2 * 2048);
          glds(src, vxo, sbase + L_X + (3 * p + wi) * 8192 + g2 * 2048 + 1024);
        }
        if (p < 2) {
#pragma unroll
          for (int per = 0; per < 3; ++per) {
#pragma unroll
            for (int m = 0; m < 3; ++m)
              glds(wqs + (size_t)(192 * th + 64 * per) * 1152, vw[m], sbase + L_B + p * 27648 + per * 9216 + (wi + 3 * m) * 1024);
            if (p == 1 && per == 0) { VM_WAIT(7); lds_signal(mi + M_C1); }
          }
        }
      }
      STAMP(1);
      VM_WAIT(4);
      lds_signal(mi + M_C2);
    } else if (w < 4) {
      if (hg < 4) {
        const int mt = w & 1, nt = w >> 1, row = 32 * pt + 16 * mt + l15;
        lds_ptr wb = sm + L_B + 2 * 27648 + (8 * g4 + tq) * 48 + (nt ? 32 + (tp & 1) * 8 : tp * 8);
        lds_ptr xb = sm + L_X + row * 128;
        const int T = 2 * qt + pt, i4 = T & 3, gb = WS_KV + (T >> 2) * KV_GROUP, rp = swap23(16 * mt + l15);
        int so;
        bool st;
        if (sel) {
          const int d = nt ? (l15 < 8 ? 10 + l15 + 18 * (g4 & 1) : 36 + (g4 & 1)) : l15 + 18 * (g4 & 1);
          so = gb + 4096 + i4 * 1216 + mt * 608 + d * 16 + (g4 >> 1) * 8;
          st = !nt || (l15 >= 6 && l15 <= 8);
        } else {
          so = nt ? gb + 8960 + i4 * 256 + rp * 8 : gb + i4 * 1024 + (rp + 32 * (g4 & 1)) * 16 + (g4 >> 1) * 8;
          st = !nt || g4 == 1;
        }
        lds_wait(mi + M_KD, 3);
        PSTAMP(5);
        bf16x8 fa[18], fb[18];
#pragma unroll
        for (int k = 0; k < 18; ++k) {
          fa[k] = tr_frag(wb + k * 1536, wb + k * 1536 + 192);
          fb[k] = lds_frag(xb + (k >> 1) * 8192 + (((4 * (k & 1) + g4) ^ (l15 >> 1)) << 4));
        }
        f32x4 acc = {0}, acc1 = {0};
        if (sel) {
#pragma unroll
          for (int k = 0; k < 18; k += 2) { acc = MFMA16(fb[k], fa[k], acc); acc1 = MFMA16(fb[k + 1], fa[k + 1], acc1); }
        } else {
#pragma unroll
          for (int k = 0; k < 18; k += 2) { acc = MFMA16(fa[k], fb[k], acc); acc1 = MFMA16(fa[k + 1], fb[k + 1], acc1); }
        }
        acc += acc1;
        u32x2 a = {pk2(acc[0], acc[1]), pk2(acc[2], acc[3])};
        if (nt) {
          if (!sel) a = (u32x2){a[1], 0x3f803f80u};
          else if (l15 == 8) a[0] = a[1] = (g4 & 1) ? 0u : 0x3f803f80u;
        }
        if (st) ST_SC1_8(a, so);
        PSTAMP(6);
        lds_signal(mi + M_KVC);
        float n2 = bf_lo(a[0]) * bf_lo(a[0]) + bf_hi(a[0]) * bf_hi(a[0]);
        if (!nt) n2 += bf_lo(a[1]) * bf_lo(a[1]) + bf_hi(a[1]) * bf_hi(a[1]);
        if (!st) n2 = 0.f;
        n2 += xhalf(n2);
        n2 += u2f(__builtin_amdgcn_ds_swizzle(__builtin_bit_cast(int, n2), 0x401f));
        n2 = max16(n2);
        if (lane == 0) *(LAS float*)(mi + M_N2 + 4 * w) = n2;
        lds_wait(mi + M_TAGR, 1);
        PSTAMP(7);
        VM_WAIT(0);
        PSTAMP(8);
        if (lds_ticket(mi + M_KVP) == 3u && lane == 0) {
          const f32x4 n = *(LAS f32x4*)(mi + M_N2);
          const float nn = sel ? 0.f : fmaxf(n[0], n[1]) + fmaxf(n[2], n[3]);
          __hip_atomic_store((gu64*)(gws + WS_FKV + T * 128 + sel * 8),
                             ((unsigned long long)*(LAS unsigned*)(mi + M_TAG) << 32) | __builtin_bit_cast(unsigned, nn), RLX_AGENT);
        }
        PSTAMP(9);
      }
    } else {
      {
      *(LAS u32x4*)(sm + L_GW + lane * 16) = *(const u32x4*)(gw + 8 * lane);
      if (lane < 8) *(LAS u32x4*)(sm + L_GW + 1024 + lane * 16) = *(const u32x4*)(gw + 512 + 8 * lane);
      lds_signal(mi + M_C1);
      UNIT_LOAD(32 + (lane & 3), l31 >> 2);
      UNIT_DO();
      ST_SC1_16(h ? o1 : o0, WS_UT + (qt * 8 + hg) * 9216 + 8192 + lane * 16);
      }
    }
    if (w < 4) {
      const int gt = w >> 1, tt = w & 1, arow = 32 * tt + l31, xsw = (arow >> 1) & 7;
      lds_ptr xb = sm + L_X + arow * 128;
      lds_ptr wb = sm + L_B + (8 * h + tq) * 144 + (36 * gt + 20 * (g4 & 1) + 4 * tp) * 2;
      f32x16 acc = {0};
#pragma unroll 1
      for (int p = 0; p < 3; ++p) {
        if (p == 1) CSTAMP(6);
        if (p == 2) CSTAMP(8);
        lds_wait(mi + M_C1 + 4 * p, p == 0 ? 7 : 8 * p - 5);
        if (p == 0) CSTAMP(5);
        if (p == 1) CSTAMP(7);
        if (p == 2) CSTAMP(9);
        bf16x8 fa[12], fb[12];
#pragma unroll
        for (int k = 0; k < 12; ++k) {
          fa[k] = tr_frag(wb + k * 2304, wb + k * 2304 + 576);
          fb[k] = lds_frag(xb + (k >> 2) * 8192 + (((2 * (k & 3) + h) ^ xsw) << 4));
        }
        __builtin_amdgcn_sched_barrier(0);
#pragma unroll
        for (int k = 0; k < 12; ++k) acc = MFMA32(fa[k], fb[k], acc);
        if (p == 1) { lds_signal(mi + M_C3B); lds_signal(mi + M_C3); }
        xb += 24576;
        wb += 27648;
      }
      STAMP(2);
      u32x4 a;
#pragma unroll
      for (int i = 0; i < 4; ++i) a[i] = pk2(acc[2 * i] * QS, acc[2 * i + 1] * QS);
      lds_ptr qe = sm + L_Q + (4 * gt + tt) * 1024;
      *(LAS u32x4*)(qe + lane * 16) = a;
      lds_ptr qo = qe + 2048 + l31 * 16;
      *(LAS unsigned*)(qo + h * 512 + 4) = pk2(acc[8] * QS, acc[9] * QS);
      *(LAS unsigned*)(qo + (h ? 8 : 512)) = pk2(acc[10] * QS, acc[11] * QS);
      *(LAS unsigned*)(qo + h * 512 + 12) = pk2(acc[12] * QS, acc[13] * QS);
      *(LAS unsigned*)(h ? sm + L_Q + 8192 + ((2 * gt + 1) * 64 + arow) * 4 : qo + 520) = pk2(acc[14] * QS, acc[15] * QS);
    } else {
      const int tok = 16 * (w - 4) + l15;
      lds_ptr xb = sm + L_X + tok * 128;
      lds_ptr wb = sm + L_B + (8 * g4 + tq) * 144 + (tp == 1 ? 104 : 32);
      f32x4 acc = {0};
#pragma unroll 1
      for (int p = 0; p < 3; ++p) {
        lds_wait(mi + M_C1 + 4 * p, p == 0 ? 7 : 8 * p - 5);
        lds_ptr gp = sm + L_GW + ((p + rot) % 3) * 384 + g4 * 16;
        bf16x8 fa[6], fg[6], fb[6];
#pragma unroll
        for (int k = 0; k < 6; ++k) {
          fa[k] = tr_frag(wb + k * 4608, wb + k * 4608 + 576);
          fg[k] = lds_frag(gp + k * 64);
          fb[k] = lds_frag(xb + (k >> 1) * 8192 + (((4 * (k & 1) + g4) ^ (l15 >> 1)) << 4));
        }
        __builtin_amdgcn_sched_barrier(0);
#pragma unroll
        for (int k = 0; k < 6; ++k) acc = MFMA16(l15 == 8 ? fg[k] : fa[k], fb[k], acc);
        if (p == 1) { lds_signal(mi + M_C3B); VM_WAIT(0); lds_signal(mi + M_C3); }
        xb += 24576;
        wb += 27648;
      }
      STAMP(2);
      if (g4 < 2) {
        *(LAS unsigned*)(sm + L_Q + 8192 + (2 * g4 * 64 + tok) * 4) = pk2(acc[0] * QS, acc[1] * QS);
        *(LAS unsigned*)(sm + L_Q + ((2 * g4 + 1) * 2 + (tok >> 5)) * 1024 + (tok & 31) * 16) = pk2(acc[2] * QS, acc[3] * QS);
      } else if (g4 == 2) {
        *(LAS float*)(mi + M_G + tok * 4) = acc[0];
      }
    }
    lds_signal(mi + M_QD);
    const int hl = w & 3, kh = w >> 2;
    const int voff = 4096 + (l31 < 18 ? h * 288 + l31 * 16 : (l31 == 18 ? 576 : 592));
    lds_wait(mi + M_QD, 12);
    STAMP(3);
    const u32x4 q0u = *(LAS u32x4*)(sm + L_Q + (2 * hl) * 1024 + lane * 16);
    const u32x4 q1u = *(LAS u32x4*)(sm + L_Q + (2 * hl + 1) * 1024 + lane * 16);
    const unsigned qr = *(LAS unsigned*)(sm + L_Q + 8192 + (hl * 64 + lane) * 4);
    const float kn0 = *(LAS float*)(mi + M_KN0);
    lds_signal(mi + M_QR);
    float n0 = 0.f, n1 = 0.f;
#pragma unroll
    for (int i = 0; i < 4; ++i) { n0 = dot2(q0u[i], n0); n1 = dot2(q1u[i], n1); }
    const float nq = dot2(qr, (h ? n1 : n0) + xhalf(h ? n0 : n1));
    const u32x2 qrem_u = {qr, pk2(-__builtin_amdgcn_sqrtf(nq * kn0), 0.f)};
    f32x16 o0, o1;
    float m0 = 0.f, m1 = 0.f;
    STAMP(4); RSTAMP(2);
    {
      int cnt = 8, off = 0, tmp, t2, so = ((s0 + 4) & 7) * RSLOT;
      unsigned ca = sbase + L_M + M_DONE - 4, dst = sbase + w * 2496;
      const unsigned long long ksrc = (unsigned long long)ws + WS_KV + w * 2496 - (sbase + kh * KV_GROUP);
      asm volatile(
      "v_mov_b32 v107, v126\n"
      "v_mov_b32 v108, v127\n"
      "v_mov_b32 v109, v128\n"
      "v_mov_b32 v130, 1\n"
      "ds_read2st64_b64 v[110:113], v109 offset0:0 offset1:1\n"
      "ds_read_b128 v[114:117], v107 offset:0\n"
      "ds_read_b128 v[118:121], v108 offset:0\n"
      "s_mov_b32 vcc_lo, %[first]\n"
      "s_mov_b32 vcc_hi, 0\n"
      "s_setprio 1\n"
      "v_mov_b32 v56, 0\n"
      "v_mov_b32 v57, 0\n"
      "v_mov_b32 v58, 0\n"
      "v_mov_b32 v59, 0\n"
      "v_mov_b32 v60, 0\n"
      "v_mov_b32 v61, 0\n"
      "v_mov_b32 v62, 0\n"
      "v_mov_b32 v63, 0\n"
      "v_mov_b32 v122, 0\n"
      "v_mov_b32 v123, 0\n"
      "v_mov_b32 v124, 0\n"
      "v_mov_b32 v125, 0\n"
      "v_mov_b32 v64, 0\n"
      "v_mov_b32 v65, 0\n"
      "v_mov_b32 v66, 0\n"
      "v_mov_b32 v67, 0\n"
      "v_mov_b32 v68, 0\n"
      "v_mov_b32 v69, 0\n"
      "v_mov_b32 v70, 0\n"
      "v_mov_b32 v71, 0\n"
      "v_mov_b32 v72, 0\n"
      "v_mov_b32 v73, 0\n"
      "v_mov_b32 v74, 0\n"
      "v_mov_b32 v75, 0\n"
      "v_mov_b32 v76, 0\n"
      "v_mov_b32 v77, 0\n"
      "v_mov_b32 v78, 0\n"
      "v_mov_b32 v79, 0\n"
      "s_waitcnt lgkmcnt(1)\n"
      "v_mfma_f32_32x32x4_2b_bf16 v[0:31], v[110:111], v[104:105], 0 cbsz:1 abid:0\n"
      "v_mov_b32 v80, 0\n"
      "v_mov_b32 v81, 0\n"
      "v_mov_b32 v82, 0\n"
      "v_mov_b32 v83, 0\n"
      "v_mov_b32 v84, 0\n"
      "v_mov_b32 v85, 0\n"
      "v_mov_b32 v86, 0\n"
      "v_mov_b32 v87, 0\n"
      "v_mov_b32 v88, 0\n"
      "v_mov_b32 v89, 0\n"
      "v_mov_b32 v90, 0\n"
      "v_mov_b32 v91, 0\n"
      "v_mov_b32 v92, 0\n"
      "v_mov_b32 v93, 0\n"
      "v_mov_b32 v94, 0\n"
      "v_mov_b32 v95, 0\n"
      "s_nop 2\n"
      "v_mfma_f32_32x32x16_bf16 v[0:15], v[114:117], v[96:99], v[0:15]\n"
      "v_mfma_f32_32x32x16_bf16 v[16:31], v[114:117], v[100:103], v[16:31]\n"
      "s_nop 11\n"
      "1:\n"
      "s_cbranch_vccz 4f\n"
      "s_setprio 2\n"
      "4:\n"
      "v_exp_f32 v0, v0\n"
      "v_exp_f32 v1, v1\n"
      "v_smfmac_f32_32x32x32_bf16 v[80:95], v[122:125], v[56:63], v106\n"
      "ds_read_b128 v[114:117], v107 offset:1024\n"
      "ds_read_b128 v[122:125], v108 offset:608\n"
      "v_exp_f32 v2, v2\n"
      "v_exp_f32 v3, v3\n"
      "v_exp_f32 v4, v4\n"
      "v_exp_f32 v5, v5\n"
      "v_mfma_f32_32x32x4_2b_bf16 v[32:63], v[110:111], v[104:105], 0 cbsz:1 abid:1\n"
      "v_exp_f32 v6, v6\n"
      "v_exp_f32 v7, v7\n"
      "v_exp_f32 v8, v8\n"
      "v_exp_f32 v9, v9\n"
      "v_exp_f32 v10, v10\n"
      "v_exp_f32 v11, v11\n"
      "v_exp_f32 v12, v12\n"
      "s_waitcnt lgkmcnt(2)\n"
      "v_exp_f32 v13, v13\n"
      "v_smfmac_f32_32x32x32_bf16 v[64:79], v[118:121], v[0:7], v106\n"
      "v_exp_f32 v14, v14\n"
      "v_exp_f32 v15, v15\n"
      "v_exp_f32 v16, v16\n"
      "s_waitcnt lgkmcnt(0)\n"
      "v_exp_f32 v17, v17\n"
      "v_smfmac_f32_32x32x32_bf16 v[64:79], v[122:125], v[8:15], v106\n"
      "v_exp_f32 v18, v18\n"
      "v_exp_f32 v19, v19\n"
      "v_exp_f32 v20, v20\n"
      "v_exp_f32 v21, v21\n"
      "v_mfma_f32_32x32x16_bf16 v[32:47], v[114:117], v[96:99], v[32:47]\n"
      "v_exp_f32 v22, v22\n"
      "v_exp_f32 v23, v23\n"
      "v_exp_f32 v24, v24\n"
      "v_exp_f32 v25, v25\n"
      "v_smfmac_f32_32x32x32_bf16 v[80:95], v[118:121], v[16:23], v106\n"
      "v_exp_f32 v26, v26\n"
      "ds_read_b128 v[118:121], v108 offset:1216\n"
      "v_exp_f32 v27, v27\n"
      "v_exp_f32 v28, v28\n"
      "v_exp_f32 v29, v29\n"
      "v_mfma_f32_32x32x16_bf16 v[48:63], v[114:117], v[100:103], v[48:63]\n"
      "v_exp_f32 v30, v30\n"
      "v_exp_f32 v31, v31\n"
      "v_exp_f32 v32, v32\n"
      "v_exp_f32 v33, v33\n"
      "v_smfmac_f32_32x32x32_bf16 v[80:95], v[122:125], v[24:31], v106\n"
      "ds_read_b128 v[114:117], v107 offset:2048\n"
      "ds_read_b128 v[122:125], v108 offset:1824\n"
      "ds_read_b32 v131, v129\n"
      "v_exp_f32 v34, v34\n"
      "v_exp_f32 v35, v35\n"
      "v_exp_f32 v36, v36\n"
      "v_exp_f32 v37, v37\n"
      "v_mfma_f32_32x32x4_2b_bf16 v[0:31], v[112:113], v[104:105], 0 cbsz:1 abid:0\n"
      "v_exp_f32 v38, v38\n"
      "v_exp_f32 v39, v39\n"
      "v_exp_f32 v40, v40\n"
      "v_exp_f32 v41, v41\n"
      "v_exp_f32 v42, v42\n"
      "v_exp_f32 v43, v43\n"
      "v_exp_f32 v44, v44\n"
      "s_waitcnt lgkmcnt(3)\n"
      "v_exp_f32 v45, v45\n"
      "v_smfmac_f32_32x32x32_bf16 v[64:79], v[118:121], v[32:39], v106\n"
      "v_exp_f32 v46, v46\n"
      "v_exp_f32 v47, v47\n"
      "v_exp_f32 v48, v48\n"
      "s_waitcnt lgkmcnt(0)\n"
      "s_waitcnt vmcnt(0)\n"
      "s_mov_b64 exec, 1\n"
      "ds_add_u32 v129, v130 offset:76\n"
      "s_mov_b64 exec, -1\n"
      "v_exp_f32 v49, v49\n"
      "v_smfmac_f32_32x32x32_bf16 v[64:79], v[122:125], v[40:47], v106\n"
      "v_exp_f32 v50, v50\n"
      "v_exp_f32 v51, v51\n"
      "v_exp_f32 v52, v52\n"
      "v_exp_f32 v53, v53\n"
      "v_mfma_f32_32x32x16_bf16 v[0:15], v[114:117], v[96:99], v[0:15]\n"
      "v_exp_f32 v54, v54\n"
      "v_exp_f32 v55, v55\n"
      "v_exp_f32 v56, v56\n"
      "v_exp_f32 v57, v57\n"
      "v_smfmac_f32_32x32x32_bf16 v[80:95], v[118:121], v[48:55], v106\n"
      "v_exp_f32 v58, v58\n"
      "ds_read_b128 v[118:121], v108 offset:2432\n"
      "v_exp_f32 v59, v59\n"
      "v_exp_f32 v60, v60\n"
      "v_exp_f32 v61, v61\n"
      "v_mfma_f32_32x32x16_bf16 v[16:31], v[114:117], v[100:103], v[16:31]\n"
      "v_exp_f32 v62, v62\n"
      "v_exp_f32 v63, v63\n"
      "v_readfirstlane_b32 %[tmp], v131\n"
      "s_and_b32 %[t2], %[cnt], 12\n"
      "s_cmp_lg_u32 %[t2], 4\n"
      "s_cbranch_scc1 8f\n"
      "s_cmp_lt_u32 %[tmp], 8\n"
      "s_cbranch_scc1 7f\n"
      "9:\n"
      "s_mov_b32 m0, %[dst]\n"
      "s_add_u32 s96, %[slo], %[so]\n"
      "s_addc_u32 s97, %[shi], 0\n"
      "global_load_lds_dwordx4 v126, s[96:97] sc1\n"
      "global_load_lds_dwordx4 v126, s[96:97] offset:1024 sc1\n"
      "s_mov_b64 exec, 0xfffffff\n"
      "global_load_lds_dwordx4 v126, s[96:97] offset:2048 sc1\n"
      "s_mov_b64 exec, -1\n"
      "s_add_u32 %[dst], %[dst], 19968\n"
      "s_add_u32 %[so], %[so], 19968\n"
      "s_cmp_eq_u32 %[so], 159744\n"
      "s_cselect_b32 %[so], 0, %[so]\n"
      "8:\n"
      "v_exp_f32 v0, v0\n"
      "v_exp_f32 v1, v1\n"
      "v_smfmac_f32_32x32x32_bf16 v[80:95], v[122:125], v[56:63], v106\n"
      "ds_read_b128 v[114:117], v107 offset:3072\n"
      "ds_read_b128 v[122:125], v108 offset:3040\n"
      "ds_read_b32 v131, v129 offset:72\n"
      "v_exp_f32 v2, v2\n"
      "v_exp_f32 v3, v3\n"
      "v_exp_f32 v4, v4\n"
      "v_exp_f32 v5, v5\n"
      "v_mfma_f32_32x32x4_2b_bf16 v[32:63], v[112:113], v[104:105], 0 cbsz:1 abid:1\n"
      "v_exp_f32 v6, v6\n"
      "v_exp_f32 v7, v7\n"
      "v_exp_f32 v8, v8\n"
      "v_exp_f32 v9, v9\n"
      "v_exp_f32 v10, v10\n"
      "v_exp_f32 v11, v11\n"
      "v_exp_f32 v12, v12\n"
      "s_waitcnt lgkmcnt(3)\n"
      "v_exp_f32 v13, v13\n"
      "v_smfmac_f32_32x32x32_bf16 v[64:79], v[118:121], v[0:7], v106\n"
      "v_exp_f32 v14, v14\n"
      "v_exp_f32 v15, v15\n"
      "v_exp_f32 v16, v16\n"
      "s_waitcnt lgkmcnt(0)\n"
      "v_exp_f32 v17, v17\n"
      "v_smfmac_f32_32x32x32_bf16 v[64:79], v[122:125], v[8:15], v106\n"
      "v_exp_f32 v18, v18\n"
      "v_exp_f32 v19, v19\n"
      "v_exp_f32 v20, v20\n"
      "v_exp_f32 v21, v21\n"
      "v_mfma_f32_32x32x16_bf16 v[32:47], v[114:117], v[96:99], v[32:47]\n"
      "v_exp_f32 v22, v22\n"
      "v_exp_f32 v23, v23\n"
      "v_exp_f32 v24, v24\n"
      "v_exp_f32 v25, v25\n"
      "v_smfmac_f32_32x32x32_bf16 v[80:95], v[118:121], v[16:23], v106\n"
      "v_exp_f32 v26, v26\n"
      "ds_read_b128 v[118:121], v108 offset:3648\n"
      "v_exp_f32 v27, v27\n"
      "v_exp_f32 v28, v28\n"
      "v_exp_f32 v29, v29\n"
      "v_mfma_f32_32x32x16_bf16 v[48:63], v[114:117], v[100:103], v[48:63]\n"
      "v_exp_f32 v30, v30\n"
      "v_exp_f32 v31, v31\n"
      "s_cbranch_vccz 5f\n"
      "s_setprio 0\n"
      "5:\n"
      "v_readfirstlane_b32 %[tmp], v131\n"
      "s_add_u32 %[off], %[off], 19968\n"
      "s_cmp_eq_u32 %[off], 79872\n"
      "s_cselect_b32 %[off], 0, %[off]\n"
      "s_cmp_lt_i32 %[tmp], 8\n"
      "s_cbranch_scc1 3f\n"
      "2:\n"
      "v_add_u32 v109, %[off], v128\n"
      "ds_read2st64_b64 v[110:113], v109 offset0:0 offset1:1\n"
      "v_add_u32 v107, %[off], v126\n"
      "v_exp_f32 v32, v32\n"
      "v_exp_f32 v33, v33\n"
      "v_smfmac_f32_32x32x32_bf16 v[80:95], v[122:125], v[24:31], v106\n"
      "ds_read_b128 v[114:117], v107 offset:0\n"
      "ds_read_b128 v[122:125], v108 offset:4256\n"
      "v_exp_f32 v34, v34\n"
      "v_exp_f32 v35, v35\n"
      "v_exp_f32 v36, v36\n"
      "s_waitcnt lgkmcnt(2)\n"
      "v_exp_f32 v37, v37\n"
      "v_mfma_f32_32x32x4_2b_bf16 v[0:31], v[110:111], v[104:105], 0 cbsz:1 abid:0\n"
      "v_exp_f32 v38, v38\n"
      "v_exp_f32 v39, v39\n"
      "v_exp_f32 v40, v40\n"
      "v_exp_f32 v41, v41\n"
      "v_exp_f32 v42, v42\n"
      "v_exp_f32 v43, v43\n"
      "v_exp_f32 v44, v44\n"
      "v_exp_f32 v45, v45\n"
      "v_smfmac_f32_32x32x32_bf16 v[64:79], v[118:121], v[32:39], v106\n"
      "v_exp_f32 v46, v46\n"
      "v_exp_f32 v47, v47\n"
      "v_exp_f32 v48, v48\n"
      "s_waitcnt lgkmcnt(0)\n"
      "s_mov_b64 exec, 1\n"
      "ds_add_u32 v129, v130 offset:4\n"
      "s_mov_b64 exec, -1\n"
      "v_exp_f32 v49, v49\n"
      "v_smfmac_f32_32x32x32_bf16 v[64:79], v[122:125], v[40:47], v106\n"
      "v_exp_f32 v50, v50\n"
      "v_exp_f32 v51, v51\n"
      "v_exp_f32 v52, v52\n"
      "v_exp_f32 v53, v53\n"
      "v_mfma_f32_32x32x16_bf16 v[0:15], v[114:117], v[96:99], v[0:15]\n"
      "v_exp_f32 v54, v54\n"
      "v_exp_f32 v55, v55\n"
      "v_exp_f32 v56, v56\n"
      "v_exp_f32 v57, v57\n"
      "v_smfmac_f32_32x32x32_bf16 v[80:95], v[118:121], v[48:55], v106\n"
      "v_add_u32 v108, %[off], v127\n"
      "v_add_u32 v129, 4, v129\n"
      "v_exp_f32 v58, v58\n"
      "ds_read_b128 v[118:121], v108 offset:0\n"
      "v_exp_f32 v59, v59\n"
      "v_exp_f32 v60, v60\n"
      "v_exp_f32 v61, v61\n"
      "v_mfma_f32_32x32x16_bf16 v[16:31], v[114:117], v[100:103], v[16:31]\n"
      "v_exp_f32 v62, v62\n"
      "v_exp_f32 v63, v63\n"
      "s_sub_u32 %[cnt], %[cnt], 1\n"
      "s_cmp_lg_u32 %[cnt], 0\n"
      "s_cbranch_scc1 1b\n"
      "s_branch 6f\n"
      "3:\n"
      "s_sleep 1\n"
      "ds_read_b32 v131, v129 offset:72\n"
      "s_waitcnt lgkmcnt(0)\n"
      "v_readfirstlane_b32 %[tmp], v131\n"
      "s_nop 0\n"
      "s_cmp_lt_i32 %[tmp], 8\n"
      "s_cbranch_scc1 3b\n"
      "s_branch 2b\n"
      "7:\n"
      "s_sleep 1\n"
      "ds_read_b32 v131, v129\n"
      "s_waitcnt lgkmcnt(0)\n"
      "v_readfirstlane_b32 %[tmp], v131\n"
      "s_nop 0\n"
      "s_cmp_lt_u32 %[tmp], 8\n"
      "s_cbranch_scc1 7b\n"
      "s_branch 9b\n"
      "6:\n"
      "v_smfmac_f32_32x32x32_bf16 v[80:95], v[122:125], v[56:63], v106\n"
      "s_setprio 0\n"
      "s_waitcnt lgkmcnt(0)\n"
      "s_nop 15\n"
      "s_nop 7\n"
      : "=&{v[64:79]}"(o0), "=&{v[80:95]}"(o1), "+{v129}"(ca), [cnt] "+s"(cnt), [off] "+s"(off), [tmp] "=&s"(tmp),
        [t2] "=&s"(t2), [so] "+s"(so), [dst] "+s"(dst)
      : "{v[96:99]}"(q0u), "{v[100:103]}"(q1u), "{v[104:105]}"(qrem_u), "{v106}"(SPIDX),
        "{v126}"(sbase + kh * KV_GROUP + lane * 16), "{v127}"(sbase + kh * KV_GROUP + voff),
        "{v128}"(sbase + kh * KV_GROUP + 8960 + lane * 8), [first] "s"(1 - kh), [slo] "s"((unsigned)ksrc), [shi] "s"((unsigned)(ksrc >> 32))
      : "v0", "v1", "v2", "v3", "v4", "v5", "v6", "v7", "v8", "v9", "v10", "v11", "v12", "v13", "v14", "v15",
        "v16", "v17", "v18", "v19", "v20", "v21", "v22", "v23", "v24", "v25", "v26", "v27", "v28", "v29", "v30", "v31",
        "v32", "v33", "v34", "v35", "v36", "v37", "v38", "v39", "v40", "v41", "v42", "v43", "v44", "v45", "v46", "v47",
        "v48", "v49", "v50", "v51", "v52", "v53", "v54", "v55", "v56", "v57", "v58", "v59", "v60", "v61", "v62", "v63",
        "v107", "v108", "v109", "v110", "v111", "v112", "v113", "v114", "v115", "v116", "v117", "v118", "v119", "v120", "v121", "v122",
        "v123", "v124", "v125", "v130", "v131", "s96", "s97", "vcc", "scc", "memory");
    }
    if (!__all(nq * *(LAS float*)(mi + M_KN) < 47.f * 47.f)) {
      m0 = m1 = -INFINITY;
      o0 = o1 = (f32x16){0};
      const bf16x8 q0 = __builtin_bit_cast(bf16x8, q0u), q1 = __builtin_bit_cast(bf16x8, q1u);
      const s16x4 qrem = __builtin_bit_cast(s16x4, (u32x2){qr, 0u});
#pragma unroll 1
      for (int t = 0; t < 32; ++t) {
        const int g = WS_KV + (t >> 2) * RSLOT + kh * KV_GROUP;
        const u32x2 kr = __builtin_bit_cast(u32x2, __builtin_amdgcn_raw_buffer_load_b64(rs, g + 8960 + (t & 3) * 256 + l31 * 8, 0, 16));
        const bf16x8 km = __builtin_bit_cast(bf16x8, LD_SC1_16(g + (t & 3) * 1024 + lane * 16));
        const bf16x8 va = __builtin_bit_cast(bf16x8, LD_SC1_16(g + voff + (t & 3) * 1216));
        const bf16x8 vb = __builtin_bit_cast(bf16x8, LD_SC1_16(g + voff + (t & 3) * 1216 + 608));
        const f32x32 z = {0};
        const f32x32 r = __builtin_amdgcn_mfma_f32_32x32x4bf16_1k(__builtin_bit_cast(s16x4, kr), qrem, z, 0, 0, 0);
        online_step(MFMA32(km, q0, lo16(r)), m0, o0, va, vb);
        online_step(MFMA32(km, q1, hi16(r)), m1, o1, va, vb);
      }
    }
    HSTAMP(9);
    lds_wait(mi + M_DONE + 20, 8);
    {
      const f32x16 give = kh ? o0 : o1;
      lds_ptr p = sm + L_EX + w * 3072 + lane * 16;
      *(LAS f32x4*)(p) = (f32x4){give[0], give[1], give[2], give[3]};
      *(LAS f32x4*)(p + 1024) = (f32x4){give[4], give[5], give[6], give[7]};
      *(LAS f32x4*)(p + 2048) = (f32x4){give[8], give[9], give[10], kh ? m0 : m1};
    }
    lds_signal(mi + M_PAIR + 4 * hl);
    lds_wait(mi + M_PAIR + 4 * hl, 2);
    STAMP(10); RSTAMP(3);
    {
      const f32x16 keep = kh ? o1 : o0;
      lds_ptr p = sm + L_EX + (w ^ 4) * 3072 + lane * 16;
      const f32x4 t0 = *(LAS f32x4*)(p), t1 = *(LAS f32x4*)(p + 1024), t2 = *(LAS f32x4*)(p + 2048);
      const float mk = kh ? m1 : m0, mm = fmaxf(mk, t2[3]);
      const float fa = __builtin_amdgcn_exp2f(mk - mm), fb = __builtin_amdgcn_exp2f(t2[3] - mm);
      float o[11];
#pragma unroll
      for (int i = 0; i < 11; ++i) o[i] = keep[i] * fa + (i < 4 ? t0[i & 3] : (i < 8 ? t1[i & 3] : t2[i & 3])) * fb;
      const float ox = xhalf(o[10]);
      const float inv = __builtin_amdgcn_rcpf(h ? ox : o[10]);
      u32x4 a;
#pragma unroll
      for (int i = 0; i < 4; ++i) a[i] = pk2(o[2 * i] * inv, o[2 * i + 1] * inv);
      const int ob = WS_O + (2 * qt + kh) * 36 * 1024;
      ST_SC1_16(a, ob + (4 * hg + hl) * 1024 + lane * 16);
      if (!h) ST_SC1_4(pk2(o[8] * inv, o[9] * inv), ob + 32768 + (4 * hg + hl) * 128 + l31 * 4);
    }
  }

  const unsigned tag = *(LAS unsigned*)(mi + M_TAG);
  if (w < 8) {
    VM_WAIT(0);
    if (lds_ticket(mi + M_OD) == 7u && lane == 0)
      __hip_atomic_store((gu32*)(gws + WS_FO) + (qt * 8 + hg) * 32, tag, RLX_AGENT);
  }
  bf16x8 wf[18];
  u32x4 bt = {0, 0, 0, 0};
  float gq = 0.f;
  lds_wait(mi + M_WO, 4);
  {
    const int ka = w < 8 ? (w >> 2) : (g4 >> 1), kb = w < 8 ? h : (g4 & 1);
    const int cb = w < 8 ? (32 * ((w >> 1) & 1) + 16 * (g4 & 1) + 4 * tp) * 2 : (64 + 4 * (tp & 1)) * 2;
    lds_ptr wm = sm + L_B + (DH * ka + 4 * kb + tq) * 144 + cb;
    lds_ptr wr = sm + L_B + (DH * (8 * ka + 4 * kb + (tq >> 1)) + 16 + (tq & 1)) * 144 + cb;
#pragma unroll
    for (int k = 0; k < 12; ++k) wf[k] = tr_frag(wm + k * 5184, wm + k * 5184 + 1152);
    wm += 12 * 5184;
#pragma unroll
    for (int k = 0; k < 4; ++k) wf[12 + k] = tr_frag(wm + k * 5184, wm + k * 5184 + 1152);
#pragma unroll
    for (int k = 0; k < 2; ++k) wf[16 + k] = tr_frag(wr + k * 41472, wr + k * 41472 + 5184);
  }
  if (w < 9) {
    lds_wait(mi + M_BT, 4);
    bt = *(LAS u32x4*)(sm + L_BT + tid * 16);
    gq = *(LAS float*)(mi + M_G + otok * 4);
    lds_signal(mi + M_EXD);
  }
  if (w == 11) {
    lds_wait<1>(mi + M_OD, 8);
#pragma unroll 1
    for (unsigned spins = 0; spins < SPIN_CAP; ++spins) {
      if (__all(lane >= 8 || __hip_atomic_load((gu32*)(gws + WS_FO) + (qt * 8 + lane) * 32, RLX_AGENT) == tag)) break;
      __builtin_amdgcn_s_sleep(1);
    }
    lds_signal(mi + M_OR);
  } else {
    lds_wait<1>(mi + M_OR, 1);
  }
  if (w < 8) HSTAMP(5);
  {
    u32x4 of[6];
#pragma unroll
    for (int i = 0; i < 6; ++i) of[i] = LD_SC1_16(WS_O + 2 * qt * 36 * 1024 + (12 * i + w) * 1024 + lane * 16);
    lds_wait(mi + M_EXD, 9);
#pragma unroll
    for (int i = 0; i < 6; ++i) {
      if ((i == 2 || i == 5) && w >= 8) {
        lds_ptr p = sm + (12 * i + w) * 1024 + (4 * (lane & 7) + 32 * h) * 16 + ((lane >> 3) & 3) * 4;
#pragma unroll
        for (int e = 0; e < 4; ++e) *(LAS unsigned*)(p + e * 16) = of[i][e];
      } else {
        *(LAS u32x4*)(sm + (12 * i + w) * 1024 + lane * 16) = of[i];
      }
    }
  }
  BAR();
  if (w < 8) HSTAMP(6);
  if (w < 8) {
    lds_ptr ob = sm + ((w & 1) * 36 + (w >> 2)) * 1024 + lane * 16;
    f32x16 acc = {0};
#pragma unroll
    for (int k = 0; k < 18; ++k) acc = MFMA32(lds_frag(ob + k * 2048), wf[k], acc);
    lds_ptr ap = sm + L_B + (((w >> 2) * 64 + 32 * (w & 1) + 4 * h) * 72 + 32 * ((w >> 1) & 1) + l31) * 4;
#pragma unroll
    for (int r = 0; r < 16; ++r) *(LAS float*)(ap + ((r & 3) + 8 * (r >> 2)) * 288) = acc[r];
  } else {
    const int j = w - 8;
    lds_ptr ob = sm + ((j >> 1) * 36 + (g4 >> 1)) * 1024 + (16 * (j & 1) + l15 + 32 * (g4 & 1)) * 16;
    f32x4 acc = {0};
#pragma unroll
    for (int k = 0; k < 18; ++k) acc = MFMA16(lds_frag(ob + k * 2048), wf[k], acc);
    lds_ptr ap = sm + L_B + (((l15 >> 3) * 64 + 16 * j + 4 * g4) * 72 + 64 + (l15 & 7)) * 4;
#pragma unroll
    for (int r = 0; r < 4; ++r) *(LAS float*)(ap + r * 288) = (l15 < 8) ? acc[r] : 0.f;
  }
  if (w < 8) HSTAMP(7);
  BAR();
  if (w < 8) HSTAMP(8);
  if (tid < 576) {
    lds_ptr ap = sm + L_B + tid * 32;
    const f32x4 a0 = *(LAS f32x4*)(ap) + *(LAS f32x4*)(ap + 64 * 72 * 4);
    const f32x4 a1 = *(LAS f32x4*)(ap + 16) + *(LAS f32x4*)(ap + 64 * 72 * 4 + 16);
    u32x4 o;
#pragma unroll
    for (int i = 0; i < 4; ++i) {
      const float e0 = i < 2 ? a0[2 * i] : a1[2 * i - 4], e1 = i < 2 ? a0[2 * i + 1] : a1[2 * i - 3];
      o[i] = pk2(fmaf(gq, e0, bf_lo(bt[i])), fmaf(gq, e1, bf_hi(bt[i])));
    }
    *(u32x4*)(out + (size_t)(64 * qt + otok) * D_ + 72 * hg + 8 * opc) = o;
  }
  STAMP_FLUSH(w < 4 ? 0 : (w < 8 ? 1 : (w < 11 ? 2 : 3)));
  asm volatile("fk_end:");
}

extern "C" void kernel_launch(void* const* d_in, const int* in_sizes, int n_in, void* d_out, int out_size,
                              void* d_ws, size_t ws_size, hipStream_t stream) {
  fused_kernel<<<256, 768, 0, stream>>>((const __bf16*)d_in[0], (const __bf16*)d_in[1], (const __bf16*)d_in[2],
                                        (const __bf16*)d_in[3], (const __bf16*)d_in[4], (const __bf16*)d_in[6],
                                        (const __bf16*)d_in[7], (const __bf16*)d_in[8], (const __bf16*)d_in[9],
                                        (const __bf16*)d_in[10], (const __bf16*)d_in[11], (__bf16*)d_out, (char*)d_ws);
}
```

```cpp
#include <hip/hip_runtime.h>
#include <hip/hip_bf16.h>
#include <stdint.h>
#define NO_TOUCH
#define STAMPS
#include <stdio.h>

typedef __attribute__((ext_vector_type(2))) __bf16 bf16x2;
typedef __attribute__((ext_vector_type(4))) __bf16 bf16x4;
typedef __attribute__((ext_vector_type(8))) __bf16 bf16x8;
typedef __attribute__((ext_vector_type(16))) __bf16 bf16x16;
typedef __attribute__((ext_vector_type(4))) short s16x4;
typedef __attribute__((ext_vector_type(2))) float f32x2;
typedef __attribute__((ext_vector_type(4))) float f32x4;
typedef __attribute__((ext_vector_type(16))) float f32x16;
typedef __attribute__((ext_vector_type(32))) float f32x32;
typedef __attribute__((ext_vector_type(2))) unsigned u32x2;
typedef __attribute__((ext_vector_type(4))) unsigned u32x4;
#define LAS __attribute__((address_space(3)))
#define GAS __attribute__((address_space(1)))
typedef LAS char* lds_ptr;
typedef GAS unsigned gu32;
typedef GAS unsigned long long gu64;
#define DEVI __device__ __forceinline__
#define RLX_AGENT __ATOMIC_RELAXED, __HIP_MEMORY_SCOPE_AGENT
#define RLX_WG __ATOMIC_RELAXED, __HIP_MEMORY_SCOPE_WORKGROUP

constexpr int S_ = 2048, D_ = 576, DH = 18;

constexpr int KV_GROUP = 9984;
constexpr int WS_KV = 0;
constexpr int WS_EPOCH = 192 * 1024;
constexpr int WS_FKV = WS_EPOCH + 1024;
constexpr int WS_FU = WS_FKV + 8192;
constexpr int WS_FZ = WS_FU + 32768;
constexpr int WS_FO = WS_FZ + 32768;
constexpr int WS_UT = 384 * 1024;
constexpr int WS_Z = WS_UT + D_ * S_ * 2;
constexpr int WS_O = WS_Z + D_ * S_ * 2;
constexpr int WS_ZP = WS_O + D_ * S_ * 2;
constexpr int WS_END = WS_ZP + 64 * 4 * 8192;

constexpr int RSLOT = 2 * KV_GROUP;
constexpr int L_X = 0;
constexpr int L_GW = 78848;
constexpr int L_B = 80000;
constexpr int L_M = 162944;
constexpr int CV_COPY = 4160;
constexpr int CV_H = L_B, CV_HB = 8 * CV_COPY, CV_U = L_B + 2 * CV_HB, CV_UB = 7680;
constexpr int L_Q = L_B + 32768;
constexpr int M_DONE = 0, M_C1 = 32, M_C2 = 36, M_C3 = 40, M_C3B = 44, M_KVC = 48, M_KVR = 52, M_UR = 56, M_OR = 60, M_RDY = 64,
              M_QD = 192, M_UD = 104, M_HS = 108, M_ZR = 112, M_BT = 116, M_WO = 120, M_OD = 124, M_PAIR = 128, M_TAGR = 144,
              M_TAG = 148, M_KN = 152, M_EXD = 156, M_KVP = 160, M_KN0 = 164, M_KD = 168, M_QR = 172,
              M_N2 = 176, M_G = 256;
constexpr int L_EX = 0, L_BT = 28672;
#define CV_PRIO 3
#define CV_SLEEP 0
#define POLL_AT M_C2
#define POLL_N 3
#define BUBBLE 0
#define POLL_SLEEP 4
#define BT_AT 28
constexpr unsigned SPIN_CAP = 1u << 18;

constexpr size_t WS_DBG = 64u << 20;
#define STAMP_DECL unsigned long long st_c[12] = {0}, st_r[4] = {0}; st_r[0] = __builtin_amdgcn_s_memrealtime()
#define STAMP(I) do { asm volatile("" ::: "memory"); st_c[I] = __builtin_amdgcn_s_memtime(); asm volatile("" ::: "memory"); } while (0)
#define STAMP_FLUSH(KID) do { asm volatile("s_waitcnt vmcnt(0)" ::: "memory"); STAMP(11); st_r[1] = __builtin_amdgcn_s_memrealtime(); \
  if ((threadIdx.x & 63) == 0) { unsigned long long* p_ = (unsigned long long*)((char*)ws + WS_DBG) + (((size_t)(KID) * 1024 + blockIdx.x) * 16 + (threadIdx.x >> 6)) * 16; \
  for (int i_ = 0; i_ < 12; ++i_) p_[i_] = st_c[i_]; p_[12] = st_r[0]; p_[13] = st_r[1]; p_[14] = st_r[2]; p_[15] = st_r[3]; \
  if ((blockIdx.x & 63) == 5 && (threadIdx.x == 0 || threadIdx.x == 704)) printf("b%d t%d cyc: %d %d %d %d %d %d %d %d %d %d %d rt: %d %d %d\n", blockIdx.x, threadIdx.x, (int)(st_c[1]-st_c[0]), (int)(st_c[2]-st_c[0]), (int)(st_c[3]-st_c[0]), (int)(st_c[4]-st_c[0]), (int)(st_c[5]-st_c[0]), (int)(st_c[6]-st_c[0]), (int)(st_c[7]-st_c[0]), (int)(st_c[8]-st_c[0]), (int)(st_c[9]-st_c[0]), (int)(st_c[10]-st_c[0]), (int)(st_c[11]-st_c[0]), (int)(st_r[2]-st_r[0]), (int)(st_r[3]-st_r[0]), (int)(st_r[1]-st_r[0])); } } while (0)
#define RSTAMP(I) do { asm volatile("" ::: "memory"); st_r[I] = __builtin_amdgcn_s_memrealtime(); asm volatile("" ::: "memory"); } while (0)

#define PSTAMP(I) do {} while (0)
#define NSTAMP(I) STAMP(I)
#define CSTAMP(I) do {} while (0)
#define HSTAMP(I) NSTAMP(I)

DEVI unsigned pk2(float a, float b) {
  bf16x2 v = {(__bf16)a, (__bf16)b};
  return __builtin_bit_cast(unsigned, v);
}
DEVI float u2f(unsigned u) { return __builtin_bit_cast(float, u); }
DEVI float bf_lo(unsigned u) { return __builtin_bit_cast(float, u << 16); }
DEVI float bf_hi(unsigned u) { return __builtin_bit_cast(float, u & 0xffff0000u); }
DEVI float dot2(unsigned u, float c) { const bf16x2 v = __builtin_bit_cast(bf16x2, u); return __builtin_amdgcn_fdot2_f32_bf16(v, v, c, false); }
DEVI float rbf(float a) { return (float)(__bf16)a; }
DEVI int swap23(int r) { return (r & ~12) | ((r & 4) << 1) | ((r & 8) >> 1); }
DEVI float xhalf(float v) {
  const unsigned u = __builtin_bit_cast(unsigned, v);
  const auto r = __builtin_amdgcn_permlane32_swap(u, u, false, false);
  return __builtin_bit_cast(float, (threadIdx.x & 32) ? r[0] : r[1]);
}

template <int CTRL>
DEVI float dpp_max(float v) {
  const int u = __builtin_bit_cast(int, v);
  return fmaxf(v, __builtin_bit_cast(float, __builtin_amdgcn_update_dpp(u, u, CTRL, 0xf, 0xf, false)));
}
DEVI float max16(float v) {
  return dpp_max<0x140>(dpp_max<0x141>(dpp_max<0x4e>(dpp_max<0xb1>(v))));
}
DEVI float wave_max(float v) {
  v = max16(v);
  const unsigned u = __builtin_bit_cast(unsigned, v);
  return fmaxf(fmaxf(u2f(__builtin_amdgcn_readlane(u, 0)), u2f(__builtin_amdgcn_readlane(u, 16))),
               fmaxf(u2f(__builtin_amdgcn_readlane(u, 32)), u2f(__builtin_amdgcn_readlane(u, 48))));
}
DEVI void glds(const void* sbase, unsigned voff, unsigned lds_dst) {
  asm volatile("s_mov_b32 m0, %2\n\ts_nop 0\n\tglobal_load_lds_dwordx4 %0, %1" :: "v"(voff), "s"(sbase), "s"(lds_dst) : "memory");
}
DEVI void glds_sc1(const void* sbase, unsigned voff, unsigned lds_dst) {
  asm volatile("s_mov_b32 m0, %2\n\ts_nop 0\n\tglobal_load_lds_dwordx4 %0, %1 sc1" :: "v"(voff), "s"(sbase), "s"(lds_dst) : "memory");
}
#define GLDS4(SC, sbase_, v0_, v3_, dst_)                                                                                  \
  asm volatile("s_mov_b32 m0, %3\n\ts_nop 0\n\tglobal_load_lds_dwordx4 %0, %2" SC "\n\tglobal_load_lds_dwordx4 %0, %2 offset:1024" SC   \
               "\n\tglobal_load_lds_dwordx4 %0, %2 offset:2048" SC "\n\tglobal_load_lds_dwordx4 %1, %2 offset:3072" SC           \
               :: "v"(v0_), "v"(v3_), "s"(sbase_), "s"(dst_) : "memory")
#define VM_WAIT(N) asm volatile("s_waitcnt vmcnt(" #N ")" ::: "memory")
#define BAR() asm volatile("s_waitcnt lgkmcnt(0)\n\ts_barrier" ::: "memory")

DEVI bf16x8 tr_frag(lds_ptr p0, lds_ptr p1) {
  bf16x4 a = __builtin_amdgcn_ds_read_tr16_b64_v4bf16((LAS bf16x4*)p0);
  bf16x4 b = __builtin_amdgcn_ds_read_tr16_b64_v4bf16((LAS bf16x4*)p1);
  return __builtin_shufflevector(a, b, 0, 1, 2, 3, 4, 5, 6, 7);
}
DEVI bf16x8 lds_frag(lds_ptr p) { return *(LAS bf16x8*)p; }
DEVI void lds_signal(lds_ptr p, unsigned n = 1u) {
  asm volatile("s_waitcnt lgkmcnt(0)\n\ts_mov_b64 exec, 1\n\tds_add_u32 %0, %1\n\ts_mov_b64 exec, -1" :: "v"(p), "v"(n) : "memory");
}
DEVI unsigned lds_ticket(lds_ptr p) {
  unsigned old;
  asm volatile("s_waitcnt lgkmcnt(0)\n\ts_mov_b64 exec, 1\n\tds_add_rtn_u32 %0, %1, %2\n\ts_mov_b64 exec, -1\n\ts_waitcnt lgkmcnt(0)"
               : "=&v"(old) : "v"(p), "v"(1u) : "memory");
  return __builtin_amdgcn_readfirstlane(old);
}
template <int SLEEP = 0>
DEVI void lds_wait(lds_ptr p, unsigned n) {
  unsigned v, t;
  asm volatile("1:\n\tds_read_b32 %0, %2\n\ts_waitcnt lgkmcnt(0)\n\tv_readfirstlane_b32 %1, %0\n\ts_nop 0\n\ts_cmp_ge_u32 %1, %3\n\t"
               "s_cbranch_scc1 2f\n\ts_sleep %4\n\ts_branch 1b\n2:"
               : "=&v"(v), "=&s"(t) : "v"(p), "s"(n), "i"(SLEEP) : "scc", "memory");
}
DEVI unsigned lds_peek(lds_ptr p) {
  unsigned v;
  asm volatile("ds_read_b32 %0, %1\n\ts_waitcnt lgkmcnt(0)" : "=&v"(v) : "v"(p) : "memory");
  return __builtin_amdgcn_readfirstlane(v);
}
#define LD_SC1_16(off) __builtin_bit_cast(u32x4, __builtin_amdgcn_raw_buffer_load_b128(rs, (off), 0, 16))
#define ST_SC1_16(v, off) __builtin_amdgcn_raw_buffer_store_b128((v), rs, (off), 0, 16)
#define ST_SC1_8(v, off) __builtin_amdgcn_raw_buffer_store_b64((v), rs, (off), 0, 16)
#define ST_SC1_4(v, off) __builtin_amdgcn_raw_buffer_store_b32((v), rs, (off), 0, 16)

DEVI f32x16 lo16(f32x32 v) { return __builtin_shufflevector(v, v, 0, 1, 2, 3, 4, 5, 6, 7, 8, 9, 10, 11, 12, 13, 14, 15); }
DEVI f32x16 hi16(f32x32 v) { return __builtin_shufflevector(v, v, 16, 17, 18, 19, 20, 21, 22, 23, 24, 25, 26, 27, 28, 29, 30, 31); }
DEVI bf16x16 raw_lo(f32x16 v) { return __builtin_bit_cast(bf16x16, __builtin_shufflevector(v, v, 0, 1, 2, 3, 4, 5, 6, 7)); }
DEVI bf16x16 raw_hi(f32x16 v) { return __builtin_bit_cast(bf16x16, __builtin_shufflevector(v, v, 8, 9, 10, 11, 12, 13, 14, 15)); }
constexpr int SPIDX = (int)0xddddddddu;
#define MFMA32(a, b, c) __builtin_amdgcn_mfma_f32_32x32x16_bf16((a), (b), (c), 0, 0, 0)
#define MFMA16(a, b, c) __builtin_amdgcn_mfma_f32_16x16x32_bf16((a), (b), (c), 0, 0, 0)

DEVI void online_step(f32x16 s, float& m, f32x16& o, bf16x8 va, bf16x8 vb) {
  float t = s[0];
#pragma unroll
  for (int i = 1; i < 16; ++i) t = fmaxf(t, s[i]);
  t = fmaxf(t, xhalf(t));
  const float mn = fmaxf(m, t);
  const float al = __builtin_amdgcn_exp2f(m - mn);
  m = mn;
#pragma unroll
  for (int i = 0; i < 16; ++i) { o[i] *= al; s[i] = __builtin_amdgcn_exp2f(s[i] - mn); }
  o = __builtin_amdgcn_smfmac_f32_32x32x32_bf16(va, raw_lo(s), o, SPIDX, 0, 0);
  o = __builtin_amdgcn_smfmac_f32_32x32x32_bf16(vb, raw_hi(s), o, SPIDX, 0, 0);
}

#define UNIT_LOAD(CP, TG)                                                                                        \
  const int uc0 = 72 * hg + 2 * (CP), ut0 = 64 * qt + 8 * (TG);                                                  \
  const u32x4 usw = *(const u32x4*)(shk + uc0 * 4);                                                              \
  unsigned ukr[11], uvr[8];                                                                                      \
  {                                                                                                              \
    const unsigned bo = (ut0 * D_ + uc0) * 2, bo3 = ut0 ? bo - 3 * 1152 : bo;                                    \
    _Pragma("unroll") for (int i = 0; i < 3; ++i) ukr[i] = *(const unsigned*)((const char*)bk + bo3 + i * 1152); \
    _Pragma("unroll") for (int i = 0; i < 8; ++i) {                                                              \
      ukr[3 + i] = *(const unsigned*)((const char*)bk + bo + i * 1152);                                          \
      uvr[i] = *(const unsigned*)((const char*)bv + bo + i * 1152);                                              \
    }                                                                                                            \
  }
#define UNIT_DO()                                                                                                \
  u32x4 o0, o1;                                                                                                  \
  {                                                                                                              \
    const f32x2 w3 = {bf_hi(usw[1]), bf_hi(usw[3])}, w2 = {bf_lo(usw[1]), bf_lo(usw[3])};                        \
    const f32x2 w1 = {bf_hi(usw[0]), bf_hi(usw[2])}, w0 = {bf_lo(usw[0]), bf_lo(usw[2])};                        \
    f32x2 kf[11], r[8];                                                                                          \
    _Pragma("unroll") for (int i = 0; i < 11; ++i) {                                                             \
      const unsigned kk = (i < 3 && !ut0) ? 0u : ukr[i];                                                         \
      kf[i] = (f32x2){bf_lo(kk), bf_hi(kk)};                                                                     \
    }                                                                                                            \
    _Pragma("unroll") for (int i = 0; i < 8; ++i)                                                                \
      r[i] = __builtin_elementwise_fma(kf[i + 3], w0, __builtin_elementwise_fma(kf[i + 2], w1,                   \
             __builtin_elementwise_fma(kf[i + 1], w2, kf[i] * w3))) * (f32x2){bf_lo(uvr[i]), bf_hi(uvr[i])};     \
    _Pragma("unroll") for (int i = 0; i < 4; ++i) {                                                              \
      o0[i] = pk2(r[7 - 2 * i][0], r[6 - 2 * i][0]);                                                             \
      o1[i] = pk2(r[7 - 2 * i][1], r[6 - 2 * i][1]);                                                             \
    }                                                                                                            \
  }

__global__ void __launch_bounds__(768, 3)
fused_kernel(const __bf16* __restrict__ x, const __bf16* __restrict__ wq, const __bf16* __restrict__ wk,
             const __bf16* __restrict__ wv, const __bf16* __restrict__ wo, const __bf16* __restrict__ bk,
             const __bf16* __restrict__ bv, const __bf16* __restrict__ shk, const __bf16* __restrict__ ssmk,
             const __bf16* __restrict__ ssmd, const __bf16* __restrict__ gw, __bf16* __restrict__ out, char* ws) {
  __shared__ __attribute__((aligned(16))) char smem[160 * 1024];
  asm volatile("fk_beg:");
  STAMP_DECL; STAMP(0);
  const int tid = threadIdx.x, lane = tid & 63;
  const int w = __builtin_amdgcn_readfirstlane(tid >> 6);
  const int bid = blockIdx.x, jj = bid >> 3;
  const int qt = (bid & 7) * 4 + (jj & 3), hg = jj >> 2;
  const unsigned sbase = (unsigned)(size_t)smem;
  lds_ptr sm = (lds_ptr)smem;
  lds_ptr mi = sm + L_M;
  const int h = lane >> 5, l31 = lane & 31, l15 = lane & 15, g4 = lane >> 4;
  const int tq = (lane & 15) >> 2, tp = lane & 3;
  const auto rs = __builtin_amdgcn_make_buffer_rsrc(ws, 0, WS_END, 0x00020000);
  GAS char* gws = (GAS char*)ws;
  const int rot = (hg + qt) % 3;
  const int s0 = bid & 7;
  const int pt = (hg >> 1) & 1, sel = hg & 1;
  const char* xt = (const char*)x + (size_t)qt * 73728;
  const char* wqs = (const char*)wq + 144 * hg;
  const unsigned vxe = (lane >> 3) * 1152 + (((lane & 7) ^ g4) << 4), vxo = 9216 + (lane >> 3) * 1152 + (((lane & 7) ^ (4 + g4)) << 4);
  const int otok = tid / 9, opc = tid - 9 * otok;

  if (w >= 8) {
    const int j = w - 8, ht = tid - 512;
    __builtin_amdgcn_s_setprio(3);
    if (j < 3) {
      const int th = (j + rot) % 3;
      const unsigned stg = L_B + 2 * 27648 + j * 9216;
#pragma unroll
      for (int c = 0; c < 6; ++c) {
        const char* src = xt + (2 * pt + (c & 1)) * 18432 + (3 * th + (c >> 1)) * 128;
        const unsigned dst = sbase + L_X + (3 * j + (c >> 1)) * 8192 + (2 * pt + (c & 1)) * 2048;
        glds(src, vxe, dst);
        glds(src, vxo, dst + 1024);
      }
      if (hg < 4) {
        const char* wsrc = (const char*)(sel ? wv : wk) + th * 6912;
#pragma unroll
        for (int i = 0; i < 3; ++i) {
          const int f = 64 * i + lane, row = f / 3, pc = f - 3 * row;
          const unsigned vo = row * 36 + (pc == 2 ? 20 : 16 * pc);
#pragma unroll
          for (int p = 0; p < 3; ++p) glds(wsrc + p * 2304, vo, sbase + stg + p * 3072 + i * 1024);
        }
      }
      BAR();
      VM_WAIT(0);
      STAMP(1);
      lds_signal(mi + M_KD);
      lds_signal(mi + M_C1);
      if (hg < 4) lds_wait<1>(mi + M_KVC, 4);
      STAMP(2);
      {
        const char* src = wqs + (size_t)(192 * ((2 + rot) % 3) + 64 * j) * 1152;
        int pc = lane % 9;
        unsigned vo = (lane / 9) * 1152 + pc * 16;
#pragma unroll
        for (int i = 0; i < 9; ++i) {
          glds(src, vo, sbase + stg + i * 1024);
          vo += pc == 8 ? 9088 : 8080;
          pc = pc == 8 ? 0 : pc + 1;
        }
      }
      VM_WAIT(0);
      lds_signal(mi + M_C3);
      lds_wait<1>(mi + M_KVR, 1);
    } else {
      const unsigned epoch = __hip_atomic_load((gu32*)(gws + WS_EPOCH), RLX_AGENT);
      unsigned pf = 0;
      {
        unsigned long long pc0;
        unsigned back, len;
        asm volatile("s_getpc_b64 %0\n\tfk_pc:\n\ts_mov_b32 %1, fk_pc-fk_beg\n\ts_mov_b32 %2, fk_end-fk_beg" : "=s"(pc0), "=s"(back), "=s"(len));
        const unsigned o = min((unsigned)(lane * 8 + (jj & 7)) * 128u, len);
        asm volatile("global_load_dword %0, %1, %2" : "=v"(pf) : "v"(o), "s"(pc0 - back) : "memory");
      }
      BAR();
      asm volatile("s_waitcnt vmcnt(0)" : "+v"(pf) :: "memory");
      const unsigned tg = __builtin_amdgcn_readfirstlane(epoch) + 1u;
      if (lane == 0) *(LAS unsigned*)(mi + M_TAG) = tg;
      lds_signal(mi + M_TAGR);
      lds_wait<1>(mi + POLL_AT, POLL_N);
      PSTAMP(5);
      float kn = 0.f;
#pragma unroll 1
      for (unsigned spins = 0; spins < SPIN_CAP; ++spins) {
        u32x4 v0 = {0, tg, 0, tg};
        if (lane < 8) v0 = LD_SC1_16(WS_FKV + (8 * s0 + lane) * 128);
        const bool ok = v0[1] == tg && v0[3] == tg;
        kn = u2f(v0[0]);
        asm volatile("" ::: "memory");
        if (spins == 0) PSTAMP(6);
        if (spins == 1) PSTAMP(7);
        if (spins == 2) PSTAMP(8);
        if (spins == 3) PSTAMP(9);
        if (__all(ok)) break;
        __builtin_amdgcn_s_sleep(POLL_SLEEP);
      }
      lds_signal(mi + M_KVR);
      STAMP(2); RSTAMP(2);
      kn = max16(kn);
      if (lane == 0) *(LAS float*)(mi + M_KN0) = kn;
    }
    const unsigned tag = *(LAS unsigned*)(mi + M_TAG);
    const int kvo = j * 1024 + lane * 16;
    const bool kvlast = kvo + 4 * 4096 < RSLOT;
    const int kvo4 = kvlast ? kvo + 16384 : kvo;
    auto kv_dma = [&](int st) {
      const char* src = ws + WS_KV + ((s0 + st) & 7) * RSLOT + j * 4992;
      const unsigned dst = sbase + st * RSLOT + j * 4992;
      GLDS4(" sc1", src, lane * 16, lane * 16, dst);
      if (lane < 56) glds_sc1(src + 4096, lane * 16, dst + 4096);
    };
    const unsigned vh3 = (lane == 63 ? 62 : lane) * 16;
    auto hload = [&](int cl, int b) {
      const char* hsrc = (const char*)(ssmk + (size_t)(72 * hg + cl) * S_) + 4 * j + 2;
      const unsigned dst = sbase + CV_H + b * CV_HB + 2 * j * CV_COPY + 64;
      GLDS4("", hsrc, lane * 16, vh3, dst);
      GLDS4("", hsrc + 2, lane * 16, vh3, dst + CV_COPY);
    };
    auto uload = [&](int cl) {
      const int tgq = ht & 7;
      const int o = cl < 64 ? (tgq >> 1) * 2048 + (cl & 1) * 1024 + ((tgq & 1) * 32 + (cl >> 1)) * 16
                            : 8192 + (cl & 1) * 512 + (tgq * 4 + ((cl - 64) >> 1)) * 16;
      return LD_SC1_16(WS_UT + ((ht >> 3) * 8 + hg) * 9216 + o);
    };
    const int ce = 64 + (qt >> 2);
    const unsigned short h8a = *(const unsigned short*)(ssmk + (size_t)(72 * hg + 2 * qt + (j >> 1)) * S_ + (lane & 7));
    const unsigned short h8e = *(const unsigned short*)(ssmk + (size_t)(72 * hg + ce) * S_ + (lane & 7));
    const float dva = (float)ssmd[72 * hg + 2 * qt + (j >> 1)], dve = (qt & 3) ? 0.f : (float)ssmd[72 * hg + ce];
    {
      u32x4 ra[5];
#pragma unroll
      for (int i = 0; i < 5; ++i) ra[i] = LD_SC1_16(WS_KV + s0 * RSLOT + (i < 4 ? kvo + i * 4096 : kvo4));
      UNIT_LOAD(l31, 2 * j + h);
      lds_wait(mi + M_C3B, 8);
#pragma unroll
      for (int i = 0; i < 5; ++i) if (i < 4 || kvlast) *(LAS u32x4*)(sm + kvo + i * 4096) = ra[i];
      lds_signal(mi + M_QD);
      STAMP(3);
      if (j == 3) {
        float kn = 0.f;
#pragma unroll 1
        for (unsigned spins = 0; spins < SPIN_CAP; ++spins) {
          const u32x4 v0 = LD_SC1_16(WS_FKV + lane * 128);
          const bool ok = v0[1] == tag && v0[3] == tag;
          kn = u2f(v0[0]);
          asm volatile("" ::: "memory");
          if (__all(ok)) break;
          __builtin_amdgcn_s_sleep(POLL_SLEEP);
        }
        lds_signal(mi + M_KVR);
        kv_dma(1);
        kn = wave_max(kn);
        if (lane == 0) *(LAS float*)(mi + M_KN) = kn;
      }
      UNIT_DO();
      ST_SC1_16(o0, WS_UT + (qt * 8 + hg) * 9216 + j * 2048 + lane * 16);
      ST_SC1_16(o1, WS_UT + (qt * 8 + hg) * 9216 + j * 2048 + 1024 + lane * 16);
      if (j < 3) { lds_wait(mi + M_KVR, 2); kv_dma(1); }
      STAMP(4);
      VM_WAIT(0);
      lds_signal(mi + M_RDY + 4, 2);
      lds_wait<1>(mi + M_QR, 8);
      if (lds_ticket(mi + M_UD) == 3u && lane == 0) __hip_atomic_store((gu32*)(gws + WS_FU) + (hg * 32 + qt) * 32, tag, RLX_AGENT);
      kv_dma(2);
      kv_dma(3);
    }
    hload(2 * qt, 0);
    hload(2 * qt + 1, 1);
    {
      *(LAS u32x4*)(sm + CV_U + (ht >> 7) * CV_UB + ((ht & 127) >> 2) * 80 + (ht & 3) * 16) = (u32x4){0, 0, 0, 0};
      lds_ptr hz = sm + CV_H + (j >> 1) * CV_HB + (lane >> 3) * CV_COPY;
      *(LAS u32x2*)(hz + (lane & 7) * 8) = (u32x2){0, 0};
      if ((lane & 7) <= (lane >> 3)) *(LAS unsigned short*)(hz + 48 + (7 - (lane >> 3) + (lane & 7)) * 2) = h8a;
      if (j == 0) {
        const float lg = *(LAS float*)(mi + M_G + lane * 4);
        *(LAS float*)(mi + M_G + lane * 4) = rbf(__builtin_amdgcn_rcpf(1.f + __expf(-rbf(lg))));
      }
    }
    VM_WAIT(16);
    lds_signal(mi + M_RDY + 8, 2);
    lds_signal(mi + M_RDY + 12, 2);
    if (j == 3) {
#pragma unroll 1
      for (unsigned spins = 0; spins < SPIN_CAP; ++spins) {
        bool ok = h || __hip_atomic_load((gu32*)(gws + WS_FU) + (hg * 32 + lane) * 32, RLX_AGENT) == tag;
        if (bid == 0) {
#pragma unroll
          for (int i = 0; i < 4; ++i) ok &= __hip_atomic_load((gu32*)(gws + WS_FU) + (64 * i + lane) * 32, RLX_AGENT) == tag;
        }
        if (__all(ok)) break;
        __builtin_amdgcn_s_sleep(4);
      }
      if (bid == 0 && lane == 0) __hip_atomic_store((gu32*)(gws + WS_EPOCH), tag, RLX_AGENT);
      lds_signal(mi + M_UR);
    } else {
      lds_wait<2>(mi + M_UR, 1);
    }
    unsigned hs = 0;
#define HSYNC() do { lds_signal(mi + M_HS); hs += 4; lds_wait<1>(mi + M_HS, hs); } while (0)
    NSTAMP(5);
    CSTAMP(1);
    const u32x4 ue = uload(ce);
    {
      const u32x4 ua = uload(2 * qt), ub = uload(2 * qt + 1);
      *(LAS u32x4*)(sm + CV_U + 2560 + (ht >> 2) * 80 + (ht & 3) * 16) = ua;
      *(LAS u32x4*)(sm + CV_U + CV_UB + 2560 + (ht >> 2) * 80 + (ht & 3) * 16) = ub;
    }
    constexpr int CV_UE = CV_H + CV_HB + 24576;
    lds_ptr px = sm + CV_H + CV_HB + lane * 16;
#pragma unroll 1
    for (int ph = 0; ph < 2; ++ph) {
      const int d0 = ph ? 8 * (qt & 3) + 2 * j : 16 * (j & 1);
      VM_WAIT(0);
      if (ph == 0) CSTAMP(2);
      HSYNC();
      if (ph == 0) CSTAMP(3);
      lds_ptr ub = sm + (ph ? CV_UE : CV_U + (j >> 1) * CV_UB) + 2560;
      lds_ptr ab = sm + CV_H + (ph ? 0 : j >> 1) * CV_HB + (l31 & 7) * CV_COPY + (h + (l31 >> 3)) * 16 + 64 * d0;
      lds_ptr bb = ub + (l31 - d0 - 2) * 80 + (1 - h) * 16;
      f32x16 y0 = {0}, y1 = {0};
      bf16x8 fx[8], fy[8];
#define PACE() do { if (CV_SLEEP) { __builtin_amdgcn_sched_barrier(0); __builtin_amdgcn_s_sleep(CV_SLEEP); __builtin_amdgcn_sched_barrier(0); } } while (0)
#define CV_LD(F, D) _Pragma("unroll") for (int s = 0; s < 2; ++s) {                                     \
        F[4 * s] = lds_frag(ab + 32 * (2 * (D) + 1 - s)); F[4 * s + 1] = lds_frag(bb + 80 * (2 - (D)) + 32 * s);  \
        F[4 * s + 2] = lds_frag(bb + 80 * (34 - (D)) + 32 * s); F[4 * s + 3] = lds_frag(ab + 32 * (2 * (D) + 65 - s)); } \
        __builtin_amdgcn_sched_barrier(0)
#define CV_MM(F) _Pragma("unroll") for (int s = 0; s < 2; ++s) {                                        \
        y0 = MFMA32(F[4 * s], F[4 * s + 1], y0); PACE(); y1 = MFMA32(F[4 * s], F[4 * s + 2], y1); PACE(); y1 = MFMA32(F[4 * s + 3], F[4 * s + 1], y1); PACE(); } \
        __builtin_amdgcn_sched_barrier(0)
      __builtin_amdgcn_s_setprio(CV_PRIO);
      CV_LD(fx, 0);
#pragma unroll 1
      for (int it = ph ? 7 : 0; it < 8; ++it) {
        CV_LD(fy, 1);
        CV_MM(fx);
        CV_LD(fx, 2);
        CV_MM(fy);
        ab += 128;
        bb -= 160;
      }
      __builtin_amdgcn_s_setprio(3);
      if (ph == 0) {
        CSTAMP(4);
        HSYNC();
        hload(ce, 0);
        if (j == 0 && (lane & 7) <= (lane >> 3))
          *(LAS unsigned short*)(sm + CV_H + (lane >> 3) * CV_COPY + 48 + (7 - (lane >> 3) + (lane & 7)) * 2) = h8e;
        if (ht < 160) *(LAS u32x4*)(sm + CV_UE + ht * 16) = (u32x4){0, 0, 0, 0};
        *(LAS u32x4*)(sm + CV_UE + 2560 + (ht >> 2) * 80 + (ht & 3) * 16) = ue;
      }
      {
        const f32x16 give = (j & 1) ? y0 : y1;
#pragma unroll
        for (int g = 0; g < 4; ++g) *(LAS f32x4*)(px + j * 4096 + g * 1024) = (f32x4){give[4 * g], give[4 * g + 1], give[4 * g + 2], give[4 * g + 3]};
        if (j & 1) y0 = y1;
        if (ph && j >= 2) {
#pragma unroll
          for (int g = 0; g < 4; ++g) *(LAS f32x4*)(px + (j + 2) * 4096 + g * 1024) = (f32x4){y0[4 * g], y0[4 * g + 1], y0[4 * g + 2], y0[4 * g + 3]};
        }
      }
      if (ph == 0) CSTAMP(9);
      HSYNC();
      if (ph == 0) CSTAMP(10);
      if (!ph || j < 2) {
        const float dv = ph ? dve : dva;
        u32x4 outv[2];
#pragma unroll
        for (int g = 0; g < 4; ++g) {
          f32x4 s = *(LAS f32x4*)(px + (j ^ 1) * 4096 + g * 1024);
          if (ph) s += *(LAS f32x4*)(px + (3 - j) * 4096 + g * 1024) + *(LAS f32x4*)(px + (j + 4) * 4096 + g * 1024);
          const u32x2 uu = *(LAS u32x2*)(ub + (32 * (j & 1) + l31) * 80 + g * 16 + (1 - h) * 8);
          s = (f32x4){fmaf(bf_hi(uu[1]), dv, s[0] + y0[4 * g]), fmaf(bf_lo(uu[1]), dv, s[1] + y0[4 * g + 1]),
                      fmaf(bf_hi(uu[0]), dv, s[2] + y0[4 * g + 2]), fmaf(bf_lo(uu[0]), dv, s[3] + y0[4 * g + 3])};
          if (ph) ST_SC1_16(__builtin_bit_cast(u32x4, s), WS_ZP + (hg * 32 + qt) * 8192 + j * 4096 + g * 1024 + lane * 16);
          outv[g >> 1][2 * (g & 1)] = pk2(s[0], s[1]);
          outv[g >> 1][2 * (g & 1) + 1] = pk2(s[2], s[3]);
        }
        if (!ph) {
          const int zo = WS_Z + (72 * hg + 2 * qt + (j >> 1)) * 4096 + (j & 1) * 2048 + lane * 16;
          ST_SC1_16(outv[0], zo);
          ST_SC1_16(outv[1], zo + 1024);
        }
      }
      if (ph == 0) CSTAMP(7);
    }
    NSTAMP(6);
    VM_WAIT(0);
    HSYNC();
    if (ht == 192) __hip_atomic_store((gu32*)(gws + WS_FZ) + (hg * 32 + qt) * 32, tag, RLX_AGENT);
    if (lane < 54) {
      const unsigned vo = (lane / 9) * 1152 + (lane % 9) * 16;
#pragma unroll 1
      for (int p = 24 * j; p < 24 * j + 24; p += 4)
        asm volatile("s_mov_b32 m0, %5\n\ts_nop 0\n\tglobal_load_lds_dwordx4 %0, %4\n\tglobal_load_lds_dwordx4 %1, %4 offset:864\n\t"
                     "global_load_lds_dwordx4 %2, %4 offset:1728\n\tglobal_load_lds_dwordx4 %3, %4 offset:2592"
                     :: "v"(vo), "v"(vo + 6048), "v"(vo + 12096), "v"(vo + 18144), "s"((const char*)wo + 144 * hg + p * 6912),
                        "s"(sbase + L_B + p * 864) : "memory");
    }
    VM_WAIT(0);
    lds_signal(mi + M_WO);
    HSTAMP(7);
    if (j == 3) {
#pragma unroll 1
      for (unsigned spins = 0; spins < SPIN_CAP; ++spins) {
        if (__all(h || __hip_atomic_load((gu32*)(gws + WS_FZ) + (hg * 32 + lane) * 32, RLX_AGENT) == tag)) break;
        __builtin_amdgcn_s_sleep(16);
      }
      lds_signal(mi + M_ZR);
    } else {
      lds_wait<8>(mi + M_ZR, 1);
    }
    {
      u32x4 zv[3], xv[3];
#pragma unroll
      for (int i = 0; i < 3; ++i) {
        const int n = (i < 2 || ht < 64) ? ht + 256 * i : ht;
        const int zl = (2 * (qt & 15) + (n & 1) + 32 * ((n >> 1) & 1)) * 16;
        if (i < 2) {
          zv[i] = LD_SC1_16(WS_Z + (72 * hg + (n >> 3)) * 4096 + (qt >> 4) * 2048 + ((n >> 2) & 1) * 1024 + zl);
        } else if (j == 0) {
          f32x4 sa = {0}, sb = {0};
#pragma unroll
          for (int p = 0; p < 4; ++p) {
            const int zo = WS_ZP + (hg * 32 + (lane >> 3) * 4 + p) * 8192 + (qt >> 4) * 4096 + ((n >> 2) & 1) * 2048 + zl;
            sa += __builtin_bit_cast(f32x4, LD_SC1_16(zo));
            sb += __builtin_bit_cast(f32x4, LD_SC1_16(zo + 1024));
          }
          zv[i] = (u32x4){pk2(sa[0], sa[1]), pk2(sa[2], sa[3]), pk2(sb[0], sb[1]), pk2(sb[2], sb[3])};
        } else {
          zv[i] = zv[0];
        }
        xv[i] = *(const u32x4*)(xt + (n / 9) * 1152 + 144 * hg + (n % 9) * 16);
      }
      lds_wait<4>(mi + M_DONE + BT_AT, 8);
#pragma unroll
      for (int i = 0; i < 3; ++i) {
        const int n = (i < 2 || ht < 64) ? ht + 256 * i : ht;
        lds_ptr zp = sm + L_BT + ((32 * (n & 1) + 16 * ((n >> 2) & 1) + 4 * ((n >> 1) & 1)) * 72 + (n >> 3)) * 2;
#pragma unroll
        for (int e = 0; e < 8; ++e)
          *(LAS unsigned short*)(zp + ((e & 3) + 8 * (e >> 2)) * 144) = (unsigned short)((e & 1) ? zv[i][e >> 1] >> 16 : zv[i][e >> 1]);
      }
      lds_signal(mi + M_HS);
      lds_wait(mi + M_HS, hs + 4);
#pragma unroll
      for (int i = 0; i < 3; ++i) {
        if (i < 2 || ht < 64) {
          const int n = ht + 256 * i;
          const u32x4 z = *(LAS u32x4*)(sm + L_BT + n * 16);
          const float omg = rbf(1.f - *(LAS float*)(mi + M_G + (n / 9) * 4));
          u32x4 o;
#pragma unroll
          for (int e = 0; e < 4; ++e)
            o[e] = pk2(omg * bf_lo(xv[i][e]) * bf_lo(z[e]), omg * bf_hi(xv[i][e]) * bf_hi(z[e]));
          *(LAS u32x4*)(sm + L_BT + n * 16) = o;
        }
      }
      lds_signal(mi + M_BT);
    }
    HSTAMP(8);
    __builtin_amdgcn_s_setprio(0);
  } else {
    if (w == 0) *(LAS int*)(mi + lane * 4) = (lane == M_RDY / 4 + 2 || lane == M_RDY / 4 + 3) ? -8 : 0;
    BAR();
    const float QS = 0.23570226039551584f * 1.4426950408889634f;
    if (w >= 5) {
      const int wi = w - 5;
      unsigned vw[3];
#pragma unroll
      for (int m = 0; m < 3; ++m) {
        const int f = 64 * (wi + 3 * m) + lane, row = f / 9;
        vw[m] = row * 1152 + (f - 9 * row) * 16;
      }
#pragma unroll
      for (int p = 0; p < 3; ++p) {
        const int th = (p + rot) % 3;
#pragma unroll
        for (int pr = 0; pr < 2; ++pr) {
          const int g2 = 2 * (1 - pt) + pr;
          const char* src = xt + g2 * 18432 + (3 * th + wi) * 128;
          glds(src, vxe, sbase + L_X + (3 * p + wi) * 8192 + g2 * 2048);
          glds(src, vxo, sbase + L_X + (3 * p + wi) * 8192 + g2 * 2048 + 1024);
        }
        if (p < 2) {
#pragma unroll
          for (int per = 0; per < 3; ++per) {
#pragma unroll
            for (int m = 0; m < 3; ++m)
              glds(wqs + (size_t)(192 * th + 64 * per) * 1152, vw[m], sbase + L_B + p * 27648 + per * 9216 + (wi + 3 * m) * 1024);
            if (p == 1 && per == 0) { VM_WAIT(7); lds_signal(mi + M_C1); }
          }
        }
      }
      STAMP(1);
      VM_WAIT(4);
      lds_signal(mi + M_C2);
    } else if (w < 4) {
      if (hg < 4) {
        const int mt = w & 1, nt = w >> 1, row = 32 * pt + 16 * mt + l15;
        lds_ptr wb = sm + L_B + 2 * 27648 + (8 * g4 + tq) * 48 + (nt ? 32 + (tp & 1) * 8 : tp * 8);
        lds_ptr xb = sm + L_X + row * 128;
        const int T = 2 * qt + pt, i4 = T & 3, gb = WS_KV + (T >> 2) * KV_GROUP, rp = swap23(16 * mt + l15);
        int so;
        bool st;
        if (sel) {
          const int d = nt ? (l15 < 8 ? 10 + l15 + 18 * (g4 & 1) : 36 + (g4 & 1)) : l15 + 18 * (g4 & 1);
          so = gb + 4096 + i4 * 1216 + mt * 608 + d * 16 + (g4 >> 1) * 8;
          st = !nt || (l15 >= 6 && l15 <= 8);
        } else {
          so = nt ? gb + 8960 + i4 * 256 + rp * 8 : gb + i4 * 1024 + (rp + 32 * (g4 & 1)) * 16 + (g4 >> 1) * 8;
          st = !nt || g4 == 1;
        }
        lds_wait(mi + M_KD, 3);
        PSTAMP(5);
        bf16x8 fa[18], fb[18];
#pragma unroll
        for (int k = 0; k < 18; ++k) {
          fa[k] = tr_frag(wb + k * 1536, wb + k * 1536 + 192);
          fb[k] = lds_frag(xb + (k >> 1) * 8192 + (((4 * (k & 1) + g4) ^ (l15 >> 1)) << 4));
        }
        f32x4 acc = {0}, acc1 = {0};
        if (sel) {
#pragma unroll
          for (int k = 0; k < 18; k += 2) { acc = MFMA16(fb[k], fa[k], acc); acc1 = MFMA16(fb[k + 1], fa[k + 1], acc1); }
        } else {
#pragma unroll
          for (int k = 0; k < 18; k += 2) { acc = MFMA16(fa[k], fb[k], acc); acc1 = MFMA16(fa[k + 1], fb[k + 1], acc1); }
        }
        acc += acc1;
        u32x2 a = {pk2(acc[0], acc[1]), pk2(acc[2], acc[3])};
        if (nt) {
          if (!sel) a = (u32x2){a[1], 0x3f803f80u};
          else if (l15 == 8) a[0] = a[1] = (g4 & 1) ? 0u : 0x3f803f80u;
        }
        if (st) ST_SC1_8(a, so);
        PSTAMP(6);
        lds_signal(mi + M_KVC);
        float n2 = bf_lo(a[0]) * bf_lo(a[0]) + bf_hi(a[0]) * bf_hi(a[0]);
        if (!nt) n2 += bf_lo(a[1]) * bf_lo(a[1]) + bf_hi(a[1]) * bf_hi(a[1]);
        if (!st) n2 = 0.f;
        n2 += xhalf(n2);
        n2 += u2f(__builtin_amdgcn_ds_swizzle(__builtin_bit_cast(int, n2), 0x401f));
        n2 = max16(n2);
        if (lane == 0) *(LAS float*)(mi + M_N2 + 4 * w) = n2;
        lds_wait(mi + M_TAGR, 1);
        PSTAMP(7);
        VM_WAIT(0);
        PSTAMP(8);
        if (lds_ticket(mi + M_KVP) == 3u && lane == 0) {
          const f32x4 n = *(LAS f32x4*)(mi + M_N2);
          const float nn = sel ? 0.f : fmaxf(n[0], n[1]) + fmaxf(n[2], n[3]);
          __hip_atomic_store((gu64*)(gws + WS_FKV + T * 128 + sel * 8),
                             ((unsigned long long)*(LAS unsigned*)(mi + M_TAG) << 32) | __builtin_bit_cast(unsigned, nn), RLX_AGENT);
        }
        PSTAMP(9);
      }
    } else {
      {
      *(LAS u32x4*)(sm + L_GW + lane * 16) = *(const u32x4*)(gw + 8 * lane);
      if (lane < 8) *(LAS u32x4*)(sm + L_GW + 1024 + lane * 16) = *(const u32x4*)(gw + 512 + 8 * lane);
      lds_signal(mi + M_C1);
      UNIT_LOAD(32 + (lane & 3), l31 >> 2);
      UNIT_DO();
      ST_SC1_16(h ? o1 : o0, WS_UT + (qt * 8 + hg) * 9216 + 8192 + lane * 16);
      }
    }
    if (w < 4) {
      const int gt = w >> 1, tt = w & 1, arow = 32 * tt + l31, xsw = (arow >> 1) & 7;
      lds_ptr xb = sm + L_X + arow * 128;
      lds_ptr wb = sm + L_B + (8 * h + tq) * 144 + (36 * gt + 20 * (g4 & 1) + 4 * tp) * 2;
      f32x16 acc = {0};
#pragma unroll 1
      for (int p = 0; p < 3; ++p) {
        if (p == 1) CSTAMP(6);
        if (p == 2) CSTAMP(8);
        lds_wait(mi + M_C1 + 4 * p, p == 0 ? 7 : 8 * p - 5);
        if (p == 0) CSTAMP(5);
        if (p == 1) CSTAMP(7);
        if (p == 2) CSTAMP(9);
        bf16x8 fa[12], fb[12];
#pragma unroll
        for (int k = 0; k < 12; ++k) {
          fa[k] = tr_frag(wb + k * 2304, wb + k * 2304 + 576);
          fb[k] = lds_frag(xb + (k >> 2) * 8192 + (((2 * (k & 3) + h) ^ xsw) << 4));
        }
        __builtin_amdgcn_sched_barrier(0);
#pragma unroll
        for (int k = 0; k < 12; ++k) acc = MFMA32(fa[k], fb[k], acc);
        if (p == 1) { lds_signal(mi + M_C3B); lds_signal(mi + M_C3); }
        xb += 24576;
        wb += 27648;
      }
      STAMP(2);
      u32x4 a;
#pragma unroll
      for (int i = 0; i < 4; ++i) a[i] = pk2(acc[2 * i] * QS, acc[2 * i + 1] * QS);
      lds_ptr qe = sm + L_Q + (4 * gt + tt) * 1024;
      *(LAS u32x4*)(qe + lane * 16) = a;
      lds_ptr qo = qe + 2048 + l31 * 16;
      *(LAS unsigned*)(qo + h * 512 + 4) = pk2(acc[8] * QS, acc[9] * QS);
      *(LAS unsigned*)(qo + (h ? 8 : 512)) = pk2(acc[10] * QS, acc[11] * QS);
      *(LAS unsigned*)(qo + h * 512 + 12) = pk2(acc[12] * QS, acc[13] * QS);
      *(LAS unsigned*)(h ? sm + L_Q + 8192 + ((2 * gt + 1) * 64 + arow) * 4 : qo + 520) = pk2(acc[14] * QS, acc[15] * QS);
    } else {
      const int tok = 16 * (w - 4) + l15;
      lds_ptr xb = sm + L_X + tok * 128;
      lds_ptr wb = sm + L_B + (8 * g4 + tq) * 144 + (tp == 1 ? 104 : 32);
      f32x4 acc = {0};
#pragma unroll 1
      for (int p = 0; p < 3; ++p) {
        lds_wait(mi + M_C1 + 4 * p, p == 0 ? 7 : 8 * p - 5);
        lds_ptr gp = sm + L_GW + ((p + rot) % 3) * 384 + g4 * 16;
        bf16x8 fa[6], fg[6], fb[6];
#pragma unroll
        for (int k = 0; k < 6; ++k) {
          fa[k] = tr_frag(wb + k * 4608, wb + k * 4608 + 576);
          fg[k] = lds_frag(gp + k * 64);
          fb[k] = lds_frag(xb + (k >> 1) * 8192 + (((4 * (k & 1) + g4) ^ (l15 >> 1)) << 4));
        }
        __builtin_amdgcn_sched_barrier(0);
#pragma unroll
        for (int k = 0; k < 6; ++k) acc = MFMA16(l15 == 8 ? fg[k] : fa[k], fb[k], acc);
        if (p == 1) { lds_signal(mi + M_C3B); VM_WAIT(0); lds_signal(mi + M_C3); }
        xb += 24576;
        wb += 27648;
      }
      STAMP(2);
      if (g4 < 2) {
        *(LAS unsigned*)(sm + L_Q + 8192 + (2 * g4 * 64 + tok) * 4) = pk2(acc[0] * QS, acc[1] * QS);
        *(LAS unsigned*)(sm + L_Q + ((2 * g4 + 1) * 2 + (tok >> 5)) * 1024 + (tok & 31) * 16) = pk2(acc[2] * QS, acc[3] * QS);
      } else if (g4 == 2) {
        *(LAS float*)(mi + M_G + tok * 4) = acc[0];
      }
    }
    lds_signal(mi + M_QD);
    const int hl = w & 3, kh = w >> 2;
    const int voff = 4096 + (l31 < 18 ? h * 288 + l31 * 16 : (l31 == 18 ? 576 : 592));
    lds_wait(mi + M_QD, 12);
    STAMP(3);
    const u32x4 q0u = *(LAS u32x4*)(sm + L_Q + (2 * hl) * 1024 + lane * 16);
    const u32x4 q1u = *(LAS u32x4*)(sm + L_Q + (2 * hl + 1) * 1024 + lane * 16);
    const unsigned qr = *(LAS unsigned*)(sm + L_Q + 8192 + (hl * 64 + lane) * 4);
    const float kn0 = *(LAS float*)(mi + M_KN0);
    lds_signal(mi + M_QR);
    float n0 = 0.f, n1 = 0.f;
#pragma unroll
    for (int i = 0; i < 4; ++i) { n0 = dot2(q0u[i], n0); n1 = dot2(q1u[i], n1); }
    const float nq = dot2(qr, (h ? n1 : n0) + xhalf(h ? n0 : n1));
    const u32x2 qrem_u = {qr, pk2(-__builtin_amdgcn_sqrtf(nq * kn0), 0.f)};
    f32x16 o0, o1;
    float m0 = 0.f, m1 = 0.f;
    STAMP(4); RSTAMP(2);
    {
      int cnt = 8, off = 0, tmp, t2, so = ((s0 + 4) & 7) * RSLOT;
      unsigned ca = sbase + L_M + M_DONE - 4, dst = sbase + w * 2496;
      const unsigned long long ksrc = (unsigned long long)ws + WS_KV + w * 2496 - (sbase + kh * KV_GROUP);
      asm volatile(
      "v_mov_b32 v107, v126\n"
      "v_mov_b32 v108, v127\n"
      "v_mov_b32 v109, v128\n"
      "v_mov_b32 v130, 1\n"
      "ds_read2st64_b64 v[110:113], v109 offset0:0 offset1:1\n"
      "ds_read_b128 v[114:117], v107 offset:0\n"
      "ds_read_b128 v[118:121], v108 offset:0\n"
      "s_mov_b32 vcc_lo, %[first]\n"
      "s_mov_b32 vcc_hi, 0\n"
      "s_setprio 1\n"
      "v_mov_b32 v56, 0\n"
      "v_mov_b32 v57, 0\n"
      "v_mov_b32 v58, 0\n"
      "v_mov_b32 v59, 0\n"
      "v_mov_b32 v60, 0\n"
      "v_mov_b32 v61, 0\n"
      "v_mov_b32 v62, 0\n"
      "v_mov_b32 v63, 0\n"
      "v_mov_b32 v122, 0\n"
      "v_mov_b32 v123, 0\n"
      "v_mov_b32 v124, 0\n"
      "v_mov_b32 v125, 0\n"
      "v_mov_b32 v64, 0\n"
      "v_mov_b32 v65, 0\n"
      "v_mov_b32 v66, 0\n"
      "v_mov_b32 v67, 0\n"
      "v_mov_b32 v68, 0\n"
      "v_mov_b32 v69, 0\n"
      "v_mov_b32 v70, 0\n"
      "v_mov_b32 v71, 0\n"
      "v_mov_b32 v72, 0\n"
      "v_mov_b32 v73, 0\n"
      "v_mov_b32 v74, 0\n"
      "v_mov_b32 v75, 0\n"
      "v_mov_b32 v76, 0\n"
      "v_mov_b32 v77, 0\n"
      "v_mov_b32 v78, 0\n"
      "v_mov_b32 v79, 0\n"
      "s_waitcnt lgkmcnt(1)\n"
      "v_mfma_f32_32x32x4_2b_bf16 v[0:31], v[110:111], v[104:105], 0 cbsz:1 abid:0\n"
      "v_mov_b32 v80, 0\n"
      "v_mov_b32 v81, 0\n"
      "v_mov_b32 v82, 0\n"
      "v_mov_b32 v83, 0\n"
      "v_mov_b32 v84, 0\n"
      "v_mov_b32 v85, 0\n"
      "v_mov_b32 v86, 0\n"
      "v_mov_b32 v87, 0\n"
      "v_mov_b32 v88, 0\n"
      "v_mov_b32 v89, 0\n"
      "v_mov_b32 v90, 0\n"
      "v_mov_b32 v91, 0\n"
      "v_mov_b32 v92, 0\n"
      "v_mov_b32 v93, 0\n"
      "v_mov_b32 v94, 0\n"
      "v_mov_b32 v95, 0\n"
      "s_nop 2\n"
      "v_mfma_f32_32x32x16_bf16 v[0:15], v[114:117], v[96:99], v[0:15]\n"
      "v_mfma_f32_32x32x16_bf16 v[16:31], v[114:117], v[100:103], v[16:31]\n"
      "s_nop 11\n"
      "1:\n"
      "s_cbranch_vccz 4f\n"
      "s_setprio 2\n"
      "4:\n"
      "v_exp_f32 v0, v0\n"
      "v_exp_f32 v1, v1\n"
      "v_smfmac_f32_32x32x32_bf16 v[80:95], v[122:125], v[56:63], v106\n"
      "ds_read_b128 v[114:117], v107 offset:1024\n"
      "ds_read_b128 v[122:125], v108 offset:608\n"
      "v_exp_f32 v2, v2\n"
      "v_exp_f32 v3, v3\n"
      "v_exp_f32 v4, v4\n"
      "v_exp_f32 v5, v5\n"
      "v_mfma_f32_32x32x4_2b_bf16 v[32:63], v[110:111], v[104:105], 0 cbsz:1 abid:1\n"
      "v_exp_f32 v6, v6\n"
      "v_exp_f32 v7, v7\n"
      "v_exp_f32 v8, v8\n"
      "v_exp_f32 v9, v9\n"
      "v_exp_f32 v10, v10\n"
      "v_exp_f32 v11, v11\n"
      "v_exp_f32 v12, v12\n"
      "s_waitcnt lgkmcnt(2)\n"
      "v_exp_f32 v13, v13\n"
      "v_smfmac_f32_32x32x32_bf16 v[64:79], v[118:121], v[0:7], v106\n"
      "v_exp_f32 v14, v14\n"
      "v_exp_f32 v15, v15\n"
      "v_exp_f32 v16, v16\n"
      "s_waitcnt lgkmcnt(0)\n"
      "v_exp_f32 v17, v17\n"
      "v_smfmac_f32_32x32x32_bf16 v[64:79], v[122:125], v[8:15], v106\n"
      "v_exp_f32 v18, v18\n"
      "v_exp_f32 v19, v19\n"
      "v_exp_f32 v20, v20\n"
      "v_exp_f32 v21, v21\n"
      "v_mfma_f32_32x32x16_bf16 v[32:47], v[114:117], v[96:99], v[32:47]\n"
      "v_exp_f32 v22, v22\n"
      "v_exp_f32 v23, v23\n"
      "v_exp_f32 v24, v24\n"
      "v_exp_f32 v25, v25\n"
      "v_smfmac_f32_32x32x32_bf16 v[80:95], v[118:121], v[16:23], v106\n"
      "v_exp_f32 v26, v26\n"
      "ds_read_b128 v[118:121], v108 offset:1216\n"
      "v_exp_f32 v27, v27\n"
      "v_exp_f32 v28, v28\n"
      "v_exp_f32 v29, v29\n"
      "v_mfma_f32_32x32x16_bf16 v[48:63], v[114:117], v[100:103], v[48:63]\n"
      "v_exp_f32 v30, v30\n"
      "v_exp_f32 v31, v31\n"
      "v_exp_f32 v32, v32\n"
      "v_exp_f32 v33, v33\n"
      "v_smfmac_f32_32x32x32_bf16 v[80:95], v[122:125], v[24:31], v106\n"
      "ds_read_b128 v[114:117], v107 offset:2048\n"
      "ds_read_b128 v[122:125], v108 offset:1824\n"
      "ds_read_b32 v131, v129\n"
      "v_exp_f32 v34, v34\n"
      "v_exp_f32 v35, v35\n"
      "v_exp_f32 v36, v36\n"
      "v_exp_f32 v37, v37\n"
      "v_mfma_f32_32x32x4_2b_bf16 v[0:31], v[112:113], v[104:105], 0 cbsz:1 abid:0\n"
      "v_exp_f32 v38, v38\n"
      "v_exp_f32 v39, v39\n"
      "v_exp_f32 v40, v40\n"
      "v_exp_f32 v41, v41\n"
      "v_exp_f32 v42, v42\n"
      "v_exp_f32 v43, v43\n"
      "v_exp_f32 v44, v44\n"
      "s_waitcnt lgkmcnt(3)\n"
      "v_exp_f32 v45, v45\n"
      "v_smfmac_f32_32x32x32_bf16 v[64:79], v[118:121], v[32:39], v106\n"
      "v_exp_f32 v46, v46\n"
      "v_exp_f32 v47, v47\n"
      "v_exp_f32 v48, v48\n"
      "s_waitcnt lgkmcnt(0)\n"
      "s_waitcnt vmcnt(0)\n"
      "s_mov_b64 exec, 1\n"
      "ds_add_u32 v129, v130 offset:76\n"
      "s_mov_b64 exec, -1\n"
      "v_exp_f32 v49, v49\n"
      "v_smfmac_f32_32x32x32_bf16 v[64:79], v[122:125], v[40:47], v106\n"
      "v_exp_f32 v50, v50\n"
      "v_exp_f32 v51, v51\n"
      "v_exp_f32 v52, v52\n"
      "v_exp_f32 v53, v53\n"
      "v_mfma_f32_32x32x16_bf16 v[0:15], v[114:117], v[96:99], v[0:15]\n"
      "v_exp_f32 v54, v54\n"
      "v_exp_f32 v55, v55\n"
      "v_exp_f32 v56, v56\n"
      "v_exp_f32 v57, v57\n"
      "v_smfmac_f32_32x32x32_bf16 v[80:95], v[118:121], v[48:55], v106\n"
      "v_exp_f32 v58, v58\n"
      "ds_read_b128 v[118:121], v108 offset:2432\n"
      "v_exp_f32 v59, v59\n"
      "v_exp_f32 v60, v60\n"
      "v_exp_f32 v61, v61\n"
      "v_mfma_f32_32x32x16_bf16 v[16:31], v[114:117], v[100:103], v[16:31]\n"
      "v_exp_f32 v62, v62\n"
      "v_exp_f32 v63, v63\n"
      "v_readfirstlane_b32 %[tmp], v131\n"
      "s_and_b32 %[t2], %[cnt], 12\n"
      "s_cmp_lg_u32 %[t2], 4\n"
      "s_cbranch_scc1 8f\n"
      "s_cmp_lt_u32 %[tmp], 8\n"
      "s_cbranch_scc1 7f\n"
      "9:\n"
      "s_mov_b32 m0, %[dst]\n"
      "s_add_u32 s96, %[slo], %[so]\n"
      "s_addc_u32 s97, %[shi], 0\n"
      "global_load_lds_dwordx4 v126, s[96:97] sc1\n"
      "global_load_lds_dwordx4 v126, s[96:97] offset:1024 sc1\n"
      "s_mov_b64 exec, 0xfffffff\n"
      "global_load_lds_dwordx4 v126, s[96:97] offset:2048 sc1\n"
      "s_mov_b64 exec, -1\n"
      "s_add_u32 %[dst], %[dst], 19968\n"
      "s_add_u32 %[so], %[so], 19968\n"
      "s_cmp_eq_u32 %[so], 159744\n"
      "s_cselect_b32 %[so], 0, %[so]\n"
      "8:\n"
      "v_exp_f32 v0, v0\n"
      "v_exp_f32 v1, v1\n"
      "v_smfmac_f32_32x32x32_bf16 v[80:95], v[122:125], v[56:63], v106\n"
      "ds_read_b128 v[114:117], v107 offset:3072\n"
      "ds_read_b128 v[122:125], v108 offset:3040\n"
      "ds_read_b32 v131, v129 offset:72\n"
      "v_exp_f32 v2, v2\n"
      "v_exp_f32 v3, v3\n"
      "v_exp_f32 v4, v4\n"
      "v_exp_f32 v5, v5\n"
      "v_mfma_f32_32x32x4_2b_bf16 v[32:63], v[112:113], v[104:105], 0 cbsz:1 abid:1\n"
      "v_exp_f32 v6, v6\n"
      "v_exp_f32 v7, v7\n"
      "v_exp_f32 v8, v8\n"
      "v_exp_f32 v9, v9\n"
      "v_exp_f32 v10, v10\n"
      "v_exp_f32 v11, v11\n"
      "v_exp_f32 v12, v12\n"
      "s_waitcnt lgkmcnt(3)\n"
      "v_exp_f32 v13, v13\n"
      "v_smfmac_f32_32x32x32_bf16 v[64:79], v[118:121], v[0:7], v106\n"
      "v_exp_f32 v14, v14\n"
      "v_exp_f32 v15, v15\n"
      "v_exp_f32 v16, v16\n"
      "s_waitcnt lgkmcnt(0)\n"
      "v_exp_f32 v17, v17\n"
      "v_smfmac_f32_32x32x32_bf16 v[64:79], v[122:125], v[8:15], v106\n"
      "v_exp_f32 v18, v18\n"
      "v_exp_f32 v19, v19\n"
      "v_exp_f32 v20, v20\n"
      "v_exp_f32 v21, v21\n"
      "v_mfma_f32_32x32x16_bf16 v[32:47], v[114:117], v[96:99], v[32:47]\n"
      "v_exp_f32 v22, v22\n"
      "v_exp_f32 v23, v23\n"
      "v_exp_f32 v24, v24\n"
      "v_exp_f32 v25, v25\n"
      "v_smfmac_f32_32x32x32_bf16 v[80:95], v[118:121], v[16:23], v106\n"
      "v_exp_f32 v26, v26\n"
      "ds_read_b128 v[118:121], v108 offset:3648\n"
      "v_exp_f32 v27, v27\n"
      "v_exp_f32 v28, v28\n"
      "v_exp_f32 v29, v29\n"
      "v_mfma_f32_32x32x16_bf16 v[48:63], v[114:117], v[100:103], v[48:63]\n"
      "v_exp_f32 v30, v30\n"
      "v_exp_f32 v31, v31\n"
      "s_cbranch_vccz 5f\n"
      "s_setprio 0\n"
      "5:\n"
      "v_readfirstlane_b32 %[tmp], v131\n"
      "s_add_u32 %[off], %[off], 19968\n"
      "s_cmp_eq_u32 %[off], 79872\n"
      "s_cselect_b32 %[off], 0, %[off]\n"
      "s_cmp_lt_i32 %[tmp], 8\n"
      "s_cbranch_scc1 3f\n"
      "2:\n"
      "v_add_u32 v109, %[off], v128\n"
      "ds_read2st64_b64 v[110:113], v109 offset0:0 offset1:1\n"
      "v_add_u32 v107, %[off], v126\n"
      "v_exp_f32 v32, v32\n"
      "v_exp_f32 v33, v33\n"
      "v_smfmac_f32_32x32x32_bf16 v[80:95], v[122:125], v[24:31], v106\n"
      "ds_read_b128 v[114:117], v107 offset:0\n"
      "ds_read_b128 v[122:125], v108 offset:4256\n"
      "v_exp_f32 v34, v34\n"
      "v_exp_f32 v35, v35\n"
      "v_exp_f32 v36, v36\n"
      "s_waitcnt lgkmcnt(2)\n"
      "v_exp_f32 v37, v37\n"
      "v_mfma_f32_32x32x4_2b_bf16 v[0:31], v[110:111], v[104:105], 0 cbsz:1 abid:0\n"
      "v_exp_f32 v38, v38\n"
      "v_exp_f32 v39, v39\n"
      "v_exp_f32 v40, v40\n"
      "v_exp_f32 v41, v41\n"
      "v_exp_f32 v42, v42\n"
      "v_exp_f32 v43, v43\n"
      "v_exp_f32 v44, v44\n"
      "v_exp_f32 v45, v45\n"
      "v_smfmac_f32_32x32x32_bf16 v[64:79], v[118:121], v[32:39], v106\n"
      "v_exp_f32 v46, v46\n"
      "v_exp_f32 v47, v47\n"
      "v_exp_f32 v48, v48\n"
      "s_waitcnt lgkmcnt(0)\n"
      "s_mov_b64 exec, 1\n"
      "ds_add_u32 v129, v130 offset:4\n"
      "s_mov_b64 exec, -1\n"
      "v_exp_f32 v49, v49\n"
      "v_smfmac_f32_32x32x32_bf16 v[64:79], v[122:125], v[40:47], v106\n"
      "v_exp_f32 v50, v50\n"
      "v_exp_f32 v51, v51\n"
      "v_exp_f32 v52, v52\n"
      "v_exp_f32 v53, v53\n"
      "v_mfma_f32_32x32x16_bf16 v[0:15], v[114:117], v[96:99], v[0:15]\n"
      "v_exp_f32 v54, v54\n"
      "v_exp_f32 v55, v55\n"
      "v_exp_f32 v56, v56\n"
      "v_exp_f32 v57, v57\n"
      "v_smfmac_f32_32x32x32_bf16 v[80:95], v[118:121], v[48:55], v106\n"
      "v_add_u32 v108, %[off], v127\n"
      "v_add_u32 v129, 4, v129\n"
      "v_exp_f32 v58, v58\n"
      "ds_read_b128 v[118:121], v108 offset:0\n"
      "v_exp_f32 v59, v59\n"
      "v_exp_f32 v60, v60\n"
      "v_exp_f32 v61, v61\n"
      "v_mfma_f32_32x32x16_bf16 v[16:31], v[114:117], v[100:103], v[16:31]\n"
      "v_exp_f32 v62, v62\n"
      "v_exp_f32 v63, v63\n"
      "s_sub_u32 %[cnt], %[cnt], 1\n"
      "s_cmp_lg_u32 %[cnt], 0\n"
      "s_cbranch_scc1 1b\n"
      "s_branch 6f\n"
      "3:\n"
      "s_sleep 1\n"
      "ds_read_b32 v131, v129 offset:72\n"
      "s_waitcnt lgkmcnt(0)\n"
      "v_readfirstlane_b32 %[tmp], v131\n"
      "s_nop 0\n"
      "s_cmp_lt_i32 %[tmp], 8\n"
      "s_cbranch_scc1 3b\n"
      "s_branch 2b\n"
      "7:\n"
      "s_sleep 1\n"
      "ds_read_b32 v131, v129\n"
      "s_waitcnt lgkmcnt(0)\n"
      "v_readfirstlane_b32 %[tmp], v131\n"
      "s_nop 0\n"
      "s_cmp_lt_u32 %[tmp], 8\n"
      "s_cbranch_scc1 7b\n"
      "s_branch 9b\n"
      "6:\n"
      "v_smfmac_f32_32x32x32_bf16 v[80:95], v[122:125], v[56:63], v106\n"
      "s_setprio 0\n"
      "s_waitcnt lgkmcnt(0)\n"
      "s_nop 15\n"
      "s_nop 7\n"
      : "=&{v[64:79]}"(o0), "=&{v[80:95]}"(o1), "+{v129}"(ca), [cnt] "+s"(cnt), [off] "+s"(off), [tmp] "=&s"(tmp),
        [t2] "=&s"(t2), [so] "+s"(so), [dst] "+s"(dst)
      : "{v[96:99]}"(q0u), "{v[100:103]}"(q1u), "{v[104:105]}"(qrem_u), "{v106}"(SPIDX),
        "{v126}"(sbase + kh * KV_GROUP + lane * 16), "{v127}"(sbase + kh * KV_GROUP + voff),
        "{v128}"(sbase + kh * KV_GROUP + 8960 + lane * 8), [first] "s"(1 - kh), [slo] "s"((unsigned)ksrc), [shi] "s"((unsigned)(ksrc >> 32))
      : "v0", "v1", "v2", "v3", "v4", "v5", "v6", "v7", "v8", "v9", "v10", "v11", "v12", "v13", "v14", "v15",
        "v16", "v17", "v18", "v19", "v20", "v21", "v22", "v23", "v24", "v25", "v26", "v27", "v28", "v29", "v30", "v31",
        "v32", "v33", "v34", "v35", "v36", "v37", "v38", "v39", "v40", "v41", "v42", "v43", "v44", "v45", "v46", "v47",
        "v48", "v49", "v50", "v51", "v52", "v53", "v54", "v55", "v56", "v57", "v58", "v59", "v60", "v61", "v62", "v63",
        "v107", "v108", "v109", "v110", "v111", "v112", "v113", "v114", "v115", "v116", "v117", "v118", "v119", "v120", "v121", "v122",
        "v123", "v124", "v125", "v130", "v131", "s96", "s97", "vcc", "scc", "memory");
    }
    if (!__all(nq * *(LAS float*)(mi + M_KN) < 47.f * 47.f)) {
      m0 = m1 = -INFINITY;
      o0 = o1 = (f32x16){0};
      const bf16x8 q0 = __builtin_bit_cast(bf16x8, q0u), q1 = __builtin_bit_cast(bf16x8, q1u);
      const s16x4 qrem = __builtin_bit_cast(s16x4, (u32x2){qr, 0u});
#pragma unroll 1
      for (int t = 0; t < 32; ++t) {
        const int g = WS_KV + (t >> 2) * RSLOT + kh * KV_GROUP;
        const u32x2 kr = __builtin_bit_cast(u32x2, __builtin_amdgcn_raw_buffer_load_b64(rs, g + 8960 + (t & 3) * 256 + l31 * 8, 0, 16));
        const bf16x8 km = __builtin_bit_cast(bf16x8, LD_SC1_16(g + (t & 3) * 1024 + lane * 16));
        const bf16x8 va = __builtin_bit_cast(bf16x8, LD_SC1_16(g + voff + (t & 3) * 1216));
        const bf16x8 vb = __builtin_bit_cast(bf16x8, LD_SC1_16(g + voff + (t & 3) * 1216 + 608));
        const f32x32 z = {0};
        const f32x32 r = __builtin_amdgcn_mfma_f32_32x32x4bf16_1k(__builtin_bit_cast(s16x4, kr), qrem, z, 0, 0, 0);
        online_step(MFMA32(km, q0, lo16(r)), m0, o0, va, vb);
        online_step(MFMA32(km, q1, hi16(r)), m1, o1, va, vb);
      }
    }
    HSTAMP(9);
    lds_wait(mi + M_DONE + 20, 8);
    {
      const f32x16 give = kh ? o0 : o1;
      lds_ptr p = sm + L_EX + w * 3072 + lane * 16;
      *(LAS f32x4*)(p) = (f32x4){give[0], give[1], give[2], give[3]};
      *(LAS f32x4*)(p + 1024) = (f32x4){give[4], give[5], give[6], give[7]};
      *(LAS f32x4*)(p + 2048) = (f32x4){give[8], give[9], give[10], kh ? m0 : m1};
    }
    lds_signal(mi + M_PAIR + 4 * hl);
    lds_wait(mi + M_PAIR + 4 * hl, 2);
    STAMP(10); RSTAMP(3);
    {
      const f32x16 keep = kh ? o1 : o0;
      lds_ptr p = sm + L_EX + (w ^ 4) * 3072 + lane * 16;
      const f32x4 t0 = *(LAS f32x4*)(p), t1 = *(LAS f32x4*)(p + 1024), t2 = *(LAS f32x4*)(p + 2048);
      const float mk = kh ? m1 : m0, mm = fmaxf(mk, t2[3]);
      const float fa = __builtin_amdgcn_exp2f(mk - mm), fb = __builtin_amdgcn_exp2f(t2[3] - mm);
      float o[11];
#pragma unroll
      for (int i = 0; i < 11; ++i) o[i] = keep[i] * fa + (i < 4 ? t0[i & 3] : (i < 8 ? t1[i & 3] : t2[i & 3])) * fb;
      const float ox = xhalf(o[10]);
      const float inv = __builtin_amdgcn_rcpf(h ? ox : o[10]);
      u32x4 a;
#pragma unroll
      for (int i = 0; i < 4; ++i) a[i] = pk2(o[2 * i] * inv, o[2 * i + 1] * inv);
      const int ob = WS_O + (2 * qt + kh) * 36 * 1024;
      ST_SC1_16(a, ob + (4 * hg + hl) * 1024 + lane * 16);
      if (!h) ST_SC1_4(pk2(o[8] * inv, o[9] * inv), ob + 32768 + (4 * hg + hl) * 128 + l31 * 4);
    }
  }

  const unsigned tag = *(LAS unsigned*)(mi + M_TAG);
  if (w < 8) {
    VM_WAIT(0);
    if (lds_ticket(mi + M_OD) == 7u && lane == 0)
      __hip_atomic_store((gu32*)(gws + WS_FO) + (qt * 8 + hg) * 32, tag, RLX_AGENT);
  }
  bf16x8 wf[18];
  u32x4 bt = {0, 0, 0, 0};
  float gq = 0.f;
  lds_wait(mi + M_WO, 4);
  {
    const int ka = w < 8 ? (w >> 2) : (g4 >> 1), kb = w < 8 ? h : (g4 & 1);
    const int cb = w < 8 ? (32 * ((w >> 1) & 1) + 16 * (g4 & 1) + 4 * tp) * 2 : (64 + 4 * (tp & 1)) * 2;
    lds_ptr wm = sm + L_B + (DH * ka + 4 * kb + tq) * 144 + cb;
    lds_ptr wr = sm + L_B + (DH * (8 * ka + 4 * kb + (tq >> 1)) + 16 + (tq & 1)) * 144 + cb;
#pragma unroll
    for (int k = 0; k < 12; ++k) wf[k] = tr_frag(wm + k * 5184, wm + k * 5184 + 1152);
    wm += 12 * 5184;
#pragma unroll
    for (int k = 0; k < 4; ++k) wf[12 + k] = tr_frag(wm + k * 5184, wm + k * 5184 + 1152);
#pragma unroll
    for (int k = 0; k < 2; ++k) wf[16 + k] = tr_frag(wr + k * 41472, wr + k * 41472 + 5184);
  }
  if (w < 9) {
    lds_wait(mi + M_BT, 4);
    bt = *(LAS u32x4*)(sm + L_BT + tid * 16);
    gq = *(LAS float*)(mi + M_G + otok * 4);
    lds_signal(mi + M_EXD);
  }
  if (w == 11) {
    lds_wait<1>(mi + M_OD, 8);
#pragma unroll 1
    for (unsigned spins = 0; spins < SPIN_CAP; ++spins) {
      if (__all(lane >= 8 || __hip_atomic_load((gu32*)(gws + WS_FO) + (qt * 8 + lane) * 32, RLX_AGENT) == tag)) break;
      __builtin_amdgcn_s_sleep(1);
    }
    lds_signal(mi + M_OR);
  } else {
    lds_wait<1>(mi + M_OR, 1);
  }
  if (w < 8) HSTAMP(5);
  {
    u32x4 of[6];
#pragma unroll
    for (int i = 0; i < 6; ++i) of[i] = LD_SC1_16(WS_O + 2 * qt * 36 * 1024 + (12 * i + w) * 1024 + lane * 16);
    lds_wait(mi + M_EXD, 9);
#pragma unroll
    for (int i = 0; i < 6; ++i) {
      if ((i == 2 || i == 5) && w >= 8) {
        lds_ptr p = sm + (12 * i + w) * 1024 + (4 * (lane & 7) + 32 * h) * 16 + ((lane >> 3) & 3) * 4;
#pragma unroll
        for (int e = 0; e < 4; ++e) *(LAS unsigned*)(p + e * 16) = of[i][e];
      } else {
        *(LAS u32x4*)(sm + (12 * i + w) * 1024 + lane * 16) = of[i];
      }
    }
  }
  BAR();
  if (w < 8) HSTAMP(6);
  if (w < 8) {
    lds_ptr ob = sm + ((w & 1) * 36 + (w >> 2)) * 1024 + lane * 16;
    f32x16 acc = {0};
#pragma unroll
    for (int k = 0; k < 18; ++k) acc = MFMA32(lds_frag(ob + k * 2048), wf[k], acc);
    lds_ptr ap = sm + L_B + (((w >> 2) * 64 + 32 * (w & 1) + 4 * h) * 72 + 32 * ((w >> 1) & 1) + l31) * 4;
#pragma unroll
    for (int r = 0; r < 16; ++r) *(LAS float*)(ap + ((r & 3) + 8 * (r >> 2)) * 288) = acc[r];
  } else {
    const int j = w - 8;
    lds_ptr ob = sm + ((j >> 1) * 36 + (g4 >> 1)) * 1024 + (16 * (j & 1) + l15 + 32 * (g4 & 1)) * 16;
    f32x4 acc = {0};
#pragma unroll
    for (int k = 0; k < 18; ++k) acc = MFMA16(lds_frag(ob + k * 2048), wf[k], acc);
    lds_ptr ap = sm + L_B + (((l15 >> 3) * 64 + 16 * j + 4 * g4) * 72 + 64 + (l15 & 7)) * 4;
#pragma unroll
    for (int r = 0; r < 4; ++r) *(LAS float*)(ap + r * 288) = (l15 < 8) ? acc[r] : 0.f;
  }
  if (w < 8) HSTAMP(7);
  BAR();
  if (w < 8) HSTAMP(8);
  if (tid < 576) {
    lds_ptr ap = sm + L_B + tid * 32;
    const f32x4 a0 = *(LAS f32x4*)(ap) + *(LAS f32x4*)(ap + 64 * 72 * 4);
    const f32x4 a1 = *(LAS f32x4*)(ap + 16) + *(LAS f32x4*)(ap + 64 * 72 * 4 + 16);
    u32x4 o;
#pragma unroll
    for (int i = 0; i < 4; ++i) {
      const float e0 = i < 2 ? a0[2 * i] : a1[2 * i - 4], e1 = i < 2 ? a0[2 * i + 1] : a1[2 * i - 3];
      o[i] = pk2(fmaf(gq, e0, bf_lo(bt[i])), fmaf(gq, e1, bf_hi(bt[i])));
    }
    *(u32x4*)(out + (size_t)(64 * qt + otok) * D_ + 72 * hg + 8 * opc) = o;
  }
  STAMP_FLUSH(w < 4 ? 0 : (w < 8 ? 1 : (w < 11 ? 2 : 3)));
  asm volatile("fk_end:");
}

extern "C" void kernel_launch(void* const* d_in, const int* in_sizes, int n_in, void* d_out, int out_size,
                              void* d_ws, size_t ws_size, hipStream_t stream) {
  fused_kernel<<<256, 768, 0, stream>>>((const __bf16*)d_in[0], (const __bf16*)d_in[1], (const __bf16*)d_in[2],
                                        (const __bf16*)d_in[3], (const __bf16*)d_in[4], (const __bf16*)d_in[6],
                                        (const __bf16*)d_in[7], (const __bf16*)d_in[8], (const __bf16*)d_in[9],
                                        (const __bf16*)d_in[10], (const __bf16*)d_in[11], (__bf16*)d_out, (char*)d_ws);
}
```

```cpp
#include <hip/hip_runtime.h>
#include <hip/hip_bf16.h>
#include <stdint.h>
#define NO_TOUCH

typedef __attribute__((ext_vector_type(2))) __bf16 bf16x2;
typedef __attribute__((ext_vector_type(4))) __bf16 bf16x4;
typedef __attribute__((ext_vector_type(8))) __bf16 bf16x8;
typedef __attribute__((ext_vector_type(16))) __bf16 bf16x16;
typedef __attribute__((ext_vector_type(4))) short s16x4;
typedef __attribute__((ext_vector_type(2))) float f32x2;
typedef __attribute__((ext_vector_type(4))) float f32x4;
typedef __attribute__((ext_vector_type(16))) float f32x16;
typedef __attribute__((ext_vector_type(32))) float f32x32;
typedef __attribute__((ext_vector_type(2))) unsigned u32x2;
typedef __attribute__((ext_vector_type(4))) unsigned u32x4;
#define LAS __attribute__((address_space(3)))
#define GAS __attribute__((address_space(1)))
typedef LAS char* lds_ptr;
typedef GAS unsigned gu32;
typedef GAS unsigned long long gu64;
#define DEVI __device__ __forceinline__
#define RLX_AGENT __ATOMIC_RELAXED, __HIP_MEMORY_SCOPE_AGENT
#define RLX_WG __ATOMIC_RELAXED, __HIP_MEMORY_SCOPE_WORKGROUP

constexpr int S_ = 2048, D_ = 576, DH = 18;

constexpr int KV_GROUP = 9984;
constexpr int WS_KV = 0;
constexpr int WS_EPOCH = 192 * 1024;
constexpr int WS_FKV = WS_EPOCH + 1024;
constexpr int WS_FU = WS_FKV + 8192;
constexpr int WS_FZ = WS_FU + 32768;
constexpr int WS_FO = WS_FZ + 32768;
constexpr int WS_UT = 384 * 1024;
constexpr int WS_Z = WS_UT + D_ * S_ * 2;
constexpr int WS_O = WS_Z + D_ * S_ * 2;
constexpr int WS_ZP = WS_O + D_ * S_ * 2;
constexpr int WS_END = WS_ZP + 64 * 4 * 8192;

constexpr int RSLOT = 2 * KV_GROUP;
constexpr int L_X = 0;
constexpr int L_GW = 78848;
constexpr int L_B = 80000;
constexpr int L_M = 162944;
constexpr int CV_COPY = 4160;
constexpr int CV_H = L_B, CV_HB = 8 * CV_COPY, CV_U = L_B + 2 * CV_HB, CV_UB = 7680;
constexpr int L_Q = L_B + 32768;
constexpr int M_DONE = 0, M_C1 = 32, M_C2 = 36, M_C3 = 40, M_C3B = 44, M_KVC = 48, M_KVR = 52, M_UR = 56, M_OR = 60, M_RDY = 64,
              M_QD = 192, M_UD = 104, M_HS = 108, M_ZR = 112, M_BT = 116, M_WO = 120, M_OD = 124, M_PAIR = 128, M_TAGR = 144,
              M_TAG = 148, M_KN = 152, M_EXD = 156, M_KVP = 160, M_KN0 = 164, M_KD = 168, M_QR = 172,
              M_N2 = 176, M_G = 256;
constexpr int L_EX = 0, L_BT = 28672;
#define CV_PRIO 3
#define CV_SLEEP 0
#define POLL_AT M_C2
#define POLL_N 3
#define BUBBLE 0
#define POLL_SLEEP 4
#define BT_AT 28
constexpr unsigned SPIN_CAP = 1u << 18;

#define STAMP_DECL do {} while (0)
#define STAMP(I) do {} while (0)
#define STAMP_FLUSH(KID) do {} while (0)
#define RSTAMP(I) do {} while (0)

#define PSTAMP(I) do {} while (0)
#define NSTAMP(I) STAMP(I)
#define CSTAMP(I) do {} while (0)
#define HSTAMP(I) NSTAMP(I)

DEVI unsigned pk2(float a, float b) {
  bf16x2 v = {(__bf16)a, (__bf16)b};
  return __builtin_bit_cast(unsigned, v);
}
DEVI float u2f(unsigned u) { return __builtin_bit_cast(float, u); }
DEVI float bf_lo(unsigned u) { return __builtin_bit_cast(float, u << 16); }
DEVI float bf_hi(unsigned u) { return __builtin_bit_cast(float, u & 0xffff0000u); }
DEVI float dot2(unsigned u, float c) { const bf16x2 v = __builtin_bit_cast(bf16x2, u); return __builtin_amdgcn_fdot2_f32_bf16(v, v, c, false); }
DEVI float rbf(float a) { return (float)(__bf16)a; }
DEVI int swap23(int r) { return (r & ~12) | ((r & 4) << 1) | ((r & 8) >> 1); }
DEVI float xhalf(float v) {
  const unsigned u = __builtin_bit_cast(unsigned, v);
  const auto r = __builtin_amdgcn_permlane32_swap(u, u, false, false);
  return __builtin_bit_cast(float, (threadIdx.x & 32) ? r[0] : r[1]);
}

template <int CTRL>
DEVI float dpp_max(float v) {
  const int u = __builtin_bit_cast(int, v);
  return fmaxf(v, __builtin_bit_cast(float, __builtin_amdgcn_update_dpp(u, u, CTRL, 0xf, 0xf, false)));
}
DEVI float max16(float v) {
  return dpp_max<0x140>(dpp_max<0x141>(dpp_max<0x4e>(dpp_max<0xb1>(v))));
}
DEVI float wave_max(float v) {
  v = max16(v);
  const unsigned u = __builtin_bit_cast(unsigned, v);
  return fmaxf(fmaxf(u2f(__builtin_amdgcn_readlane(u, 0)), u2f(__builtin_amdgcn_readlane(u, 16))),
               fmaxf(u2f(__builtin_amdgcn_readlane(u, 32)), u2f(__builtin_amdgcn_readlane(u, 48))));
}
DEVI void glds(const void* sbase, unsigned voff, unsigned lds_dst) {
  asm volatile("s_mov_b32 m0, %2\n\ts_nop 0\n\tglobal_load_lds_dwordx4 %0, %1" :: "v"(voff), "s"(sbase), "s"(lds_dst) : "memory");
}
DEVI void glds_sc1(const void* sbase, unsigned voff, unsigned lds_dst) {
  asm volatile("s_mov_b32 m0, %2\n\ts_nop 0\n\tglobal_load_lds_dwordx4 %0, %1 sc1" :: "v"(voff), "s"(sbase), "s"(lds_dst) : "memory");
}
#define GLDS4(SC, sbase_, v0_, v3_, dst_)                                                                                  \
  asm volatile("s_mov_b32 m0, %3\n\ts_nop 0\n\tglobal_load_lds_dwordx4 %0, %2" SC "\n\tglobal_load_lds_dwordx4 %0, %2 offset:1024" SC   \
               "\n\tglobal_load_lds_dwordx4 %0, %2 offset:2048" SC "\n\tglobal_load_lds_dwordx4 %1, %2 offset:3072" SC           \
               :: "v"(v0_), "v"(v3_), "s"(sbase_), "s"(dst_) : "memory")
#define VM_WAIT(N) asm volatile("s_waitcnt vmcnt(" #N ")" ::: "memory")
#define BAR() asm volatile("s_waitcnt lgkmcnt(0)\n\ts_barrier" ::: "memory")

DEVI bf16x8 tr_frag(lds_ptr p0, lds_ptr p1) {
  bf16x4 a = __builtin_amdgcn_ds_read_tr16_b64_v4bf16((LAS bf16x4*)p0);
  bf16x4 b = __builtin_amdgcn_ds_read_tr16_b64_v4bf16((LAS bf16x4*)p1);
  return __builtin_shufflevector(a, b, 0, 1, 2, 3, 4, 5, 6, 7);
}
DEVI bf16x8 lds_frag(lds_ptr p) { return *(LAS bf16x8*)p; }
DEVI void lds_signal(lds_ptr p, unsigned n = 1u) {
  asm volatile("s_waitcnt lgkmcnt(0)\n\ts_mov_b64 exec, 1\n\tds_add_u32 %0, %1\n\ts_mov_b64 exec, -1" :: "v"(p), "v"(n) : "memory");
}
DEVI unsigned lds_ticket(lds_ptr p) {
  unsigned old;
  asm volatile("s_waitcnt lgkmcnt(0)\n\ts_mov_b64 exec, 1\n\tds_add_rtn_u32 %0, %1, %2\n\ts_mov_b64 exec, -1\n\ts_waitcnt lgkmcnt(0)"
               : "=&v"(old) : "v"(p), "v"(1u) : "memory");
  return __builtin_amdgcn_readfirstlane(old);
}
template <int SLEEP = 0>
DEVI void lds_wait(lds_ptr p, unsigned n) {
  unsigned v, t;
  asm volatile("1:\n\tds_read_b32 %0, %2\n\ts_waitcnt lgkmcnt(0)\n\tv_readfirstlane_b32 %1, %0\n\ts_nop 0\n\ts_cmp_ge_u32 %1, %3\n\t"
               "s_cbranch_scc1 2f\n\ts_sleep %4\n\ts_branch 1b\n2:"
               : "=&v"(v), "=&s"(t) : "v"(p), "s"(n), "i"(SLEEP) : "scc", "memory");
}
DEVI unsigned lds_peek(lds_ptr p) {
  unsigned v;
  asm volatile("ds_read_b32 %0, %1\n\ts_waitcnt lgkmcnt(0)" : "=&v"(v) : "v"(p) : "memory");
  return __builtin_amdgcn_readfirstlane(v);
}
#define LD_SC1_16(off) __builtin_bit_cast(u32x4, __builtin_amdgcn_raw_buffer_load_b128(rs, (off), 0, 16))
#define ST_SC1_16(v, off) __builtin_amdgcn_raw_buffer_store_b128((v), rs, (off), 0, 16)
#define ST_SC1_8(v, off) __builtin_amdgcn_raw_buffer_store_b64((v), rs, (off), 0, 16)
#define ST_SC1_4(v, off) __builtin_amdgcn_raw_buffer_store_b32((v), rs, (off), 0, 16)

DEVI f32x16 lo16(f32x32 v) { return __builtin_shufflevector(v, v, 0, 1, 2, 3, 4, 5, 6, 7, 8, 9, 10, 11, 12, 13, 14, 15); }
DEVI f32x16 hi16(f32x32 v) { return __builtin_shufflevector(v, v, 16, 17, 18, 19, 20, 21, 22, 23, 24, 25, 26, 27, 28, 29, 30, 31); }
DEVI bf16x16 raw_lo(f32x16 v) { return __builtin_bit_cast(bf16x16, __builtin_shufflevector(v, v, 0, 1, 2, 3, 4, 5, 6, 7)); }
DEVI bf16x16 raw_hi(f32x16 v) { return __builtin_bit_cast(bf16x16, __builtin_shufflevector(v, v, 8, 9, 10, 11, 12, 13, 14, 15)); }
constexpr int SPIDX = (int)0xddddddddu;
#define MFMA32(a, b, c) __builtin_amdgcn_mfma_f32_32x32x16_bf16((a), (b), (c), 0, 0, 0)
#define MFMA16(a, b, c) __builtin_amdgcn_mfma_f32_16x16x32_bf16((a), (b), (c), 0, 0, 0)

DEVI void online_step(f32x16 s, float& m, f32x16& o, bf16x8 va, bf16x8 vb) {
  float t = s[0];
#pragma unroll
  for (int i = 1; i < 16; ++i) t = fmaxf(t, s[i]);
  t = fmaxf(t, xhalf(t));
  const float mn = fmaxf(m, t);
  const float al = __builtin_amdgcn_exp2f(m - mn);
  m = mn;
#pragma unroll
  for (int i = 0; i < 16; ++i) { o[i] *= al; s[i] = __builtin_amdgcn_exp2f(s[i] - mn); }
  o = __builtin_amdgcn_smfmac_f32_32x32x32_bf16(va, raw_lo(s), o, SPIDX, 0, 0);
  o = __builtin_amdgcn_smfmac_f32_32x32x32_bf16(vb, raw_hi(s), o, SPIDX, 0, 0);
}

#define UNIT_LOAD(CP, TG)                                                                                        \
  const int uc0 = 72 * hg + 2 * (CP), ut0 = 64 * qt + 8 * (TG);                                                  \
  const u32x4 usw = *(const u32x4*)(shk + uc0 * 4);                                                              \
  unsigned ukr[11], uvr[8];                                                                                      \
  {                                                                                                              \
    const unsigned bo = (ut0 * D_ + uc0) * 2, bo3 = ut0 ? bo - 3 * 1152 : bo;                                    \
    _Pragma("unroll") for (int i = 0; i < 3; ++i) ukr[i] = *(const unsigned*)((const char*)bk + bo3 + i * 1152); \
    _Pragma("unroll") for (int i = 0; i < 8; ++i) {                                                              \
      ukr[3 + i] = *(const unsigned*)((const char*)bk + bo + i * 1152);                                          \
      uvr[i] = *(const unsigned*)((const char*)bv + bo + i * 1152);                                              \
    }                                                                                                            \
  }
#define UNIT_DO()                                                                                                \
  u32x4 o0, o1;                                                                                                  \
  {                                                                                                              \
    const f32x2 w3 = {bf_hi(usw[1]), bf_hi(usw[3])}, w2 = {bf_lo(usw[1]), bf_lo(usw[3])};                        \
    const f32x2 w1 = {bf_hi(usw[0]), bf_hi(usw[2])}, w0 = {bf_lo(usw[0]), bf_lo(usw[2])};                        \
    f32x2 kf[11], r[8];                                                                                          \
    _Pragma("unroll") for (int i = 0; i < 11; ++i) {                                                             \
      const unsigned kk = (i < 3 && !ut0) ? 0u : ukr[i];                                                         \
      kf[i] = (f32x2){bf_lo(kk), bf_hi(kk)};                                                                     \
    }                                                                                                            \
    _Pragma("unroll") for (int i = 0; i < 8; ++i)                                                                \
      r[i] = __builtin_elementwise_fma(kf[i + 3], w0, __builtin_elementwise_fma(kf[i + 2], w1,                   \
             __builtin_elementwise_fma(kf[i + 1], w2, kf[i] * w3))) * (f32x2){bf_lo(uvr[i]), bf_hi(uvr[i])};     \
    _Pragma("unroll") for (int i = 0; i < 4; ++i) {                                                              \
      o0[i] = pk2(r[7 - 2 * i][0], r[6 - 2 * i][0]);                                                             \
      o1[i] = pk2(r[7 - 2 * i][1], r[6 - 2 * i][1]);                                                             \
    }                                                                                                            \
  }

__global__ void __launch_bounds__(768, 3)
fused_kernel(const __bf16* __restrict__ x, const __bf16* __restrict__ wq, const __bf16* __restrict__ wk,
             const __bf16* __restrict__ wv, const __bf16* __restrict__ wo, const __bf16* __restrict__ bk,
             const __bf16* __restrict__ bv, const __bf16* __restrict__ shk, const __bf16* __restrict__ ssmk,
             const __bf16* __restrict__ ssmd, const __bf16* __restrict__ gw, __bf16* __restrict__ out, char* ws) {
  __shared__ __attribute__((aligned(16))) char smem[160 * 1024];
  asm volatile("fk_beg:");
  unsigned long long xr = __builtin_amdgcn_s_memrealtime();
  STAMP_DECL; STAMP(0);
  const int tid = threadIdx.x, lane = tid & 63;
  const int w = __builtin_amdgcn_readfirstlane(tid >> 6);
  const int bid = blockIdx.x, jj = bid >> 3;
  const int qt = (bid & 7) * 4 + (jj & 3), hg = jj >> 2;
  const unsigned sbase = (unsigned)(size_t)smem;
  lds_ptr sm = (lds_ptr)smem;
  lds_ptr mi = sm + L_M;
  const int h = lane >> 5, l31 = lane & 31, l15 = lane & 15, g4 = lane >> 4;
  const int tq = (lane & 15) >> 2, tp = lane & 3;
  const auto rs = __builtin_amdgcn_make_buffer_rsrc(ws, 0, WS_END, 0x00020000);
  GAS char* gws = (GAS char*)ws;
  const int rot = (hg + qt) % 3;
  const int s0 = bid & 7;
  const int pt = (hg >> 1) & 1, sel = hg & 1;
  const char* xt = (const char*)x + (size_t)qt * 73728;
  const char* wqs = (const char*)wq + 144 * hg;
  const unsigned vxe = (lane >> 3) * 1152 + (((lane & 7) ^ g4) << 4), vxo = 9216 + (lane >> 3) * 1152 + (((lane & 7) ^ (4 + g4)) << 4);
  const int otok = tid / 9, opc = tid - 9 * otok;

  if (w >= 8) {
    const int j = w - 8, ht = tid - 512;
    __builtin_amdgcn_s_setprio(3);
    if (j < 3) {
      const int th = (j + rot) % 3;
      const unsigned stg = L_B + 2 * 27648 + j * 9216;
#pragma unroll
      for (int c = 0; c < 6; ++c) {
        const char* src = xt + (2 * pt + (c & 1)) * 18432 + (3 * th + (c >> 1)) * 128;
        const unsigned dst = sbase + L_X + (3 * j + (c >> 1)) * 8192 + (2 * pt + (c & 1)) * 2048;
        glds(src, vxe, dst);
        glds(src, vxo, dst + 1024);
      }
      if (hg < 4) {
        const char* wsrc = (const char*)(sel ? wv : wk) + th * 6912;
#pragma unroll
        for (int i = 0; i < 3; ++i) {
          const int f = 64 * i + lane, row = f / 3, pc = f - 3 * row;
          const unsigned vo = row * 36 + (pc == 2 ? 20 : 16 * pc);
#pragma unroll
          for (int p = 0; p < 3; ++p) glds(wsrc + p * 2304, vo, sbase + stg + p * 3072 + i * 1024);
        }
      }
      BAR();
      VM_WAIT(0);
      STAMP(1);
      lds_signal(mi + M_KD);
      lds_signal(mi + M_C1);
      if (hg < 4) lds_wait<1>(mi + M_KVC, 4);
      STAMP(2);
      {
        const char* src = wqs + (size_t)(192 * ((2 + rot) % 3) + 64 * j) * 1152;
        int pc = lane % 9;
        unsigned vo = (lane / 9) * 1152 + pc * 16;
#pragma unroll
        for (int i = 0; i < 9; ++i) {
          glds(src, vo, sbase + stg + i * 1024);
          vo += pc == 8 ? 9088 : 8080;
          pc = pc == 8 ? 0 : pc + 1;
        }
      }
      VM_WAIT(0);
      lds_signal(mi + M_C3);
      lds_wait<1>(mi + M_KVR, 1);
    } else {
      const unsigned epoch = __hip_atomic_load((gu32*)(gws + WS_EPOCH), RLX_AGENT);
      unsigned pf = 0;
      {
        unsigned long long pc0;
        unsigned back, len;
        asm volatile("s_getpc_b64 %0\n\tfk_pc:\n\ts_mov_b32 %1, fk_pc-fk_beg\n\ts_mov_b32 %2, fk_end-fk_beg" : "=s"(pc0), "=s"(back), "=s"(len));
        const unsigned o = min((unsigned)(lane * 8 + (jj & 7)) * 128u, len);
        asm volatile("global_load_dword %0, %1, %2" : "=v"(pf) : "v"(o), "s"(pc0 - back) : "memory");
      }
      BAR();
      asm volatile("s_waitcnt vmcnt(0)" : "+v"(pf) :: "memory");
      const unsigned tg = __builtin_amdgcn_readfirstlane(epoch) + 1u;
      if (lane == 0) *(LAS unsigned*)(mi + M_TAG) = tg;
      lds_signal(mi + M_TAGR);
      lds_wait<1>(mi + POLL_AT, POLL_N);
      PSTAMP(5);
      float kn = 0.f;
#pragma unroll 1
      for (unsigned spins = 0; spins < SPIN_CAP; ++spins) {
        u32x4 v0 = {0, tg, 0, tg};
        if (lane < 8) v0 = LD_SC1_16(WS_FKV + (8 * s0 + lane) * 128);
        const bool ok = v0[1] == tg && v0[3] == tg;
        kn = u2f(v0[0]);
        asm volatile("" ::: "memory");
        if (spins == 0) PSTAMP(6);
        if (spins == 1) PSTAMP(7);
        if (spins == 2) PSTAMP(8);
        if (spins == 3) PSTAMP(9);
        if (__all(ok)) break;
        __builtin_amdgcn_s_sleep(POLL_SLEEP);
      }
      lds_signal(mi + M_KVR);
      STAMP(2); RSTAMP(2);
      kn = max16(kn);
      if (lane == 0) *(LAS float*)(mi + M_KN0) = kn;
    }
    const unsigned tag = *(LAS unsigned*)(mi + M_TAG);
    const int kvo = j * 1024 + lane * 16;
    const bool kvlast = kvo + 4 * 4096 < RSLOT;
    const int kvo4 = kvlast ? kvo + 16384 : kvo;
    auto kv_dma = [&](int st) {
      const char* src = ws + WS_KV + ((s0 + st) & 7) * RSLOT + j * 4992;
      const unsigned dst = sbase + st * RSLOT + j * 4992;
      GLDS4(" sc1", src, lane * 16, lane * 16, dst);
      if (lane < 56) glds_sc1(src + 4096, lane * 16, dst + 4096);
    };
    const unsigned vh3 = (lane == 63 ? 62 : lane) * 16;
    auto hload = [&](int cl, int b) {
      const char* hsrc = (const char*)(ssmk + (size_t)(72 * hg + cl) * S_) + 4 * j + 2;
      const unsigned dst = sbase + CV_H + b * CV_HB + 2 * j * CV_COPY + 64;
      GLDS4("", hsrc, lane * 16, vh3, dst);
      GLDS4("", hsrc + 2, lane * 16, vh3, dst + CV_COPY);
    };
    auto uload = [&](int cl) {
      const int tgq = ht & 7;
      const int o = cl < 64 ? (tgq >> 1) * 2048 + (cl & 1) * 1024 + ((tgq & 1) * 32 + (cl >> 1)) * 16
                            : 8192 + (cl & 1) * 512 + (tgq * 4 + ((cl - 64) >> 1)) * 16;
      return LD_SC1_16(WS_UT + ((ht >> 3) * 8 + hg) * 9216 + o);
    };
    const int ce = 64 + (qt >> 2);
    const unsigned short h8a = *(const unsigned short*)(ssmk + (size_t)(72 * hg + 2 * qt + (j >> 1)) * S_ + (lane & 7));
    const unsigned short h8e = *(const unsigned short*)(ssmk + (size_t)(72 * hg + ce) * S_ + (lane & 7));
    const float dva = (float)ssmd[72 * hg + 2 * qt + (j >> 1)], dve = (qt & 3) ? 0.f : (float)ssmd[72 * hg + ce];
    {
      u32x4 ra[5];
#pragma unroll
      for (int i = 0; i < 5; ++i) ra[i] = LD_SC1_16(WS_KV + s0 * RSLOT + (i < 4 ? kvo + i * 4096 : kvo4));
      UNIT_LOAD(l31, 2 * j + h);
      lds_wait(mi + M_C3B, 8);
#pragma unroll
      for (int i = 0; i < 5; ++i) if (i < 4 || kvlast) *(LAS u32x4*)(sm + kvo + i * 4096) = ra[i];
      lds_signal(mi + M_QD);
      STAMP(3);
      if (j == 3) {
        float kn = 0.f;
#pragma unroll 1
        for (unsigned spins = 0; spins < SPIN_CAP; ++spins) {
          const u32x4 v0 = LD_SC1_16(WS_FKV + lane * 128);
          const bool ok = v0[1] == tag && v0[3] == tag;
          kn = u2f(v0[0]);
          asm volatile("" ::: "memory");
          if (__all(ok)) break;
          __builtin_amdgcn_s_sleep(POLL_SLEEP);
        }
        lds_signal(mi + M_KVR);
        kv_dma(1);
        kn = wave_max(kn);
        if (lane == 0) *(LAS float*)(mi + M_KN) = kn;
      }
      UNIT_DO();
      ST_SC1_16(o0, WS_UT + (qt * 8 + hg) * 9216 + j * 2048 + lane * 16);
      ST_SC1_16(o1, WS_UT + (qt * 8 + hg) * 9216 + j * 2048 + 1024 + lane * 16);
      if (j < 3) { lds_wait(mi + M_KVR, 2); kv_dma(1); }
      STAMP(4);
      VM_WAIT(0);
      lds_signal(mi + M_RDY + 4, 2);
      lds_wait<1>(mi + M_QR, 8);
      if (lds_ticket(mi + M_UD) == 3u && lane == 0) __hip_atomic_store((gu32*)(gws + WS_FU) + (hg * 32 + qt) * 32, tag, RLX_AGENT);
      kv_dma(2);
      kv_dma(3);
    }
    hload(2 * qt, 0);
    hload(2 * qt + 1, 1);
    {
      *(LAS u32x4*)(sm + CV_U + (ht >> 7) * CV_UB + ((ht & 127) >> 2) * 80 + (ht & 3) * 16) = (u32x4){0, 0, 0, 0};
      lds_ptr hz = sm + CV_H + (j >> 1) * CV_HB + (lane >> 3) * CV_COPY;
      *(LAS u32x2*)(hz + (lane & 7) * 8) = (u32x2){0, 0};
      if ((lane & 7) <= (lane >> 3)) *(LAS unsigned short*)(hz + 48 + (7 - (lane >> 3) + (lane & 7)) * 2) = h8a;
      if (j == 0) {
        const float lg = *(LAS float*)(mi + M_G + lane * 4);
        *(LAS float*)(mi + M_G + lane * 4) = rbf(__builtin_amdgcn_rcpf(1.f + __expf(-rbf(lg))));
      }
    }
    VM_WAIT(16);
    lds_signal(mi + M_RDY + 8, 2);
    lds_signal(mi + M_RDY + 12, 2);
    if (j == 3) {
#pragma unroll 1
      for (unsigned spins = 0; spins < SPIN_CAP; ++spins) {
        bool ok = h || __hip_atomic_load((gu32*)(gws + WS_FU) + (hg * 32 + lane) * 32, RLX_AGENT) == tag;
        if (bid == 0) {
#pragma unroll
          for (int i = 0; i < 4; ++i) ok &= __hip_atomic_load((gu32*)(gws + WS_FU) + (64 * i + lane) * 32, RLX_AGENT) == tag;
        }
        if (__all(ok)) break;
        __builtin_amdgcn_s_sleep(4);
      }
      if (bid == 0 && lane == 0) __hip_atomic_store((gu32*)(gws + WS_EPOCH), tag, RLX_AGENT);
      lds_signal(mi + M_UR);
    } else {
      lds_wait<2>(mi + M_UR, 1);
    }
    unsigned hs = 0;
#define HSYNC() do { lds_signal(mi + M_HS); hs += 4; lds_wait<1>(mi + M_HS, hs); } while (0)
    NSTAMP(5);
    CSTAMP(1);
    const u32x4 ue = uload(ce);
    {
      const u32x4 ua = uload(2 * qt), ub = uload(2 * qt + 1);
      *(LAS u32x4*)(sm + CV_U + 2560 + (ht >> 2) * 80 + (ht & 3) * 16) = ua;
      *(LAS u32x4*)(sm + CV_U + CV_UB + 2560 + (ht >> 2) * 80 + (ht & 3) * 16) = ub;
    }
    constexpr int CV_UE = CV_H + CV_HB + 24576;
    lds_ptr px = sm + CV_H + CV_HB + lane * 16;
#pragma unroll 1
    for (int ph = 0; ph < 2; ++ph) {
      const int d0 = ph ? 8 * (qt & 3) + 2 * j : 16 * (j & 1);
      VM_WAIT(0);
      if (ph == 0) CSTAMP(2);
      HSYNC();
      if (ph == 0) CSTAMP(3);
      lds_ptr ub = sm + (ph ? CV_UE : CV_U + (j >> 1) * CV_UB) + 2560;
      lds_ptr ab = sm + CV_H + (ph ? 0 : j >> 1) * CV_HB + (l31 & 7) * CV_COPY + (h + (l31 >> 3)) * 16 + 64 * d0;
      lds_ptr bb = ub + (l31 - d0 - 2) * 80 + (1 - h) * 16;
      f32x16 y0 = {0}, y1 = {0};
      bf16x8 fx[8], fy[8];
#define PACE() do { if (CV_SLEEP) { __builtin_amdgcn_sched_barrier(0); __builtin_amdgcn_s_sleep(CV_SLEEP); __builtin_amdgcn_sched_barrier(0); } } while (0)
#define CV_LD(F, D) _Pragma("unroll") for (int s = 0; s < 2; ++s) {                                     \
        F[4 * s] = lds_frag(ab + 32 * (2 * (D) + 1 - s)); F[4 * s + 1] = lds_frag(bb + 80 * (2 - (D)) + 32 * s);  \
        F[4 * s + 2] = lds_frag(bb + 80 * (34 - (D)) + 32 * s); F[4 * s + 3] = lds_frag(ab + 32 * (2 * (D) + 65 - s)); } \
        __builtin_amdgcn_sched_barrier(0)
#define CV_MM(F) _Pragma("unroll") for (int s = 0; s < 2; ++s) {                                        \
        y0 = MFMA32(F[4 * s], F[4 * s + 1], y0); PACE(); y1 = MFMA32(F[4 * s], F[4 * s + 2], y1); PACE(); y1 = MFMA32(F[4 * s + 3], F[4 * s + 1], y1); PACE(); } \
        __builtin_amdgcn_sched_barrier(0)
      __builtin_amdgcn_s_setprio(CV_PRIO);
      CV_LD(fx, 0);
#pragma unroll 1
      for (int it = ph ? 7 : 0; it < 8; ++it) {
        CV_LD(fy, 1);
        CV_MM(fx);
        CV_LD(fx, 2);
        CV_MM(fy);
        ab += 128;
        bb -= 160;
      }
      __builtin_amdgcn_s_setprio(3);
      if (ph == 0) {
        CSTAMP(4);
        HSYNC();
        hload(ce, 0);
        if (j == 0 && (lane & 7) <= (lane >> 3))
          *(LAS unsigned short*)(sm + CV_H + (lane >> 3) * CV_COPY + 48 + (7 - (lane >> 3) + (lane & 7)) * 2) = h8e;
        if (ht < 160) *(LAS u32x4*)(sm + CV_UE + ht * 16) = (u32x4){0, 0, 0, 0};
        *(LAS u32x4*)(sm + CV_UE + 2560 + (ht >> 2) * 80 + (ht & 3) * 16) = ue;
      }
      {
        const f32x16 give = (j & 1) ? y0 : y1;
#pragma unroll
        for (int g = 0; g < 4; ++g) *(LAS f32x4*)(px + j * 4096 + g * 1024) = (f32x4){give[4 * g], give[4 * g + 1], give[4 * g + 2], give[4 * g + 3]};
        if (j & 1) y0 = y1;
        if (ph && j >= 2) {
#pragma unroll
          for (int g = 0; g < 4; ++g) *(LAS f32x4*)(px + (j + 2) * 4096 + g * 1024) = (f32x4){y0[4 * g], y0[4 * g + 1], y0[4 * g + 2], y0[4 * g + 3]};
        }
      }
      if (ph == 0) CSTAMP(9);
      HSYNC();
      if (ph == 0) CSTAMP(10);
      if (!ph || j < 2) {
        const float dv = ph ? dve : dva;
        u32x4 outv[2];
#pragma unroll
        for (int g = 0; g < 4; ++g) {
          f32x4 s = *(LAS f32x4*)(px + (j ^ 1) * 4096 + g * 1024);
          if (ph) s += *(LAS f32x4*)(px + (3 - j) * 4096 + g * 1024) + *(LAS f32x4*)(px + (j + 4) * 4096 + g * 1024);
          const u32x2 uu = *(LAS u32x2*)(ub + (32 * (j & 1) + l31) * 80 + g * 16 + (1 - h) * 8);
          s = (f32x4){fmaf(bf_hi(uu[1]), dv, s[0] + y0[4 * g]), fmaf(bf_lo(uu[1]), dv, s[1] + y0[4 * g + 1]),
                      fmaf(bf_hi(uu[0]), dv, s[2] + y0[4 * g + 2]), fmaf(bf_lo(uu[0]), dv, s[3] + y0[4 * g + 3])};
          if (ph) ST_SC1_16(__builtin_bit_cast(u32x4, s), WS_ZP + (hg * 32 + qt) * 8192 + j * 4096 + g * 1024 + lane * 16);
          outv[g >> 1][2 * (g & 1)] = pk2(s[0], s[1]);
          outv[g >> 1][2 * (g & 1) + 1] = pk2(s[2], s[3]);
        }
        if (!ph) {
          const int zo = WS_Z + (72 * hg + 2 * qt + (j >> 1)) * 4096 + (j & 1) * 2048 + lane * 16;
          ST_SC1_16(outv[0], zo);
          ST_SC1_16(outv[1], zo + 1024);
        }
      }
      if (ph == 0) CSTAMP(7);
    }
    NSTAMP(6);
    VM_WAIT(0);
    HSYNC();
    if (ht == 192) __hip_atomic_store((gu32*)(gws + WS_FZ) + (hg * 32 + qt) * 32, tag, RLX_AGENT);
    if (lane < 54) {
      const unsigned vo = (lane / 9) * 1152 + (lane % 9) * 16;
#pragma unroll 1
      for (int p = 24 * j; p < 24 * j + 24; p += 4)
        asm volatile("s_mov_b32 m0, %5\n\ts_nop 0\n\tglobal_load_lds_dwordx4 %0, %4\n\tglobal_load_lds_dwordx4 %1, %4 offset:864\n\t"
                     "global_load_lds_dwordx4 %2, %4 offset:1728\n\tglobal_load_lds_dwordx4 %3, %4 offset:2592"
                     :: "v"(vo), "v"(vo + 6048), "v"(vo + 12096), "v"(vo + 18144), "s"((const char*)wo + 144 * hg + p * 6912),
                        "s"(sbase + L_B + p * 864) : "memory");
    }
    VM_WAIT(0);
    lds_signal(mi + M_WO);
    HSTAMP(7);
    if (j == 3) {
#pragma unroll 1
      for (unsigned spins = 0; spins < SPIN_CAP; ++spins) {
        if (__all(h || __hip_atomic_load((gu32*)(gws + WS_FZ) + (hg * 32 + lane) * 32, RLX_AGENT) == tag)) break;
        __builtin_amdgcn_s_sleep(16);
      }
      lds_signal(mi + M_ZR);
    } else {
      lds_wait<8>(mi + M_ZR, 1);
    }
    {
      u32x4 zv[3], xv[3];
#pragma unroll
      for (int i = 0; i < 3; ++i) {
        const int n = (i < 2 || ht < 64) ? ht + 256 * i : ht;
        const int zl = (2 * (qt & 15) + (n & 1) + 32 * ((n >> 1) & 1)) * 16;
        if (i < 2) {
          zv[i] = LD_SC1_16(WS_Z + (72 * hg + (n >> 3)) * 4096 + (qt >> 4) * 2048 + ((n >> 2) & 1) * 1024 + zl);
        } else if (j == 0) {
          f32x4 sa = {0}, sb = {0};
#pragma unroll
          for (int p = 0; p < 4; ++p) {
            const int zo = WS_ZP + (hg * 32 + (lane >> 3) * 4 + p) * 8192 + (qt >> 4) * 4096 + ((n >> 2) & 1) * 2048 + zl;
            sa += __builtin_bit_cast(f32x4, LD_SC1_16(zo));
            sb += __builtin_bit_cast(f32x4, LD_SC1_16(zo + 1024));
          }
          zv[i] = (u32x4){pk2(sa[0], sa[1]), pk2(sa[2], sa[3]), pk2(sb[0], sb[1]), pk2(sb[2], sb[3])};
        } else {
          zv[i] = zv[0];
        }
        xv[i] = *(const u32x4*)(xt + (n / 9) * 1152 + 144 * hg + (n % 9) * 16);
      }
      lds_wait<4>(mi + M_DONE + BT_AT, 8);
#pragma unroll
      for (int i = 0; i < 3; ++i) {
        const int n = (i < 2 || ht < 64) ? ht + 256 * i : ht;
        lds_ptr zp = sm + L_BT + ((32 * (n & 1) + 16 * ((n >> 2) & 1) + 4 * ((n >> 1) & 1)) * 72 + (n >> 3)) * 2;
#pragma unroll
        for (int e = 0; e < 8; ++e)
          *(LAS unsigned short*)(zp + ((e & 3) + 8 * (e >> 2)) * 144) = (unsigned short)((e & 1) ? zv[i][e >> 1] >> 16 : zv[i][e >> 1]);
      }
      lds_signal(mi + M_HS);
      lds_wait(mi + M_HS, hs + 4);
#pragma unroll
      for (int i = 0; i < 3; ++i) {
        if (i < 2 || ht < 64) {
          const int n = ht + 256 * i;
          const u32x4 z = *(LAS u32x4*)(sm + L_BT + n * 16);
          const float omg = rbf(1.f - *(LAS float*)(mi + M_G + (n / 9) * 4));
          u32x4 o;
#pragma unroll
          for (int e = 0; e < 4; ++e)
            o[e] = pk2(omg * bf_lo(xv[i][e]) * bf_lo(z[e]), omg * bf_hi(xv[i][e]) * bf_hi(z[e]));
          *(LAS u32x4*)(sm + L_BT + n * 16) = o;
        }
      }
      lds_signal(mi + M_BT);
    }
    HSTAMP(8);
    __builtin_amdgcn_s_setprio(0);
  } else {
    if (w == 0) *(LAS int*)(mi + lane * 4) = (lane == M_RDY / 4 + 2 || lane == M_RDY / 4 + 3) ? -8 : 0;
    BAR();
    const float QS = 0.23570226039551584f * 1.4426950408889634f;
    if (w >= 5) {
      const int wi = w - 5;
      unsigned vw[3];
#pragma unroll
      for (int m = 0; m < 3; ++m) {
        const int f = 64 * (wi + 3 * m) + lane, row = f / 9;
        vw[m] = row * 1152 + (f - 9 * row) * 16;
      }
#pragma unroll
      for (int p = 0; p < 3; ++p) {
        const int th = (p + rot) % 3;
#pragma unroll
        for (int pr = 0; pr < 2; ++pr) {
          const int g2 = 2 * (1 - pt) + pr;
          const char* src = xt + g2 * 18432 + (3 * th + wi) * 128;
          glds(src, vxe, sbase + L_X + (3 * p + wi) * 8192 + g2 * 2048);
          glds(src, vxo, sbase + L_X + (3 * p + wi) * 8192 + g2 * 2048 + 1024);
        }
        if (p < 2) {
#pragma unroll
          for (int per = 0; per < 3; ++per) {
#pragma unroll
            for (int m = 0; m < 3; ++m)
              glds(wqs + (size_t)(192 * th + 64 * per) * 1152, vw[m], sbase + L_B + p * 27648 + per * 9216 + (wi + 3 * m) * 1024);
            if (p == 1 && per == 0) { VM_WAIT(7); lds_signal(mi + M_C1); }
          }
        }
      }
      STAMP(1);
      VM_WAIT(4);
      lds_signal(mi + M_C2);
    } else if (w < 4) {
      if (hg < 4) {
        const int mt = w & 1, nt = w >> 1, row = 32 * pt + 16 * mt + l15;
        lds_ptr wb = sm + L_B + 2 * 27648 + (8 * g4 + tq) * 48 + (nt ? 32 + (tp & 1) * 8 : tp * 8);
        lds_ptr xb = sm + L_X + row * 128;
        const int T = 2 * qt + pt, i4 = T & 3, gb = WS_KV + (T >> 2) * KV_GROUP, rp = swap23(16 * mt + l15);
        int so;
        bool st;
        if (sel) {
          const int d = nt ? (l15 < 8 ? 10 + l15 + 18 * (g4 & 1) : 36 + (g4 & 1)) : l15 + 18 * (g4 & 1);
          so = gb + 4096 + i4 * 1216 + mt * 608 + d * 16 + (g4 >> 1) * 8;
          st = !nt || (l15 >= 6 && l15 <= 8);
        } else {
          so = nt ? gb + 8960 + i4 * 256 + rp * 8 : gb + i4 * 1024 + (rp + 32 * (g4 & 1)) * 16 + (g4 >> 1) * 8;
          st = !nt || g4 == 1;
        }
        lds_wait(mi + M_KD, 3);
        PSTAMP(5);
        bf16x8 fa[18], fb[18];
#pragma unroll
        for (int k = 0; k < 18; ++k) {
          fa[k] = tr_frag(wb + k * 1536, wb + k * 1536 + 192);
          fb[k] = lds_frag(xb + (k >> 1) * 8192 + (((4 * (k & 1) + g4) ^ (l15 >> 1)) << 4));
        }
        f32x4 acc = {0}, acc1 = {0};
        if (sel) {
#pragma unroll
          for (int k = 0; k < 18; k += 2) { acc = MFMA16(fb[k], fa[k], acc); acc1 = MFMA16(fb[k + 1], fa[k + 1], acc1); }
        } else {
#pragma unroll
          for (int k = 0; k < 18; k += 2) { acc = MFMA16(fa[k], fb[k], acc); acc1 = MFMA16(fa[k + 1], fb[k + 1], acc1); }
        }
        acc += acc1;
        u32x2 a = {pk2(acc[0], acc[1]), pk2(acc[2], acc[3])};
        if (nt) {
          if (!sel) a = (u32x2){a[1], 0x3f803f80u};
          else if (l15 == 8) a[0] = a[1] = (g4 & 1) ? 0u : 0x3f803f80u;
        }
        if (st) ST_SC1_8(a, so);
        PSTAMP(6);
        lds_signal(mi + M_KVC);
        float n2 = bf_lo(a[0]) * bf_lo(a[0]) + bf_hi(a[0]) * bf_hi(a[0]);
        if (!nt) n2 += bf_lo(a[1]) * bf_lo(a[1]) + bf_hi(a[1]) * bf_hi(a[1]);
        if (!st) n2 = 0.f;
        n2 += xhalf(n2);
        n2 += u2f(__builtin_amdgcn_ds_swizzle(__builtin_bit_cast(int, n2), 0x401f));
        n2 = max16(n2);
        if (lane == 0) *(LAS float*)(mi + M_N2 + 4 * w) = n2;
        lds_wait(mi + M_TAGR, 1);
        PSTAMP(7);
        VM_WAIT(0);
        PSTAMP(8);
        if (lds_ticket(mi + M_KVP) == 3u && lane == 0) {
          const f32x4 n = *(LAS f32x4*)(mi + M_N2);
          const float nn = sel ? 0.f : fmaxf(n[0], n[1]) + fmaxf(n[2], n[3]);
          __hip_atomic_store((gu64*)(gws + WS_FKV + T * 128 + sel * 8),
                             ((unsigned long long)*(LAS unsigned*)(mi + M_TAG) << 32) | __builtin_bit_cast(unsigned, nn), RLX_AGENT);
        }
        PSTAMP(9);
      }
    } else {
      {
      *(LAS u32x4*)(sm + L_GW + lane * 16) = *(const u32x4*)(gw + 8 * lane);
      if (lane < 8) *(LAS u32x4*)(sm + L_GW + 1024 + lane * 16) = *(const u32x4*)(gw + 512 + 8 * lane);
      lds_signal(mi + M_C1);
      UNIT_LOAD(32 + (lane & 3), l31 >> 2);
      UNIT_DO();
      ST_SC1_16(h ? o1 : o0, WS_UT + (qt * 8 + hg) * 9216 + 8192 + lane * 16);
      }
    }
    if (w < 4) {
      const int gt = w >> 1, tt = w & 1, arow = 32 * tt + l31, xsw = (arow >> 1) & 7;
      lds_ptr xb = sm + L_X + arow * 128;
      lds_ptr wb = sm + L_B + (8 * h + tq) * 144 + (36 * gt + 20 * (g4 & 1) + 4 * tp) * 2;
      f32x16 acc = {0};
#pragma unroll 1
      for (int p = 0; p < 3; ++p) {
        if (p == 1) CSTAMP(6);
        if (p == 2) CSTAMP(8);
        lds_wait(mi + M_C1 + 4 * p, p == 0 ? 7 : 8 * p - 5);
        if (p == 0) CSTAMP(5);
        if (p == 1) CSTAMP(7);
        if (p == 2) CSTAMP(9);
        bf16x8 fa[12], fb[12];
#pragma unroll
        for (int k = 0; k < 12; ++k) {
          fa[k] = tr_frag(wb + k * 2304, wb + k * 2304 + 576);
          fb[k] = lds_frag(xb + (k >> 2) * 8192 + (((2 * (k & 3) + h) ^ xsw) << 4));
        }
        __builtin_amdgcn_sched_barrier(0);
#pragma unroll
        for (int k = 0; k < 12; ++k) acc = MFMA32(fa[k], fb[k], acc);
        if (p == 1) { lds_signal(mi + M_C3B); lds_signal(mi + M_C3); }
        xb += 24576;
        wb += 27648;
      }
      STAMP(2);
      u32x4 a;
#pragma unroll
      for (int i = 0; i < 4; ++i) a[i] = pk2(acc[2 * i] * QS, acc[2 * i + 1] * QS);
      lds_ptr qe = sm + L_Q + (4 * gt + tt) * 1024;
      *(LAS u32x4*)(qe + lane * 16) = a;
      lds_ptr qo = qe + 2048 + l31 * 16;
      *(LAS unsigned*)(qo + h * 512 + 4) = pk2(acc[8] * QS, acc[9] * QS);
      *(LAS unsigned*)(qo + (h ? 8 : 512)) = pk2(acc[10] * QS, acc[11] * QS);
      *(LAS unsigned*)(qo + h * 512 + 12) = pk2(acc[12] * QS, acc[13] * QS);
      *(LAS unsigned*)(h ? sm + L_Q + 8192 + ((2 * gt + 1) * 64 + arow) * 4 : qo + 520) = pk2(acc[14] * QS, acc[15] * QS);
    } else {
      const int tok = 16 * (w - 4) + l15;
      lds_ptr xb = sm + L_X + tok * 128;
      lds_ptr wb = sm + L_B + (8 * g4 + tq) * 144 + (tp == 1 ? 104 : 32);
      f32x4 acc = {0};
#pragma unroll 1
      for (int p = 0; p < 3; ++p) {
        lds_wait(mi + M_C1 + 4 * p, p == 0 ? 7 : 8 * p - 5);
        lds_ptr gp = sm + L_GW + ((p + rot) % 3) * 384 + g4 * 16;
        bf16x8 fa[6], fg[6], fb[6];
#pragma unroll
        for (int k = 0; k < 6; ++k) {
          fa[k] = tr_frag(wb + k * 4608, wb + k * 4608 + 576);
          fg[k] = lds_frag(gp + k * 64);
          fb[k] = lds_frag(xb + (k >> 1) * 8192 + (((4 * (k & 1) + g4) ^ (l15 >> 1)) << 4));
        }
        __builtin_amdgcn_sched_barrier(0);
#pragma unroll
        for (int k = 0; k < 6; ++k) acc = MFMA16(l15 == 8 ? fg[k] : fa[k], fb[k], acc);
        if (p == 1) { lds_signal(mi + M_C3B); VM_WAIT(0); lds_signal(mi + M_C3); }
        xb += 24576;
        wb += 27648;
      }
      STAMP(2);
      if (g4 < 2) {
        *(LAS unsigned*)(sm + L_Q + 8192 + (2 * g4 * 64 + tok) * 4) = pk2(acc[0] * QS, acc[1] * QS);
        *(LAS unsigned*)(sm + L_Q + ((2 * g4 + 1) * 2 + (tok >> 5)) * 1024 + (tok & 31) * 16) = pk2(acc[2] * QS, acc[3] * QS);
      } else if (g4 == 2) {
        *(LAS float*)(mi + M_G + tok * 4) = acc[0];
      }
    }
    lds_signal(mi + M_QD);
    const int hl = w & 3, kh = w >> 2;
    const int voff = 4096 + (l31 < 18 ? h * 288 + l31 * 16 : (l31 == 18 ? 576 : 592));
    lds_wait(mi + M_QD, 12);
    STAMP(3);
    const u32x4 q0u = *(LAS u32x4*)(sm + L_Q + (2 * hl) * 1024 + lane * 16);
    const u32x4 q1u = *(LAS u32x4*)(sm + L_Q + (2 * hl + 1) * 1024 + lane * 16);
    const unsigned qr = *(LAS unsigned*)(sm + L_Q + 8192 + (hl * 64 + lane) * 4);
    const float kn0 = *(LAS float*)(mi + M_KN0);
    lds_signal(mi + M_QR);
    float n0 = 0.f, n1 = 0.f;
#pragma unroll
    for (int i = 0; i < 4; ++i) { n0 = dot2(q0u[i], n0); n1 = dot2(q1u[i], n1); }
    const float nq = dot2(qr, (h ? n1 : n0) + xhalf(h ? n0 : n1));
    const u32x2 qrem_u = {qr, pk2(-__builtin_amdgcn_sqrtf(nq * kn0), 0.f)};
    f32x16 o0, o1;
    float m0 = 0.f, m1 = 0.f;
    STAMP(4); RSTAMP(2);
    {
      int cnt = 8, off = 0, tmp, t2, so = ((s0 + 4) & 7) * RSLOT;
      unsigned ca = sbase + L_M + M_DONE - 4, dst = sbase + w * 2496;
      const unsigned long long ksrc = (unsigned long long)ws + WS_KV + w * 2496 - (sbase + kh * KV_GROUP);
      asm volatile(
      "v_mov_b32 v107, v126\n"
      "v_mov_b32 v108, v127\n"
      "v_mov_b32 v109, v128\n"
      "v_mov_b32 v130, 1\n"
      "ds_read2st64_b64 v[110:113], v109 offset0:0 offset1:1\n"
      "ds_read_b128 v[114:117], v107 offset:0\n"
      "ds_read_b128 v[118:121], v108 offset:0\n"
      "s_mov_b32 vcc_lo, %[first]\n"
      "s_mov_b32 vcc_hi, 0\n"
      "s_setprio 1\n"
      "v_mov_b32 v56, 0\n"
      "v_mov_b32 v57, 0\n"
      "v_mov_b32 v58, 0\n"
      "v_mov_b32 v59, 0\n"
      "v_mov_b32 v60, 0\n"
      "v_mov_b32 v61, 0\n"
      "v_mov_b32 v62, 0\n"
      "v_mov_b32 v63, 0\n"
      "v_mov_b32 v122, 0\n"
      "v_mov_b32 v123, 0\n"
      "v_mov_b32 v124, 0\n"
      "v_mov_b32 v125, 0\n"
      "v_mov_b32 v64, 0\n"
      "v_mov_b32 v65, 0\n"
      "v_mov_b32 v66, 0\n"
      "v_mov_b32 v67, 0\n"
      "v_mov_b32 v68, 0\n"
      "v_mov_b32 v69, 0\n"
      "v_mov_b32 v70, 0\n"
      "v_mov_b32 v71, 0\n"
      "v_mov_b32 v72, 0\n"
      "v_mov_b32 v73, 0\n"
      "v_mov_b32 v74, 0\n"
      "v_mov_b32 v75, 0\n"
      "v_mov_b32 v76, 0\n"
      "v_mov_b32 v77, 0\n"
      "v_mov_b32 v78, 0\n"
      "v_mov_b32 v79, 0\n"
      "s_waitcnt lgkmcnt(1)\n"
      "v_mfma_f32_32x32x4_2b_bf16 v[0:31], v[110:111], v[104:105], 0 cbsz:1 abid:0\n"
      "v_mov_b32 v80, 0\n"
      "v_mov_b32 v81, 0\n"
      "v_mov_b32 v82, 0\n"
      "v_mov_b32 v83, 0\n"
      "v_mov_b32 v84, 0\n"
      "v_mov_b32 v85, 0\n"
      "v_mov_b32 v86, 0\n"
      "v_mov_b32 v87, 0\n"
      "v_mov_b32 v88, 0\n"
      "v_mov_b32 v89, 0\n"
      "v_mov_b32 v90, 0\n"
      "v_mov_b32 v91, 0\n"
      "v_mov_b32 v92, 0\n"
      "v_mov_b32 v93, 0\n"
      "v_mov_b32 v94, 0\n"
      "v_mov_b32 v95, 0\n"
      "s_nop 2\n"
      "v_mfma_f32_32x32x16_bf16 v[0:15], v[114:117], v[96:99], v[0:15]\n"
      "v_mfma_f32_32x32x16_bf16 v[16:31], v[114:117], v[100:103], v[16:31]\n"
      "s_nop 11\n"
      "1:\n"
      "s_cbranch_vccz 4f\n"
      "s_setprio 2\n"
      "4:\n"
      "v_exp_f32 v0, v0\n"
      "v_exp_f32 v1, v1\n"
      "v_smfmac_f32_32x32x32_bf16 v[80:95], v[122:125], v[56:63], v106\n"
      "ds_read_b128 v[114:117], v107 offset:1024\n"
      "ds_read_b128 v[122:125], v108 offset:608\n"
      "v_exp_f32 v2, v2\n"
      "v_exp_f32 v3, v3\n"
      "v_exp_f32 v4, v4\n"
      "v_exp_f32 v5, v5\n"
      "v_mfma_f32_32x32x4_2b_bf16 v[32:63], v[110:111], v[104:105], 0 cbsz:1 abid:1\n"
      "v_exp_f32 v6, v6\n"
      "v_exp_f32 v7, v7\n"
      "v_exp_f32 v8, v8\n"
      "v_exp_f32 v9, v9\n"
      "v_exp_f32 v10, v10\n"
      "v_exp_f32 v11, v11\n"
      "v_exp_f32 v12, v12\n"
      "s_waitcnt lgkmcnt(2)\n"
      "v_exp_f32 v13, v13\n"
      "v_smfmac_f32_32x32x32_bf16 v[64:79], v[118:121], v[0:7], v106\n"
      "v_exp_f32 v14, v14\n"
      "v_exp_f32 v15, v15\n"
      "v_exp_f32 v16, v16\n"
      "s_waitcnt lgkmcnt(0)\n"
      "v_exp_f32 v17, v17\n"
      "v_smfmac_f32_32x32x32_bf16 v[64:79], v[122:125], v[8:15], v106\n"
      "v_exp_f32 v18, v18\n"
      "v_exp_f32 v19, v19\n"
      "v_exp_f32 v20, v20\n"
      "v_exp_f32 v21, v21\n"
      "v_mfma_f32_32x32x16_bf16 v[32:47], v[114:117], v[96:99], v[32:47]\n"
      "v_exp_f32 v22, v22\n"
      "v_exp_f32 v23, v23\n"
      "v_exp_f32 v24, v24\n"
      "v_exp_f32 v25, v25\n"
      "v_smfmac_f32_32x32x32_bf16 v[80:95], v[118:121], v[16:23], v106\n"
      "v_exp_f32 v26, v26\n"
      "ds_read_b128 v[118:121], v108 offset:1216\n"
      "v_exp_f32 v27, v27\n"
      "v_exp_f32 v28, v28\n"
      "v_exp_f32 v29, v29\n"
      "v_mfma_f32_32x32x16_bf16 v[48:63], v[114:117], v[100:103], v[48:63]\n"
      "v_exp_f32 v30, v30\n"
      "v_exp_f32 v31, v31\n"
      "v_exp_f32 v32, v32\n"
      "v_exp_f32 v33, v33\n"
      "v_smfmac_f32_32x32x32_bf16 v[80:95], v[122:125], v[24:31], v106\n"
      "ds_read_b128 v[114:117], v107 offset:2048\n"
      "ds_read_b128 v[122:125], v108 offset:1824\n"
      "ds_read_b32 v131, v129\n"
      "v_exp_f32 v34, v34\n"
      "v_exp_f32 v35, v35\n"
      "v_exp_f32 v36, v36\n"
      "v_exp_f32 v37, v37\n"
      "v_mfma_f32_32x32x4_2b_bf16 v[0:31], v[112:113], v[104:105], 0 cbsz:1 abid:0\n"
      "v_exp_f32 v38, v38\n"
      "v_exp_f32 v39, v39\n"
      "v_exp_f32 v40, v40\n"
      "v_exp_f32 v41, v41\n"
      "v_exp_f32 v42, v42\n"
      "v_exp_f32 v43, v43\n"
      "v_exp_f32 v44, v44\n"
      "s_waitcnt lgkmcnt(3)\n"
      "v_exp_f32 v45, v45\n"
      "v_smfmac_f32_32x32x32_bf16 v[64:79], v[118:121], v[32:39], v106\n"
      "v_exp_f32 v46, v46\n"
      "v_exp_f32 v47, v47\n"
      "v_exp_f32 v48, v48\n"
      "s_waitcnt lgkmcnt(0)\n"
      "s_waitcnt vmcnt(0)\n"
      "s_mov_b64 exec, 1\n"
      "ds_add_u32 v129, v130 offset:76\n"
      "s_mov_b64 exec, -1\n"
      "v_exp_f32 v49, v49\n"
      "v_smfmac_f32_32x32x32_bf16 v[64:79], v[122:125], v[40:47], v106\n"
      "v_exp_f32 v50, v50\n"
      "v_exp_f32 v51, v51\n"
      "v_exp_f32 v52, v52\n"
      "v_exp_f32 v53, v53\n"
      "v_mfma_f32_32x32x16_bf16 v[0:15], v[114:117], v[96:99], v[0:15]\n"
      "v_exp_f32 v54, v54\n"
      "v_exp_f32 v55, v55\n"
      "v_exp_f32 v56, v56\n"
      "v_exp_f32 v57, v57\n"
      "v_smfmac_f32_32x32x32_bf16 v[80:95], v[118:121], v[48:55], v106\n"
      "v_exp_f32 v58, v58\n"
      "ds_read_b128 v[118:121], v108 offset:2432\n"
      "v_exp_f32 v59, v59\n"
      "v_exp_f32 v60, v60\n"
      "v_exp_f32 v61, v61\n"
      "v_mfma_f32_32x32x16_bf16 v[16:31], v[114:117], v[100:103], v[16:31]\n"
      "v_exp_f32 v62, v62\n"
      "v_exp_f32 v63, v63\n"
      "v_readfirstlane_b32 %[tmp], v131\n"
      "s_and_b32 %[t2], %[cnt], 12\n"
      "s_cmp_lg_u32 %[t2], 4\n"
      "s_cbranch_scc1 8f\n"
      "s_cmp_lt_u32 %[tmp], 8\n"
      "s_cbranch_scc1 7f\n"
      "9:\n"
      "s_mov_b32 m0, %[dst]\n"
      "s_add_u32 s96, %[slo], %[so]\n"
      "s_addc_u32 s97, %[shi], 0\n"
      "global_load_lds_dwordx4 v126, s[96:97] sc1\n"
      "global_load_lds_dwordx4 v126, s[96:97] offset:1024 sc1\n"
      "s_mov_b64 exec, 0xfffffff\n"
      "global_load_lds_dwordx4 v126, s[96:97] offset:2048 sc1\n"
      "s_mov_b64 exec, -1\n"
      "s_add_u32 %[dst], %[dst], 19968\n"
      "s_add_u32 %[so], %[so], 19968\n"
      "s_cmp_eq_u32 %[so], 159744\n"
      "s_cselect_b32 %[so], 0, %[so]\n"
      "8:\n"
      "v_exp_f32 v0, v0\n"
      "v_exp_f32 v1, v1\n"
      "v_smfmac_f32_32x32x32_bf16 v[80:95], v[122:125], v[56:63], v106\n"
      "ds_read_b128 v[114:117], v107 offset:3072\n"
      "ds_read_b128 v[122:125], v108 offset:3040\n"
      "ds_read_b32 v131, v129 offset:72\n"
      "v_exp_f32 v2, v2\n"
      "v_exp_f32 v3, v3\n"
      "v_exp_f32 v4, v4\n"
      "v_exp_f32 v5, v5\n"
      "v_mfma_f32_32x32x4_2b_bf16 v[32:63], v[112:113], v[104:105], 0 cbsz:1 abid:1\n"
      "v_exp_f32 v6, v6\n"
      "v_exp_f32 v7, v7\n"
      "v_exp_f32 v8, v8\n"
      "v_exp_f32 v9, v9\n"
      "v_exp_f32 v10, v10\n"
      "v_exp_f32 v11, v11\n"
      "v_exp_f32 v12, v12\n"
      "s_waitcnt lgkmcnt(3)\n"
      "v_exp_f32 v13, v13\n"
      "v_smfmac_f32_32x32x32_bf16 v[64:79], v[118:121], v[0:7], v106\n"
      "v_exp_f32 v14, v14\n"
      "v_exp_f32 v15, v15\n"
      "v_exp_f32 v16, v16\n"
      "s_waitcnt lgkmcnt(0)\n"
      "v_exp_f32 v17, v17\n"
      "v_smfmac_f32_32x32x32_bf16 v[64:79], v[122:125], v[8:15], v106\n"
      "v_exp_f32 v18, v18\n"
      "v_exp_f32 v19, v19\n"
      "v_exp_f32 v20, v20\n"
      "v_exp_f32 v21, v21\n"
      "v_mfma_f32_32x32x16_bf16 v[32:47], v[114:117], v[96:99], v[32:47]\n"
      "v_exp_f32 v22, v22\n"
      "v_exp_f32 v23, v23\n"
      "v_exp_f32 v24, v24\n"
      "v_exp_f32 v25, v25\n"
      "v_smfmac_f32_32x32x32_bf16 v[80:95], v[118:121], v[16:23], v106\n"
      "v_exp_f32 v26, v26\n"
      "ds_read_b128 v[118:121], v108 offset:3648\n"
      "v_exp_f32 v27, v27\n"
      "v_exp_f32 v28, v28\n"
      "v_exp_f32 v29, v29\n"
      "v_mfma_f32_32x32x16_bf16 v[48:63], v[114:117], v[100:103], v[48:63]\n"
      "v_exp_f32 v30, v30\n"
      "v_exp_f32 v31, v31\n"
      "s_cbranch_vccz 5f\n"
      "s_setprio 0\n"
      "5:\n"
      "v_readfirstlane_b32 %[tmp], v131\n"
      "s_add_u32 %[off], %[off], 19968\n"
      "s_cmp_eq_u32 %[off], 79872\n"
      "s_cselect_b32 %[off], 0, %[off]\n"
      "s_cmp_lt_i32 %[tmp], 8\n"
      "s_cbranch_scc1 3f\n"
      "2:\n"
      "v_add_u32 v109, %[off], v128\n"
      "ds_read2st64_b64 v[110:113], v109 offset0:0 offset1:1\n"
      "v_add_u32 v107, %[off], v126\n"
      "v_exp_f32 v32, v32\n"
      "v_exp_f32 v33, v33\n"
      "v_smfmac_f32_32x32x32_bf16 v[80:95], v[122:125], v[24:31], v106\n"
      "ds_read_b128 v[114:117], v107 offset:0\n"
      "ds_read_b128 v[122:125], v108 offset:4256\n"
      "v_exp_f32 v34, v34\n"
      "v_exp_f32 v35, v35\n"
      "v_exp_f32 v36, v36\n"
      "s_waitcnt lgkmcnt(2)\n"
      "v_exp_f32 v37, v37\n"
      "v_mfma_f32_32x32x4_2b_bf16 v[0:31], v[110:111], v[104:105], 0 cbsz:1 abid:0\n"
      "v_exp_f32 v38, v38\n"
      "v_exp_f32 v39, v39\n"
      "v_exp_f32 v40, v40\n"
      "v_exp_f32 v41, v41\n"
      "v_exp_f32 v42, v42\n"
      "v_exp_f32 v43, v43\n"
      "v_exp_f32 v44, v44\n"
      "v_exp_f32 v45, v45\n"
      "v_smfmac_f32_32x32x32_bf16 v[64:79], v[118:121], v[32:39], v106\n"
      "v_exp_f32 v46, v46\n"
      "v_exp_f32 v47, v47\n"
      "v_exp_f32 v48, v48\n"
      "s_waitcnt lgkmcnt(0)\n"
      "s_mov_b64 exec, 1\n"
      "ds_add_u32 v129, v130 offset:4\n"
      "s_mov_b64 exec, -1\n"
      "v_exp_f32 v49, v49\n"
      "v_smfmac_f32_32x32x32_bf16 v[64:79], v[122:125], v[40:47], v106\n"
      "v_exp_f32 v50, v50\n"
      "v_exp_f32 v51, v51\n"
      "v_exp_f32 v52, v52\n"
      "v_exp_f32 v53, v53\n"
      "v_mfma_f32_32x32x16_bf16 v[0:15], v[114:117], v[96:99], v[0:15]\n"
      "v_exp_f32 v54, v54\n"
      "v_exp_f32 v55, v55\n"
      "v_exp_f32 v56, v56\n"
      "v_exp_f32 v57, v57\n"
      "v_smfmac_f32_32x32x32_bf16 v[80:95], v[118:121], v[48:55], v106\n"
      "v_add_u32 v108, %[off], v127\n"
      "v_add_u32 v129, 4, v129\n"
      "v_exp_f32 v58, v58\n"
      "ds_read_b128 v[118:121], v108 offset:0\n"
      "v_exp_f32 v59, v59\n"
      "v_exp_f32 v60, v60\n"
      "v_exp_f32 v61, v61\n"
      "v_mfma_f32_32x32x16_bf16 v[16:31], v[114:117], v[100:103], v[16:31]\n"
      "v_exp_f32 v62, v62\n"
      "v_exp_f32 v63, v63\n"
      "s_sub_u32 %[cnt], %[cnt], 1\n"
      "s_cmp_lg_u32 %[cnt], 0\n"
      "s_cbranch_scc1 1b\n"
      "s_branch 6f\n"
      "3:\n"
      "s_sleep 1\n"
      "ds_read_b32 v131, v129 offset:72\n"
      "s_waitcnt lgkmcnt(0)\n"
      "v_readfirstlane_b32 %[tmp], v131\n"
      "s_nop 0\n"
      "s_cmp_lt_i32 %[tmp], 8\n"
      "s_cbranch_scc1 3b\n"
      "s_branch 2b\n"
      "7:\n"
      "s_sleep 1\n"
      "ds_read_b32 v131, v129\n"
      "s_waitcnt lgkmcnt(0)\n"
      "v_readfirstlane_b32 %[tmp], v131\n"
      "s_nop 0\n"
      "s_cmp_lt_u32 %[tmp], 8\n"
      "s_cbranch_scc1 7b\n"
      "s_branch 9b\n"
      "6:\n"
      "v_smfmac_f32_32x32x32_bf16 v[80:95], v[122:125], v[56:63], v106\n"
      "s_setprio 0\n"
      "s_waitcnt lgkmcnt(0)\n"
      "s_nop 15\n"
      "s_nop 7\n"
      : "=&{v[64:79]}"(o0), "=&{v[80:95]}"(o1), "+{v129}"(ca), [cnt] "+s"(cnt), [off] "+s"(off), [tmp] "=&s"(tmp),
        [t2] "=&s"(t2), [so] "+s"(so), [dst] "+s"(dst)
      : "{v[96:99]}"(q0u), "{v[100:103]}"(q1u), "{v[104:105]}"(qrem_u), "{v106}"(SPIDX),
        "{v126}"(sbase + kh * KV_GROUP + lane * 16), "{v127}"(sbase + kh * KV_GROUP + voff),
        "{v128}"(sbase + kh * KV_GROUP + 8960 + lane * 8), [first] "s"(1 - kh), [slo] "s"((unsigned)ksrc), [shi] "s"((unsigned)(ksrc >> 32))
      : "v0", "v1", "v2", "v3", "v4", "v5", "v6", "v7", "v8", "v9", "v10", "v11", "v12", "v13", "v14", "v15",
        "v16", "v17", "v18", "v19", "v20", "v21", "v22", "v23", "v24", "v25", "v26", "v27", "v28", "v29", "v30", "v31",
        "v32", "v33", "v34", "v35", "v36", "v37", "v38", "v39", "v40", "v41", "v42", "v43", "v44", "v45", "v46", "v47",
        "v48", "v49", "v50", "v51", "v52", "v53", "v54", "v55", "v56", "v57", "v58", "v59", "v60", "v61", "v62", "v63",
        "v107", "v108", "v109", "v110", "v111", "v112", "v113", "v114", "v115", "v116", "v117", "v118", "v119", "v120", "v121", "v122",
        "v123", "v124", "v125", "v130", "v131", "s96", "s97", "vcc", "scc", "memory");
    }
    if (!__all(nq * *(LAS float*)(mi + M_KN) < 47.f * 47.f)) {
      m0 = m1 = -INFINITY;
      o0 = o1 = (f32x16){0};
      const bf16x8 q0 = __builtin_bit_cast(bf16x8, q0u), q1 = __builtin_bit_cast(bf16x8, q1u);
      const s16x4 qrem = __builtin_bit_cast(s16x4, (u32x2){qr, 0u});
#pragma unroll 1
      for (int t = 0; t < 32; ++t) {
        const int g = WS_KV + (t >> 2) * RSLOT + kh * KV_GROUP;
        const u32x2 kr = __builtin_bit_cast(u32x2, __builtin_amdgcn_raw_buffer_load_b64(rs, g + 8960 + (t & 3) * 256 + l31 * 8, 0, 16));
        const bf16x8 km = __builtin_bit_cast(bf16x8, LD_SC1_16(g + (t & 3) * 1024 + lane * 16));
        const bf16x8 va = __builtin_bit_cast(bf16x8, LD_SC1_16(g + voff + (t & 3) * 1216));
        const bf16x8 vb = __builtin_bit_cast(bf16x8, LD_SC1_16(g + voff + (t & 3) * 1216 + 608));
        const f32x32 z = {0};
        const f32x32 r = __builtin_amdgcn_mfma_f32_32x32x4bf16_1k(__builtin_bit_cast(s16x4, kr), qrem, z, 0, 0, 0);
        online_step(MFMA32(km, q0, lo16(r)), m0, o0, va, vb);
        online_step(MFMA32(km, q1, hi16(r)), m1, o1, va, vb);
      }
    }
    HSTAMP(9);
    lds_wait(mi + M_DONE + 20, 8);
    {
      const f32x16 give = kh ? o0 : o1;
      lds_ptr p = sm + L_EX + w * 3072 + lane * 16;
      *(LAS f32x4*)(p) = (f32x4){give[0], give[1], give[2], give[3]};
      *(LAS f32x4*)(p + 1024) = (f32x4){give[4], give[5], give[6], give[7]};
      *(LAS f32x4*)(p + 2048) = (f32x4){give[8], give[9], give[10], kh ? m0 : m1};
    }
    lds_signal(mi + M_PAIR + 4 * hl);
    lds_wait(mi + M_PAIR + 4 * hl, 2);
    STAMP(10); RSTAMP(3);
    {
      const f32x16 keep = kh ? o1 : o0;
      lds_ptr p = sm + L_EX + (w ^ 4) * 3072 + lane * 16;
      const f32x4 t0 = *(LAS f32x4*)(p), t1 = *(LAS f32x4*)(p + 1024), t2 = *(LAS f32x4*)(p + 2048);
      const float mk = kh ? m1 : m0, mm = fmaxf(mk, t2[3]);
      const float fa = __builtin_amdgcn_exp2f(mk - mm), fb = __builtin_amdgcn_exp2f(t2[3] - mm);
      float o[11];
#pragma unroll
      for (int i = 0; i < 11; ++i) o[i] = keep[i] * fa + (i < 4 ? t0[i & 3] : (i < 8 ? t1[i & 3] : t2[i & 3])) * fb;
      const float ox = xhalf(o[10]);
      const float inv = __builtin_amdgcn_rcpf(h ? ox : o[10]);
      u32x4 a;
#pragma unroll
      for (int i = 0; i < 4; ++i) a[i] = pk2(o[2 * i] * inv, o[2 * i + 1] * inv);
      const int ob = WS_O + (2 * qt + kh) * 36 * 1024;
      ST_SC1_16(a, ob + (4 * hg + hl) * 1024 + lane * 16);
      if (!h) ST_SC1_4(pk2(o[8] * inv, o[9] * inv), ob + 32768 + (4 * hg + hl) * 128 + l31 * 4);
    }
  }

  const unsigned tag = *(LAS unsigned*)(mi + M_TAG);
  if (w < 8) {
    VM_WAIT(0);
    if (lds_ticket(mi + M_OD) == 7u && lane == 0)
      __hip_atomic_store((gu32*)(gws + WS_FO) + (qt * 8 + hg) * 32, tag, RLX_AGENT);
  }
  bf16x8 wf[18];
  u32x4 bt = {0, 0, 0, 0};
  float gq = 0.f;
  lds_wait(mi + M_WO, 4);
  {
    const int ka = w < 8 ? (w >> 2) : (g4 >> 1), kb = w < 8 ? h : (g4 & 1);
    const int cb = w < 8 ? (32 * ((w >> 1) & 1) + 16 * (g4 & 1) + 4 * tp) * 2 : (64 + 4 * (tp & 1)) * 2;
    lds_ptr wm = sm + L_B + (DH * ka + 4 * kb + tq) * 144 + cb;
    lds_ptr wr = sm + L_B + (DH * (8 * ka + 4 * kb + (tq >> 1)) + 16 + (tq & 1)) * 144 + cb;
#pragma unroll
    for (int k = 0; k < 12; ++k) wf[k] = tr_frag(wm + k * 5184, wm + k * 5184 + 1152);
    wm += 12 * 5184;
#pragma unroll
    for (int k = 0; k < 4; ++k) wf[12 + k] = tr_frag(wm + k * 5184, wm + k * 5184 + 1152);
#pragma unroll
    for (int k = 0; k < 2; ++k) wf[16 + k] = tr_frag(wr + k * 41472, wr + k * 41472 + 5184);
  }
  if (w < 9) {
    lds_wait(mi + M_BT, 4);
    bt = *(LAS u32x4*)(sm + L_BT + tid * 16);
    gq = *(LAS float*)(mi + M_G + otok * 4);
    lds_signal(mi + M_EXD);
  }
  if (w == 11) {
    lds_wait<1>(mi + M_OD, 8);
#pragma unroll 1
    for (unsigned spins = 0; spins < SPIN_CAP; ++spins) {
      if (__all(lane >= 8 || __hip_atomic_load((gu32*)(gws + WS_FO) + (qt * 8 + lane) * 32, RLX_AGENT) == tag)) break;
      __builtin_amdgcn_s_sleep(1);
    }
    lds_signal(mi + M_OR);
  } else {
    lds_wait<1>(mi + M_OR, 1);
  }
  if (w < 8) HSTAMP(5);
  {
    u32x4 of[6];
#pragma unroll
    for (int i = 0; i < 6; ++i) of[i] = LD_SC1_16(WS_O + 2 * qt * 36 * 1024 + (12 * i + w) * 1024 + lane * 16);
    lds_wait(mi + M_EXD, 9);
#pragma unroll
    for (int i = 0; i < 6; ++i) {
      if ((i == 2 || i == 5) && w >= 8) {
        lds_ptr p = sm + (12 * i + w) * 1024 + (4 * (lane & 7) + 32 * h) * 16 + ((lane >> 3) & 3) * 4;
#pragma unroll
        for (int e = 0; e < 4; ++e) *(LAS unsigned*)(p + e * 16) = of[i][e];
      } else {
        *(LAS u32x4*)(sm + (12 * i + w) * 1024 + lane * 16) = of[i];
      }
    }
  }
  BAR();
  if (w < 8) HSTAMP(6);
  if (w < 8) {
    lds_ptr ob = sm + ((w & 1) * 36 + (w >> 2)) * 1024 + lane * 16;
    f32x16 acc = {0};
#pragma unroll
    for (int k = 0; k < 18; ++k) acc = MFMA32(lds_frag(ob + k * 2048), wf[k], acc);
    lds_ptr ap = sm + L_B + (((w >> 2) * 64 + 32 * (w & 1) + 4 * h) * 72 + 32 * ((w >> 1) & 1) + l31) * 4;
#pragma unroll
    for (int r = 0; r < 16; ++r) *(LAS float*)(ap + ((r & 3) + 8 * (r >> 2)) * 288) = acc[r];
  } else {
    const int j = w - 8;
    lds_ptr ob = sm + ((j >> 1) * 36 + (g4 >> 1)) * 1024 + (16 * (j & 1) + l15 + 32 * (g4 & 1)) * 16;
    f32x4 acc = {0};
#pragma unroll
    for (int k = 0; k < 18; ++k) acc = MFMA16(lds_frag(ob + k * 2048), wf[k], acc);
    lds_ptr ap = sm + L_B + (((l15 >> 3) * 64 + 16 * j + 4 * g4) * 72 + 64 + (l15 & 7)) * 4;
#pragma unroll
    for (int r = 0; r < 4; ++r) *(LAS float*)(ap + r * 288) = (l15 < 8) ? acc[r] : 0.f;
  }
  if (w < 8) HSTAMP(7);
  BAR();
  if (w < 8) HSTAMP(8);
  if (tid < 576) {
    lds_ptr ap = sm + L_B + tid * 32;
    const f32x4 a0 = *(LAS f32x4*)(ap) + *(LAS f32x4*)(ap + 64 * 72 * 4);
    const f32x4 a1 = *(LAS f32x4*)(ap + 16) + *(LAS f32x4*)(ap + 64 * 72 * 4 + 16);
    u32x4 o;
#pragma unroll
    for (int i = 0; i < 4; ++i) {
      const float e0 = i < 2 ? a0[2 * i] : a1[2 * i - 4], e1 = i < 2 ? a0[2 * i + 1] : a1[2 * i - 3];
      o[i] = pk2(fmaf(gq, e0, bf_lo(bt[i])), fmaf(gq, e1, bf_hi(bt[i])));
    }
    *(u32x4*)(out + (size_t)(64 * qt + otok) * D_ + 72 * hg + 8 * opc) = o;
  }
  STAMP_FLUSH(w < 4 ? 0 : (w < 8 ? 1 : (w < 11 ? 2 : 3)));
  while (__builtin_amdgcn_s_memrealtime() < xr + 3600) {}
  asm volatile("fk_end:");
}

extern "C" void kernel_launch(void* const* d_in, const int* in_sizes, int n_in, void* d_out, int out_size,
                              void* d_ws, size_t ws_size, hipStream_t stream) {
  fused_kernel<<<256, 768, 0, stream>>>((const __bf16*)d_in[0], (const __bf16*)d_in[1], (const __bf16*)d_in[2],
                                        (const __bf16*)d_in[3], (const __bf16*)d_in[4], (const __bf16*)d_in[6],
                                        (const __bf16*)d_in[7], (const __bf16*)d_in[8], (const __bf16*)d_in[9],
                                        (const __bf16*)d_in[10], (const __bf16*)d_in[11], (__bf16*)d_out, (char*)d_ws);
}
```

```cpp
#include <hip/hip_runtime.h>
#include <hip/hip_bf16.h>
#include <stdint.h>

typedef __attribute__((ext_vector_type(2))) __bf16 bf16x2;
typedef __attribute__((ext_vector_type(4))) __bf16 bf16x4;
typedef __attribute__((ext_vector_type(8))) __bf16 bf16x8;
typedef __attribute__((ext_vector_type(16))) __bf16 bf16x16;
typedef __attribute__((ext_vector_type(4))) short s16x4;
typedef __attribute__((ext_vector_type(2))) float f32x2;
typedef __attribute__((ext_vector_type(4))) float f32x4;
typedef __attribute__((ext_vector_type(16))) float f32x16;
typedef __attribute__((ext_vector_type(32))) float f32x32;
typedef __attribute__((ext_vector_type(2))) unsigned u32x2;
typedef __attribute__((ext_vector_type(4))) unsigned u32x4;
#define LAS __attribute__((address_space(3)))
#define GAS __attribute__((address_space(1)))
typedef LAS char* lds_ptr;
typedef GAS unsigned gu32;
typedef GAS unsigned long long gu64;
#ifdef __HIP_DEVICE_COMPILE__
#define NOPK __attribute__((target("no-packed-fp32-ops")))
#else
#define NOPK
#endif
#define DEVI __device__ __forceinline__ NOPK
#define LAMI __attribute__((always_inline)) NOPK
#define TIDX __builtin_amdgcn_workitem_id_x()
#define BIDX __builtin_amdgcn_workgroup_id_x()
#define ALL(p) (__builtin_amdgcn_ballot_w64(!(p)) == 0ull)
#define UMIN(a, b) __builtin_elementwise_min((unsigned)(a), (unsigned)(b))
#define UMAX(a, b) __builtin_elementwise_max((unsigned)(a), (unsigned)(b))
#define RLX_AGENT __ATOMIC_RELAXED, __HIP_MEMORY_SCOPE_AGENT
#define RLX_WG __ATOMIC_RELAXED, __HIP_MEMORY_SCOPE_WORKGROUP

constexpr int S_ = 2048, D_ = 576, DH = 18;

constexpr int KV_GROUP = 9984;
constexpr int WS_KV = 0;
constexpr int WS_EPOCH = 192 * 1024;
constexpr int WS_FKV = WS_EPOCH + 1024;
constexpr int WS_FU = WS_FKV + 8192;
constexpr int WS_FZ = WS_FU + 32768;
constexpr int WS_G = WS_FZ + 32768;
constexpr int WS_KT = 320 * 1024;
constexpr int WS_UT = 640 * 1024;
constexpr int WS_Z = WS_UT + D_ * S_ * 2;
constexpr int WS_O = WS_Z + D_ * S_ * 2;
constexpr int WS_ZP = WS_O + D_ * S_ * 2;
constexpr int WS_END = WS_ZP + 64 * 4 * 8192;

constexpr int RSLOT = 2 * KV_GROUP;
constexpr int L_X = 0;
constexpr int L_KVP = 73728;
constexpr int L_B = 80000;
constexpr int L_M = 162944;
constexpr int CV_COPY = 4160;
constexpr int CV_H = L_B, CV_HB = 8 * CV_COPY, CV_U = L_B + 2 * CV_HB, CV_UB = 7680;
constexpr int L_Q = L_B + 8192;
constexpr int M_DONE = 0, M_C1 = 32, M_C2 = 36, M_C3 = 40, M_C3B = 44, M_KVC = 48, M_KVR = 52, M_UR = 56, M_OR = 60, M_RDY = 64,
              M_QD = 192, M_UD = 104, M_HS = 108, M_APR = 112, M_CVR = 116, M_CE = 124, M_PAIR = 128, M_TAGR = 144,
              M_TAG = 148, M_KN = 152, M_EXD = 156, M_KVP = 160, M_KN0 = 164, M_KD = 168, M_QR = 172,
              M_N2 = 176, M_AS = 196, M_SE = 204, M_G = 256;
constexpr int L_EX = 0;
constexpr int L_AP = L_B, L_AP2 = L_B + 32768, L_ZT = L_AP2 + 8192;
#define CV_PRIO 3
#define CV_SLEEP 0
#define POLL_AT M_C2
#define POLL_N 3
#define BUBBLE 0
#define POLL_SLEEP 4
#define WO_INFL "2"
#define DMA_FIRST 0
#define ENTRY 0
#define O_DELAY 12
#define Z_DELAY 8
constexpr unsigned SPIN_CAP = 1u << 18;

#define STAMP_DECL do {} while (0)
#define STAMP(I) do {} while (0)
#define STAMP_FLUSH(KID) do {} while (0)
#define RSTAMP(I) do {} while (0)

#define PSTAMP(I) do {} while (0)
#define NSTAMP(I) STAMP(I)
#define CSTAMP(I) do {} while (0)
#define HSTAMP(I) NSTAMP(I)

DEVI unsigned pk2(float a, float b) {
  bf16x2 v = {(__bf16)a, (__bf16)b};
  return __builtin_bit_cast(unsigned, v);
}
DEVI float u2f(unsigned u) { return __builtin_bit_cast(float, u); }
DEVI float bf_lo(unsigned u) { return __builtin_bit_cast(float, u << 16); }
DEVI float bf_hi(unsigned u) { return __builtin_bit_cast(float, u & 0xffff0000u); }
DEVI float dot2(unsigned u, float c) { const bf16x2 v = __builtin_bit_cast(bf16x2, u); return __builtin_amdgcn_fdot2_f32_bf16(v, v, c, false); }
DEVI float rbf(float a) { return (float)(__bf16)a; }
DEVI int swap23(int r) { return (r & ~12) | ((r & 4) << 1) | ((r & 8) >> 1); }
DEVI float xhalf(float v) {
  const unsigned u = __builtin_bit_cast(unsigned, v);
  const auto r = __builtin_amdgcn_permlane32_swap(u, u, false, false);
  return __builtin_bit_cast(float, (TIDX & 32) ? r[0] : r[1]);
}

template <int CTRL>
DEVI float dpp_max(float v) {
  const int u = __builtin_bit_cast(int, v);
  return __builtin_fmaxf(v, __builtin_bit_cast(float, __builtin_amdgcn_update_dpp(u, u, CTRL, 0xf, 0xf, false)));
}
DEVI float max16(float v) {
  return dpp_max<0x140>(dpp_max<0x141>(dpp_max<0x4e>(dpp_max<0xb1>(v))));
}
template <int CTRL>
DEVI float dpp_add(float v) {
  const int u = __builtin_bit_cast(int, v);
  return v + __builtin_bit_cast(float, __builtin_amdgcn_update_dpp(u, u, CTRL, 0xf, 0xf, false));
}
DEVI float sum16(float v) { return dpp_add<0x140>(dpp_add<0x141>(dpp_add<0x4e>(dpp_add<0xb1>(v)))); }
DEVI float wave_max(float v) {
  v = max16(v);
  const unsigned u = __builtin_bit_cast(unsigned, v);
  return __builtin_fmaxf(__builtin_fmaxf(u2f(__builtin_amdgcn_readlane(u, 0)), u2f(__builtin_amdgcn_readlane(u, 16))),
               __builtin_fmaxf(u2f(__builtin_amdgcn_readlane(u, 32)), u2f(__builtin_amdgcn_readlane(u, 48))));
}
DEVI void glds(const void* sbase, unsigned voff, unsigned lds_dst) {
  asm volatile("s_mov_b32 m0, %2\n\ts_nop 0\n\tglobal_load_lds_dwordx4 %0, %1" :: "v"(voff), "s"(sbase), "s"(lds_dst) : "memory");
}
DEVI void glds_sc1(const void* sbase, unsigned voff, unsigned lds_dst) {
  asm volatile("s_mov_b32 m0, %2\n\ts_nop 0\n\tglobal_load_lds_dwordx4 %0, %1 sc1" :: "v"(voff), "s"(sbase), "s"(lds_dst) : "memory");
}
#define GLDS4(SC, sbase_, v0_, v3_, dst_)                                                                                  \
  asm volatile("s_mov_b32 m0, %3\n\ts_nop 0\n\tglobal_load_lds_dwordx4 %0, %2" SC "\n\tglobal_load_lds_dwordx4 %0, %2 offset:1024" SC   \
               "\n\tglobal_load_lds_dwordx4 %0, %2 offset:2048" SC "\n\tglobal_load_lds_dwordx4 %1, %2 offset:3072" SC           \
               :: "v"(v0_), "v"(v3_), "s"(sbase_), "s"(dst_) : "memory")
#define GLDSX(src_, dst_)                                                                                                  \
  asm volatile("s_mov_b32 m0, %5\n\ts_nop 0\n\tglobal_load_lds_dwordx4 %0, %4\n\tglobal_load_lds_dwordx4 %1, %4 offset:1024\n\t"   \
               "global_load_lds_dwordx4 %2, %4 offset:2048\n\tglobal_load_lds_dwordx4 %3, %4 offset:3072"                 \
               :: "v"(vxe), "v"(vxo), "v"(vxe + 16384), "v"(vxo + 16384), "s"(src_), "s"(dst_) : "memory")
#define GLDSX2(src_, dst_)                                                                                                 \
  asm volatile("s_mov_b32 m0, %3\n\ts_nop 0\n\tglobal_load_lds_dwordx4 %0, %2\n\tglobal_load_lds_dwordx4 %1, %2 offset:1024"       \
               :: "v"(vxe), "v"(vxo), "s"(src_), "s"(dst_) : "memory")
#define GLDS3(STEP1, STEP2, v0_, v1_, v2_, src_, dst_)                                                                     \
  asm volatile("s_mov_b32 m0, %4\n\ts_nop 0\n\tglobal_load_lds_dwordx4 %0, %3\n\tglobal_load_lds_dwordx4 %1, %3 offset:" STEP1  \
               "\n\tglobal_load_lds_dwordx4 %2, %3 offset:" STEP2                                                        \
               :: "v"(v0_), "v"(v1_), "v"(v2_), "s"(src_), "s"(dst_) : "memory")
#define TOUCH(src_, v_) asm volatile("s_mov_b32 m0, %2\n\ts_nop 0\n\tglobal_load_lds_dword %0, %1 sc1" :: "v"(v_), "s"(src_), "s"(sbase + L_M + 512) : "memory")
#define VM_WAIT(N) asm volatile("s_waitcnt vmcnt(" #N ")" ::: "memory")
#define BAR() asm volatile("s_waitcnt lgkmcnt(0)\n\ts_barrier" ::: "memory")

DEVI bf16x8 tr_frag(lds_ptr p0, lds_ptr p1) {
  bf16x4 a = __builtin_amdgcn_ds_read_tr16_b64_v4bf16((LAS bf16x4*)p0);
  bf16x4 b = __builtin_amdgcn_ds_read_tr16_b64_v4bf16((LAS bf16x4*)p1);
  return __builtin_shufflevector(a, b, 0, 1, 2, 3, 4, 5, 6, 7);
}
DEVI bf16x8 lds_frag(lds_ptr p) { return *(LAS bf16x8*)p; }
DEVI void lds_signal(lds_ptr p, unsigned n = 1u) {
  asm volatile("s_waitcnt lgkmcnt(0)\n\ts_mov_b64 exec, 1\n\tds_add_u32 %0, %1\n\ts_mov_b64 exec, -1" :: "v"(p), "v"(n) : "memory");
}
DEVI unsigned lds_ticket(lds_ptr p) {
  unsigned old;
  asm volatile("s_waitcnt lgkmcnt(0)\n\ts_mov_b64 exec, 1\n\tds_add_rtn_u32 %0, %1, %2\n\ts_mov_b64 exec, -1\n\ts_waitcnt lgkmcnt(0)"
               : "=&v"(old) : "v"(p), "v"(1u) : "memory");
  return __builtin_amdgcn_readfirstlane(old);
}
template <int SLEEP = 0>
DEVI void lds_wait(lds_ptr p, unsigned n) {
  unsigned v, t;
  asm volatile("1:\n\tds_read_b32 %0, %2\n\ts_waitcnt lgkmcnt(0)\n\tv_readfirstlane_b32 %1, %0\n\ts_nop 0\n\ts_cmp_ge_u32 %1, %3\n\t"
               "s_cbranch_scc1 2f\n\ts_sleep %4\n\ts_branch 1b\n2:"
               : "=&v"(v), "=&s"(t) : "v"(p), "s"(n), "i"(SLEEP) : "scc", "memory");
}
DEVI unsigned lds_peek(lds_ptr p) {
  unsigned v;
  asm volatile("ds_read_b32 %0, %1\n\ts_waitcnt lgkmcnt(0)" : "=&v"(v) : "v"(p) : "memory");
  return __builtin_amdgcn_readfirstlane(v);
}
#define LD_SC1_16(off) __builtin_bit_cast(u32x4, __builtin_amdgcn_raw_buffer_load_b128(rs, (off), 0, 16))
#define ST_SC1_16(v, off) __builtin_amdgcn_raw_buffer_store_b128((v), rs, (off), 0, 16)
#define ST_SC1_8(v, off) __builtin_amdgcn_raw_buffer_store_b64((v), rs, (off), 0, 16)
#define ST_SC1_4(v, off) __builtin_amdgcn_raw_buffer_store_b32((v), rs, (off), 0, 16)

DEVI f32x16 lo16(f32x32 v) { return __builtin_shufflevector(v, v, 0, 1, 2, 3, 4, 5, 6, 7, 8, 9, 10, 11, 12, 13, 14, 15); }
DEVI f32x16 hi16(f32x32 v) { return __builtin_shufflevector(v, v, 16, 17, 18, 19, 20, 21, 22, 23, 24, 25, 26, 27, 28, 29, 30, 31); }
DEVI bf16x16 raw_lo(f32x16 v) { return __builtin_bit_cast(bf16x16, __builtin_shufflevector(v, v, 0, 1, 2, 3, 4, 5, 6, 7)); }
DEVI bf16x16 raw_hi(f32x16 v) { return __builtin_bit_cast(bf16x16, __builtin_shufflevector(v, v, 8, 9, 10, 11, 12, 13, 14, 15)); }
constexpr int SPIDX = (int)0xddddddddu;
#define MFMA32(a, b, c) __builtin_amdgcn_mfma_f32_32x32x16_bf16((a), (b), (c), 0, 0, 0)
#define MFMA16(a, b, c) __builtin_amdgcn_mfma_f32_16x16x32_bf16((a), (b), (c), 0, 0, 0)

DEVI void online_step(f32x16 s, float& m, f32x16& o, bf16x8 va, bf16x8 vb) {
  float t = s[0];
#pragma unroll
  for (int i = 1; i < 16; ++i) t = __builtin_fmaxf(t, s[i]);
  t = __builtin_fmaxf(t, xhalf(t));
  const float mn = __builtin_fmaxf(m, t);
  const float al = __builtin_amdgcn_exp2f(m - mn);
  m = mn;
#pragma unroll
  for (int i = 0; i < 16; ++i) { o[i] *= al; s[i] = __builtin_amdgcn_exp2f(s[i] - mn); }
  o = __builtin_amdgcn_smfmac_f32_32x32x32_bf16(va, raw_lo(s), o, SPIDX, 0, 0);
  o = __builtin_amdgcn_smfmac_f32_32x32x32_bf16(vb, raw_hi(s), o, SPIDX, 0, 0);
}

#define UNIT_LOAD(CP, TG)                                                                                        \
  const int uc0 = 72 * hg + 2 * (CP), ut0 = 64 * qt + 8 * (TG);                                                  \
  const u32x4 usw = *(const u32x4*)(shk + uc0 * 4);                                                              \
  unsigned ukr[11], uvr[8];                                                                                      \
  {                                                                                                              \
    const unsigned bo = (ut0 * D_ + uc0) * 2, bo3 = ut0 ? bo - 3 * 1152 : bo;                                    \
    _Pragma("unroll") for (int i = 0; i < 3; ++i) ukr[i] = *(const unsigned*)((const char*)bk + bo3 + i * 1152); \
    _Pragma("unroll") for (int i = 0; i < 8; ++i) {                                                              \
      ukr[3 + i] = *(const unsigned*)((const char*)bk + bo + i * 1152);                                          \
      uvr[i] = *(const unsigned*)((const char*)bv + bo + i * 1152);                                              \
    }                                                                                                            \
  }
#define UNIT_DO()                                                                                                \
  u32x4 o0, o1;                                                                                                  \
  {                                                                                                              \
    const f32x2 w3 = {bf_hi(usw[1]), bf_hi(usw[3])}, w2 = {bf_lo(usw[1]), bf_lo(usw[3])};                        \
    const f32x2 w1 = {bf_hi(usw[0]), bf_hi(usw[2])}, w0 = {bf_lo(usw[0]), bf_lo(usw[2])};                        \
    f32x2 kf[11], r[8];                                                                                          \
    _Pragma("unroll") for (int i = 0; i < 11; ++i) {                                                             \
      const unsigned kk = (i < 3 && !ut0) ? 0u : ukr[i];                                                         \
      kf[i] = (f32x2){bf_lo(kk), bf_hi(kk)};                                                                     \
    }                                                                                                            \
    _Pragma("unroll") for (int i = 0; i < 8; ++i)                                                                \
      r[i] = __builtin_elementwise_fma(kf[i + 3], w0, __builtin_elementwise_fma(kf[i + 2], w1,                   \
             __builtin_elementwise_fma(kf[i + 1], w2, kf[i] * w3))) * (f32x2){bf_lo(uvr[i]), bf_hi(uvr[i])};     \
    _Pragma("unroll") for (int i = 0; i < 4; ++i) {                                                              \
      o0[i] = pk2(r[7 - 2 * i][0], r[6 - 2 * i][0]);                                                             \
      o1[i] = pk2(r[7 - 2 * i][1], r[6 - 2 * i][1]);                                                             \
    }                                                                                                            \
  }

#define CV_LD(F, D) _Pragma("unroll") for (int s = 0; s < 2; ++s) {                                              \
    F[4 * s] = lds_frag(ab + 32 * (2 * (D) + 1 - s)); F[4 * s + 1] = lds_frag(bb + 80 * (7 - (D)) + 32 * s);     \
    F[4 * s + 2] = lds_frag(bb + 80 * (39 - (D)) + 32 * s); F[4 * s + 3] = lds_frag(ab + 32 * (2 * (D) + 65 - s)); } \
  __builtin_amdgcn_sched_barrier(0)
#define CV_MM(F) _Pragma("unroll") for (int s = 0; s < 2; ++s) {                                                 \
    y0 = MFMA32(F[4 * s], F[4 * s + 1], y0); y1 = MFMA32(F[4 * s], F[4 * s + 2], y1);                            \
    y1 = MFMA32(F[4 * s + 3], F[4 * s + 1], y1); }                                                               \
  __builtin_amdgcn_sched_barrier(0)
#define SB0 __builtin_amdgcn_sched_barrier(0)
#define CV_STEP(F, N, D) _Pragma("unroll") for (int s = 0; s < 2; ++s) {                                         \
    y0 = MFMA32(F[4 * s], F[4 * s + 1], y0); SB0;                                                                \
    N[4 * s] = lds_frag(ab + 32 * (2 * (D) + 1 - s)); N[4 * s + 1] = lds_frag(bb + 80 * (7 - (D)) + 32 * s); SB0; \
    y1 = MFMA32(F[4 * s], F[4 * s + 2], y1); SB0;                                                                \
    N[4 * s + 2] = lds_frag(bb + 80 * (39 - (D)) + 32 * s); SB0;                                                 \
    y1 = MFMA32(F[4 * s + 3], F[4 * s + 1], y1); SB0;                                                            \
    N[4 * s + 3] = lds_frag(ab + 32 * (2 * (D) + 65 - s)); SB0; }
#define CV_PUT(P) _Pragma("unroll") for (int g = 0; g < 4; ++g) {                                                \
    *(LAS f32x4*)((P) + g * 1024) = (f32x4){y0[4 * g], y0[4 * g + 1], y0[4 * g + 2], y0[4 * g + 3]};             \
    *(LAS f32x4*)((P) + 4096 + g * 1024) = (f32x4){y1[4 * g], y1[4 * g + 1], y1[4 * g + 2], y1[4 * g + 3]}; }
DEVI f32x4 cv_sum(lds_ptr p, lds_ptr u, float dv) {
  const f32x4 s = (*(LAS f32x4*)(p) + *(LAS f32x4*)(p + 8192)) + (*(LAS f32x4*)(p + 16384) + *(LAS f32x4*)(p + 24576));
  const u32x2 uu = *(LAS u32x2*)u;
  return (f32x4){__builtin_fmaf(bf_hi(uu[1]), dv, s[0]), __builtin_fmaf(bf_lo(uu[1]), dv, s[1]), __builtin_fmaf(bf_hi(uu[0]), dv, s[2]), __builtin_fmaf(bf_lo(uu[0]), dv, s[3])};
}

__global__ void __launch_bounds__(768, 3) NOPK
fused_kernel(const __bf16* __restrict__ x, const __bf16* __restrict__ wq, const __bf16* __restrict__ wk,
             const __bf16* __restrict__ wv, const __bf16* __restrict__ wo, const __bf16* __restrict__ bk,
             const __bf16* __restrict__ bv, const __bf16* __restrict__ shk, const __bf16* __restrict__ ssmk,
             const __bf16* __restrict__ ssmd, const __bf16* __restrict__ gw, __bf16* __restrict__ out, char* ws) {
  __shared__ __attribute__((aligned(16))) char smem[160 * 1024];
  STAMP_DECL; STAMP(0);
  const int tid = TIDX, lane = tid & 63;
  const int w = __builtin_amdgcn_readfirstlane(tid >> 6);
  const int bid = BIDX, jj = bid >> 3;
  const unsigned sbase = (unsigned)(size_t)smem;
  if (w == 0) {
    unsigned long long pc0;
    unsigned len;
    asm volatile("s_getpc_b64 %0\n\tfk_pc:\n\ts_mov_b32 %1, fk_end-fk_pc" : "=s"(pc0), "=s"(len));
    TOUCH(pc0, UMIN((unsigned)(lane * 8 + (jj & 7)) * 128u, len));
  }
  const int qt = (bid & 7) * 4 + (jj & 3), hg = jj >> 2;
  lds_ptr sm = (lds_ptr)smem;
  lds_ptr mi = sm + L_M;
  const int h = lane >> 5, l31 = lane & 31, l15 = lane & 15, g4 = lane >> 4;
  const int tq = (lane & 15) >> 2, tp = lane & 3;
  const auto rs = __builtin_amdgcn_make_buffer_rsrc(ws, 0, WS_END, 0x00020000);
  GAS char* gws = (GAS char*)ws;
  const int rot = (hg + qt) % 3;
  const int s0 = bid & 7;
  const int tq4 = hg >> 1, sel = hg & 1;
  const char* xt = (const char*)x + (size_t)qt * 73728;
  const char* wqs = (const char*)wq + 144 * hg;
  const unsigned vxe = (lane >> 3) * 1152 + (((lane & 7) ^ g4) << 4), vxo = 8192 + (lane >> 3) * 1152 + (((lane & 7) ^ (4 + g4)) << 4);
  const int otok = tid / 9, opc = tid - 9 * otok;

  u32x4 zv, xv;

  if (w == 4) *(LAS int*)(mi + lane * 4) = (lane == M_RDY / 4 + 2 || lane == M_RDY / 4 + 3) ? -8 : (lane == 0 ? -1 : 0);
  if (w >= 8 && w < 11) {
    const int j = w - 8;
    const int th = (j + rot) % 3;
    const unsigned stg = L_B + 2 * 27648 + j * 9216;
#pragma unroll
    for (int c = 0; c < 3; ++c) GLDSX2(xt + tq4 * 18432 + (3 * th + c) * 128, sbase + L_X + (3 * j + c) * 8192 + tq4 * 2048);
    {
      const char* wsrc = (const char*)(sel ? wv : wk) + th * 6912 - 2048;
      unsigned vo[3];
#pragma unroll
      for (int i = 0; i < 3; ++i) {
        const int f = 64 * i + lane, row = f / 3, pc = f - 3 * row;
        vo[i] = row * 36 + (pc == 2 ? 20 : 16 * pc) + 2048 - 1024 * i;
      }
#pragma unroll
      for (int p = 0; p < 3; ++p) GLDS3("1024", "2048", vo[0], vo[1], vo[2], wsrc + p * 2304, sbase + stg + p * 3072);
    }
  }
  unsigned epoch = 0;
  if (w == 11) epoch = __hip_atomic_load((gu32*)(gws + WS_EPOCH), RLX_AGENT);
  PSTAMP(5);
  BAR();
  if (w >= 1 && w < 4) TOUCH((const char*)wq + (jj * 162 + (w - 1) * 54) * 128, UMIN(lane, 53) * 128u);
  PSTAMP(6);
  if (w >= 5 && w < 8) {
    const int wi = w - 5;
    unsigned vw[3];
#pragma unroll
    for (int m = 0; m < 3; ++m) {
      const int f = 64 * (3 * wi + m) + lane, row = f / 9;
      vw[m] = row * 1152 + (f - 9 * row) * 16 - 1024 * m;
    }
    const char* xs = xt + (1 - (tq4 >> 1)) * 36864 + wi * 128;
    const char* xs2 = xt + (tq4 ^ 1) * 18432 + wi * 128;
    const unsigned xd = sbase + L_X + wi * 8192 + (1 - (tq4 >> 1)) * 4096, xd2 = sbase + L_X + wi * 8192 + (tq4 ^ 1) * 2048;
    const unsigned wd = sbase + L_B + wi * 3072;
    const int t1 = (rot + 1) % 3, t2 = (rot + 2) % 3;
    GLDSX(xs + rot * 384, xd);
    GLDSX2(xs2 + rot * 384, xd2);
#pragma unroll
    for (int per = 0; per < 3; ++per) GLDS3("1024", "2048", vw[0], vw[1], vw[2], wqs + (192 * rot + 64 * per) * 1152, wd + per * 9216);
    GLDSX(xs + t1 * 384, xd + 24576);
    GLDSX2(xs2 + t1 * 384, xd2 + 24576);
    GLDS3("1024", "2048", vw[0], vw[1], vw[2], wqs + 192 * t1 * 1152, wd + 27648);
    VM_WAIT(9);
    lds_signal(mi + M_C1);
#pragma unroll
    for (int per = 1; per < 3; ++per) GLDS3("1024", "2048", vw[0], vw[1], vw[2], wqs + (192 * t1 + 64 * per) * 1152, wd + 27648 + per * 9216);
    GLDSX(xs + t2 * 384, xd + 49152);
    GLDSX2(xs2 + t2 * 384, xd2 + 49152);
    lds_wait(mi + M_KVC, 3);
    GLDS3("1024", "2048", vw[0], vw[1], vw[2], wqs + 192 * t2 * 1152, wd + 55296);
    VM_WAIT(9);
    lds_signal(mi + M_C2);
#pragma unroll
    for (int per = 1; per < 3; ++per) GLDS3("1024", "2048", vw[0], vw[1], vw[2], wqs + (192 * t2 + 64 * per) * 1152, wd + 55296 + per * 9216);
    STAMP(1);
    VM_WAIT(0);
    lds_signal(mi + M_C3);
    lds_signal(mi + M_SE);
  }

  if (w >= 8) {
    const int j = w - 8, ht = tid - 512;
    __builtin_amdgcn_s_setprio(3);
    if (j < 3) {
      const unsigned stg = L_B + 2 * 27648 + j * 9216;
      lds_ptr wb = sm + stg + (8 * g4 + tq) * 48 + tp * 8;
      lds_ptr wc = sm + stg + (8 * g4 + tq) * 48 + 32 + (tp & 1) * 8;
      lds_ptr xb = sm + L_X + 3 * j * 8192 + (16 * tq4 + l15) * 128;
      VM_WAIT(0);
      STAMP(1);
      bf16x8 fa[6], fc[6], fb[6];
#pragma unroll
      for (int k = 0; k < 6; ++k) {
        fb[k] = lds_frag(xb + (k >> 1) * 8192 + (((4 * (k & 1) + g4) ^ (l15 >> 1)) << 4));
        fa[k] = tr_frag(wb + k * 1536, wb + k * 1536 + 192);
        fc[k] = tr_frag(wc + k * 1536, wc + k * 1536 + 192);
      }
      f32x4 acc = {0}, acr = {0};
      if (sel) {
#pragma unroll
        for (int k = 0; k < 6; ++k) { acc = MFMA16(fb[k], fa[k], acc); acr = MFMA16(fb[k], fc[k], acr); }
      } else {
#pragma unroll
        for (int k = 0; k < 6; ++k) { acc = MFMA16(fa[k], fb[k], acc); acr = MFMA16(fc[k], fb[k], acr); }
      }
      *(LAS f32x4*)(sm + L_KVP + j * 2048 + lane * 16) = acc;
      *(LAS f32x4*)(sm + L_KVP + j * 2048 + 1024 + lane * 16) = acr;
      lds_signal(mi + M_KVC);
      lds_signal(mi + M_C1);
      STAMP(2);
      lds_wait<1>(mi + M_KD, 1);
    } else {
      const unsigned tg = __builtin_amdgcn_readfirstlane(epoch) + 1u;
      if (lane == 0) *(LAS unsigned*)(mi + M_TAG) = tg;
      const int T = 2 * qt + (tq4 >> 1), mt = tq4 & 1, i4 = T & 3, gb = (T >> 2) * KV_GROUP, rp = swap23(16 * mt + l15);
      int sa, sr;
      bool st;
      if (sel) {
        sa = gb + 4096 + i4 * 1216 + mt * 608 + (l15 + 18 * (g4 & 1)) * 16 + (g4 >> 1) * 8;
        sr = gb + 4096 + i4 * 1216 + mt * 608 + (l15 < 8 ? 10 + l15 + 18 * (g4 & 1) : 36 + (g4 & 1)) * 16 + (g4 >> 1) * 8;
        st = l15 >= 6 && l15 <= 8;
      } else {
        sa = gb + i4 * 1024 + (rp + 32 * (g4 & 1)) * 16 + (g4 >> 1) * 8;
        sr = gb + 8960 + i4 * 256 + rp * 8;
        st = g4 == 1;
      }
      lds_wait(mi + M_KVC, 3);
      lds_ptr pp = sm + L_KVP + lane * 16;
      const f32x4 va = *(LAS f32x4*)(pp) + *(LAS f32x4*)(pp + 2048) + *(LAS f32x4*)(pp + 4096);
      const f32x4 vr = *(LAS f32x4*)(pp + 1024) + *(LAS f32x4*)(pp + 3072) + *(LAS f32x4*)(pp + 5120);
      const u32x2 a = {pk2(va[0], va[1]), pk2(va[2], va[3])};
      u32x2 r = {pk2(vr[0], vr[1]), pk2(vr[2], vr[3])};
      if (!sel) r = (u32x2){r[1], 0x3f803f80u};
      else if (l15 == 8) r[0] = r[1] = (g4 & 1) ? 0u : 0x3f803f80u;
      ST_SC1_16(((u32x4){a[0], tg, a[1], tg}), WS_KT + 2 * sa);
      if (st) ST_SC1_16(((u32x4){r[0], tg, r[1], tg}), WS_KT + 2 * sr);
      ST_SC1_8(a, WS_KV + sa);
      if (st) ST_SC1_8(r, WS_KV + sr);
      STAMP(1);
      lds_signal(mi + M_KD);
      float n2 = dot2(a[1], dot2(a[0], st ? dot2(r[0], 0.f) : 0.f));
      n2 += xhalf(n2);
      n2 += u2f(__builtin_amdgcn_ds_swizzle(__builtin_bit_cast(int, n2), 0x401f));
      n2 = sel ? 0.f : max16(n2);
      VM_WAIT(0);
      if (lane == 0)
        __hip_atomic_store((gu64*)(gws + WS_FKV + T * 128 + (hg & 3) * 8), ((unsigned long long)tg << 32) | __builtin_bit_cast(unsigned, n2), RLX_AGENT);
      STAMP(2);
    }
    const unsigned tag = *(LAS unsigned*)(mi + M_TAG);
    auto kv_tagged = [&](int st) LAMI {
      u32x4 r[10];
      const int o = WS_KT + ((s0 + st) & 7) * (2 * RSLOT) + lane * 16;
#pragma unroll 1
      for (unsigned spins = 0; spins < SPIN_CAP; ++spins) {
        unsigned bad = 0;
#pragma unroll
        for (int i = 0; i < 10; ++i) r[i] = LD_SC1_16(o + (i == 9 && j == 3 ? 38 : 4 * i + j) * 1024);
#pragma unroll
        for (int i = 0; i < 10; ++i) bad |= (r[i][1] ^ tag) | (r[i][3] ^ tag);
        asm volatile("" ::: "memory");
        if (ALL(bad == 0)) break;
        __builtin_amdgcn_s_sleep(POLL_SLEEP);
      }
      if (st == 0) lds_wait(mi + M_C3B, 5);
#pragma unroll
      for (int i = 0; i < 10; ++i)
        *(LAS u32x2*)(sm + st * RSLOT + (i == 9 && j == 3 ? 38 : 4 * i + j) * 512 + lane * 8) = (u32x2){r[i][0], r[i][2]};
    };
    auto kv_dma = [&](int st) LAMI {
      const char* src = ws + WS_KV + ((s0 + st) & 7) * RSLOT + j * 4992;
      const unsigned dst = sbase + st * RSLOT + j * 4992;
      GLDS4(" sc1", src, lane * 16, lane * 16, dst);
      if (lane < 56) glds_sc1(src + 4096, lane * 16, dst + 4096);
    };
    const unsigned vh3 = (lane == 63 ? 62 : lane) * 16;
    auto hload = [&](int cl, int b) LAMI {
      const char* hsrc = (const char*)(ssmk + (size_t)(72 * hg + cl) * S_) + 4 * j + 2;
      const unsigned dst = sbase + CV_H + b * CV_HB + 2 * j * CV_COPY + 64;
      GLDS4("", hsrc, lane * 16, vh3, dst);
      GLDS4("", hsrc + 2, lane * 16, vh3, dst + CV_COPY);
    };
    auto uload = [&](int cl) LAMI {
      const int tgq = ht & 7;
      const int o = cl < 64 ? (tgq >> 1) * 2048 + (cl & 1) * 1024 + ((tgq & 1) * 32 + (cl >> 1)) * 16
                            : 8192 + (cl & 1) * 512 + (tgq * 4 + ((cl - 64) >> 1)) * 16;
      return LD_SC1_16(WS_UT + ((ht >> 3) * 8 + hg) * 9216 + o);
    };
    const int ce = 64 + (qt >> 2);
    const unsigned short h8a = *(const unsigned short*)(ssmk + (size_t)(72 * hg + 2 * qt + (j >> 1)) * S_ + (lane & 7));
    const unsigned short h8e = *(const unsigned short*)(ssmk + (size_t)(72 * hg + ce) * S_ + (lane & 7));
    const float dve = (qt & 3) ? 0.f : (float)ssmd[72 * hg + ce];
    {
      lds_wait<1>(mi + POLL_AT, POLL_N);
      kv_tagged(0);
      lds_signal(mi + M_QD);
      STAMP(3);
      kv_tagged(1);
      lds_signal(mi + M_RDY + 4, 2);
      if (j == 3) {
        float kn = 0.f;
#pragma unroll 1
        for (unsigned spins = 0; spins < SPIN_CAP; ++spins) {
          const u32x4 v0 = LD_SC1_16(WS_FKV + lane * 128), v1 = LD_SC1_16(WS_FKV + lane * 128 + 16);
          const bool ok = v0[1] == tag && v0[3] == tag && v1[1] == tag && v1[3] == tag;
          kn = __builtin_fmaxf(u2f(v0[0]), u2f(v1[0]));
          asm volatile("" ::: "memory");
          if (ALL(ok)) break;
          __builtin_amdgcn_s_sleep(POLL_SLEEP);
        }
        lds_signal(mi + M_QR);
        lds_signal(mi + M_DONE);
        kn = wave_max(kn);
        if (lane == 0) *(LAS float*)(mi + M_KN) = kn;
      }
      lds_wait<1>(mi + M_QR, 9);
      kv_dma(2);
      kv_dma(3);
      UNIT_LOAD(l31, 2 * j + h);
      u32x4 gx[3], gv[3];
#pragma unroll
      for (int i = 0; i < 3; ++i) {
        const int cg = UMIN(l31 + 32 * i, 71) * 16;
        gx[i] = *(const u32x4*)(xt + (8 * hg + 2 * j + h) * 1152 + cg);
        gv[i] = *(const u32x4*)((const char*)gw + cg);
      }
      if (j < 3) TOUCH((const char*)wo + (jj * 162 + j * 54) * 128, UMIN(lane, 53) * 128u);
      else TOUCH(ssmk + (size_t)(72 * hg + 2 * qt) * S_, lane * 128u);
      VM_WAIT(20);
      lds_signal(mi + M_RDY + 8, 2);
      lds_signal(mi + M_RDY + 12, 2);
      if (j == 1) {
        UNIT_LOAD(32 + (lane & 3), l31 >> 2);
        UNIT_DO();
        ST_SC1_16(h ? o1 : o0, WS_UT + (qt * 8 + hg) * 9216 + 8192 + lane * 16);
      }
      UNIT_DO();
      {
        float lg = 0.f;
#pragma unroll
        for (int i = 2; i >= 0; --i) {
#pragma unroll
          for (int e = 0; e < 4; ++e)
            lg = __builtin_amdgcn_fdot2_f32_bf16(__builtin_bit_cast(bf16x2, (unsigned)gx[i][e]), __builtin_bit_cast(bf16x2, (unsigned)gv[i][e]), lg, false);
          if (i == 2 && l31 >= 8) lg = 0.f;
        }
        lg = sum16(lg);
        lg += u2f(__builtin_amdgcn_ds_swizzle(__builtin_bit_cast(int, lg), 0x401f));
        const float g = rbf(__builtin_amdgcn_rcpf(1.f + __builtin_amdgcn_exp2f(-1.4426950408889634f * rbf(lg))));
        if (l31 == 0) ST_SC1_4(__builtin_bit_cast(unsigned, g), WS_G + (64 * qt + 8 * hg + 2 * j + h) * 4);
      }
      ST_SC1_16(o0, WS_UT + (qt * 8 + hg) * 9216 + j * 2048 + lane * 16);
      ST_SC1_16(o1, WS_UT + (qt * 8 + hg) * 9216 + j * 2048 + 1024 + lane * 16);
      {
        const u32x4 ones = {~0u, ~0u, ~0u, ~0u};
        const int ob = WS_O + 2 * qt * 36 * 1024 + (4 * hg + j) * 1024 + lane * 16;
        ST_SC1_16(ones, ob);
        ST_SC1_16(ones, ob + 36 * 1024);
        if (j == 0) ST_SC1_16(ones, WS_O + (2 * qt + h) * 36 * 1024 + 32768 + hg * 512 + l31 * 16);
        ST_SC1_16(ones, WS_Z + (72 * hg + 2 * qt) * 4096 + ht * 16);
        ST_SC1_16(ones, WS_Z + (72 * hg + 2 * qt + 1) * 4096 + ht * 16);
      }
      STAMP(4);
      VM_WAIT(0);
      if (lds_ticket(mi + M_UD) == 3u && lane == 0) __hip_atomic_store((gu32*)(gws + WS_FU) + (hg * 32 + qt) * 32, tag, RLX_AGENT);
    }
    auto cvprep = [&](int b, unsigned short h8) LAMI {
      *(LAS u32x4*)(sm + CV_U + (ht >> 7) * CV_UB + ((ht & 127) >> 2) * 80 + (ht & 3) * 16) = (u32x4){0, 0, 0, 0};
      lds_ptr hz = sm + CV_H + b * CV_HB + (lane >> 3) * CV_COPY;
      *(LAS u32x2*)(hz + (lane & 7) * 8) = (u32x2){0, 0};
      if ((lane & 7) <= (lane >> 3)) *(LAS unsigned short*)(hz + 48 + (7 - (lane >> 3) + (lane & 7)) * 2) = h8;
    };
    unsigned hs = 0;
#define HSYNC() do { lds_signal(mi + M_HS); hs += 4; lds_wait<2>(mi + M_HS, hs); } while (0)
    if (lane < 54) {
      const unsigned vo = (lane / 9) * 1152 + (lane % 9) * 16;
#pragma unroll 1
      for (int p = 24 * j; p < 24 * j + 24; p += 4)
        asm volatile("s_mov_b32 m0, %5\n\ts_nop 0\n\tglobal_load_lds_dwordx4 %0, %4\n\tglobal_load_lds_dwordx4 %1, %4 offset:864\n\t"
                     "global_load_lds_dwordx4 %2, %4 offset:1728\n\tglobal_load_lds_dwordx4 %3, %4 offset:2592\n\ts_waitcnt vmcnt(" WO_INFL ")"
                     :: "v"(vo), "v"(vo + 6048), "v"(vo + 12096), "v"(vo + 18144), "s"((const char*)wo + 144 * hg + p * 6912),
                        "s"(sbase + L_B + p * 864) : "memory");
    }
    VM_WAIT(0);
    HSYNC();
    bf16x8 wf[18], we[5];
    {
      const int cb = (32 * (j & 1) + 16 * (g4 & 1) + 4 * tp) * 2;
      lds_ptr wm = sm + L_B + (DH * (j >> 1) + 4 * h + tq) * 144 + cb;
      lds_ptr wr = sm + L_B + (DH * (8 * (j >> 1) + 4 * h + (tq >> 1)) + 16 + (tq & 1)) * 144 + cb;
#pragma unroll
      for (int k = 0; k < 12; ++k) wf[k] = tr_frag(wm + k * 5184, wm + k * 5184 + 1152);
      wm += 12 * 5184;
#pragma unroll
      for (int k = 0; k < 4; ++k) wf[12 + k] = tr_frag(wm + k * 5184, wm + k * 5184 + 1152);
#pragma unroll
      for (int k = 0; k < 2; ++k) wf[16 + k] = tr_frag(wr + k * 41472, wr + k * 41472 + 5184);
      lds_ptr em = sm + L_B + (DH * (8 * j + (g4 >> 1)) + 4 * (g4 & 1) + tq) * 144 + (64 + 4 * (tp & 1)) * 2;
      lds_ptr er = sm + L_B + (DH * (16 * (j & 1) + 4 * g4 + (tq >> 1)) + 16 + (tq & 1)) * 144 + (64 + 4 * (tp & 1)) * 2;
#pragma unroll
      for (int k = 0; k < 4; ++k) we[k] = tr_frag(em + k * 5184, em + k * 5184 + 1152);
      we[4] = tr_frag(er, er + 5184);
    }
    HSYNC();
    HSTAMP(7);
    hload(ce, 0);
    cvprep(0, h8e);
    if (j == 3) {
#pragma unroll 1
      for (unsigned spins = 0; spins < SPIN_CAP; ++spins) {
        bool ok = lane >= 40 || __hip_atomic_load((gu32*)(gws + WS_FU) + (h ? l31 * 32 + qt : hg * 32 + lane) * 32, RLX_AGENT) == tag;
        if (bid == 0) {
#pragma unroll
          for (int i = 0; i < 4; ++i) ok &= __hip_atomic_load((gu32*)(gws + WS_FU) + (64 * i + lane) * 32, RLX_AGENT) == tag;
        }
        if (ALL(ok)) break;
        __builtin_amdgcn_s_sleep(8);
      }
      if (bid == 0 && lane == 0) __hip_atomic_store((gu32*)(gws + WS_EPOCH), tag, RLX_AGENT);
      lds_signal(mi + M_UR);
    } else {
      lds_wait<4>(mi + M_UR, 1);
    }
    NSTAMP(5);
    if (j == 1) *(LAS unsigned*)(mi + M_G + lane * 4) = __builtin_amdgcn_raw_buffer_load_b32(rs, WS_G + (64 * qt + lane) * 4, 0, 16);
    *(LAS u32x4*)(sm + CV_U + 2560 + (ht >> 2) * 80 + (ht & 3) * 16) = uload(ce);
    VM_WAIT(0);
    HSYNC();
    {
      const int d0 = 8 * (qt & 3) + 2 * j;
      lds_ptr ab = sm + CV_H + (l31 & 7) * CV_COPY + (h + (l31 >> 3)) * 16 + 64 * d0;
      lds_ptr bb = sm + CV_U + 2560 + (l31 - d0 - 7) * 80 + (1 - h) * 16;
      asm volatile("" : "+v"(ab), "+v"(bb));
      f32x16 y0 = {0}, y1 = {0};
#pragma unroll
      for (int d = 0; d < 2; ++d) {
#pragma unroll
        for (int s = 0; s < 2; ++s) {
          const bf16x8 a0 = lds_frag(ab + 32 * (2 * d + 1 - s)), b0 = lds_frag(bb + 80 * (7 - d) + 32 * s);
          const bf16x8 b1 = lds_frag(bb + 80 * (39 - d) + 32 * s), a1 = lds_frag(ab + 32 * (2 * d + 65 - s));
          y0 = MFMA32(a0, b0, y0);
          y1 = MFMA32(a0, b1, y1);
          y1 = MFMA32(a1, b0, y1);
          SB0;
        }
      }
      CV_PUT(sm + CV_H + CV_HB + j * 8192 + lane * 16);
      HSYNC();
#pragma unroll
      for (int q = 0; q < 2; ++q)
        ST_SC1_16(__builtin_bit_cast(u32x4, cv_sum(sm + CV_H + CV_HB + (2 * j + q) * 1024 + lane * 16,
                                                   sm + CV_U + 2560 + (32 * (j >> 1) + l31) * 80 + (2 * (j & 1) + q) * 16 + (1 - h) * 8, dve)),
                  WS_ZP + (hg * 32 + qt) * 8192 + (2 * j + q) * 1024 + lane * 16);
    }
    HSYNC();
    NSTAMP(6);
    hload(2 * qt, 0);
    hload(2 * qt + 1, 1);
    cvprep(j >> 1, h8a);
    *(LAS u32x4*)(sm + CV_U + 2560 + (ht >> 2) * 80 + (ht & 3) * 16) = uload(2 * qt);
    *(LAS u32x4*)(sm + CV_U + CV_UB + 2560 + (ht >> 2) * 80 + (ht & 3) * 16) = uload(2 * qt + 1);
    VM_WAIT(0);
#pragma unroll
    for (int i = 0; i < 4; ++i) lds_signal(mi + M_PAIR + 4 * i);
    if (lds_ticket(mi + M_CVR) == 3u && lane == 0) __hip_atomic_store((gu32*)(gws + WS_FZ) + (hg * 32 + qt) * 32, tag, RLX_AGENT);
    if (j == 0) {
#pragma unroll 1
      for (unsigned spins = 0; spins < SPIN_CAP; ++spins) {
        if (ALL(h || __hip_atomic_load((gu32*)(gws + WS_FZ) + (hg * 32 + lane) * 32, RLX_AGENT) == tag)) break;
        __builtin_amdgcn_s_sleep(16);
      }
      asm volatile("" ::: "memory");
      f32x4 sa = {0}, sb = {0};
#pragma unroll
      for (int p = 0; p < 4; ++p) {
        const int zo = WS_ZP + (hg * 32 + (lane >> 3) * 4 + p) * 8192 + (qt >> 4) * 4096 + ((lane >> 2) & 1) * 2048 +
                       (2 * (qt & 15) + (lane & 1) + 32 * ((lane >> 1) & 1)) * 16;
        sa += __builtin_bit_cast(f32x4, LD_SC1_16(zo));
        sb += __builtin_bit_cast(f32x4, LD_SC1_16(zo + 1024));
      }
      zv = (u32x4){pk2(sa[0], sa[1]), pk2(sa[2], sa[3]), pk2(sb[0], sb[1]), pk2(sb[2], sb[3])};
      xv = *(const u32x4*)(xt + otok * 1152 + 144 * hg + opc * 16);
    }
    HSTAMP(8);
    lds_wait<8>(mi + M_DONE + 28, 8);
    lds_wait<1>(mi + M_OR, 4);
    NSTAMP(9);
    __builtin_amdgcn_s_setprio(0);
    {
      f32x16 a0 = {0}, a1 = {0};
      f32x4 ae[4] = {{0}, {0}, {0}, {0}};
      lds_ptr oe = sm + (8 * j + (g4 >> 1)) * 1024 + (l15 + 32 * (g4 & 1)) * 16;
      lds_ptr or_ = sm + (36 * (j >> 1) + 32 + 2 * (j & 1) + (g4 >> 1)) * 1024 + (l15 + 32 * (g4 & 1)) * 16;
      {
        const f32x4 r0 = MFMA16(we[4], lds_frag(or_), ae[0]), r1 = MFMA16(we[4], lds_frag(or_ + 256), ae[0]);
        if (j >> 1) { ae[2] = r0; ae[3] = r1; } else { ae[0] = r0; ae[1] = r1; }
      }
#pragma unroll
      for (int t = 0; t < 4; ++t) {
#pragma unroll
        for (int k = 0; k < 4; ++k) ae[t] = MFMA16(we[k], lds_frag(oe + (t >> 1) * 36864 + (t & 1) * 256 + k * 2048), ae[t]);
        if (t & 1) __builtin_amdgcn_sched_barrier(0);
      }
      lds_ptr ob = sm + (j >> 1) * 1024 + lane * 16;
      bf16x8 f0[4], f1[4];
#define OP_LD(F, K) F[0] = lds_frag(ob + (K) * 2048); F[1] = lds_frag(ob + 36864 + (K) * 2048);               \
      F[2] = lds_frag(ob + (K) * 2048 + 2048); F[3] = lds_frag(ob + 36864 + 2048 + (K) * 2048); __builtin_amdgcn_sched_barrier(0)
#define OP_MM(F, K) a0 = MFMA32(F[0], wf[K], a0); a1 = MFMA32(F[1], wf[K], a1);                               \
      a0 = MFMA32(F[2], wf[(K) + 1], a0); a1 = MFMA32(F[3], wf[(K) + 1], a1); __builtin_amdgcn_sched_barrier(0)
      OP_LD(f0, 0);
#pragma unroll
      for (int k = 0; k < 16; k += 4) {
        OP_LD(f1, 2);
        OP_MM(f0, k);
        OP_LD(f0, 4);
        OP_MM(f1, k + 2);
        ob += 8192;
      }
      OP_MM(f0, 16);
      NSTAMP(10);
      lds_wait<1>(mi + M_CE, 4);
      lds_ptr ap = sm + L_AP + (((j >> 1) * 64 + 4 * h) * 64 + 32 * (j & 1) + l31) * 4;
#pragma unroll
      for (int r = 0; r < 16; ++r) {
        *(LAS float*)(ap + ((r & 3) + 8 * (r >> 2)) * 256) = a0[r];
        *(LAS float*)(ap + 8192 + ((r & 3) + 8 * (r >> 2)) * 256) = a1[r];
      }
      if (!h) {
#pragma unroll
        for (int t = 0; t < 4; ++t) *(LAS f32x4*)(sm + L_AP2 + (j * 64 + 16 * t + l15) * 32 + g4 * 16) = ae[t];
      }
    }
  } else {
    const float QS = 0.23570226039551584f * 1.4426950408889634f;
    if (w < 4) {
      const int gt = w >> 1, tt = w & 1, arow = 32 * tt + l31, xsw = (arow >> 1) & 7;
      lds_ptr xb = sm + L_X + arow * 128;
      lds_ptr wb = sm + L_B + (8 * h + tq) * 144 + (36 * gt + 20 * (g4 & 1) + 4 * tp) * 2;
      f32x16 acc = {0};
#pragma unroll 1
      for (int p = 0; p < 3; ++p) {
        if (p == 1) CSTAMP(6);
        if (p == 2) CSTAMP(8);
        lds_wait(mi + M_C1 + 4 * p, p == 1 ? 3 : 6 + p);
        if (p == 0) CSTAMP(5);
        if (p == 1) CSTAMP(7);
        if (p == 2) CSTAMP(9);
        bf16x8 fa[12], fb[12];
#pragma unroll
        for (int k = 0; k < 16; ++k) {
          if (k < 12) {
            fa[k] = tr_frag(wb + k * 2304, wb + k * 2304 + 576);
            fb[k] = lds_frag(xb + (k >> 2) * 8192 + (((2 * (k & 3) + h) ^ xsw) << 4));
          }
          SB0;
          if (k >= 4) acc = MFMA32(fa[k - 4], fb[k - 4], acc);
          SB0;
        }
        if (p < 2) lds_signal(mi + M_C3 + 4 * p);
        xb += 24576;
        wb += 27648;
      }
      STAMP(2);
      u32x4 a;
#pragma unroll
      for (int i = 0; i < 4; ++i) a[i] = pk2(acc[2 * i] * QS, acc[2 * i + 1] * QS);
      lds_ptr qe = sm + L_Q + (4 * gt + tt) * 1024;
      *(LAS u32x4*)(qe + lane * 16) = a;
      lds_ptr qo = qe + 2048 + l31 * 16;
      *(LAS unsigned*)(qo + h * 512 + 4) = pk2(acc[8] * QS, acc[9] * QS);
      *(LAS unsigned*)(qo + (h ? 8 : 512)) = pk2(acc[10] * QS, acc[11] * QS);
      *(LAS unsigned*)(qo + h * 512 + 12) = pk2(acc[12] * QS, acc[13] * QS);
      *(LAS unsigned*)(h ? sm + L_Q + 8192 + ((2 * gt + 1) * 64 + arow) * 4 : qo + 520) = pk2(acc[14] * QS, acc[15] * QS);
    } else if (w == 4) {
      lds_ptr xb = sm + L_X + l15 * 128;
      lds_ptr wb = sm + L_B + (8 * g4 + tq) * 144 + (tp == 1 ? 104 : 32);
      f32x4 acc[4] = {{0}, {0}, {0}, {0}};
#pragma unroll 1
      for (int p = 0; p < 3; ++p) {
        lds_wait(mi + M_C1 + 4 * p, p == 1 ? 3 : 6 + p);
        bf16x8 fa[6], fb[24];
#pragma unroll
        for (int k = 0; k < 8; ++k) {
          if (k < 6) {
            fa[k] = tr_frag(wb + k * 4608, wb + k * 4608 + 576);
#pragma unroll
            for (int t = 0; t < 4; ++t) fb[4 * k + t] = lds_frag(xb + (k >> 1) * 8192 + t * 2048 + (((4 * (k & 1) + g4) ^ (l15 >> 1)) << 4));
          }
          SB0;
          if (k >= 2) {
#pragma unroll
            for (int t = 0; t < 4; ++t) acc[t] = MFMA16(fa[k - 2], fb[4 * (k - 2) + t], acc[t]);
          }
          SB0;
        }
        if (p < 2) lds_signal(mi + M_C3 + 4 * p);
        xb += 24576;
        wb += 27648;
      }
      STAMP(2);
      if (g4 < 2) {
#pragma unroll
        for (int t = 0; t < 4; ++t) {
          const int tok = 16 * t + l15;
          *(LAS unsigned*)(sm + L_Q + 8192 + (2 * g4 * 64 + tok) * 4) = pk2(acc[t][0] * QS, acc[t][1] * QS);
          *(LAS unsigned*)(sm + L_Q + ((2 * g4 + 1) * 2 + (t >> 1)) * 1024 + (tok & 31) * 16) = pk2(acc[t][2] * QS, acc[t][3] * QS);
        }
      }
    }
    lds_signal(mi + M_QD);
    const int hl = w & 3, kh = w >> 2;
    const int voff = 4096 + (l31 < 18 ? h * 288 + l31 * 16 : (l31 == 18 ? 576 : 592));
    lds_wait(mi + M_QD, 12);
    STAMP(3);
    const u32x4 q0u = *(LAS u32x4*)(sm + L_Q + (2 * hl) * 1024 + lane * 16);
    const u32x4 q1u = *(LAS u32x4*)(sm + L_Q + (2 * hl + 1) * 1024 + lane * 16);
    const unsigned qr = *(LAS unsigned*)(sm + L_Q + 8192 + (hl * 64 + lane) * 4);
    const u32x4 k0u = *(LAS u32x4*)(sm + lane * 16);
    lds_signal(mi + M_QR);
    float n0 = 0.f, n1 = 0.f, kn0 = 0.f;
#pragma unroll
    for (int i = 0; i < 4; ++i) { n0 = dot2(q0u[i], n0); n1 = dot2(q1u[i], n1); kn0 = dot2(k0u[i], kn0); }
    kn0 = wave_max(kn0 + xhalf(kn0));
    const float nq = dot2(qr, (h ? n1 : n0) + xhalf(h ? n0 : n1));
    const u32x2 qrem_u = {qr, pk2(-__builtin_amdgcn_sqrtf(nq * kn0), 0.f)};
    f32x16 o0, o1;
    float m0 = 0.f, m1 = 0.f;
    STAMP(4); RSTAMP(2);
    {
      int cnt = 8, off = 0, tmp, t2, so = ((s0 + 4) & 7) * RSLOT;
      unsigned ca = sbase + L_M + M_DONE - 4, dst = sbase + w * 2496;
      const unsigned long long ksrc = (unsigned long long)ws + WS_KV + w * 2496 - (sbase + kh * KV_GROUP);
      asm volatile(
      "v_mov_b32 v107, v126\n"
      "v_mov_b32 v108, v127\n"
      "v_mov_b32 v109, v128\n"
      "v_mov_b32 v130, 1\n"
      "ds_read2st64_b64 v[110:113], v109 offset0:0 offset1:1\n"
      "ds_read_b128 v[114:117], v107 offset:0\n"
      "ds_read_b128 v[118:121], v108 offset:0\n"
      "s_mov_b32 vcc_lo, %[first]\n"
      "s_mov_b32 vcc_hi, 0\n"
      "s_setprio 1\n"
      "v_mov_b32 v56, 0\n"
      "v_mov_b32 v57, 0\n"
      "v_mov_b32 v58, 0\n"
      "v_mov_b32 v59, 0\n"
      "v_mov_b32 v60, 0\n"
      "v_mov_b32 v61, 0\n"
      "v_mov_b32 v62, 0\n"
      "v_mov_b32 v63, 0\n"
      "v_mov_b32 v122, 0\n"
      "v_mov_b32 v123, 0\n"
      "v_mov_b32 v124, 0\n"
      "v_mov_b32 v125, 0\n"
      "v_mov_b32 v64, 0\n"
      "v_mov_b32 v65, 0\n"
      "v_mov_b32 v66, 0\n"
      "v_mov_b32 v67, 0\n"
      "v_mov_b32 v68, 0\n"
      "v_mov_b32 v69, 0\n"
      "v_mov_b32 v70, 0\n"
      "v_mov_b32 v71, 0\n"
      "v_mov_b32 v72, 0\n"
      "v_mov_b32 v73, 0\n"
      "v_mov_b32 v74, 0\n"
      "v_mov_b32 v75, 0\n"
      "v_mov_b32 v76, 0\n"
      "v_mov_b32 v77, 0\n"
      "v_mov_b32 v78, 0\n"
      "v_mov_b32 v79, 0\n"
      "s_waitcnt lgkmcnt(1)\n"
      "v_mfma_f32_32x32x4_2b_bf16 v[0:31], v[110:111], v[104:105], 0 cbsz:1 abid:0\n"
      "v_mov_b32 v80, 0\n"
      "v_mov_b32 v81, 0\n"
      "v_mov_b32 v82, 0\n"
      "v_mov_b32 v83, 0\n"
      "v_mov_b32 v84, 0\n"
      "v_mov_b32 v85, 0\n"
      "v_mov_b32 v86, 0\n"
      "v_mov_b32 v87, 0\n"
      "v_mov_b32 v88, 0\n"
      "v_mov_b32 v89, 0\n"
      "v_mov_b32 v90, 0\n"
      "v_mov_b32 v91, 0\n"
      "v_mov_b32 v92, 0\n"
      "v_mov_b32 v93, 0\n"
      "v_mov_b32 v94, 0\n"
      "v_mov_b32 v95, 0\n"
      "s_nop 2\n"
      "v_mfma_f32_32x32x16_bf16 v[0:15], v[114:117], v[96:99], v[0:15]\n"
      "v_mfma_f32_32x32x16_bf16 v[16:31], v[114:117], v[100:103], v[16:31]\n"
      "s_nop 11\n"
      "1:\n"
      "s_cbranch_vccz 4f\n"
      "s_setprio 2\n"
      "4:\n"
      "v_exp_f32 v0, v0\n"
      "v_exp_f32 v1, v1\n"
      "v_smfmac_f32_32x32x32_bf16 v[80:95], v[122:125], v[56:63], v106\n"
      "ds_read_b128 v[114:117], v107 offset:1024\n"
      "ds_read_b128 v[122:125], v108 offset:608\n"
      "v_exp_f32 v2, v2\n"
      "v_exp_f32 v3, v3\n"
      "v_exp_f32 v4, v4\n"
      "v_exp_f32 v5, v5\n"
      "v_mfma_f32_32x32x4_2b_bf16 v[32:63], v[110:111], v[104:105], 0 cbsz:1 abid:1\n"
      "v_exp_f32 v6, v6\n"
      "v_exp_f32 v7, v7\n"
      "v_exp_f32 v8, v8\n"
      "v_exp_f32 v9, v9\n"
      "v_exp_f32 v10, v10\n"
      "v_exp_f32 v11, v11\n"
      "v_exp_f32 v12, v12\n"
      "s_waitcnt lgkmcnt(2)\n"
      "v_exp_f32 v13, v13\n"
      "v_smfmac_f32_32x32x32_bf16 v[64:79], v[118:121], v[0:7], v106\n"
      "v_exp_f32 v14, v14\n"
      "v_exp_f32 v15, v15\n"
      "v_exp_f32 v16, v16\n"
      "s_waitcnt lgkmcnt(0)\n"
      "v_exp_f32 v17, v17\n"
      "v_smfmac_f32_32x32x32_bf16 v[64:79], v[122:125], v[8:15], v106\n"
      "v_exp_f32 v18, v18\n"
      "v_exp_f32 v19, v19\n"
      "v_exp_f32 v20, v20\n"
      "v_exp_f32 v21, v21\n"
      "v_mfma_f32_32x32x16_bf16 v[32:47], v[114:117], v[96:99], v[32:47]\n"
      "v_exp_f32 v22, v22\n"
      "v_exp_f32 v23, v23\n"
      "v_exp_f32 v24, v24\n"
      "v_exp_f32 v25, v25\n"
      "v_smfmac_f32_32x32x32_bf16 v[80:95], v[118:121], v[16:23], v106\n"
      "v_exp_f32 v26, v26\n"
      "ds_read_b128 v[118:121], v108 offset:1216\n"
      "v_exp_f32 v27, v27\n"
      "v_exp_f32 v28, v28\n"
      "v_exp_f32 v29, v29\n"
      "v_mfma_f32_32x32x16_bf16 v[48:63], v[114:117], v[100:103], v[48:63]\n"
      "v_exp_f32 v30, v30\n"
      "v_exp_f32 v31, v31\n"
      "v_exp_f32 v32, v32\n"
      "v_exp_f32 v33, v33\n"
      "v_smfmac_f32_32x32x32_bf16 v[80:95], v[122:125], v[24:31], v106\n"
      "ds_read_b128 v[114:117], v107 offset:2048\n"
      "ds_read_b128 v[122:125], v108 offset:1824\n"
      "ds_read_b32 v131, v129\n"
      "v_exp_f32 v34, v34\n"
      "v_exp_f32 v35, v35\n"
      "v_exp_f32 v36, v36\n"
      "v_exp_f32 v37, v37\n"
      "v_mfma_f32_32x32x4_2b_bf16 v[0:31], v[112:113], v[104:105], 0 cbsz:1 abid:0\n"
      "v_exp_f32 v38, v38\n"
      "v_exp_f32 v39, v39\n"
      "v_exp_f32 v40, v40\n"
      "v_exp_f32 v41, v41\n"
      "v_exp_f32 v42, v42\n"
      "v_exp_f32 v43, v43\n"
      "v_exp_f32 v44, v44\n"
      "s_waitcnt lgkmcnt(3)\n"
      "v_exp_f32 v45, v45\n"
      "v_smfmac_f32_32x32x32_bf16 v[64:79], v[118:121], v[32:39], v106\n"
      "v_exp_f32 v46, v46\n"
      "v_exp_f32 v47, v47\n"
      "v_exp_f32 v48, v48\n"
      "s_waitcnt lgkmcnt(0)\n"
      "s_waitcnt vmcnt(0)\n"
      "s_mov_b64 exec, 1\n"
      "ds_add_u32 v129, v130 offset:76\n"
      "s_mov_b64 exec, -1\n"
      "v_exp_f32 v49, v49\n"
      "v_smfmac_f32_32x32x32_bf16 v[64:79], v[122:125], v[40:47], v106\n"
      "v_exp_f32 v50, v50\n"
      "v_exp_f32 v51, v51\n"
      "v_exp_f32 v52, v52\n"
      "v_exp_f32 v53, v53\n"
      "v_mfma_f32_32x32x16_bf16 v[0:15], v[114:117], v[96:99], v[0:15]\n"
      "v_exp_f32 v54, v54\n"
      "v_exp_f32 v55, v55\n"
      "v_exp_f32 v56, v56\n"
      "v_exp_f32 v57, v57\n"
      "v_smfmac_f32_32x32x32_bf16 v[80:95], v[118:121], v[48:55], v106\n"
      "v_exp_f32 v58, v58\n"
      "ds_read_b128 v[118:121], v108 offset:2432\n"
      "v_exp_f32 v59, v59\n"
      "v_exp_f32 v60, v60\n"
      "v_exp_f32 v61, v61\n"
      "v_mfma_f32_32x32x16_bf16 v[16:31], v[114:117], v[100:103], v[16:31]\n"
      "v_exp_f32 v62, v62\n"
      "v_exp_f32 v63, v63\n"
      "v_readfirstlane_b32 %[tmp], v131\n"
      "s_and_b32 %[t2], %[cnt], 12\n"
      "s_cmp_lg_u32 %[t2], 4\n"
      "s_cbranch_scc1 8f\n"
      "s_cmp_lt_u32 %[tmp], 8\n"
      "s_cbranch_scc1 7f\n"
      "9:\n"
      "s_mov_b32 m0, %[dst]\n"
      "s_add_u32 s96, %[slo], %[so]\n"
      "s_addc_u32 s97, %[shi], 0\n"
      "global_load_lds_dwordx4 v126, s[96:97] sc1\n"
      "global_load_lds_dwordx4 v126, s[96:97] offset:1024 sc1\n"
      "s_mov_b64 exec, 0xfffffff\n"
      "global_load_lds_dwordx4 v126, s[96:97] offset:2048 sc1\n"
      "s_mov_b64 exec, -1\n"
      "s_add_u32 %[dst], %[dst], 19968\n"
      "s_add_u32 %[so], %[so], 19968\n"
      "s_cmp_eq_u32 %[so], 159744\n"
      "s_cselect_b32 %[so], 0, %[so]\n"
      "8:\n"
      "v_exp_f32 v0, v0\n"
      "v_exp_f32 v1, v1\n"
      "v_smfmac_f32_32x32x32_bf16 v[80:95], v[122:125], v[56:63], v106\n"
      "ds_read_b128 v[114:117], v107 offset:3072\n"
      "ds_read_b128 v[122:125], v108 offset:3040\n"
      "ds_read_b32 v131, v129 offset:72\n"
      "v_exp_f32 v2, v2\n"
      "v_exp_f32 v3, v3\n"
      "v_exp_f32 v4, v4\n"
      "v_exp_f32 v5, v5\n"
      "v_mfma_f32_32x32x4_2b_bf16 v[32:63], v[112:113], v[104:105], 0 cbsz:1 abid:1\n"
      "v_exp_f32 v6, v6\n"
      "v_exp_f32 v7, v7\n"
      "v_exp_f32 v8, v8\n"
      "v_exp_f32 v9, v9\n"
      "v_exp_f32 v10, v10\n"
      "v_exp_f32 v11, v11\n"
      "v_exp_f32 v12, v12\n"
      "s_waitcnt lgkmcnt(3)\n"
      "v_exp_f32 v13, v13\n"
      "v_smfmac_f32_32x32x32_bf16 v[64:79], v[118:121], v[0:7], v106\n"
      "v_exp_f32 v14, v14\n"
      "v_exp_f32 v15, v15\n"
      "v_exp_f32 v16, v16\n"
      "s_waitcnt lgkmcnt(0)\n"
      "v_exp_f32 v17, v17\n"
      "v_smfmac_f32_32x32x32_bf16 v[64:79], v[122:125], v[8:15], v106\n"
      "v_exp_f32 v18, v18\n"
      "v_exp_f32 v19, v19\n"
      "v_exp_f32 v20, v20\n"
      "v_exp_f32 v21, v21\n"
      "v_mfma_f32_32x32x16_bf16 v[32:47], v[114:117], v[96:99], v[32:47]\n"
      "v_exp_f32 v22, v22\n"
      "v_exp_f32 v23, v23\n"
      "v_exp_f32 v24, v24\n"
      "v_exp_f32 v25, v25\n"
      "v_smfmac_f32_32x32x32_bf16 v[80:95], v[118:121], v[16:23], v106\n"
      "v_exp_f32 v26, v26\n"
      "ds_read_b128 v[118:121], v108 offset:3648\n"
      "v_exp_f32 v27, v27\n"
      "v_exp_f32 v28, v28\n"
      "v_exp_f32 v29, v29\n"
      "v_mfma_f32_32x32x16_bf16 v[48:63], v[114:117], v[100:103], v[48:63]\n"
      "v_exp_f32 v30, v30\n"
      "v_exp_f32 v31, v31\n"
      "s_cbranch_vccz 5f\n"
      "s_setprio 0\n"
      "5:\n"
      "v_readfirstlane_b32 %[tmp], v131\n"
      "s_add_u32 %[off], %[off], 19968\n"
      "s_cmp_eq_u32 %[off], 79872\n"
      "s_cselect_b32 %[off], 0, %[off]\n"
      "s_cmp_lt_i32 %[tmp], 8\n"
      "s_cbranch_scc1 3f\n"
      "2:\n"
      "v_add_u32 v109, %[off], v128\n"
      "ds_read2st64_b64 v[110:113], v109 offset0:0 offset1:1\n"
      "v_add_u32 v107, %[off], v126\n"
      "v_exp_f32 v32, v32\n"
      "v_exp_f32 v33, v33\n"
      "v_smfmac_f32_32x32x32_bf16 v[80:95], v[122:125], v[24:31], v106\n"
      "ds_read_b128 v[114:117], v107 offset:0\n"
      "ds_read_b128 v[122:125], v108 offset:4256\n"
      "v_exp_f32 v34, v34\n"
      "v_exp_f32 v35, v35\n"
      "v_exp_f32 v36, v36\n"
      "s_waitcnt lgkmcnt(2)\n"
      "v_exp_f32 v37, v37\n"
      "v_mfma_f32_32x32x4_2b_bf16 v[0:31], v[110:111], v[104:105], 0 cbsz:1 abid:0\n"
      "v_exp_f32 v38, v38\n"
      "v_exp_f32 v39, v39\n"
      "v_exp_f32 v40, v40\n"
      "v_exp_f32 v41, v41\n"
      "v_exp_f32 v42, v42\n"
      "v_exp_f32 v43, v43\n"
      "v_exp_f32 v44, v44\n"
      "v_exp_f32 v45, v45\n"
      "v_smfmac_f32_32x32x32_bf16 v[64:79], v[118:121], v[32:39], v106\n"
      "v_exp_f32 v46, v46\n"
      "v_exp_f32 v47, v47\n"
      "v_exp_f32 v48, v48\n"
      "s_waitcnt lgkmcnt(0)\n"
      "s_mov_b64 exec, 1\n"
      "ds_add_u32 v129, v130 offset:4\n"
      "s_mov_b64 exec, -1\n"
      "v_exp_f32 v49, v49\n"
      "v_smfmac_f32_32x32x32_bf16 v[64:79], v[122:125], v[40:47], v106\n"
      "v_exp_f32 v50, v50\n"
      "v_exp_f32 v51, v51\n"
      "v_exp_f32 v52, v52\n"
      "v_exp_f32 v53, v53\n"
      "v_mfma_f32_32x32x16_bf16 v[0:15], v[114:117], v[96:99], v[0:15]\n"
      "v_exp_f32 v54, v54\n"
      "v_exp_f32 v55, v55\n"
      "v_exp_f32 v56, v56\n"
      "v_exp_f32 v57, v57\n"
      "v_smfmac_f32_32x32x32_bf16 v[80:95], v[118:121], v[48:55], v106\n"
      "v_add_u32 v108, %[off], v127\n"
      "v_add_u32 v129, 4, v129\n"
      "v_exp_f32 v58, v58\n"
      "ds_read_b128 v[118:121], v108 offset:0\n"
      "v_exp_f32 v59, v59\n"
      "v_exp_f32 v60, v60\n"
      "v_exp_f32 v61, v61\n"
      "v_mfma_f32_32x32x16_bf16 v[16:31], v[114:117], v[100:103], v[16:31]\n"
      "v_exp_f32 v62, v62\n"
      "v_exp_f32 v63, v63\n"
      "s_sub_u32 %[cnt], %[cnt], 1\n"
      "s_cmp_lg_u32 %[cnt], 0\n"
      "s_cbranch_scc1 1b\n"
      "s_branch 6f\n"
      "3:\n"
      "s_sleep 1\n"
      "ds_read_b32 v131, v129 offset:72\n"
      "s_waitcnt lgkmcnt(0)\n"
      "v_readfirstlane_b32 %[tmp], v131\n"
      "s_nop 0\n"
      "s_cmp_lt_i32 %[tmp], 8\n"
      "s_cbranch_scc1 3b\n"
      "s_branch 2b\n"
      "7:\n"
      "s_sleep 1\n"
      "ds_read_b32 v131, v129\n"
      "s_waitcnt lgkmcnt(0)\n"
      "v_readfirstlane_b32 %[tmp], v131\n"
      "s_nop 0\n"
      "s_cmp_lt_u32 %[tmp], 8\n"
      "s_cbranch_scc1 7b\n"
      "s_branch 9b\n"
      "6:\n"
      "v_smfmac_f32_32x32x32_bf16 v[80:95], v[122:125], v[56:63], v106\n"
      "s_setprio 0\n"
      "s_waitcnt lgkmcnt(0)\n"
      "s_nop 15\n"
      "s_nop 7\n"
      : "=&{v[64:79]}"(o0), "=&{v[80:95]}"(o1), "+{v129}"(ca), [cnt] "+s"(cnt), [off] "+s"(off), [tmp] "=&s"(tmp),
        [t2] "=&s"(t2), [so] "+s"(so), [dst] "+s"(dst)
      : "{v[96:99]}"(q0u), "{v[100:103]}"(q1u), "{v[104:105]}"(qrem_u), "{v106}"(SPIDX),
        "{v126}"(sbase + kh * KV_GROUP + lane * 16), "{v127}"(sbase + kh * KV_GROUP + voff),
        "{v128}"(sbase + kh * KV_GROUP + 8960 + lane * 8), [first] "s"(1 - kh), [slo] "s"((unsigned)ksrc), [shi] "s"((unsigned)(ksrc >> 32))
      : "v0", "v1", "v2", "v3", "v4", "v5", "v6", "v7", "v8", "v9", "v10", "v11", "v12", "v13", "v14", "v15",
        "v16", "v17", "v18", "v19", "v20", "v21", "v22", "v23", "v24", "v25", "v26", "v27", "v28", "v29", "v30", "v31",
        "v32", "v33", "v34", "v35", "v36", "v37", "v38", "v39", "v40", "v41", "v42", "v43", "v44", "v45", "v46", "v47",
        "v48", "v49", "v50", "v51", "v52", "v53", "v54", "v55", "v56", "v57", "v58", "v59", "v60", "v61", "v62", "v63",
        "v107", "v108", "v109", "v110", "v111", "v112", "v113", "v114", "v115", "v116", "v117", "v118", "v119", "v120", "v121", "v122",
        "v123", "v124", "v125", "v130", "v131", "s96", "s97", "vcc", "scc", "memory");
    }
    xv = *(const u32x4*)(xt + otok * 1152 + 144 * hg + opc * 16);
    const float dv = (float)ssmd[72 * hg + 2 * qt + (hl >> 1)];
    if (!ALL(nq * *(LAS float*)(mi + M_KN) < 47.f * 47.f)) {
      m0 = m1 = -__builtin_inff();
      o0 = o1 = (f32x16){0};
      const bf16x8 q0 = __builtin_bit_cast(bf16x8, q0u), q1 = __builtin_bit_cast(bf16x8, q1u);
      const s16x4 qrem = __builtin_bit_cast(s16x4, (u32x2){qr, 0u});
#pragma unroll 1
      for (int t = 0; t < 32; ++t) {
        const int g = WS_KV + (t >> 2) * RSLOT + kh * KV_GROUP;
        const u32x2 kr = __builtin_bit_cast(u32x2, __builtin_amdgcn_raw_buffer_load_b64(rs, g + 8960 + (t & 3) * 256 + l31 * 8, 0, 16));
        const bf16x8 km = __builtin_bit_cast(bf16x8, LD_SC1_16(g + (t & 3) * 1024 + lane * 16));
        const bf16x8 va = __builtin_bit_cast(bf16x8, LD_SC1_16(g + voff + (t & 3) * 1216));
        const bf16x8 vb = __builtin_bit_cast(bf16x8, LD_SC1_16(g + voff + (t & 3) * 1216 + 608));
        const f32x32 z = {0};
        const f32x32 r = __builtin_amdgcn_mfma_f32_32x32x4bf16_1k(__builtin_bit_cast(s16x4, kr), qrem, z, 0, 0, 0);
        online_step(MFMA32(km, q0, lo16(r)), m0, o0, va, vb);
        online_step(MFMA32(km, q1, hi16(r)), m1, o1, va, vb);
      }
    }
    HSTAMP(9);
    lds_ptr ex = sm + L_EX + hl * 6144 + lane * 16;
    if (kh) {
      *(LAS f32x4*)(ex) = (f32x4){o0[0], o0[1], o0[2], o0[3]};
      *(LAS f32x4*)(ex + 1024) = (f32x4){o0[4], o0[5], o0[6], o0[7]};
      *(LAS f32x4*)(ex + 2048) = (f32x4){o0[8], o0[9], o0[10], m0};
      *(LAS f32x4*)(ex + 3072) = (f32x4){o1[0], o1[1], o1[2], o1[3]};
      *(LAS f32x4*)(ex + 4096) = (f32x4){o1[4], o1[5], o1[6], o1[7]};
      *(LAS f32x4*)(ex + 5120) = (f32x4){o1[8], o1[9], o1[10], m1};
      lds_signal(mi + M_PAIR + 4 * hl);
    }
    lds_wait<1>(mi + M_PAIR + 4 * hl, 5);
    STAMP(10); RSTAMP(3);
    if (!kh) {
      __builtin_amdgcn_s_setprio(3);
#pragma unroll
      for (int t = 0; t < 2; ++t) {
        const f32x16 keep = t ? o1 : o0;
        const f32x4 t0 = *(LAS f32x4*)(ex + t * 3072), t1 = *(LAS f32x4*)(ex + t * 3072 + 1024), t2 = *(LAS f32x4*)(ex + t * 3072 + 2048);
        const float mk = t ? m1 : m0, mm = __builtin_fmaxf(mk, t2[3]);
        float fa = __builtin_amdgcn_exp2f(mk - mm), fb = __builtin_amdgcn_exp2f(t2[3] - mm);
        const float l = keep[10] * fa + t2[2] * fb, lx = xhalf(l);
        const float inv = __builtin_amdgcn_rcpf(h ? lx : l);
        fa *= inv;
        fb *= inv;
        float o[10];
#pragma unroll
        for (int i = 0; i < 10; ++i) o[i] = keep[i] * fa + (i < 4 ? t0[i & 3] : (i < 8 ? t1[i & 3] : t2[i & 3])) * fb;
        u32x4 a;
#pragma unroll
        for (int i = 0; i < 4; ++i) a[i] = UMIN(pk2(o[2 * i], o[2 * i + 1]), 0xfffffffeu);
        const int ob = WS_O + (2 * qt + t) * 36 * 1024;
        ST_SC1_16(a, ob + (4 * hg + hl) * 1024 + lane * 16);
        if (!h) ST_SC1_4(UMIN(pk2(o[8], o[9]), 0xfffffffeu), ob + 32768 + (4 * hg + hl) * 128 + l31 * 4);
      }
      lds_signal(mi + M_EXD);
      __builtin_amdgcn_s_setprio(1);
      HSTAMP(5);
      __builtin_amdgcn_s_sleep(O_DELAY);
      u32x4 of[18];
#pragma unroll 1
      for (unsigned spins = 0; spins < SPIN_CAP; ++spins) {
        unsigned mx = 0;
#pragma unroll
        for (int i = 0; i < 18; ++i) of[i] = LD_SC1_16(WS_O + (2 * qt * 36 + 18 * hl + i) * 1024 + lane * 16);
#pragma unroll
        for (int i = 0; i < 18; ++i) mx = UMAX(UMAX(mx, UMAX(of[i][0], of[i][1])), UMAX(of[i][2], of[i][3]));
        asm volatile("" ::: "memory");
        if (ALL(mx != ~0u)) break;
        __builtin_amdgcn_s_sleep(4);
      }
      lds_wait<1>(mi + M_EXD, 4);
#pragma unroll
      for (int i = 0; i < 18; ++i) {
        if (i >= 14 && (hl & 1)) {
          lds_ptr p = sm + (18 * hl + i) * 1024 + (4 * (lane & 7) + 32 * h) * 16 + ((lane >> 3) & 3) * 4;
#pragma unroll
          for (int e = 0; e < 4; ++e) *(LAS unsigned*)(p + e * 16) = of[i][e];
        } else {
          *(LAS u32x4*)(sm + (18 * hl + i) * 1024 + lane * 16) = of[i];
        }
      }
      lds_signal(mi + M_OR);
      __builtin_amdgcn_s_setprio(0);
      HSTAMP(6);
    } else {
      const int b = hl >> 1, o = hl & 1;
      lds_ptr ab = sm + CV_H + b * CV_HB + (l31 & 7) * CV_COPY + (h + (l31 >> 3)) * 16 + 1024 * o;
      lds_ptr bb = sm + CV_U + b * CV_UB + 2560 + (l31 - 16 * o - 7) * 80 + (1 - h) * 16;
      f32x16 y0 = {0}, y1 = {0};
      bf16x8 fx[8], fy[8];
      __builtin_amdgcn_s_setprio(2);
      asm volatile("" : "+v"(ab), "+v"(bb));
      CV_LD(fx, 0);
#pragma unroll 1
      for (int it = 0; it < 4; ++it) {
        asm volatile("" : "+v"(ab), "+v"(bb));
        CV_STEP(fx, fy, 1);
        CV_STEP(fy, fx, 2);
        CV_STEP(fx, fy, 3);
        CV_STEP(fy, fx, 4);
        ab += 256;
        bb -= 320;
      }
      HSTAMP(5);
      lds_signal(mi + M_AS + 4 * b);
      lds_wait(mi + M_AS + 4 * b, 2);
      lds_ptr px = sm + CV_H + b * CV_HB + lane * 16;
      {
        const f32x16 give = o ? y0 : y1;
#pragma unroll
        for (int g = 0; g < 4; ++g) *(LAS f32x4*)(px + o * 4096 + g * 1024) = (f32x4){give[4 * g], give[4 * g + 1], give[4 * g + 2], give[4 * g + 3]};
        if (o) y0 = y1;
      }
      lds_signal(mi + M_AS + 4 * b);
      u32x2 uu[4];
#pragma unroll
      for (int g = 0; g < 4; ++g) uu[g] = *(LAS u32x2*)(sm + CV_U + b * CV_UB + 2560 + (32 * o + l31) * 80 + g * 16 + (1 - h) * 8);
#pragma unroll
      for (int g = 0; g < 4; ++g) {
        y0[4 * g] = __builtin_fmaf(bf_hi(uu[g][1]), dv, y0[4 * g]);
        y0[4 * g + 1] = __builtin_fmaf(bf_lo(uu[g][1]), dv, y0[4 * g + 1]);
        y0[4 * g + 2] = __builtin_fmaf(bf_hi(uu[g][0]), dv, y0[4 * g + 2]);
        y0[4 * g + 3] = __builtin_fmaf(bf_lo(uu[g][0]), dv, y0[4 * g + 3]);
      }
      lds_wait(mi + M_AS + 4 * b, 4);
      u32x4 z[2];
#pragma unroll
      for (int g = 0; g < 4; ++g) {
        const f32x4 t = *(LAS f32x4*)(px + (1 - o) * 4096 + g * 1024);
        z[g >> 1][2 * (g & 1)] = UMIN(pk2(t[0] + y0[4 * g], t[1] + y0[4 * g + 1]), 0xfffffffeu);
        z[g >> 1][2 * (g & 1) + 1] = UMIN(pk2(t[2] + y0[4 * g + 2], t[3] + y0[4 * g + 3]), 0xfffffffeu);
      }
      const int zo = WS_Z + (72 * hg + 2 * qt + b) * 4096 + o * 2048 + lane * 16;
      ST_SC1_16(z[0], zo);
      ST_SC1_16(z[1], zo + 1024);
      lds_signal(mi + M_CE);
      __builtin_amdgcn_s_setprio(0);
      HSTAMP(6);
    }
    lds_wait<2>(mi + M_CE, 4);
    __builtin_amdgcn_s_sleep(Z_DELAY);
#pragma unroll 1
    for (unsigned spins = 0; spins < SPIN_CAP; ++spins) {
      zv = LD_SC1_16(WS_Z + (72 * hg + (tid >> 3)) * 4096 + (qt >> 4) * 2048 + ((tid >> 2) & 1) * 1024 +
                     (2 * (qt & 15) + (tid & 1) + 32 * ((tid >> 1) & 1)) * 16);
      asm volatile("" ::: "memory");
      if (ALL(UMAX(UMAX(zv[0], zv[1]), UMAX(zv[2], zv[3])) != ~0u)) break;
      __builtin_amdgcn_s_sleep(2);
    }
    HSTAMP(7);
  }

  if (tid < 576) {
    lds_ptr zp = sm + L_ZT + ((tid & 7) * 72 + (tid >> 3)) * 2;
#pragma unroll
    for (int e = 0; e < 8; ++e)
      *(LAS unsigned short*)(zp + e * 1152) = (unsigned short)((e & 1) ? zv[e >> 1] >> 16 : zv[e >> 1]);
  }
  BAR();
  if (w < 8) HSTAMP(8);
  if (tid < 576) {
    lds_ptr ap = opc < 8 ? sm + L_AP + otok * 256 + opc * 32 : sm + L_AP2 + otok * 32;
    const int st = opc < 8 ? 16384 : 2048;
    f32x4 a0 = *(LAS f32x4*)(ap) + *(LAS f32x4*)(ap + st), a1 = *(LAS f32x4*)(ap + 16) + *(LAS f32x4*)(ap + st + 16);
    if (opc == 8) {
      a0 += *(LAS f32x4*)(ap + 4096) + *(LAS f32x4*)(ap + 6144);
      a1 += *(LAS f32x4*)(ap + 4112) + *(LAS f32x4*)(ap + 6160);
    }
    const int zr = (otok >> 5) + ((otok >> 1) & 2) + ((otok >> 2) & 4) + 8 * (otok & 3) + 4 * (otok & 8);
    const u32x4 z = *(LAS u32x4*)(sm + L_ZT + zr * 144 + opc * 16);
    const float gq = *(LAS float*)(mi + M_G + otok * 4), omg = rbf(1.f - gq);
    u32x4 o;
#pragma unroll
    for (int i = 0; i < 4; ++i) {
      const float e0 = i < 2 ? a0[2 * i] : a1[2 * i - 4], e1 = i < 2 ? a0[2 * i + 1] : a1[2 * i - 3];
      o[i] = pk2(__builtin_fmaf(gq, e0, omg * bf_lo(xv[i]) * bf_lo(z[i])), __builtin_fmaf(gq, e1, omg * bf_hi(xv[i]) * bf_hi(z[i])));
    }
    *(u32x4*)(out + (size_t)(64 * qt + otok) * D_ + 72 * hg + 8 * opc) = o;
  }
  STAMP_FLUSH(w < 4 ? 0 : (w < 8 ? 1 : (w < 11 ? 2 : 3)));
  asm volatile("fk_end:");
}

extern "C" void kernel_launch(void* const* d_in, const int* in_sizes, int n_in, void* d_out, int out_size,
                              void* d_ws, size_t ws_size, hipStream_t stream) {
  fused_kernel<<<256, 768, 0, stream>>>((const __bf16*)d_in[0], (const __bf16*)d_in[1], (const __bf16*)d_in[2],
                                        (const __bf16*)d_in[3], (const __bf16*)d_in[4], (const __bf16*)d_in[6],
                                        (const __bf16*)d_in[7], (const __bf16*)d_in[8], (const __bf16*)d_in[9],
                                        (const __bf16*)d_in[10], (const __bf16*)d_in[11], (__bf16*)d_out, (char*)d_ws);
}
```

```cpp
#include <hip/hip_runtime.h>
#include <hip/hip_bf16.h>
#include <stdint.h>

typedef __attribute__((ext_vector_type(2))) __bf16 bf16x2;
typedef __attribute__((ext_vector_type(4))) __bf16 bf16x4;
typedef __attribute__((ext_vector_type(8))) __bf16 bf16x8;
typedef __attribute__((ext_vector_type(16))) __bf16 bf16x16;
typedef __attribute__((ext_vector_type(4))) short s16x4;
typedef __attribute__((ext_vector_type(2))) float f32x2;
typedef __attribute__((ext_vector_type(4))) float f32x4;
typedef __attribute__((ext_vector_type(16))) float f32x16;
typedef __attribute__((ext_vector_type(32))) float f32x32;
typedef __attribute__((ext_vector_type(2))) unsigned u32x2;
typedef __attribute__((ext_vector_type(4))) unsigned u32x4;
#define LAS __attribute__((address_space(3)))
#define GAS __attribute__((address_space(1)))
typedef LAS char* lds_ptr;
typedef GAS unsigned gu32;
typedef GAS unsigned long long gu64;
#ifdef __HIP_DEVICE_COMPILE__
#define NOPK __attribute__((target("no-packed-fp32-ops")))
#else
#define NOPK
#endif
#define DEVI __device__ __forceinline__ NOPK
#define LAMI __attribute__((always_inline)) NOPK
#define TIDX __builtin_amdgcn_workitem_id_x()
#define BIDX __builtin_amdgcn_workgroup_id_x()
#define ALL(p) (__builtin_amdgcn_ballot_w64(!(p)) == 0ull)
#define UMIN(a, b) __builtin_elementwise_min((unsigned)(a), (unsigned)(b))
#define UMAX(a, b) __builtin_elementwise_max((unsigned)(a), (unsigned)(b))
#define RLX_AGENT __ATOMIC_RELAXED, __HIP_MEMORY_SCOPE_AGENT
#define RLX_WG __ATOMIC_RELAXED, __HIP_MEMORY_SCOPE_WORKGROUP

constexpr int S_ = 2048, D_ = 576, DH = 18;

constexpr int KV_GROUP = 9984;
constexpr int WS_KV = 0;
constexpr int WS_EPOCH = 192 * 1024;
constexpr int WS_FKV = WS_EPOCH + 1024;
constexpr int WS_FU = WS_FKV + 8192;
constexpr int WS_FZ = WS_FU + 32768;
constexpr int WS_G = WS_FZ + 32768;
constexpr int WS_KT = 320 * 1024;
constexpr int WS_UT = 640 * 1024;
constexpr int WS_Z = WS_UT + D_ * S_ * 2;
constexpr int WS_O = WS_Z + D_ * S_ * 2;
constexpr int WS_ZP = WS_O + D_ * S_ * 2;
constexpr int WS_END = WS_ZP + 64 * 4 * 8192;

constexpr int RSLOT = 2 * KV_GROUP;
constexpr int L_X = 0;
constexpr int L_KVP = 73728;
constexpr int L_B = 80000;
constexpr int L_M = 162944;
constexpr int CV_COPY = 4160;
constexpr int CV_H = L_B, CV_HB = 8 * CV_COPY, CV_U = L_B + 2 * CV_HB, CV_UB = 7680;
constexpr int L_Q = L_B + 8192;
constexpr int M_DONE = 0, M_C1 = 32, M_C2 = 36, M_C3 = 40, M_C3B = 44, M_KVC = 48, M_KVR = 52, M_UR = 56, M_OR = 60, M_RDY = 64,
              M_QD = 192, M_UD = 104, M_HS = 108, M_APR = 112, M_CVR = 116, M_CE = 124, M_PAIR = 128, M_TAGR = 144,
              M_TAG = 148, M_KN = 152, M_EXD = 156, M_KVP = 160, M_KN0 = 164, M_KD = 168, M_QR = 172,
              M_N2 = 176, M_AS = 196, M_SE = 204, M_ZS = 208, M_MM = 212, M_G = 256;
constexpr int L_EX = 0;
constexpr int L_AP = CV_H + 512, L_ZT = CV_U;
#define CV_PRIO 3
#define CV_SLEEP 0
#define POLL_AT M_C2
#define POLL_N 3
#define BUBBLE 0
#define POLL_SLEEP 4
#define WO_INFL "2"
#define DMA_FIRST 0
#define ENTRY 0
#define O_DELAY 20
#define Z_DELAY 12
constexpr unsigned SPIN_CAP = 1u << 18;

#define STAMP_DECL do {} while (0)
#define STAMP(I) do {} while (0)
#define STAMP_FLUSH(KID) do {} while (0)
#define RSTAMP(I) do {} while (0)

#define PSTAMP(I) do {} while (0)
#define NSTAMP(I) STAMP(I)
#define CSTAMP(I) do {} while (0)
#define HSTAMP(I) NSTAMP(I)

DEVI unsigned pk2(float a, float b) {
  bf16x2 v = {(__bf16)a, (__bf16)b};
  return __builtin_bit_cast(unsigned, v);
}
DEVI float u2f(unsigned u) { return __builtin_bit_cast(float, u); }
DEVI float bf_lo(unsigned u) { return __builtin_bit_cast(float, u << 16); }
DEVI float bf_hi(unsigned u) { return __builtin_bit_cast(float, u & 0xffff0000u); }
DEVI float dot2(unsigned u, float c) { const bf16x2 v = __builtin_bit_cast(bf16x2, u); return __builtin_amdgcn_fdot2_f32_bf16(v, v, c, false); }
DEVI float rbf(float a) { return (float)(__bf16)a; }
DEVI int swap23(int r) { return (r & ~12) | ((r & 4) << 1) | ((r & 8) >> 1); }
DEVI float xhalf(float v) {
  const unsigned u = __builtin_bit_cast(unsigned, v);
  const auto r = __builtin_amdgcn_permlane32_swap(u, u, false, false);
  return __builtin_bit_cast(float, (TIDX & 32) ? r[0] : r[1]);
}

template <int CTRL>
DEVI float dpp_max(float v) {
  const int u = __builtin_bit_cast(int, v);
  return __builtin_fmaxf(v, __builtin_bit_cast(float, __builtin_amdgcn_update_dpp(u, u, CTRL, 0xf, 0xf, false)));
}
DEVI float max16(float v) {
  return dpp_max<0x140>(dpp_max<0x141>(dpp_max<0x4e>(dpp_max<0xb1>(v))));
}
template <int CTRL>
DEVI float dpp_add(float v) {
  const int u = __builtin_bit_cast(int, v);
  return v + __builtin_bit_cast(float, __builtin_amdgcn_update_dpp(u, u, CTRL, 0xf, 0xf, false));
}
DEVI float sum16(float v) { return dpp_add<0x140>(dpp_add<0x141>(dpp_add<0x4e>(dpp_add<0xb1>(v)))); }
DEVI float wave_max(float v) {
  v = max16(v);
  const unsigned u = __builtin_bit_cast(unsigned, v);
  return __builtin_fmaxf(__builtin_fmaxf(u2f(__builtin_amdgcn_readlane(u, 0)), u2f(__builtin_amdgcn_readlane(u, 16))),
               __builtin_fmaxf(u2f(__builtin_amdgcn_readlane(u, 32)), u2f(__builtin_amdgcn_readlane(u, 48))));
}
DEVI void glds(const void* sbase, unsigned voff, unsigned lds_dst) {
  asm volatile("s_mov_b32 m0, %2\n\ts_nop 0\n\tglobal_load_lds_dwordx4 %0, %1" :: "v"(voff), "s"(sbase), "s"(lds_dst) : "memory");
}
DEVI void glds_sc1(const void* sbase, unsigned voff, unsigned lds_dst) {
  asm volatile("s_mov_b32 m0, %2\n\ts_nop 0\n\tglobal_load_lds_dwordx4 %0, %1 sc1" :: "v"(voff), "s"(sbase), "s"(lds_dst) : "memory");
}
#define GLDS4(SC, sbase_, v0_, v3_, dst_)                                                                                  \
  asm volatile("s_mov_b32 m0, %3\n\ts_nop 0\n\tglobal_load_lds_dwordx4 %0, %2" SC "\n\tglobal_load_lds_dwordx4 %0, %2 offset:1024" SC   \
               "\n\tglobal_load_lds_dwordx4 %0, %2 offset:2048" SC "\n\tglobal_load_lds_dwordx4 %1, %2 offset:3072" SC           \
               :: "v"(v0_), "v"(v3_), "s"(sbase_), "s"(dst_) : "memory")
#define GLDSX(src_, dst_)                                                                                                  \
  asm volatile("s_mov_b32 m0, %5\n\ts_nop 0\n\tglobal_load_lds_dwordx4 %0, %4\n\tglobal_load_lds_dwordx4 %1, %4 offset:1024\n\t"   \
               "global_load_lds_dwordx4 %2, %4 offset:2048\n\tglobal_load_lds_dwordx4 %3, %4 offset:3072"                 \
               :: "v"(vxe), "v"(vxo), "v"(vxe + 16384), "v"(vxo + 16384), "s"(src_), "s"(dst_) : "memory")
#define GLDSX2(src_, dst_)                                                                                                 \
  asm volatile("s_mov_b32 m0, %3\n\ts_nop 0\n\tglobal_load_lds_dwordx4 %0, %2\n\tglobal_load_lds_dwordx4 %1, %2 offset:1024"       \
               :: "v"(vxe), "v"(vxo), "s"(src_), "s"(dst_) : "memory")
#define GLDS3(STEP1, STEP2, v0_, v1_, v2_, src_, dst_)                                                                     \
  asm volatile("s_mov_b32 m0, %4\n\ts_nop 0\n\tglobal_load_lds_dwordx4 %0, %3\n\tglobal_load_lds_dwordx4 %1, %3 offset:" STEP1  \
               "\n\tglobal_load_lds_dwordx4 %2, %3 offset:" STEP2                                                        \
               :: "v"(v0_), "v"(v1_), "v"(v2_), "s"(src_), "s"(dst_) : "memory")
#define TOUCH(src_, v_) asm volatile("s_mov_b32 m0, %2\n\ts_nop 0\n\tglobal_load_lds_dword %0, %1 sc1" :: "v"(v_), "s"(src_), "s"(sbase + L_M + 512) : "memory")
#define VM_WAIT(N) asm volatile("s_waitcnt vmcnt(" #N ")" ::: "memory")
#define BAR() asm volatile("s_waitcnt lgkmcnt(0)\n\ts_barrier" ::: "memory")

DEVI bf16x8 tr_frag(lds_ptr p0, lds_ptr p1) {
  bf16x4 a = __builtin_amdgcn_ds_read_tr16_b64_v4bf16((LAS bf16x4*)p0);
  bf16x4 b = __builtin_amdgcn_ds_read_tr16_b64_v4bf16((LAS bf16x4*)p1);
  return __builtin_shufflevector(a, b, 0, 1, 2, 3, 4, 5, 6, 7);
}
DEVI bf16x8 lds_frag(lds_ptr p) { return *(LAS bf16x8*)p; }
DEVI void lds_signal(lds_ptr p, unsigned n = 1u) {
  asm volatile("s_waitcnt lgkmcnt(0)\n\ts_mov_b64 exec, 1\n\tds_add_u32 %0, %1\n\ts_mov_b64 exec, -1" :: "v"(p), "v"(n) : "memory");
}
DEVI unsigned lds_ticket(lds_ptr p) {
  unsigned old;
  asm volatile("s_waitcnt lgkmcnt(0)\n\ts_mov_b64 exec, 1\n\tds_add_rtn_u32 %0, %1, %2\n\ts_mov_b64 exec, -1\n\ts_waitcnt lgkmcnt(0)"
               : "=&v"(old) : "v"(p), "v"(1u) : "memory");
  return __builtin_amdgcn_readfirstlane(old);
}
template <int SLEEP = 0>
DEVI void lds_wait(lds_ptr p, unsigned n) {
  unsigned v, t;
  asm volatile("1:\n\tds_read_b32 %0, %2\n\ts_waitcnt lgkmcnt(0)\n\tv_readfirstlane_b32 %1, %0\n\ts_nop 0\n\ts_cmp_ge_u32 %1, %3\n\t"
               "s_cbranch_scc1 2f\n\ts_sleep %4\n\ts_branch 1b\n2:"
               : "=&v"(v), "=&s"(t) : "v"(p), "s"(n), "i"(SLEEP) : "scc", "memory");
}
DEVI unsigned lds_peek(lds_ptr p) {
  unsigned v;
  asm volatile("ds_read_b32 %0, %1\n\ts_waitcnt lgkmcnt(0)" : "=&v"(v) : "v"(p) : "memory");
  return __builtin_amdgcn_readfirstlane(v);
}
#define LD_SC1_16(off) __builtin_bit_cast(u32x4, __builtin_amdgcn_raw_buffer_load_b128(rs, (off), 0, 16))
#define ST_SC1_16(v, off) __builtin_amdgcn_raw_buffer_store_b128((v), rs, (off), 0, 16)
#define ST_SC1_8(v, off) __builtin_amdgcn_raw_buffer_store_b64((v), rs, (off), 0, 16)
#define ST_SC1_4(v, off) __builtin_amdgcn_raw_buffer_store_b32((v), rs, (off), 0, 16)

DEVI f32x16 lo16(f32x32 v) { return __builtin_shufflevector(v, v, 0, 1, 2, 3, 4, 5, 6, 7, 8, 9, 10, 11, 12, 13, 14, 15); }
DEVI f32x16 hi16(f32x32 v) { return __builtin_shufflevector(v, v, 16, 17, 18, 19, 20, 21, 22, 23, 24, 25, 26, 27, 28, 29, 30, 31); }
DEVI bf16x16 raw_lo(f32x16 v) { return __builtin_bit_cast(bf16x16, __builtin_shufflevector(v, v, 0, 1, 2, 3, 4, 5, 6, 7)); }
DEVI bf16x16 raw_hi(f32x16 v) { return __builtin_bit_cast(bf16x16, __builtin_shufflevector(v, v, 8, 9, 10, 11, 12, 13, 14, 15)); }
constexpr int SPIDX = (int)0xddddddddu;
#define MFMA32(a, b, c) __builtin_amdgcn_mfma_f32_32x32x16_bf16((a), (b), (c), 0, 0, 0)
#define MFMA16(a, b, c) __builtin_amdgcn_mfma_f32_16x16x32_bf16((a), (b), (c), 0, 0, 0)

DEVI void online_step(f32x16 s, float& m, f32x16& o, bf16x8 va, bf16x8 vb) {
  float t = s[0];
#pragma unroll
  for (int i = 1; i < 16; ++i) t = __builtin_fmaxf(t, s[i]);
  t = __builtin_fmaxf(t, xhalf(t));
  const float mn = __builtin_fmaxf(m, t);
  const float al = __builtin_amdgcn_exp2f(m - mn);
  m = mn;
#pragma unroll
  for (int i = 0; i < 16; ++i) { o[i] *= al; s[i] = __builtin_amdgcn_exp2f(s[i] - mn); }
  o = __builtin_amdgcn_smfmac_f32_32x32x32_bf16(va, raw_lo(s), o, SPIDX, 0, 0);
  o = __builtin_amdgcn_smfmac_f32_32x32x32_bf16(vb, raw_hi(s), o, SPIDX, 0, 0);
}

#define UNIT_LOAD(CP, TG)                                                                                        \
  const int uc0 = 72 * hg + 2 * (CP), ut0 = 64 * qt + 8 * (TG);                                                  \
  const u32x4 usw = *(const u32x4*)(shk + uc0 * 4);                                                              \
  unsigned ukr[11], uvr[8];                                                                                      \
  {                                                                                                              \
    const unsigned bo = (ut0 * D_ + uc0) * 2, bo3 = ut0 ? bo - 3 * 1152 : bo;                                    \
    _Pragma("unroll") for (int i = 0; i < 3; ++i) ukr[i] = *(const unsigned*)((const char*)bk + bo3 + i * 1152); \
    _Pragma("unroll") for (int i = 0; i < 8; ++i) {                                                              \
      ukr[3 + i] = *(const unsigned*)((const char*)bk + bo + i * 1152);                                          \
      uvr[i] = *(const unsigned*)((const char*)bv + bo + i * 1152);                                              \
    }                                                                                                            \
  }
#define UNIT_DO()                                                                                                \
  u32x4 o0, o1;                                                                                                  \
  {                                                                                                              \
    const f32x2 w3 = {bf_hi(usw[1]), bf_hi(usw[3])}, w2 = {bf_lo(usw[1]), bf_lo(usw[3])};                        \
    const f32x2 w1 = {bf_hi(usw[0]), bf_hi(usw[2])}, w0 = {bf_lo(usw[0]), bf_lo(usw[2])};                        \
    f32x2 kf[11], r[8];                                                                                          \
    _Pragma("unroll") for (int i = 0; i < 11; ++i) {                                                             \
      const unsigned kk = (i < 3 && !ut0) ? 0u : ukr[i];                                                         \
      kf[i] = (f32x2){bf_lo(kk), bf_hi(kk)};                                                                     \
    }                                                                                                            \
    _Pragma("unroll") for (int i = 0; i < 8; ++i)                                                                \
      r[i] = __builtin_elementwise_fma(kf[i + 3], w0, __builtin_elementwise_fma(kf[i + 2], w1,                   \
             __builtin_elementwise_fma(kf[i + 1], w2, kf[i] * w3))) * (f32x2){bf_lo(uvr[i]), bf_hi(uvr[i])};     \
    _Pragma("unroll") for (int i = 0; i < 4; ++i) {                                                              \
      o0[i] = pk2(r[7 - 2 * i][0], r[6 - 2 * i][0]);                                                             \
      o1[i] = pk2(r[7 - 2 * i][1], r[6 - 2 * i][1]);                                                             \
    }                                                                                                            \
  }

#define CV_LD(F, D) _Pragma("unroll") for (int s = 0; s < 2; ++s) {                                              \
    F[4 * s] = lds_frag(ab + 32 * (2 * (D) + 1 - s)); F[4 * s + 1] = lds_frag(bb + 80 * (7 - (D)) + 32 * s);     \
    F[4 * s + 2] = lds_frag(bb + 80 * (39 - (D)) + 32 * s); F[4 * s + 3] = lds_frag(ab + 32 * (2 * (D) + 65 - s)); } \
  __builtin_amdgcn_sched_barrier(0)
#define CV_MM(F) _Pragma("unroll") for (int s = 0; s < 2; ++s) {                                                 \
    y0 = MFMA32(F[4 * s], F[4 * s + 1], y0); y1 = MFMA32(F[4 * s], F[4 * s + 2], y1);                            \
    y1 = MFMA32(F[4 * s + 3], F[4 * s + 1], y1); }                                                               \
  __builtin_amdgcn_sched_barrier(0)
#define SB0 __builtin_amdgcn_sched_barrier(0)
#define CV_STEP(F, N, D) _Pragma("unroll") for (int s = 0; s < 2; ++s) {                                         \
    y0 = MFMA32(F[4 * s], F[4 * s + 1], y0); SB0;                                                                \
    N[4 * s] = lds_frag(ab + 32 * (2 * (D) + 1 - s)); N[4 * s + 1] = lds_frag(bb + 80 * (7 - (D)) + 32 * s); SB0; \
    y1 = MFMA32(F[4 * s], F[4 * s + 2], y1); SB0;                                                                \
    N[4 * s + 2] = lds_frag(bb + 80 * (39 - (D)) + 32 * s); SB0;                                                 \
    y1 = MFMA32(F[4 * s + 3], F[4 * s + 1], y1); SB0;                                                            \
    N[4 * s + 3] = lds_frag(ab + 32 * (2 * (D) + 65 - s)); SB0; }
#define CV_PUT(P) _Pragma("unroll") for (int g = 0; g < 4; ++g) {                                                \
    *(LAS f32x4*)((P) + g * 1024) = (f32x4){y0[4 * g], y0[4 * g + 1], y0[4 * g + 2], y0[4 * g + 3]};             \
    *(LAS f32x4*)((P) + 4096 + g * 1024) = (f32x4){y1[4 * g], y1[4 * g + 1], y1[4 * g + 2], y1[4 * g + 3]}; }
DEVI f32x4 cv_sum(lds_ptr p, lds_ptr u, float dv) {
  const f32x4 s = (*(LAS f32x4*)(p) + *(LAS f32x4*)(p + 8192)) + (*(LAS f32x4*)(p + 16384) + *(LAS f32x4*)(p + 24576));
  const u32x2 uu = *(LAS u32x2*)u;
  return (f32x4){__builtin_fmaf(bf_hi(uu[1]), dv, s[0]), __builtin_fmaf(bf_lo(uu[1]), dv, s[1]), __builtin_fmaf(bf_hi(uu[0]), dv, s[2]), __builtin_fmaf(bf_lo(uu[0]), dv, s[3])};
}

__global__ void __launch_bounds__(768, 3) NOPK
fused_kernel(const __bf16* __restrict__ x, const __bf16* __restrict__ wq, const __bf16* __restrict__ wk,
             const __bf16* __restrict__ wv, const __bf16* __restrict__ wo, const __bf16* __restrict__ bk,
             const __bf16* __restrict__ bv, const __bf16* __restrict__ shk, const __bf16* __restrict__ ssmk,
             const __bf16* __restrict__ ssmd, const __bf16* __restrict__ gw, __bf16* __restrict__ out, char* ws) {
  __shared__ __attribute__((aligned(16))) char smem[160 * 1024];
  STAMP_DECL; STAMP(0);
  const int tid = TIDX, lane = tid & 63;
  const int w = __builtin_amdgcn_readfirstlane(tid >> 6);
  const int bid = BIDX, jj = bid >> 3;
  const unsigned sbase = (unsigned)(size_t)smem;
  if (w == 0) {
    unsigned long long pc0;
    unsigned len;
    asm volatile("s_getpc_b64 %0\n\tfk_pc:\n\ts_mov_b32 %1, fk_end-fk_pc" : "=s"(pc0), "=s"(len));
    TOUCH(pc0, UMIN((unsigned)(lane * 8 + (jj & 7)) * 128u, len));
  }
  const int qt = (bid & 7) * 4 + (jj & 3), hg = jj >> 2;
  lds_ptr sm = (lds_ptr)smem;
  lds_ptr mi = sm + L_M;
  const int h = lane >> 5, l31 = lane & 31, l15 = lane & 15, g4 = lane >> 4;
  const int tq = (lane & 15) >> 2, tp = lane & 3;
  const auto rs = __builtin_amdgcn_make_buffer_rsrc(ws, 0, WS_END, 0x00020000);
  GAS char* gws = (GAS char*)ws;
  const int rot = (hg + qt) % 3;
  const int s0 = bid & 7;
  const int tq4 = hg >> 1, sel = hg & 1;
  const char* xt = (const char*)x + (size_t)qt * 73728;
  const char* wqs = (const char*)wq + 144 * hg;
  const unsigned vxe = (lane >> 3) * 1152 + (((lane & 7) ^ g4) << 4), vxo = 8192 + (lane >> 3) * 1152 + (((lane & 7) ^ (4 + g4)) << 4);
  const int otok = tid / 9, opc = tid - 9 * otok;

  u32x4 zv, xv;

  if (w == 4) *(LAS int*)(mi + lane * 4) = (lane == M_RDY / 4 + 2 || lane == M_RDY / 4 + 3) ? -8 : (lane == 0 ? -1 : 0);
  auto zscatter = [&]() LAMI {
    lds_ptr zp = sm + L_ZT + ((tid & 7) * 72 + (tid >> 3)) * 2;
#pragma unroll
    for (int e = 0; e < 8; ++e)
      *(LAS unsigned short*)(zp + e * 1152) = (unsigned short)((e & 1) ? zv[e >> 1] >> 16 : zv[e >> 1]);
    lds_signal(mi + M_ZS);
  };
  if (w >= 8 && w < 11) {
    const int j = w - 8;
    const int th = (j + rot) % 3;
    const unsigned stg = L_B + 2 * 27648 + j * 9216;
#pragma unroll
    for (int c = 0; c < 3; ++c) GLDSX2(xt + tq4 * 18432 + (3 * th + c) * 128, sbase + L_X + (3 * j + c) * 8192 + tq4 * 2048);
    {
      const char* wsrc = (const char*)(sel ? wv : wk) + th * 6912 - 2048;
      unsigned vo[3];
#pragma unroll
      for (int i = 0; i < 3; ++i) {
        const int f = 64 * i + lane, row = f / 3, pc = f - 3 * row;
        vo[i] = row * 36 + (pc == 2 ? 20 : 16 * pc) + 2048 - 1024 * i;
      }
#pragma unroll
      for (int p = 0; p < 3; ++p) GLDS3("1024", "2048", vo[0], vo[1], vo[2], wsrc + p * 2304, sbase + stg + p * 3072);
    }
  }
  unsigned epoch = 0;
  if (w == 11) epoch = __hip_atomic_load((gu32*)(gws + WS_EPOCH), RLX_AGENT);
  PSTAMP(5);
  BAR();
  if (w >= 1 && w < 4) TOUCH((const char*)wq + (jj * 162 + (w - 1) * 54) * 128, UMIN(lane, 53) * 128u);
  PSTAMP(6);
  if (w >= 5 && w < 8) {
    const int wi = w - 5;
    unsigned vw[3];
#pragma unroll
    for (int m = 0; m < 3; ++m) {
      const int f = 64 * (3 * wi + m) + lane, row = f / 9;
      vw[m] = row * 1152 + (f - 9 * row) * 16 - 1024 * m;
    }
    const char* xs = xt + (1 - (tq4 >> 1)) * 36864 + wi * 128;
    const char* xs2 = xt + (tq4 ^ 1) * 18432 + wi * 128;
    const unsigned xd = sbase + L_X + wi * 8192 + (1 - (tq4 >> 1)) * 4096, xd2 = sbase + L_X + wi * 8192 + (tq4 ^ 1) * 2048;
    const unsigned wd = sbase + L_B + wi * 3072;
    const int t1 = (rot + 1) % 3, t2 = (rot + 2) % 3;
    GLDSX(xs + rot * 384, xd);
    GLDSX2(xs2 + rot * 384, xd2);
#pragma unroll
    for (int per = 0; per < 3; ++per) GLDS3("1024", "2048", vw[0], vw[1], vw[2], wqs + (192 * rot + 64 * per) * 1152, wd + per * 9216);
    GLDSX(xs + t1 * 384, xd + 24576);
    GLDSX2(xs2 + t1 * 384, xd2 + 24576);
    GLDS3("1024", "2048", vw[0], vw[1], vw[2], wqs + 192 * t1 * 1152, wd + 27648);
    VM_WAIT(9);
    lds_signal(mi + M_C1);
#pragma unroll
    for (int per = 1; per < 3; ++per) GLDS3("1024", "2048", vw[0], vw[1], vw[2], wqs + (192 * t1 + 64 * per) * 1152, wd + 27648 + per * 9216);
    GLDSX(xs + t2 * 384, xd + 49152);
    GLDSX2(xs2 + t2 * 384, xd2 + 49152);
    lds_wait(mi + M_KVC, 3);
    GLDS3("1024", "2048", vw[0], vw[1], vw[2], wqs + 192 * t2 * 1152, wd + 55296);
    VM_WAIT(9);
    lds_signal(mi + M_C2);
#pragma unroll
    for (int per = 1; per < 3; ++per) GLDS3("1024", "2048", vw[0], vw[1], vw[2], wqs + (192 * t2 + 64 * per) * 1152, wd + 55296 + per * 9216);
    STAMP(1);
    VM_WAIT(0);
    lds_signal(mi + M_C3);
    lds_signal(mi + M_SE);
  }

  if (w >= 8) {
    const int j = w - 8, ht = tid - 512;
    __builtin_amdgcn_s_setprio(3);
    if (j < 3) {
      const unsigned stg = L_B + 2 * 27648 + j * 9216;
      lds_ptr wb = sm + stg + (8 * g4 + tq) * 48 + tp * 8;
      lds_ptr wc = sm + stg + (8 * g4 + tq) * 48 + 32 + (tp & 1) * 8;
      lds_ptr xb = sm + L_X + 3 * j * 8192 + (16 * tq4 + l15) * 128;
      VM_WAIT(0);
      STAMP(1);
      bf16x8 fa[6], fc[6], fb[6];
#pragma unroll
      for (int k = 0; k < 6; ++k) {
        fb[k] = lds_frag(xb + (k >> 1) * 8192 + (((4 * (k & 1) + g4) ^ (l15 >> 1)) << 4));
        fa[k] = tr_frag(wb + k * 1536, wb + k * 1536 + 192);
        fc[k] = tr_frag(wc + k * 1536, wc + k * 1536 + 192);
      }
      f32x4 acc = {0}, acr = {0};
      if (sel) {
#pragma unroll
        for (int k = 0; k < 6; ++k) { acc = MFMA16(fb[k], fa[k], acc); acr = MFMA16(fb[k], fc[k], acr); }
      } else {
#pragma unroll
        for (int k = 0; k < 6; ++k) { acc = MFMA16(fa[k], fb[k], acc); acr = MFMA16(fc[k], fb[k], acr); }
      }
      *(LAS f32x4*)(sm + L_KVP + j * 2048 + lane * 16) = acc;
      *(LAS f32x4*)(sm + L_KVP + j * 2048 + 1024 + lane * 16) = acr;
      lds_signal(mi + M_KVC);
      lds_signal(mi + M_C1);
      STAMP(2);
      lds_wait<1>(mi + M_KD, 1);
    } else {
      const unsigned tg = __builtin_amdgcn_readfirstlane(epoch) + 1u;
      if (lane == 0) *(LAS unsigned*)(mi + M_TAG) = tg;
      const int T = 2 * qt + (tq4 >> 1), mt = tq4 & 1, i4 = T & 3, gb = (T >> 2) * KV_GROUP, rp = swap23(16 * mt + l15);
      int sa, sr;
      bool st;
      if (sel) {
        sa = gb + 4096 + i4 * 1216 + mt * 608 + (l15 + 18 * (g4 & 1)) * 16 + (g4 >> 1) * 8;
        sr = gb + 4096 + i4 * 1216 + mt * 608 + (l15 < 8 ? 10 + l15 + 18 * (g4 & 1) : 36 + (g4 & 1)) * 16 + (g4 >> 1) * 8;
        st = l15 >= 6 && l15 <= 8;
      } else {
        sa = gb + i4 * 1024 + (rp + 32 * (g4 & 1)) * 16 + (g4 >> 1) * 8;
        sr = gb + 8960 + i4 * 256 + rp * 8;
        st = g4 == 1;
      }
      lds_wait(mi + M_KVC, 3);
      lds_ptr pp = sm + L_KVP + lane * 16;
      const float ks = sel ? 1.f : 0.23570226039551584f * 1.4426950408889634f;
      const f32x4 va = (*(LAS f32x4*)(pp) + *(LAS f32x4*)(pp + 2048) + *(LAS f32x4*)(pp + 4096)) * ks;
      const f32x4 vr = (*(LAS f32x4*)(pp + 1024) + *(LAS f32x4*)(pp + 3072) + *(LAS f32x4*)(pp + 5120)) * ks;
      const u32x2 a = {pk2(va[0], va[1]), pk2(va[2], va[3])};
      u32x2 r = {pk2(vr[0], vr[1]), pk2(vr[2], vr[3])};
      if (!sel) r = (u32x2){r[1], 0x3f803f80u};
      else if (l15 == 8) r[0] = r[1] = (g4 & 1) ? 0u : 0x3f803f80u;
      ST_SC1_16(((u32x4){a[0], tg, a[1], tg}), WS_KT + 2 * sa);
      if (st) ST_SC1_16(((u32x4){r[0], tg, r[1], tg}), WS_KT + 2 * sr);
      ST_SC1_8(a, WS_KV + sa);
      if (st) ST_SC1_8(r, WS_KV + sr);
      STAMP(1);
      lds_signal(mi + M_KD);
      float n2 = dot2(a[1], dot2(a[0], st ? dot2(r[0], 0.f) : 0.f));
      n2 += xhalf(n2);
      n2 += u2f(__builtin_amdgcn_ds_swizzle(__builtin_bit_cast(int, n2), 0x401f));
      n2 = sel ? 0.f : max16(n2);
      VM_WAIT(0);
      if (lane == 0)
        __hip_atomic_store((gu64*)(gws + WS_FKV + T * 128 + (hg & 3) * 8), ((unsigned long long)tg << 32) | __builtin_bit_cast(unsigned, n2), RLX_AGENT);
      STAMP(2);
    }
    const unsigned tag = *(LAS unsigned*)(mi + M_TAG);
    auto kv_tagged = [&](int st) LAMI {
      u32x4 r[10];
      const int o = WS_KT + ((s0 + st) & 7) * (2 * RSLOT) + lane * 16;
#pragma unroll 1
      for (unsigned spins = 0; spins < SPIN_CAP; ++spins) {
        unsigned bad = 0;
#pragma unroll
        for (int i = 0; i < 10; ++i) r[i] = LD_SC1_16(o + (i == 9 && j == 3 ? 38 : 4 * i + j) * 1024);
#pragma unroll
        for (int i = 0; i < 10; ++i) bad |= (r[i][1] ^ tag) | (r[i][3] ^ tag);
        asm volatile("" ::: "memory");
        if (ALL(bad == 0)) break;
        __builtin_amdgcn_s_sleep(POLL_SLEEP);
      }
      if (st == 0) lds_wait(mi + M_C3B, 5);
#pragma unroll
      for (int i = 0; i < 10; ++i)
        *(LAS u32x2*)(sm + st * RSLOT + (i == 9 && j == 3 ? 38 : 4 * i + j) * 512 + lane * 8) = (u32x2){r[i][0], r[i][2]};
    };
    auto kv_dma = [&](int st) LAMI {
      const char* src = ws + WS_KV + ((s0 + st) & 7) * RSLOT + j * 4992;
      const unsigned dst = sbase + st * RSLOT + j * 4992;
      GLDS4(" sc1", src, lane * 16, lane * 16, dst);
      if (lane < 56) glds_sc1(src + 4096, lane * 16, dst + 4096);
    };
    const unsigned vh3 = (lane == 63 ? 62 : lane) * 16;
    auto hload = [&](int cl, int b) LAMI {
      const char* hsrc = (const char*)(ssmk + (size_t)(72 * hg + cl) * S_) + 4 * j + 2;
      const unsigned dst = sbase + CV_H + b * CV_HB + 2 * j * CV_COPY + 64;
      GLDS4("", hsrc, lane * 16, vh3, dst);
      GLDS4("", hsrc + 2, lane * 16, vh3, dst + CV_COPY);
    };
    auto uload = [&](int cl) LAMI {
      const int tgq = ht & 7;
      const int o = cl < 64 ? (tgq >> 1) * 2048 + (cl & 1) * 1024 + ((tgq & 1) * 32 + (cl >> 1)) * 16
                            : 8192 + (cl & 1) * 512 + (tgq * 4 + ((cl - 64) >> 1)) * 16;
      return LD_SC1_16(WS_UT + ((ht >> 3) * 8 + hg) * 9216 + o);
    };
    const int ce = 64 + (qt >> 2);
    const unsigned short h8a = *(const unsigned short*)(ssmk + (size_t)(72 * hg + 2 * qt + (j >> 1)) * S_ + (lane & 7));
    const unsigned short h8e = *(const unsigned short*)(ssmk + (size_t)(72 * hg + ce) * S_ + (lane & 7));
    const float dve = (qt & 3) ? 0.f : (float)ssmd[72 * hg + ce];
    {
      lds_wait<1>(mi + POLL_AT, POLL_N);
      kv_tagged(0);
      lds_signal(mi + M_RDY);
      STAMP(3);
      kv_tagged(1);
      lds_signal(mi + M_RDY + 4, 2);
      if (j == 3) {
        float kn = 0.f;
#pragma unroll 1
        for (unsigned spins = 0; spins < SPIN_CAP; ++spins) {
          const u32x4 v0 = LD_SC1_16(WS_FKV + lane * 128), v1 = LD_SC1_16(WS_FKV + lane * 128 + 16);
          const bool ok = v0[1] == tag && v0[3] == tag && v1[1] == tag && v1[3] == tag;
          kn = __builtin_fmaxf(u2f(v0[0]), u2f(v1[0]));
          asm volatile("" ::: "memory");
          if (ALL(ok)) break;
          __builtin_amdgcn_s_sleep(POLL_SLEEP);
        }
        lds_signal(mi + M_QR);
        lds_signal(mi + M_DONE);
        kn = wave_max(kn);
        if (lane == 0) *(LAS float*)(mi + M_KN) = kn;
      }
      lds_wait<1>(mi + M_QR, 9);
      kv_dma(2);
      kv_dma(3);
      UNIT_LOAD(l31, 2 * j + h);
      u32x4 gx[3], gv[3];
#pragma unroll
      for (int i = 0; i < 3; ++i) {
        const int cg = UMIN(l31 + 32 * i, 71) * 16;
        gx[i] = *(const u32x4*)(xt + (8 * hg + 2 * j + h) * 1152 + cg);
        gv[i] = *(const u32x4*)((const char*)gw + cg);
      }
      if (j < 3) TOUCH((const char*)wo + (jj * 162 + j * 54) * 128, UMIN(lane, 53) * 128u);
      else TOUCH(ssmk + (size_t)(72 * hg + 2 * qt) * S_, lane * 128u);
      VM_WAIT(20);
      lds_signal(mi + M_RDY + 8, 2);
      lds_signal(mi + M_RDY + 12, 2);
      if (j == 1) {
        UNIT_LOAD(32 + (lane & 3), l31 >> 2);
        UNIT_DO();
        ST_SC1_16(h ? o1 : o0, WS_UT + (qt * 8 + hg) * 9216 + 8192 + lane * 16);
      }
      UNIT_DO();
      {
        float lg = 0.f;
#pragma unroll
        for (int i = 2; i >= 0; --i) {
#pragma unroll
          for (int e = 0; e < 4; ++e)
            lg = __builtin_amdgcn_fdot2_f32_bf16(__builtin_bit_cast(bf16x2, (unsigned)gx[i][e]), __builtin_bit_cast(bf16x2, (unsigned)gv[i][e]), lg, false);
          if (i == 2 && l31 >= 8) lg = 0.f;
        }
        lg = sum16(lg);
        lg += u2f(__builtin_amdgcn_ds_swizzle(__builtin_bit_cast(int, lg), 0x401f));
        const float g = rbf(__builtin_amdgcn_rcpf(1.f + __builtin_amdgcn_exp2f(-1.4426950408889634f * rbf(lg))));
        if (l31 == 0) ST_SC1_4(__builtin_bit_cast(unsigned, g), WS_G + (64 * qt + 8 * hg + 2 * j + h) * 4);
      }
      ST_SC1_16(o0, WS_UT + (qt * 8 + hg) * 9216 + j * 2048 + lane * 16);
      ST_SC1_16(o1, WS_UT + (qt * 8 + hg) * 9216 + j * 2048 + 1024 + lane * 16);
      {
        const u32x4 ones = {~0u, ~0u, ~0u, ~0u};
        const int ob = WS_O + 2 * qt * 36 * 1024 + (4 * hg + j) * 1024 + lane * 16;
        ST_SC1_16(ones, ob);
        ST_SC1_16(ones, ob + 36 * 1024);
        if (j == 0) ST_SC1_16(ones, WS_O + (2 * qt + h) * 36 * 1024 + 32768 + hg * 512 + l31 * 16);
        ST_SC1_16(ones, WS_Z + (72 * hg + 2 * qt) * 4096 + ht * 16);
        ST_SC1_16(ones, WS_Z + (72 * hg + 2 * qt + 1) * 4096 + ht * 16);
      }
      STAMP(4);
      VM_WAIT(0);
      if (lds_ticket(mi + M_UD) == 3u && lane == 0) __hip_atomic_store((gu32*)(gws + WS_FU) + (hg * 32 + qt) * 32, tag, RLX_AGENT);
    }
    auto cvprep = [&](int b, unsigned short h8) LAMI {
      *(LAS u32x4*)(sm + CV_U + (ht >> 7) * CV_UB + ((ht & 127) >> 2) * 80 + (ht & 3) * 16) = (u32x4){0, 0, 0, 0};
      lds_ptr hz = sm + CV_H + b * CV_HB + (lane >> 3) * CV_COPY;
      *(LAS u32x2*)(hz + (lane & 7) * 8) = (u32x2){0, 0};
      if ((lane & 7) <= (lane >> 3)) *(LAS unsigned short*)(hz + 48 + (7 - (lane >> 3) + (lane & 7)) * 2) = h8;
    };
    unsigned hs = 0;
#define HSYNC() do { lds_signal(mi + M_HS); hs += 4; lds_wait<2>(mi + M_HS, hs); } while (0)
    if (lane < 54) {
      const unsigned vo = (lane / 9) * 1152 + (lane % 9) * 16;
#pragma unroll 1
      for (int p = 24 * j; p < 24 * j + 24; p += 4)
        asm volatile("s_mov_b32 m0, %5\n\ts_nop 0\n\tglobal_load_lds_dwordx4 %0, %4\n\tglobal_load_lds_dwordx4 %1, %4 offset:864\n\t"
                     "global_load_lds_dwordx4 %2, %4 offset:1728\n\tglobal_load_lds_dwordx4 %3, %4 offset:2592\n\ts_waitcnt vmcnt(" WO_INFL ")"
                     :: "v"(vo), "v"(vo + 6048), "v"(vo + 12096), "v"(vo + 18144), "s"((const char*)wo + 144 * hg + p * 6912),
                        "s"(sbase + L_B + p * 864) : "memory");
    }
    VM_WAIT(0);
    HSYNC();
    bf16x8 wf[18], we[5];
    {
      const int cb = (32 * (j & 1) + 16 * (g4 & 1) + 4 * tp) * 2;
      lds_ptr wm = sm + L_B + (DH * (j >> 1) + 4 * h + tq) * 144 + cb;
      lds_ptr wr = sm + L_B + (DH * (8 * (j >> 1) + 4 * h + (tq >> 1)) + 16 + (tq & 1)) * 144 + cb;
#pragma unroll
      for (int k = 0; k < 12; ++k) wf[k] = tr_frag(wm + k * 5184, wm + k * 5184 + 1152);
      wm += 12 * 5184;
#pragma unroll
      for (int k = 0; k < 4; ++k) wf[12 + k] = tr_frag(wm + k * 5184, wm + k * 5184 + 1152);
#pragma unroll
      for (int k = 0; k < 2; ++k) wf[16 + k] = tr_frag(wr + k * 41472, wr + k * 41472 + 5184);
      lds_ptr em = sm + L_B + (DH * (8 * j + (g4 >> 1)) + 4 * (g4 & 1) + tq) * 144 + (64 + 4 * (tp & 1)) * 2;
      lds_ptr er = sm + L_B + (DH * (16 * (j & 1) + 4 * g4 + (tq >> 1)) + 16 + (tq & 1)) * 144 + (64 + 4 * (tp & 1)) * 2;
#pragma unroll
      for (int k = 0; k < 4; ++k) we[k] = tr_frag(em + k * 5184, em + k * 5184 + 1152);
      we[4] = tr_frag(er, er + 5184);
    }
    HSYNC();
    HSTAMP(7);
    hload(ce, 0);
    cvprep(0, h8e);
    if (j == 3) {
#pragma unroll 1
      for (unsigned spins = 0; spins < SPIN_CAP; ++spins) {
        bool ok = lane >= 40 || __hip_atomic_load((gu32*)(gws + WS_FU) + (h ? l31 * 32 + qt : hg * 32 + lane) * 32, RLX_AGENT) == tag;
        if (bid == 0) {
#pragma unroll
          for (int i = 0; i < 4; ++i) ok &= __hip_atomic_load((gu32*)(gws + WS_FU) + (64 * i + lane) * 32, RLX_AGENT) == tag;
        }
        if (ALL(ok)) break;
        __builtin_amdgcn_s_sleep(8);
      }
      if (bid == 0 && lane == 0) __hip_atomic_store((gu32*)(gws + WS_EPOCH), tag, RLX_AGENT);
      lds_signal(mi + M_UR);
    } else {
      lds_wait<4>(mi + M_UR, 1);
    }
    NSTAMP(5);
    if (j == 1) *(LAS unsigned*)(mi + M_G + lane * 4) = __builtin_amdgcn_raw_buffer_load_b32(rs, WS_G + (64 * qt + lane) * 4, 0, 16);
    *(LAS u32x4*)(sm + CV_U + 2560 + (ht >> 2) * 80 + (ht & 3) * 16) = uload(ce);
    VM_WAIT(0);
    HSYNC();
    {
      const int d0 = 8 * (qt & 3) + 2 * j;
      lds_ptr ab = sm + CV_H + (l31 & 7) * CV_COPY + (h + (l31 >> 3)) * 16 + 64 * d0;
      lds_ptr bb = sm + CV_U + 2560 + (l31 - d0 - 7) * 80 + (1 - h) * 16;
      asm volatile("" : "+v"(ab), "+v"(bb));
      f32x16 y0 = {0}, y1 = {0};
#pragma unroll
      for (int d = 0; d < 2; ++d) {
#pragma unroll
        for (int s = 0; s < 2; ++s) {
          const bf16x8 a0 = lds_frag(ab + 32 * (2 * d + 1 - s)), b0 = lds_frag(bb + 80 * (7 - d) + 32 * s);
          const bf16x8 b1 = lds_frag(bb + 80 * (39 - d) + 32 * s), a1 = lds_frag(ab + 32 * (2 * d + 65 - s));
          y0 = MFMA32(a0, b0, y0);
          y1 = MFMA32(a0, b1, y1);
          y1 = MFMA32(a1, b0, y1);
          SB0;
        }
      }
      CV_PUT(sm + CV_H + CV_HB + j * 8192 + lane * 16);
      HSYNC();
#pragma unroll
      for (int q = 0; q < 2; ++q)
        ST_SC1_16(__builtin_bit_cast(u32x4, cv_sum(sm + CV_H + CV_HB + (2 * j + q) * 1024 + lane * 16,
                                                   sm + CV_U + 2560 + (32 * (j >> 1) + l31) * 80 + (2 * (j & 1) + q) * 16 + (1 - h) * 8, dve)),
                  WS_ZP + (hg * 32 + qt) * 8192 + (2 * j + q) * 1024 + lane * 16);
    }
    HSYNC();
    NSTAMP(6);
    hload(2 * qt, 0);
    hload(2 * qt + 1, 1);
    cvprep(j >> 1, h8a);
    *(LAS u32x4*)(sm + CV_U + 2560 + (ht >> 2) * 80 + (ht & 3) * 16) = uload(2 * qt);
    *(LAS u32x4*)(sm + CV_U + CV_UB + 2560 + (ht >> 2) * 80 + (ht & 3) * 16) = uload(2 * qt + 1);
    VM_WAIT(0);
#pragma unroll
    for (int i = 0; i < 4; ++i) lds_signal(mi + M_PAIR + 4 * i);
    if (lds_ticket(mi + M_CVR) == 3u && lane == 0) __hip_atomic_store((gu32*)(gws + WS_FZ) + (hg * 32 + qt) * 32, tag, RLX_AGENT);
    if (j == 0) {
#pragma unroll 1
      for (unsigned spins = 0; spins < SPIN_CAP; ++spins) {
        if (ALL(h || __hip_atomic_load((gu32*)(gws + WS_FZ) + (hg * 32 + lane) * 32, RLX_AGENT) == tag)) break;
        __builtin_amdgcn_s_sleep(16);
      }
      asm volatile("" ::: "memory");
      f32x4 sa = {0}, sb = {0};
#pragma unroll
      for (int p = 0; p < 4; ++p) {
        const int zo = WS_ZP + (hg * 32 + (lane >> 3) * 4 + p) * 8192 + (qt >> 4) * 4096 + ((lane >> 2) & 1) * 2048 +
                       (2 * (qt & 15) + (lane & 1) + 32 * ((lane >> 1) & 1)) * 16;
        sa += __builtin_bit_cast(f32x4, LD_SC1_16(zo));
        sb += __builtin_bit_cast(f32x4, LD_SC1_16(zo + 1024));
      }
      zv = (u32x4){pk2(sa[0], sa[1]), pk2(sa[2], sa[3]), pk2(sb[0], sb[1]), pk2(sb[2], sb[3])};
      xv = *(const u32x4*)(xt + otok * 1152 + 144 * hg + opc * 16);
    }
    HSTAMP(8);
    lds_wait<8>(mi + M_DONE + 28, 8);
    __builtin_amdgcn_s_setprio(1);
#pragma unroll 1
    for (int t = 0; t < 2; ++t) {
      lds_ptr ob = sm + (36 * t + (j >> 1)) * 1024 + lane * 16;
      lds_ptr oe = sm + (36 * t + 8 * j + (g4 >> 1)) * 1024 + (l15 + 32 * (g4 & 1)) * 16;
      lds_ptr or_ = sm + (36 * t + 32 + 2 * (j & 1) + (g4 >> 1)) * 1024 + (16 * (j >> 1) + l15 + 32 * (g4 & 1)) * 16;
      lds_wait<1>(mi + (t ? M_OR : M_KVR), 4);
      if (t) NSTAMP(9);
      f32x16 a0 = {0};
      f32x4 e0 = {0}, e1 = {0};
      bf16x8 f[27];
#pragma unroll
      for (int k = 0; k < 31; ++k) {
        if (k < 18) f[k] = lds_frag(ob + k * 2048);
        else if (k < 26) f[k] = lds_frag(oe + ((k - 18) >> 2) * 256 + ((k - 18) & 3) * 2048);
        else if (k == 26) f[k] = lds_frag(or_);
        SB0;
        const int m = k - 4;
        if (m >= 0 && m < 18) a0 = MFMA32(wf[m], f[m], a0);
        else if (m >= 18 && m < 22) e0 = MFMA16(we[m - 18], f[m], e0);
        else if (m >= 22 && m < 26) e1 = MFMA16(we[m - 22], f[m], e1);
        else if (m == 26) { if (j >> 1) e1 = MFMA16(we[4], f[m], e1); else e0 = MFMA16(we[4], f[m], e0); }
        SB0;
      }
      if (t) NSTAMP(10);
      lds_wait<1>(mi + M_MM, 4);
      lds_ptr ap = sm + L_AP + (8 * (j >> 1) + 4 * t) * CV_COPY;
#pragma unroll
      for (int g = 0; g < 4; ++g)
        *(LAS f32x4*)(ap + (l31 >> 3) * CV_COPY + (l31 & 7) * 336 + (j & 1) * 128 + h * 16 + g * 32) = (f32x4){a0[4 * g], a0[4 * g + 1], a0[4 * g + 2], a0[4 * g + 3]};
      if (!h) {
        lds_ptr ae = ap + (l15 >> 3) * CV_COPY + (l15 & 7) * 336 + 256 + (j & 1) * 32 + g4 * 16;
        *(LAS f32x4*)(ae) = e0;
        *(LAS f32x4*)(ae + 2 * CV_COPY) = e1;
      }
      if (j == 0 && t == 0) { lds_wait<1>(mi + M_CE, 4); zscatter(); }
    }
    lds_signal(mi + M_APR);
  } else {
    if (w < 4) {
      const int gt = w >> 1, tt = w & 1, arow = 32 * tt + l31, xsw = (arow >> 1) & 7;
      lds_ptr xb = sm + L_X + arow * 128;
      lds_ptr wb = sm + L_B + (8 * h + tq) * 144 + (36 * gt + 20 * (g4 & 1) + 4 * tp) * 2;
      f32x16 acc = {0};
#pragma unroll 1
      for (int p = 0; p < 3; ++p) {
        if (p == 1) CSTAMP(6);
        if (p == 2) CSTAMP(8);
        lds_wait(mi + M_C1 + 4 * p, p == 1 ? 3 : 6 + p);
        if (p == 0) CSTAMP(5);
        if (p == 1) CSTAMP(7);
        if (p == 2) CSTAMP(9);
        bf16x8 fa[12], fb[12];
#pragma unroll
        for (int k = 0; k < 16; ++k) {
          if (k < 12) {
            fa[k] = tr_frag(wb + k * 2304, wb + k * 2304 + 576);
            fb[k] = lds_frag(xb + (k >> 2) * 8192 + (((2 * (k & 3) + h) ^ xsw) << 4));
          }
          SB0;
          if (k >= 4) acc = MFMA32(fa[k - 4], fb[k - 4], acc);
          SB0;
        }
        if (p < 2) lds_signal(mi + M_C3 + 4 * p);
        xb += 24576;
        wb += 27648;
      }
      STAMP(2);
      u32x4 a;
#pragma unroll
      for (int i = 0; i < 4; ++i) a[i] = pk2(acc[2 * i], acc[2 * i + 1]);
      lds_ptr qe = sm + L_Q + (4 * gt + tt) * 1024;
      *(LAS u32x4*)(qe + lane * 16) = a;
      lds_ptr qo = qe + 2048 + l31 * 16;
      *(LAS unsigned*)(qo + h * 512 + 4) = pk2(acc[8], acc[9]);
      *(LAS unsigned*)(qo + (h ? 8 : 512)) = pk2(acc[10], acc[11]);
      *(LAS unsigned*)(qo + h * 512 + 12) = pk2(acc[12], acc[13]);
      *(LAS unsigned*)(h ? sm + L_Q + 8192 + ((2 * gt + 1) * 64 + arow) * 4 : qo + 520) = pk2(acc[14], acc[15]);
    } else if (w == 5) {
      lds_wait<1>(mi + M_RDY, 4);
      const u32x4 k0 = *(LAS u32x4*)(sm + lane * 16);
      float kn = dot2(k0[3], dot2(k0[2], dot2(k0[1], dot2(k0[0], 0.f))));
      kn = wave_max(kn + xhalf(kn));
      if (lane == 0) *(LAS float*)(mi + M_KN0) = kn;
    } else if (w == 4) {
      __builtin_amdgcn_s_setprio(2);
      lds_ptr xb = sm + L_X + l15 * 128;
      lds_ptr wb = sm + L_B + (8 * g4 + tq) * 144 + (tp == 1 ? 104 : 32);
      f32x4 acc[4] = {{0}, {0}, {0}, {0}};
#pragma unroll 1
      for (int p = 0; p < 3; ++p) {
        lds_wait(mi + M_C1 + 4 * p, p == 1 ? 3 : 6 + p);
        bf16x8 fa[6], fb[24];
#pragma unroll
        for (int k = 0; k < 8; ++k) {
          if (k < 6) {
            fa[k] = tr_frag(wb + k * 4608, wb + k * 4608 + 576);
#pragma unroll
            for (int t = 0; t < 4; ++t) fb[4 * k + t] = lds_frag(xb + (k >> 1) * 8192 + t * 2048 + (((4 * (k & 1) + g4) ^ (l15 >> 1)) << 4));
          }
          SB0;
          if (k >= 2) {
#pragma unroll
            for (int t = 0; t < 4; ++t) acc[t] = MFMA16(fa[k - 2], fb[4 * (k - 2) + t], acc[t]);
          }
          SB0;
        }
        if (p < 2) lds_signal(mi + M_C3 + 4 * p);
        xb += 24576;
        wb += 27648;
      }
      STAMP(2);
      if (g4 < 2) {
#pragma unroll
        for (int t = 0; t < 4; ++t) {
          const int tok = 16 * t + l15;
          *(LAS unsigned*)(sm + L_Q + 8192 + (2 * g4 * 64 + tok) * 4) = pk2(acc[t][0], acc[t][1]);
          *(LAS unsigned*)(sm + L_Q + ((2 * g4 + 1) * 2 + (t >> 1)) * 1024 + (tok & 31) * 16) = pk2(acc[t][2], acc[t][3]);
        }
      }
    }
    lds_signal(mi + M_QD);
    const int hl = w & 3, kh = w >> 2;
    const int voff = 4096 + (l31 < 18 ? h * 288 + l31 * 16 : (l31 == 18 ? 576 : 592));
    lds_wait(mi + M_QD, 8);
    STAMP(3);
    const u32x4 q0u = *(LAS u32x4*)(sm + L_Q + (2 * hl) * 1024 + lane * 16);
    const u32x4 q1u = *(LAS u32x4*)(sm + L_Q + (2 * hl + 1) * 1024 + lane * 16);
    const unsigned qr = *(LAS unsigned*)(sm + L_Q + 8192 + (hl * 64 + lane) * 4);
    const float kn0 = *(LAS float*)(mi + M_KN0);
    lds_signal(mi + M_QR);
    float n0 = 0.f, n1 = 0.f;
#pragma unroll
    for (int i = 0; i < 4; ++i) { n0 = dot2(q0u[i], n0); n1 = dot2(q1u[i], n1); }
    const float nq = dot2(qr, (h ? n1 : n0) + xhalf(h ? n0 : n1));
    const u32x2 qrem_u = {qr, pk2(-__builtin_amdgcn_sqrtf(nq * kn0), 0.f)};
    f32x16 o0, o1;
    float m0 = 0.f, m1 = 0.f;
    STAMP(4); RSTAMP(2);
    {
      int cnt = 8, off = 0, tmp, t2, so = ((s0 + 4) & 7) * RSLOT;
      unsigned ca = sbase + L_M + M_DONE - 4, dst = sbase + w * 2496;
      const unsigned long long ksrc = (unsigned long long)ws + WS_KV + w * 2496 - (sbase + kh * KV_GROUP);
      asm volatile(
      "v_mov_b32 v107, v126\n"
      "v_mov_b32 v108, v127\n"
      "v_mov_b32 v109, v128\n"
      "v_mov_b32 v130, 1\n"
      "ds_read2st64_b64 v[110:113], v109 offset0:0 offset1:1\n"
      "ds_read_b128 v[114:117], v107 offset:0\n"
      "ds_read_b128 v[118:121], v108 offset:0\n"
      "s_mov_b32 vcc_lo, %[first]\n"
      "s_mov_b32 vcc_hi, 0\n"
      "s_setprio 1\n"
      "v_mov_b32 v56, 0\n"
      "v_mov_b32 v57, 0\n"
      "v_mov_b32 v58, 0\n"
      "v_mov_b32 v59, 0\n"
      "v_mov_b32 v60, 0\n"
      "v_mov_b32 v61, 0\n"
      "v_mov_b32 v62, 0\n"
      "v_mov_b32 v63, 0\n"
      "v_mov_b32 v122, 0\n"
      "v_mov_b32 v123, 0\n"
      "v_mov_b32 v124, 0\n"
      "v_mov_b32 v125, 0\n"
      "v_mov_b32 v64, 0\n"
      "v_mov_b32 v65, 0\n"
      "v_mov_b32 v66, 0\n"
      "v_mov_b32 v67, 0\n"
      "v_mov_b32 v68, 0\n"
      "v_mov_b32 v69, 0\n"
      "v_mov_b32 v70, 0\n"
      "v_mov_b32 v71, 0\n"
      "v_mov_b32 v72, 0\n"
      "v_mov_b32 v73, 0\n"
      "v_mov_b32 v74, 0\n"
      "v_mov_b32 v75, 0\n"
      "v_mov_b32 v76, 0\n"
      "v_mov_b32 v77, 0\n"
      "v_mov_b32 v78, 0\n"
      "v_mov_b32 v79, 0\n"
      "s_waitcnt lgkmcnt(1)\n"
      "v_mfma_f32_32x32x4_2b_bf16 v[0:31], v[110:111], v[104:105], 0 cbsz:1 abid:0\n"
      "v_mov_b32 v80, 0\n"
      "v_mov_b32 v81, 0\n"
      "v_mov_b32 v82, 0\n"
      "v_mov_b32 v83, 0\n"
      "v_mov_b32 v84, 0\n"
      "v_mov_b32 v85, 0\n"
      "v_mov_b32 v86, 0\n"
      "v_mov_b32 v87, 0\n"
      "v_mov_b32 v88, 0\n"
      "v_mov_b32 v89, 0\n"
      "v_mov_b32 v90, 0\n"
      "v_mov_b32 v91, 0\n"
      "v_mov_b32 v92, 0\n"
      "v_mov_b32 v93, 0\n"
      "v_mov_b32 v94, 0\n"
      "v_mov_b32 v95, 0\n"
      "s_nop 2\n"
      "v_mfma_f32_32x32x16_bf16 v[0:15], v[114:117], v[96:99], v[0:15]\n"
      "v_mfma_f32_32x32x16_bf16 v[16:31], v[114:117], v[100:103], v[16:31]\n"
      "s_nop 11\n"
      "1:\n"
      "s_cbranch_vccz 4f\n"
      "s_setprio 2\n"
      "4:\n"
      "v_exp_f32 v0, v0\n"
      "v_exp_f32 v1, v1\n"
      "v_smfmac_f32_32x32x32_bf16 v[80:95], v[122:125], v[56:63], v106\n"
      "ds_read_b128 v[114:117], v107 offset:1024\n"
      "ds_read_b128 v[122:125], v108 offset:608\n"
      "v_exp_f32 v2, v2\n"
      "v_exp_f32 v3, v3\n"
      "v_exp_f32 v4, v4\n"
      "v_exp_f32 v5, v5\n"
      "v_mfma_f32_32x32x4_2b_bf16 v[32:63], v[110:111], v[104:105], 0 cbsz:1 abid:1\n"
      "v_exp_f32 v6, v6\n"
      "v_exp_f32 v7, v7\n"
      "v_exp_f32 v8, v8\n"
      "v_exp_f32 v9, v9\n"
      "v_exp_f32 v10, v10\n"
      "v_exp_f32 v11, v11\n"
      "v_exp_f32 v12, v12\n"
      "s_waitcnt lgkmcnt(2)\n"
      "v_exp_f32 v13, v13\n"
      "v_smfmac_f32_32x32x32_bf16 v[64:79], v[118:121], v[0:7], v106\n"
      "v_exp_f32 v14, v14\n"
      "v_exp_f32 v15, v15\n"
      "v_exp_f32 v16, v16\n"
      "s_waitcnt lgkmcnt(0)\n"
      "v_exp_f32 v17, v17\n"
      "v_smfmac_f32_32x32x32_bf16 v[64:79], v[122:125], v[8:15], v106\n"
      "v_exp_f32 v18, v18\n"
      "v_exp_f32 v19, v19\n"
      "v_exp_f32 v20, v20\n"
      "v_exp_f32 v21, v21\n"
      "v_mfma_f32_32x32x16_bf16 v[32:47], v[114:117], v[96:99], v[32:47]\n"
      "v_exp_f32 v22, v22\n"
      "v_exp_f32 v23, v23\n"
      "v_exp_f32 v24, v24\n"
      "v_exp_f32 v25, v25\n"
      "v_smfmac_f32_32x32x32_bf16 v[80:95], v[118:121], v[16:23], v106\n"
      "v_exp_f32 v26, v26\n"
      "ds_read_b128 v[118:121], v108 offset:1216\n"
      "v_exp_f32 v27, v27\n"
      "v_exp_f32 v28, v28\n"
      "v_exp_f32 v29, v29\n"
      "v_mfma_f32_32x32x16_bf16 v[48:63], v[114:117], v[100:103], v[48:63]\n"
      "v_exp_f32 v30, v30\n"
      "v_exp_f32 v31, v31\n"
      "v_exp_f32 v32, v32\n"
      "v_exp_f32 v33, v33\n"
      "v_smfmac_f32_32x32x32_bf16 v[80:95], v[122:125], v[24:31], v106\n"
      "ds_read_b128 v[114:117], v107 offset:2048\n"
      "ds_read_b128 v[122:125], v108 offset:1824\n"
      "ds_read_b32 v131, v129\n"
      "v_exp_f32 v34, v34\n"
      "v_exp_f32 v35, v35\n"
      "v_exp_f32 v36, v36\n"
      "v_exp_f32 v37, v37\n"
      "v_mfma_f32_32x32x4_2b_bf16 v[0:31], v[112:113], v[104:105], 0 cbsz:1 abid:0\n"
      "v_exp_f32 v38, v38\n"
      "v_exp_f32 v39, v39\n"
      "v_exp_f32 v40, v40\n"
      "v_exp_f32 v41, v41\n"
      "v_exp_f32 v42, v42\n"
      "v_exp_f32 v43, v43\n"
      "v_exp_f32 v44, v44\n"
      "s_waitcnt lgkmcnt(3)\n"
      "v_exp_f32 v45, v45\n"
      "v_smfmac_f32_32x32x32_bf16 v[64:79], v[118:121], v[32:39], v106\n"
      "v_exp_f32 v46, v46\n"
      "v_exp_f32 v47, v47\n"
      "v_exp_f32 v48, v48\n"
      "s_waitcnt lgkmcnt(0)\n"
      "s_waitcnt vmcnt(0)\n"
      "s_mov_b64 exec, 1\n"
      "ds_add_u32 v129, v130 offset:76\n"
      "s_mov_b64 exec, -1\n"
      "v_exp_f32 v49, v49\n"
      "v_smfmac_f32_32x32x32_bf16 v[64:79], v[122:125], v[40:47], v106\n"
      "v_exp_f32 v50, v50\n"
      "v_exp_f32 v51, v51\n"
      "v_exp_f32 v52, v52\n"
      "v_exp_f32 v53, v53\n"
      "v_mfma_f32_32x32x16_bf16 v[0:15], v[114:117], v[96:99], v[0:15]\n"
      "v_exp_f32 v54, v54\n"
      "v_exp_f32 v55, v55\n"
      "v_exp_f32 v56, v56\n"
      "v_exp_f32 v57, v57\n"
      "v_smfmac_f32_32x32x32_bf16 v[80:95], v[118:121], v[48:55], v106\n"
      "v_exp_f32 v58, v58\n"
      "ds_read_b128 v[118:121], v108 offset:2432\n"
      "v_exp_f32 v59, v59\n"
      "v_exp_f32 v60, v60\n"
      "v_exp_f32 v61, v61\n"
      "v_mfma_f32_32x32x16_bf16 v[16:31], v[114:117], v[100:103], v[16:31]\n"
      "v_exp_f32 v62, v62\n"
      "v_exp_f32 v63, v63\n"
      "v_readfirstlane_b32 %[tmp], v131\n"
      "s_and_b32 %[t2], %[cnt], 12\n"
      "s_cmp_lg_u32 %[t2], 4\n"
      "s_cbranch_scc1 8f\n"
      "s_cmp_lt_u32 %[tmp], 8\n"
      "s_cbranch_scc1 7f\n"
      "9:\n"
      "s_mov_b32 m0, %[dst]\n"
      "s_add_u32 s96, %[slo], %[so]\n"
      "s_addc_u32 s97, %[shi], 0\n"
      "global_load_lds_dwordx4 v126, s[96:97] sc1\n"
      "global_load_lds_dwordx4 v126, s[96:97] offset:1024 sc1\n"
      "s_mov_b64 exec, 0xfffffff\n"
      "global_load_lds_dwordx4 v126, s[96:97] offset:2048 sc1\n"
      "s_mov_b64 exec, -1\n"
      "s_add_u32 %[dst], %[dst], 19968\n"
      "s_add_u32 %[so], %[so], 19968\n"
      "s_cmp_eq_u32 %[so], 159744\n"
      "s_cselect_b32 %[so], 0, %[so]\n"
      "8:\n"
      "v_exp_f32 v0, v0\n"
      "v_exp_f32 v1, v1\n"
      "v_smfmac_f32_32x32x32_bf16 v[80:95], v[122:125], v[56:63], v106\n"
      "ds_read_b128 v[114:117], v107 offset:3072\n"
      "ds_read_b128 v[122:125], v108 offset:3040\n"
      "ds_read_b32 v131, v129 offset:72\n"
      "v_exp_f32 v2, v2\n"
      "v_exp_f32 v3, v3\n"
      "v_exp_f32 v4, v4\n"
      "v_exp_f32 v5, v5\n"
      "v_mfma_f32_32x32x4_2b_bf16 v[32:63], v[112:113], v[104:105], 0 cbsz:1 abid:1\n"
      "v_exp_f32 v6, v6\n"
      "v_exp_f32 v7, v7\n"
      "v_exp_f32 v8, v8\n"
      "v_exp_f32 v9, v9\n"
      "v_exp_f32 v10, v10\n"
      "v_exp_f32 v11, v11\n"
      "v_exp_f32 v12, v12\n"
      "s_waitcnt lgkmcnt(3)\n"
      "v_exp_f32 v13, v13\n"
      "v_smfmac_f32_32x32x32_bf16 v[64:79], v[118:121], v[0:7], v106\n"
      "v_exp_f32 v14, v14\n"
      "v_exp_f32 v15, v15\n"
      "v_exp_f32 v16, v16\n"
      "s_waitcnt lgkmcnt(0)\n"
      "v_exp_f32 v17, v17\n"
      "v_smfmac_f32_32x32x32_bf16 v[64:79], v[122:125], v[8:15], v106\n"
      "v_exp_f32 v18, v18\n"
      "v_exp_f32 v19, v19\n"
      "v_exp_f32 v20, v20\n"
      "v_exp_f32 v21, v21\n"
      "v_mfma_f32_32x32x16_bf16 v[32:47], v[114:117], v[96:99], v[32:47]\n"
      "v_exp_f32 v22, v22\n"
      "v_exp_f32 v23, v23\n"
      "v_exp_f32 v24, v24\n"
      "v_exp_f32 v25, v25\n"
      "v_smfmac_f32_32x32x32_bf16 v[80:95], v[118:121], v[16:23], v106\n"
      "v_exp_f32 v26, v26\n"
      "ds_read_b128 v[118:121], v108 offset:3648\n"
      "v_exp_f32 v27, v27\n"
      "v_exp_f32 v28, v28\n"
      "v_exp_f32 v29, v29\n"
      "v_mfma_f32_32x32x16_bf16 v[48:63], v[114:117], v[100:103], v[48:63]\n"
      "v_exp_f32 v30, v30\n"
      "v_exp_f32 v31, v31\n"
      "s_cbranch_vccz 5f\n"
      "s_setprio 0\n"
      "5:\n"
      "v_readfirstlane_b32 %[tmp], v131\n"
      "s_add_u32 %[off], %[off], 19968\n"
      "s_cmp_eq_u32 %[off], 79872\n"
      "s_cselect_b32 %[off], 0, %[off]\n"
      "s_cmp_lt_i32 %[tmp], 8\n"
      "s_cbranch_scc1 3f\n"
      "2:\n"
      "v_add_u32 v109, %[off], v128\n"
      "ds_read2st64_b64 v[110:113], v109 offset0:0 offset1:1\n"
      "v_add_u32 v107, %[off], v126\n"
      "v_exp_f32 v32, v32\n"
      "v_exp_f32 v33, v33\n"
      "v_smfmac_f32_32x32x32_bf16 v[80:95], v[122:125], v[24:31], v106\n"
      "ds_read_b128 v[114:117], v107 offset:0\n"
      "ds_read_b128 v[122:125], v108 offset:4256\n"
      "v_exp_f32 v34, v34\n"
      "v_exp_f32 v35, v35\n"
      "v_exp_f32 v36, v36\n"
      "s_waitcnt lgkmcnt(2)\n"
      "v_exp_f32 v37, v37\n"
      "v_mfma_f32_32x32x4_2b_bf16 v[0:31], v[110:111], v[104:105], 0 cbsz:1 abid:0\n"
      "v_exp_f32 v38, v38\n"
      "v_exp_f32 v39, v39\n"
      "v_exp_f32 v40, v40\n"
      "v_exp_f32 v41, v41\n"
      "v_exp_f32 v42, v42\n"
      "v_exp_f32 v43, v43\n"
      "v_exp_f32 v44, v44\n"
      "v_exp_f32 v45, v45\n"
      "v_smfmac_f32_32x32x32_bf16 v[64:79], v[118:121], v[32:39], v106\n"
      "v_exp_f32 v46, v46\n"
      "v_exp_f32 v47, v47\n"
      "v_exp_f32 v48, v48\n"
      "s_waitcnt lgkmcnt(0)\n"
      "s_mov_b64 exec, 1\n"
      "ds_add_u32 v129, v130 offset:4\n"
      "s_mov_b64 exec, -1\n"
      "v_exp_f32 v49, v49\n"
      "v_smfmac_f32_32x32x32_bf16 v[64:79], v[122:125], v[40:47], v106\n"
      "v_exp_f32 v50, v50\n"
      "v_exp_f32 v51, v51\n"
      "v_exp_f32 v52, v52\n"
      "v_exp_f32 v53, v53\n"
      "v_mfma_f32_32x32x16_bf16 v[0:15], v[114:117], v[96:99], v[0:15]\n"
      "v_exp_f32 v54, v54\n"
      "v_exp_f32 v55, v55\n"
      "v_exp_f32 v56, v56\n"
      "v_exp_f32 v57, v57\n"
      "v_smfmac_f32_32x32x32_bf16 v[80:95], v[118:121], v[48:55], v106\n"
      "v_add_u32 v108, %[off], v127\n"
      "v_add_u32 v129, 4, v129\n"
      "v_exp_f32 v58, v58\n"
      "ds_read_b128 v[118:121], v108 offset:0\n"
      "v_exp_f32 v59, v59\n"
      "v_exp_f32 v60, v60\n"
      "v_exp_f32 v61, v61\n"
      "v_mfma_f32_32x32x16_bf16 v[16:31], v[114:117], v[100:103], v[16:31]\n"
      "v_exp_f32 v62, v62\n"
      "v_exp_f32 v63, v63\n"
      "s_sub_u32 %[cnt], %[cnt], 1\n"
      "s_cmp_lg_u32 %[cnt], 0\n"
      "s_cbranch_scc1 1b\n"
      "s_branch 6f\n"
      "3:\n"
      "s_sleep 1\n"
      "ds_read_b32 v131, v129 offset:72\n"
      "s_waitcnt lgkmcnt(0)\n"
      "v_readfirstlane_b32 %[tmp], v131\n"
      "s_nop 0\n"
      "s_cmp_lt_i32 %[tmp], 8\n"
      "s_cbranch_scc1 3b\n"
      "s_branch 2b\n"
      "7:\n"
      "s_sleep 1\n"
      "ds_read_b32 v131, v129\n"
      "s_waitcnt lgkmcnt(0)\n"
      "v_readfirstlane_b32 %[tmp], v131\n"
      "s_nop 0\n"
      "s_cmp_lt_u32 %[tmp], 8\n"
      "s_cbranch_scc1 7b\n"
      "s_branch 9b\n"
      "6:\n"
      "v_smfmac_f32_32x32x32_bf16 v[80:95], v[122:125], v[56:63], v106\n"
      "s_setprio 0\n"
      "s_waitcnt lgkmcnt(0)\n"
      "s_nop 15\n"
      "s_nop 7\n"
      : "=&{v[64:79]}"(o0), "=&{v[80:95]}"(o1), "+{v129}"(ca), [cnt] "+s"(cnt), [off] "+s"(off), [tmp] "=&s"(tmp),
        [t2] "=&s"(t2), [so] "+s"(so), [dst] "+s"(dst)
      : "{v[96:99]}"(q0u), "{v[100:103]}"(q1u), "{v[104:105]}"(qrem_u), "{v106}"(SPIDX),
        "{v126}"(sbase + kh * KV_GROUP + lane * 16), "{v127}"(sbase + kh * KV_GROUP + voff),
        "{v128}"(sbase + kh * KV_GROUP + 8960 + lane * 8), [first] "s"(1 - kh), [slo] "s"((unsigned)ksrc), [shi] "s"((unsigned)(ksrc >> 32))
      : "v0", "v1", "v2", "v3", "v4", "v5", "v6", "v7", "v8", "v9", "v10", "v11", "v12", "v13", "v14", "v15",
        "v16", "v17", "v18", "v19", "v20", "v21", "v22", "v23", "v24", "v25", "v26", "v27", "v28", "v29", "v30", "v31",
        "v32", "v33", "v34", "v35", "v36", "v37", "v38", "v39", "v40", "v41", "v42", "v43", "v44", "v45", "v46", "v47",
        "v48", "v49", "v50", "v51", "v52", "v53", "v54", "v55", "v56", "v57", "v58", "v59", "v60", "v61", "v62", "v63",
        "v107", "v108", "v109", "v110", "v111", "v112", "v113", "v114", "v115", "v116", "v117", "v118", "v119", "v120", "v121", "v122",
        "v123", "v124", "v125", "v130", "v131", "s96", "s97", "vcc", "scc", "memory");
    }
    xv = *(const u32x4*)(xt + otok * 1152 + 144 * hg + opc * 16);
    const float dv = (float)ssmd[72 * hg + 2 * qt + (hl >> 1)];
    if (!ALL(nq * *(LAS float*)(mi + M_KN) < 47.f * 47.f)) {
      m0 = m1 = -__builtin_inff();
      o0 = o1 = (f32x16){0};
      const bf16x8 q0 = __builtin_bit_cast(bf16x8, q0u), q1 = __builtin_bit_cast(bf16x8, q1u);
      const s16x4 qrem = __builtin_bit_cast(s16x4, (u32x2){qr, 0u});
#pragma unroll 1
      for (int t = 0; t < 32; ++t) {
        const int g = WS_KV + (t >> 2) * RSLOT + kh * KV_GROUP;
        const u32x2 kr = __builtin_bit_cast(u32x2, __builtin_amdgcn_raw_buffer_load_b64(rs, g + 8960 + (t & 3) * 256 + l31 * 8, 0, 16));
        const bf16x8 km = __builtin_bit_cast(bf16x8, LD_SC1_16(g + (t & 3) * 1024 + lane * 16));
        const bf16x8 va = __builtin_bit_cast(bf16x8, LD_SC1_16(g + voff + (t & 3) * 1216));
        const bf16x8 vb = __builtin_bit_cast(bf16x8, LD_SC1_16(g + voff + (t & 3) * 1216 + 608));
        const f32x32 z = {0};
        const f32x32 r = __builtin_amdgcn_mfma_f32_32x32x4bf16_1k(__builtin_bit_cast(s16x4, kr), qrem, z, 0, 0, 0);
        online_step(MFMA32(km, q0, lo16(r)), m0, o0, va, vb);
        online_step(MFMA32(km, q1, hi16(r)), m1, o1, va, vb);
      }
    }
    HSTAMP(9);
    lds_ptr ex = sm + L_EX + hl * 6144 + lane * 16;
    if (kh) {
      *(LAS f32x4*)(ex) = (f32x4){o0[0], o0[1], o0[2], o0[3]};
      *(LAS f32x4*)(ex + 1024) = (f32x4){o0[4], o0[5], o0[6], o0[7]};
      *(LAS f32x4*)(ex + 2048) = (f32x4){o0[8], o0[9], o0[10], m0};
      *(LAS f32x4*)(ex + 3072) = (f32x4){o1[0], o1[1], o1[2], o1[3]};
      *(LAS f32x4*)(ex + 4096) = (f32x4){o1[4], o1[5], o1[6], o1[7]};
      *(LAS f32x4*)(ex + 5120) = (f32x4){o1[8], o1[9], o1[10], m1};
      lds_signal(mi + M_PAIR + 4 * hl);
    }
    lds_wait<1>(mi + M_PAIR + 4 * hl, 5);
    STAMP(10); RSTAMP(3);
    if (!kh) {
      __builtin_amdgcn_s_setprio(3);
#pragma unroll
      for (int t = 0; t < 2; ++t) {
        const f32x16 keep = t ? o1 : o0;
        const f32x4 t0 = *(LAS f32x4*)(ex + t * 3072), t1 = *(LAS f32x4*)(ex + t * 3072 + 1024), t2 = *(LAS f32x4*)(ex + t * 3072 + 2048);
        const float mk = t ? m1 : m0, mm = __builtin_fmaxf(mk, t2[3]);
        float fa = __builtin_amdgcn_exp2f(mk - mm), fb = __builtin_amdgcn_exp2f(t2[3] - mm);
        const float l = keep[10] * fa + t2[2] * fb, lx = xhalf(l);
        const float inv = __builtin_amdgcn_rcpf(h ? lx : l);
        fa *= inv;
        fb *= inv;
        float o[10];
#pragma unroll
        for (int i = 0; i < 10; ++i) o[i] = keep[i] * fa + (i < 4 ? t0[i & 3] : (i < 8 ? t1[i & 3] : t2[i & 3])) * fb;
        u32x4 a;
#pragma unroll
        for (int i = 0; i < 4; ++i) a[i] = UMIN(pk2(o[2 * i], o[2 * i + 1]), 0xfffffffeu);
        const int ob = WS_O + (2 * qt + t) * 36 * 1024;
        ST_SC1_16(a, ob + (4 * hg + hl) * 1024 + lane * 16);
        if (!h) ST_SC1_4(UMIN(pk2(o[8], o[9]), 0xfffffffeu), ob + 32768 + (4 * hg + hl) * 128 + l31 * 4);
      }
      lds_signal(mi + M_EXD);
      __builtin_amdgcn_s_setprio(1);
      HSTAMP(5);
      __builtin_amdgcn_s_sleep(O_DELAY);
      u32x4 of[18];
#pragma unroll
      for (int i = 0; i < 18; ++i) of[i] = LD_SC1_16(WS_O + (2 * qt * 36 + (i / 9) * 36 + 9 * hl + i % 9) * 1024 + lane * 16);
      lds_wait<1>(mi + M_EXD, 4);
#pragma unroll
      for (int t = 0; t < 2; ++t) {
        unsigned mx = 0;
#pragma unroll
        for (int i = 9 * t; i < 9 * t + 9; ++i) mx = UMAX(UMAX(mx, UMAX(of[i][0], of[i][1])), UMAX(of[i][2], of[i][3]));
        if (__builtin_expect(!ALL(mx != ~0u), 0)) {
#pragma unroll 1
          for (unsigned spins = 0; spins < SPIN_CAP; ++spins) {
            asm volatile("" ::: "memory");
#pragma unroll
            for (int i = 9 * t; i < 9 * t + 9; ++i) of[i] = LD_SC1_16(WS_O + (2 * qt * 36 + t * 36 + 9 * hl + i % 9) * 1024 + lane * 16);
            mx = 0;
#pragma unroll
            for (int i = 9 * t; i < 9 * t + 9; ++i) mx = UMAX(UMAX(mx, UMAX(of[i][0], of[i][1])), UMAX(of[i][2], of[i][3]));
            if (ALL(mx != ~0u)) break;
            __builtin_amdgcn_s_sleep(2);
          }
        }
#pragma unroll
        for (int i = 0; i < 9; ++i) {
          if (i >= 5 && hl == 3) {
            lds_ptr p = sm + (36 * t + 27 + i) * 1024 + (4 * (lane & 7) + 32 * h) * 16 + ((lane >> 3) & 3) * 4;
#pragma unroll
            for (int e = 0; e < 4; ++e) *(LAS unsigned*)(p + e * 16) = of[9 * t + i][e];
          } else {
            *(LAS u32x4*)(sm + (36 * t + 9 * hl + i) * 1024 + lane * 16) = of[9 * t + i];
          }
        }
        if (t == 0) lds_signal(mi + M_KVR);
      }
      lds_signal(mi + M_OR);
      __builtin_amdgcn_s_setprio(0);
      HSTAMP(6);
    } else {
      const int b = hl >> 1, o = hl & 1;
      lds_ptr ab = sm + CV_H + b * CV_HB + (l31 & 7) * CV_COPY + (h + (l31 >> 3)) * 16 + 1024 * o;
      lds_ptr bb = sm + CV_U + b * CV_UB + 2560 + (l31 - 16 * o - 7) * 80 + (1 - h) * 16;
      f32x16 y0 = {0}, y1 = {0};
      bf16x8 fx[8], fy[8];
      __builtin_amdgcn_s_setprio(2);
      asm volatile("" : "+v"(ab), "+v"(bb));
      CV_LD(fx, 0);
#pragma unroll 1
      for (int it = 0; it < 4; ++it) {
        asm volatile("" : "+v"(ab), "+v"(bb));
        CV_STEP(fx, fy, 1);
        CV_STEP(fy, fx, 2);
        CV_STEP(fx, fy, 3);
        CV_STEP(fy, fx, 4);
        ab += 256;
        bb -= 320;
      }
      HSTAMP(5);
      lds_ptr px = sm + CV_H + b * CV_HB + h * CV_COPY + l31 * 16;
      {
        const f32x16 give = o ? y0 : y1;
#pragma unroll
        for (int g = 0; g < 4; ++g) *(LAS f32x4*)(px + o * 3200 + g * 2 * CV_COPY) = (f32x4){give[4 * g], give[4 * g + 1], give[4 * g + 2], give[4 * g + 3]};
        if (o) y0 = y1;
      }
      lds_signal(mi + M_AS + 4 * b);
      lds_signal(mi + M_MM);
      u32x2 uu[4];
#pragma unroll
      for (int g = 0; g < 4; ++g) uu[g] = *(LAS u32x2*)(sm + CV_U + b * CV_UB + 2560 + (32 * o + l31) * 80 + g * 16 + (1 - h) * 8);
#pragma unroll
      for (int g = 0; g < 4; ++g) {
        y0[4 * g] = __builtin_fmaf(bf_hi(uu[g][1]), dv, y0[4 * g]);
        y0[4 * g + 1] = __builtin_fmaf(bf_lo(uu[g][1]), dv, y0[4 * g + 1]);
        y0[4 * g + 2] = __builtin_fmaf(bf_hi(uu[g][0]), dv, y0[4 * g + 2]);
        y0[4 * g + 3] = __builtin_fmaf(bf_lo(uu[g][0]), dv, y0[4 * g + 3]);
      }
      lds_wait(mi + M_AS + 4 * b, 2);
      u32x4 z[2];
#pragma unroll
      for (int g = 0; g < 4; ++g) {
        const f32x4 t = *(LAS f32x4*)(px + (1 - o) * 3200 + g * 2 * CV_COPY);
        z[g >> 1][2 * (g & 1)] = UMIN(pk2(t[0] + y0[4 * g], t[1] + y0[4 * g + 1]), 0xfffffffeu);
        z[g >> 1][2 * (g & 1) + 1] = UMIN(pk2(t[2] + y0[4 * g + 2], t[3] + y0[4 * g + 3]), 0xfffffffeu);
      }
      const int zo = WS_Z + (72 * hg + 2 * qt + b) * 4096 + o * 2048 + lane * 16;
      ST_SC1_16(z[0], zo);
      ST_SC1_16(z[1], zo + 1024);
      lds_signal(mi + M_CE);
      __builtin_amdgcn_s_setprio(0);
      HSTAMP(6);
    }
    lds_wait<2>(mi + M_CE, 4);
    __builtin_amdgcn_s_sleep(Z_DELAY);
#pragma unroll 1
    for (unsigned spins = 0; spins < SPIN_CAP; ++spins) {
      zv = LD_SC1_16(WS_Z + (72 * hg + (tid >> 3)) * 4096 + (qt >> 4) * 2048 + ((tid >> 2) & 1) * 1024 +
                     (2 * (qt & 15) + (tid & 1) + 32 * ((tid >> 1) & 1)) * 16);
      asm volatile("" ::: "memory");
      if (ALL(UMAX(UMAX(zv[0], zv[1]), UMAX(zv[2], zv[3])) != ~0u)) break;
      __builtin_amdgcn_s_sleep(2);
    }
    HSTAMP(7);
    zscatter();
  }

  if (w < 9) {
    lds_ptr ap = sm + L_AP + (otok >> 3) * CV_COPY + (otok & 7) * 336 + opc * 32;
    lds_wait<1>(mi + M_APR, 4);
    const float gq = *(LAS float*)(mi + M_G + otok * 4), omg = rbf(1.f - gq);
    f32x4 a0 = *(LAS f32x4*)(ap) + *(LAS f32x4*)(ap + CV_HB), a1 = *(LAS f32x4*)(ap + 16) + *(LAS f32x4*)(ap + CV_HB + 16);
    if (opc == 8) {
      a0 += *(LAS f32x4*)(ap + 32) + *(LAS f32x4*)(ap + CV_HB + 32);
      a1 += *(LAS f32x4*)(ap + 48) + *(LAS f32x4*)(ap + CV_HB + 48);
    }
    a0 *= gq;
    a1 *= gq;
    f32x4 x0 = {omg * bf_lo(xv[0]), omg * bf_hi(xv[0]), omg * bf_lo(xv[1]), omg * bf_hi(xv[1])};
    f32x4 x1 = {omg * bf_lo(xv[2]), omg * bf_hi(xv[2]), omg * bf_lo(xv[3]), omg * bf_hi(xv[3])};
    lds_ptr zq = sm + L_ZT + ((otok >> 5) + ((otok >> 1) & 2) + ((otok >> 2) & 4) + 8 * (otok & 3) + 4 * (otok & 8)) * 144 + opc * 16;
    u32x4* op = (u32x4*)(out + (size_t)(64 * qt + otok) * D_ + 72 * hg + 8 * opc);
    asm volatile("" : "+v"(a0), "+v"(a1), "+v"(x0), "+v"(x1), "+v"(zq), "+v"(op));
    lds_wait(mi + M_ZS, 9);
    if (w < 8) HSTAMP(8);
    const u32x4 z = *(LAS u32x4*)zq;
    *(GAS u32x4*)op = (u32x4){pk2(__builtin_fmaf(x0[0], bf_lo(z[0]), a0[0]), __builtin_fmaf(x0[1], bf_hi(z[0]), a0[1])),
                              pk2(__builtin_fmaf(x0[2], bf_lo(z[1]), a0[2]), __builtin_fmaf(x0[3], bf_hi(z[1]), a0[3])),
                              pk2(__builtin_fmaf(x1[0], bf_lo(z[2]), a1[0]), __builtin_fmaf(x1[1], bf_hi(z[2]), a1[1])),
                              pk2(__builtin_fmaf(x1[2], bf_lo(z[3]), a1[2]), __builtin_fmaf(x1[3], bf_hi(z[3]), a1[3]))};
  }
  STAMP_FLUSH(w < 4 ? 0 : (w < 8 ? 1 : (w < 11 ? 2 : 3)));
  asm volatile("fk_end:");
}

extern "C" void kernel_launch(void* const* d_in, const int* in_sizes, int n_in, void* d_out, int out_size,
                              void* d_ws, size_t ws_size, hipStream_t stream) {
  fused_kernel<<<256, 768, 0, stream>>>((const __bf16*)d_in[0], (const __bf16*)d_in[1], (const __bf16*)d_in[2],
                                        (const __bf16*)d_in[3], (const __bf16*)d_in[4], (const __bf16*)d_in[6],
                                        (const __bf16*)d_in[7], (const __bf16*)d_in[8], (const __bf16*)d_in[9],
                                        (const __bf16*)d_in[10], (const __bf16*)d_in[11], (__bf16*)d_out, (char*)d_ws);
}
```

```cpp
#include <hip/hip_runtime.h>
#include <hip/hip_bf16.h>
#include <stdint.h>
#define ENTRY 0

typedef __attribute__((ext_vector_type(2))) __bf16 bf16x2;
typedef __attribute__((ext_vector_type(4))) __bf16 bf16x4;
typedef __attribute__((ext_vector_type(8))) __bf16 bf16x8;
typedef __attribute__((ext_vector_type(16))) __bf16 bf16x16;
typedef __attribute__((ext_vector_type(4))) short s16x4;
typedef __attribute__((ext_vector_type(2))) float f32x2;
typedef __attribute__((ext_vector_type(4))) float f32x4;
typedef __attribute__((ext_vector_type(16))) float f32x16;
typedef __attribute__((ext_vector_type(32))) float f32x32;
typedef __attribute__((ext_vector_type(2))) unsigned u32x2;
typedef __attribute__((ext_vector_type(4))) unsigned u32x4;
#define LAS __attribute__((address_space(3)))
#define GAS __attribute__((address_space(1)))
typedef LAS char* lds_ptr;
typedef GAS unsigned gu32;
typedef GAS unsigned long long gu64;
#ifdef __HIP_DEVICE_COMPILE__
#define NOPK __attribute__((target("no-packed-fp32-ops")))
#else
#define NOPK
#endif
#define DEVI __device__ __forceinline__ NOPK
#define LAMI __attribute__((always_inline)) NOPK
#define TIDX __builtin_amdgcn_workitem_id_x()
#define BIDX __builtin_amdgcn_workgroup_id_x()
#define ALL(p) (__builtin_amdgcn_ballot_w64(!(p)) == 0ull)
#define UMIN(a, b) __builtin_elementwise_min((unsigned)(a), (unsigned)(b))
#define UMAX(a, b) __builtin_elementwise_max((unsigned)(a), (unsigned)(b))
#define RLX_AGENT __ATOMIC_RELAXED, __HIP_MEMORY_SCOPE_AGENT
#define RLX_WG __ATOMIC_RELAXED, __HIP_MEMORY_SCOPE_WORKGROUP

constexpr int S_ = 2048, D_ = 576, DH = 18;

constexpr int KV_GROUP = 9984;
constexpr int WS_KV = 0;
constexpr int WS_EPOCH = 192 * 1024;
constexpr int WS_FKV = WS_EPOCH + 1024;
constexpr int WS_FU = WS_FKV + 8192;
constexpr int WS_FZ = WS_FU + 32768;
constexpr int WS_G = WS_FZ + 32768;
constexpr int WS_KT = 320 * 1024;
constexpr int WS_UT = 640 * 1024;
constexpr int WS_Z = WS_UT + D_ * S_ * 2;
constexpr int WS_O = WS_Z + D_ * S_ * 2;
constexpr int WS_ZP = WS_O + D_ * S_ * 2;
constexpr int WS_END = WS_ZP + 64 * 4 * 8192;

constexpr int RSLOT = 2 * KV_GROUP;
constexpr int L_X = 0;
constexpr int L_KVP = 73728;
constexpr int L_B = 80000;
constexpr int L_M = 162944;
constexpr int CV_COPY = 4160;
constexpr int CV_H = L_B, CV_HB = 8 * CV_COPY, CV_U = L_B + 2 * CV_HB, CV_UB = 7680;
constexpr int L_Q = L_B + 8192;
constexpr int M_DONE = 0, M_C1 = 32, M_C2 = 36, M_C3 = 40, M_C3B = 44, M_KVC = 48, M_KVR = 52, M_UR = 56, M_OR = 60, M_RDY = 64,
              M_QD = 192, M_UD = 104, M_HS = 108, M_APR = 112, M_CVR = 116, M_CE = 124, M_PAIR = 128, M_TAGR = 144,
              M_TAG = 148, M_KN = 152, M_EXD = 156, M_KVP = 160, M_KN0 = 164, M_KD = 168, M_QR = 172,
              M_N2 = 176, M_AS = 196, M_SE = 204, M_ZS = 208, M_MM = 212, M_G = 256;
constexpr int L_EX = 0;
constexpr int L_AP = CV_H + 512, L_ZT = CV_U;
#define CV_PRIO 3
#define CV_SLEEP 0
#define POLL_AT M_C2
#define POLL_N 3
#define BUBBLE 0
#define POLL_SLEEP 4
#define WO_INFL "2"
#define DMA_FIRST 0
#define H_PRIO 3
#define TCH 0
#define O_DELAY 20
#define Z_DELAY 12
constexpr unsigned SPIN_CAP = 1u << 18;

#define STAMP_DECL do {} while (0)
#define STAMP(I) do {} while (0)
#define STAMP_FLUSH(KID) do {} while (0)
#define RSTAMP(I) do {} while (0)

#define PSTAMP(I) do {} while (0)
#define NSTAMP(I) STAMP(I)
#define CSTAMP(I) do {} while (0)
#define HSTAMP(I) NSTAMP(I)

DEVI unsigned pk2(float a, float b) {
  bf16x2 v = {(__bf16)a, (__bf16)b};
  return __builtin_bit_cast(unsigned, v);
}
DEVI float u2f(unsigned u) { return __builtin_bit_cast(float, u); }
DEVI float bf_lo(unsigned u) { return __builtin_bit_cast(float, u << 16); }
DEVI float bf_hi(unsigned u) { return __builtin_bit_cast(float, u & 0xffff0000u); }
DEVI float dot2(unsigned u, float c) { const bf16x2 v = __builtin_bit_cast(bf16x2, u); return __builtin_amdgcn_fdot2_f32_bf16(v, v, c, false); }
DEVI float rbf(float a) { return (float)(__bf16)a; }
DEVI int swap23(int r) { return (r & ~12) | ((r & 4) << 1) | ((r & 8) >> 1); }
DEVI float xhalf(float v) {
  const unsigned u = __builtin_bit_cast(unsigned, v);
  const auto r = __builtin_amdgcn_permlane32_swap(u, u, false, false);
  return __builtin_bit_cast(float, (TIDX & 32) ? r[0] : r[1]);
}

template <int CTRL>
DEVI float dpp_max(float v) {
  const int u = __builtin_bit_cast(int, v);
  return __builtin_fmaxf(v, __builtin_bit_cast(float, __builtin_amdgcn_update_dpp(u, u, CTRL, 0xf, 0xf, false)));
}
DEVI float max16(float v) {
  return dpp_max<0x140>(dpp_max<0x141>(dpp_max<0x4e>(dpp_max<0xb1>(v))));
}
template <int CTRL>
DEVI float dpp_add(float v) {
  const int u = __builtin_bit_cast(int, v);
  return v + __builtin_bit_cast(float, __builtin_amdgcn_update_dpp(u, u, CTRL, 0xf, 0xf, false));
}
DEVI float sum16(float v) { return dpp_add<0x140>(dpp_add<0x141>(dpp_add<0x4e>(dpp_add<0xb1>(v)))); }
DEVI float wave_max(float v) {
  v = max16(v);
  const unsigned u = __builtin_bit_cast(unsigned, v);
  return __builtin_fmaxf(__builtin_fmaxf(u2f(__builtin_amdgcn_readlane(u, 0)), u2f(__builtin_amdgcn_readlane(u, 16))),
               __builtin_fmaxf(u2f(__builtin_amdgcn_readlane(u, 32)), u2f(__builtin_amdgcn_readlane(u, 48))));
}
DEVI void glds(const void* sbase, unsigned voff, unsigned lds_dst) {
  asm volatile("s_mov_b32 m0, %2\n\ts_nop 0\n\tglobal_load_lds_dwordx4 %0, %1" :: "v"(voff), "s"(sbase), "s"(lds_dst) : "memory");
}
DEVI void glds_sc1(const void* sbase, unsigned voff, unsigned lds_dst) {
  asm volatile("s_mov_b32 m0, %2\n\ts_nop 0\n\tglobal_load_lds_dwordx4 %0, %1 sc1" :: "v"(voff), "s"(sbase), "s"(lds_dst) : "memory");
}
#define GLDS4(SC, sbase_, v0_, v3_, dst_)                                                                                  \
  asm volatile("s_mov_b32 m0, %3\n\ts_nop 0\n\tglobal_load_lds_dwordx4 %0, %2" SC "\n\tglobal_load_lds_dwordx4 %0, %2 offset:1024" SC   \
               "\n\tglobal_load_lds_dwordx4 %0, %2 offset:2048" SC "\n\tglobal_load_lds_dwordx4 %1, %2 offset:3072" SC           \
               :: "v"(v0_), "v"(v3_), "s"(sbase_), "s"(dst_) : "memory")
#define GLDSX(src_, dst_)                                                                                                  \
  asm volatile("s_mov_b32 m0, %5\n\ts_nop 0\n\tglobal_load_lds_dwordx4 %0, %4\n\tglobal_load_lds_dwordx4 %1, %4 offset:1024\n\t"   \
               "global_load_lds_dwordx4 %2, %4 offset:2048\n\tglobal_load_lds_dwordx4 %3, %4 offset:3072"                 \
               :: "v"(vxe), "v"(vxo), "v"(vxe + 16384), "v"(vxo + 16384), "s"(src_), "s"(dst_) : "memory")
#define GLDSX2(src_, dst_)                                                                                                 \
  asm volatile("s_mov_b32 m0, %3\n\ts_nop 0\n\tglobal_load_lds_dwordx4 %0, %2\n\tglobal_load_lds_dwordx4 %1, %2 offset:1024"       \
               :: "v"(vxe), "v"(vxo), "s"(src_), "s"(dst_) : "memory")
#define GLDS3(STEP1, STEP2, v0_, v1_, v2_, src_, dst_)                                                                     \
  asm volatile("s_mov_b32 m0, %4\n\ts_nop 0\n\tglobal_load_lds_dwordx4 %0, %3\n\tglobal_load_lds_dwordx4 %1, %3 offset:" STEP1  \
               "\n\tglobal_load_lds_dwordx4 %2, %3 offset:" STEP2                                                        \
               :: "v"(v0_), "v"(v1_), "v"(v2_), "s"(src_), "s"(dst_) : "memory")
#define TOUCH(src_, v_) asm volatile("s_mov_b32 m0, %2\n\ts_nop 0\n\tglobal_load_lds_dword %0, %1 sc1" :: "v"(v_), "s"(src_), "s"(sbase + L_M + 512) : "memory")
#define VM_WAIT(N) asm volatile("s_waitcnt vmcnt(" #N ")" ::: "memory")
#define BAR() asm volatile("s_waitcnt lgkmcnt(0)\n\ts_barrier" ::: "memory")

DEVI bf16x8 tr_frag(lds_ptr p0, lds_ptr p1) {
  bf16x4 a = __builtin_amdgcn_ds_read_tr16_b64_v4bf16((LAS bf16x4*)p0);
  bf16x4 b = __builtin_amdgcn_ds_read_tr16_b64_v4bf16((LAS bf16x4*)p1);
  return __builtin_shufflevector(a, b, 0, 1, 2, 3, 4, 5, 6, 7);
}
DEVI bf16x8 lds_frag(lds_ptr p) { return *(LAS bf16x8*)p; }
DEVI void lds_signal(lds_ptr p, unsigned n = 1u) {
  asm volatile("s_waitcnt lgkmcnt(0)\n\ts_mov_b64 exec, 1\n\tds_add_u32 %0, %1\n\ts_mov_b64 exec, -1" :: "v"(p), "v"(n) : "memory");
}
DEVI unsigned lds_ticket(lds_ptr p) {
  unsigned old;
  asm volatile("s_waitcnt lgkmcnt(0)\n\ts_mov_b64 exec, 1\n\tds_add_rtn_u32 %0, %1, %2\n\ts_mov_b64 exec, -1\n\ts_waitcnt lgkmcnt(0)"
               : "=&v"(old) : "v"(p), "v"(1u) : "memory");
  return __builtin_amdgcn_readfirstlane(old);
}
template <int SLEEP = 0>
DEVI void lds_wait(lds_ptr p, unsigned n) {
  unsigned v, t;
  asm volatile("1:\n\tds_read_b32 %0, %2\n\ts_waitcnt lgkmcnt(0)\n\tv_readfirstlane_b32 %1, %0\n\ts_nop 0\n\ts_cmp_ge_u32 %1, %3\n\t"
               "s_cbranch_scc1 2f\n\ts_sleep %4\n\ts_branch 1b\n2:"
               : "=&v"(v), "=&s"(t) : "v"(p), "s"(n), "i"(SLEEP) : "scc", "memory");
}
DEVI unsigned lds_peek(lds_ptr p) {
  unsigned v;
  asm volatile("ds_read_b32 %0, %1\n\ts_waitcnt lgkmcnt(0)" : "=&v"(v) : "v"(p) : "memory");
  return __builtin_amdgcn_readfirstlane(v);
}
#define LD_SC1_16(off) __builtin_bit_cast(u32x4, __builtin_amdgcn_raw_buffer_load_b128(rs, (off), 0, 16))
#define ST_SC1_16(v, off) __builtin_amdgcn_raw_buffer_store_b128((v), rs, (off), 0, 16)
#define ST_SC1_8(v, off) __builtin_amdgcn_raw_buffer_store_b64((v), rs, (off), 0, 16)
#define ST_SC1_4(v, off) __builtin_amdgcn_raw_buffer_store_b32((v), rs, (off), 0, 16)

DEVI f32x16 lo16(f32x32 v) { return __builtin_shufflevector(v, v, 0, 1, 2, 3, 4, 5, 6, 7, 8, 9, 10, 11, 12, 13, 14, 15); }
DEVI f32x16 hi16(f32x32 v) { return __builtin_shufflevector(v, v, 16, 17, 18, 19, 20, 21, 22, 23, 24, 25, 26, 27, 28, 29, 30, 31); }
DEVI bf16x16 raw_lo(f32x16 v) { return __builtin_bit_cast(bf16x16, __builtin_shufflevector(v, v, 0, 1, 2, 3, 4, 5, 6, 7)); }
DEVI bf16x16 raw_hi(f32x16 v) { return __builtin_bit_cast(bf16x16, __builtin_shufflevector(v, v, 8, 9, 10, 11, 12, 13, 14, 15)); }
constexpr int SPIDX = (int)0xddddddddu;
#define MFMA32(a, b, c) __builtin_amdgcn_mfma_f32_32x32x16_bf16((a), (b), (c), 0, 0, 0)
#define MFMA16(a, b, c) __builtin_amdgcn_mfma_f32_16x16x32_bf16((a), (b), (c), 0, 0, 0)

DEVI void online_step(f32x16 s, float& m, f32x16& o, bf16x8 va, bf16x8 vb) {
  float t = s[0];
#pragma unroll
  for (int i = 1; i < 16; ++i) t = __builtin_fmaxf(t, s[i]);
  t = __builtin_fmaxf(t, xhalf(t));
  const float mn = __builtin_fmaxf(m, t);
  const float al = __builtin_amdgcn_exp2f(m - mn);
  m = mn;
#pragma unroll
  for (int i = 0; i < 16; ++i) { o[i] *= al; s[i] = __builtin_amdgcn_exp2f(s[i] - mn); }
  o = __builtin_amdgcn_smfmac_f32_32x32x32_bf16(va, raw_lo(s), o, SPIDX, 0, 0);
  o = __builtin_amdgcn_smfmac_f32_32x32x32_bf16(vb, raw_hi(s), o, SPIDX, 0, 0);
}

#define UNIT_LOAD(CP, TG)                                                                                        \
  const int uc0 = 72 * hg + 2 * (CP), ut0 = 64 * qt + 8 * (TG);                                                  \
  const u32x4 usw = *(const u32x4*)(shk + uc0 * 4);                                                              \
  unsigned ukr[11], uvr[8];                                                                                      \
  {                                                                                                              \
    const unsigned bo = (ut0 * D_ + uc0) * 2, bo3 = ut0 ? bo - 3 * 1152 : bo;                                    \
    _Pragma("unroll") for (int i = 0; i < 3; ++i) ukr[i] = *(const unsigned*)((const char*)bk + bo3 + i * 1152); \
    _Pragma("unroll") for (int i = 0; i < 8; ++i) {                                                              \
      ukr[3 + i] = *(const unsigned*)((const char*)bk + bo + i * 1152);                                          \
      uvr[i] = *(const unsigned*)((const char*)bv + bo + i * 1152);                                              \
    }                                                                                                            \
  }
#define UNIT_DO()                                                                                                \
  u32x4 o0, o1;                                                                                                  \
  {                                                                                                              \
    const f32x2 w3 = {bf_hi(usw[1]), bf_hi(usw[3])}, w2 = {bf_lo(usw[1]), bf_lo(usw[3])};                        \
    const f32x2 w1 = {bf_hi(usw[0]), bf_hi(usw[2])}, w0 = {bf_lo(usw[0]), bf_lo(usw[2])};                        \
    f32x2 kf[11], r[8];                                                                                          \
    _Pragma("unroll") for (int i = 0; i < 11; ++i) {                                                             \
      const unsigned kk = (i < 3 && !ut0) ? 0u : ukr[i];                                                         \
      kf[i] = (f32x2){bf_lo(kk), bf_hi(kk)};                                                                     \
    }                                                                                                            \
    _Pragma("unroll") for (int i = 0; i < 8; ++i)                                                                \
      r[i] = __builtin_elementwise_fma(kf[i + 3], w0, __builtin_elementwise_fma(kf[i + 2], w1,                   \
             __builtin_elementwise_fma(kf[i + 1], w2, kf[i] * w3))) * (f32x2){bf_lo(uvr[i]), bf_hi(uvr[i])};     \
    _Pragma("unroll") for (int i = 0; i < 4; ++i) {                                                              \
      o0[i] = pk2(r[7 - 2 * i][0], r[6 - 2 * i][0]);                                                             \
      o1[i] = pk2(r[7 - 2 * i][1], r[6 - 2 * i][1]);                                                             \
    }                                                                                                            \
  }

#define CV_LD(F, D) _Pragma("unroll") for (int s = 0; s < 2; ++s) {                                              \
    F[4 * s] = lds_frag(ab + 32 * (2 * (D) + 1 - s)); F[4 * s + 1] = lds_frag(bb + 80 * (7 - (D)) + 32 * s);     \
    F[4 * s + 2] = lds_frag(bb + 80 * (39 - (D)) + 32 * s); F[4 * s + 3] = lds_frag(ab + 32 * (2 * (D) + 65 - s)); } \
  __builtin_amdgcn_sched_barrier(0)
#define CV_MM(F) _Pragma("unroll") for (int s = 0; s < 2; ++s) {                                                 \
    y0 = MFMA32(F[4 * s], F[4 * s + 1], y0); y1 = MFMA32(F[4 * s], F[4 * s + 2], y1);                            \
    y1 = MFMA32(F[4 * s + 3], F[4 * s + 1], y1); }                                                               \
  __builtin_amdgcn_sched_barrier(0)
#define SB0 __builtin_amdgcn_sched_barrier(0)
#define CV_STEP(F, N, D) _Pragma("unroll") for (int s = 0; s < 2; ++s) {                                         \
    y0 = MFMA32(F[4 * s], F[4 * s + 1], y0); SB0;                                                                \
    N[4 * s] = lds_frag(ab + 32 * (2 * (D) + 1 - s)); N[4 * s + 1] = lds_frag(bb + 80 * (7 - (D)) + 32 * s); SB0; \
    y1 = MFMA32(F[4 * s], F[4 * s + 2], y1); SB0;                                                                \
    N[4 * s + 2] = lds_frag(bb + 80 * (39 - (D)) + 32 * s); SB0;                                                 \
    y1 = MFMA32(F[4 * s + 3], F[4 * s + 1], y1); SB0;                                                            \
    N[4 * s + 3] = lds_frag(ab + 32 * (2 * (D) + 65 - s)); SB0; }
#define CV_PUT(P) _Pragma("unroll") for (int g = 0; g < 4; ++g) {                                                \
    *(LAS f32x4*)((P) + g * 1024) = (f32x4){y0[4 * g], y0[4 * g + 1], y0[4 * g + 2], y0[4 * g + 3]};             \
    *(LAS f32x4*)((P) + 4096 + g * 1024) = (f32x4){y1[4 * g], y1[4 * g + 1], y1[4 * g + 2], y1[4 * g + 3]}; }
DEVI f32x4 cv_sum(lds_ptr p, lds_ptr u, float dv) {
  const f32x4 s = (*(LAS f32x4*)(p) + *(LAS f32x4*)(p + 8192)) + (*(LAS f32x4*)(p + 16384) + *(LAS f32x4*)(p + 24576));
  const u32x2 uu = *(LAS u32x2*)u;
  return (f32x4){__builtin_fmaf(bf_hi(uu[1]), dv, s[0]), __builtin_fmaf(bf_lo(uu[1]), dv, s[1]), __builtin_fmaf(bf_hi(uu[0]), dv, s[2]), __builtin_fmaf(bf_lo(uu[0]), dv, s[3])};
}

__global__ void __launch_bounds__(768, 3) NOPK
fused_kernel(const __bf16* __restrict__ x, const __bf16* __restrict__ wq, const __bf16* __restrict__ wk,
             const __bf16* __restrict__ wv, const __bf16* __restrict__ wo, const __bf16* __restrict__ bk,
             const __bf16* __restrict__ bv, const __bf16* __restrict__ shk, const __bf16* __restrict__ ssmk,
             const __bf16* __restrict__ ssmd, const __bf16* __restrict__ gw, __bf16* __restrict__ out, char* ws) {
  __shared__ __attribute__((aligned(16))) char smem[160 * 1024];
  STAMP_DECL; STAMP(0);
  const int tid = TIDX, lane = tid & 63;
  const int w = __builtin_amdgcn_readfirstlane(tid >> 6);
  const int bid = BIDX, jj = bid >> 3;
  const unsigned sbase = (unsigned)(size_t)smem;
  if (w == 0) {
    unsigned long long pc0;
    unsigned len;
    asm volatile("s_getpc_b64 %0\n\tfk_pc:\n\ts_mov_b32 %1, fk_end-fk_pc" : "=s"(pc0), "=s"(len));
    TOUCH(pc0, UMIN((unsigned)(lane * 8 + (jj & 7)) * 128u, len));
  }
  const int qt = (bid & 7) * 4 + (jj & 3), hg = jj >> 2;
  lds_ptr sm = (lds_ptr)smem;
  lds_ptr mi = sm + L_M;
  const int h = lane >> 5, l31 = lane & 31, l15 = lane & 15, g4 = lane >> 4;
  const int tq = (lane & 15) >> 2, tp = lane & 3;
  const auto rs = __builtin_amdgcn_make_buffer_rsrc(ws, 0, WS_END, 0x00020000);
  GAS char* gws = (GAS char*)ws;
  const int rot = (hg + qt) % 3;
  const int s0 = bid & 7;
  const int tq4 = hg >> 1, sel = hg & 1;
  const char* xt = (const char*)x + (size_t)qt * 73728;
  const char* wqs = (const char*)wq + 144 * hg;
  const unsigned vxe = (lane >> 3) * 1152 + (((lane & 7) ^ g4) << 4), vxo = 8192 + (lane >> 3) * 1152 + (((lane & 7) ^ (4 + g4)) << 4);
  const int otok = tid / 9, opc = tid - 9 * otok;

  u32x4 zv, xv;

  if (w == 4) *(LAS int*)(mi + lane * 4) = (lane == M_RDY / 4 + 2 || lane == M_RDY / 4 + 3) ? -8 : (lane == 0 ? -1 : 0);
  auto zscatter = [&]() LAMI {
    lds_ptr zp = sm + L_ZT + ((tid & 7) * 72 + (tid >> 3)) * 2;
#pragma unroll
    for (int e = 0; e < 8; ++e)
      *(LAS unsigned short*)(zp + e * 1152) = (unsigned short)((e & 1) ? zv[e >> 1] >> 16 : zv[e >> 1]);
    lds_signal(mi + M_ZS);
  };
  if (w >= 8 && w < 11) {
    const int j = w - 8;
    const int th = (j + rot) % 3;
    const unsigned stg = L_B + 2 * 27648 + j * 9216;
#pragma unroll
    for (int c = 0; c < 3; ++c) GLDSX2(xt + tq4 * 18432 + (3 * th + c) * 128, sbase + L_X + (3 * j + c) * 8192 + tq4 * 2048);
    {
      const char* wsrc = (const char*)(sel ? wv : wk) + th * 6912 - 2048;
      unsigned vo[3];
#pragma unroll
      for (int i = 0; i < 3; ++i) {
        const int f = 64 * i + lane, row = f / 3, pc = f - 3 * row;
        vo[i] = row * 36 + (pc == 2 ? 20 : 16 * pc) + 2048 - 1024 * i;
      }
#pragma unroll
      for (int p = 0; p < 3; ++p) GLDS3("1024", "2048", vo[0], vo[1], vo[2], wsrc + p * 2304, sbase + stg + p * 3072);
    }
    glds(gw + 192 * th, UMIN(lane, 23) * 16, sbase + L_KVP + j * 2048);
  }
  unsigned epoch = 0;
  if (w == 11) epoch = __hip_atomic_load((gu32*)(gws + WS_EPOCH), RLX_AGENT);
  PSTAMP(5);
  BAR();
  if (w >= 1 && w < 4) TOUCH((const char*)wq + (jj * 162 + (w - 1) * 54) * 128, UMIN(lane, 53) * 128u);
  PSTAMP(6);
  if (w >= 5 && w < 8) {
    const int wi = w - 5;
    unsigned vw[3];
#pragma unroll
    for (int m = 0; m < 3; ++m) {
      const int f = 64 * (3 * wi + m) + lane, row = f / 9;
      vw[m] = row * 1152 + (f - 9 * row) * 16 - 1024 * m;
    }
    const char* xs = xt + (1 - (tq4 >> 1)) * 36864 + wi * 128;
    const char* xs2 = xt + (tq4 ^ 1) * 18432 + wi * 128;
    const unsigned xd = sbase + L_X + wi * 8192 + (1 - (tq4 >> 1)) * 4096, xd2 = sbase + L_X + wi * 8192 + (tq4 ^ 1) * 2048;
    const unsigned wd = sbase + L_B + wi * 3072;
    const int t1 = (rot + 1) % 3, t2 = (rot + 2) % 3;
    GLDSX(xs + rot * 384, xd);
    GLDSX2(xs2 + rot * 384, xd2);
#pragma unroll
    for (int per = 0; per < 3; ++per) GLDS3("1024", "2048", vw[0], vw[1], vw[2], wqs + (192 * rot + 64 * per) * 1152, wd + per * 9216);
    GLDSX(xs + t1 * 384, xd + 24576);
    GLDSX2(xs2 + t1 * 384, xd2 + 24576);
    GLDS3("1024", "2048", vw[0], vw[1], vw[2], wqs + 192 * t1 * 1152, wd + 27648);
    VM_WAIT(9);
    lds_signal(mi + M_C1);
#pragma unroll
    for (int per = 1; per < 3; ++per) GLDS3("1024", "2048", vw[0], vw[1], vw[2], wqs + (192 * t1 + 64 * per) * 1152, wd + 27648 + per * 9216);
    GLDSX(xs + t2 * 384, xd + 49152);
    GLDSX2(xs2 + t2 * 384, xd2 + 49152);
    lds_wait(mi + M_KVC, 3);
    GLDS3("1024", "2048", vw[0], vw[1], vw[2], wqs + 192 * t2 * 1152, wd + 55296);
    VM_WAIT(9);
    lds_signal(mi + M_C2);
#pragma unroll
    for (int per = 1; per < 3; ++per) GLDS3("1024", "2048", vw[0], vw[1], vw[2], wqs + (192 * t2 + 64 * per) * 1152, wd + 55296 + per * 9216);
    STAMP(1);
    VM_WAIT(0);
    lds_signal(mi + M_C3);
    lds_signal(mi + M_SE);
  }

  if (w >= 8) {
    const int j = w - 8, ht = tid - 512;
    __builtin_amdgcn_s_setprio(3);
    if (j < 3) {
      const unsigned stg = L_B + 2 * 27648 + j * 9216;
      lds_ptr wb = sm + stg + (8 * g4 + tq) * 48 + tp * 8;
      lds_ptr wc = sm + stg + (8 * g4 + tq) * 48 + 32 + (tp & 1) * 8;
      lds_ptr xb = sm + L_X + 3 * j * 8192 + (16 * tq4 + l15) * 128;
      VM_WAIT(0);
      STAMP(1);
      bf16x8 fa[6], fc[6], fb[6], fg[6];
#pragma unroll
      for (int k = 0; k < 6; ++k) {
        fg[k] = lds_frag(sm + L_KVP + j * 2048 + k * 64 + g4 * 16);
        fb[k] = lds_frag(xb + (k >> 1) * 8192 + (((4 * (k & 1) + g4) ^ (l15 >> 1)) << 4));
        fa[k] = tr_frag(wb + k * 1536, wb + k * 1536 + 192);
        fc[k] = tr_frag(wc + k * 1536, wc + k * 1536 + 192);
      }
      f32x4 acc = {0}, acr = {0};
      if (sel) {
#pragma unroll
        for (int k = 0; k < 6; ++k) { acc = MFMA16(fb[k], fa[k], acc); acr = MFMA16(fb[k], fc[k], acr); }
      } else {
#pragma unroll
        for (int k = 0; k < 6; ++k) { acc = MFMA16(fa[k], fb[k], acc); acr = MFMA16(l15 == 8 ? fg[k] : fc[k], fb[k], acr); }
      }
      *(LAS f32x4*)(sm + L_KVP + j * 2048 + lane * 16) = acc;
      *(LAS f32x4*)(sm + L_KVP + j * 2048 + 1024 + lane * 16) = acr;
      lds_signal(mi + M_KVC);
      lds_signal(mi + M_C1);
      STAMP(2);
      lds_wait<1>(mi + M_KD, 1);
    } else {
      const unsigned tg = __builtin_amdgcn_readfirstlane(epoch) + 1u;
      if (lane == 0) *(LAS unsigned*)(mi + M_TAG) = tg;
      const int T = 2 * qt + (tq4 >> 1), mt = tq4 & 1, i4 = T & 3, gb = (T >> 2) * KV_GROUP, rp = swap23(16 * mt + l15);
      int sa, sr;
      bool st;
      if (sel) {
        sa = gb + 4096 + i4 * 1216 + mt * 608 + (l15 + 18 * (g4 & 1)) * 16 + (g4 >> 1) * 8;
        sr = gb + 4096 + i4 * 1216 + mt * 608 + (l15 < 8 ? 10 + l15 + 18 * (g4 & 1) : 36 + (g4 & 1)) * 16 + (g4 >> 1) * 8;
        st = l15 >= 6 && l15 <= 8;
      } else {
        sa = gb + i4 * 1024 + (rp + 32 * (g4 & 1)) * 16 + (g4 >> 1) * 8;
        sr = gb + 8960 + i4 * 256 + rp * 8;
        st = g4 == 1;
      }
      lds_wait(mi + M_KVC, 3);
      lds_ptr pp = sm + L_KVP + lane * 16;
      const float ks = sel ? 1.f : 0.23570226039551584f * 1.4426950408889634f;
      const f32x4 va = (*(LAS f32x4*)(pp) + *(LAS f32x4*)(pp + 2048) + *(LAS f32x4*)(pp + 4096)) * ks;
      const f32x4 vs = *(LAS f32x4*)(pp + 1024) + *(LAS f32x4*)(pp + 3072) + *(LAS f32x4*)(pp + 5120), vr = vs * ks;
      const u32x2 a = {pk2(va[0], va[1]), pk2(va[2], va[3])};
      u32x2 r = {pk2(vr[0], vr[1]), pk2(vr[2], vr[3])};
      if (!sel) r = (u32x2){r[1], 0x3f803f80u};
      else if (l15 == 8) r[0] = r[1] = (g4 & 1) ? 0u : 0x3f803f80u;
      ST_SC1_16(((u32x4){a[0], tg, a[1], tg}), WS_KT + 2 * sa);
      if (st) ST_SC1_16(((u32x4){r[0], tg, r[1], tg}), WS_KT + 2 * sr);
      ST_SC1_8(a, WS_KV + sa);
      if (st) ST_SC1_8(r, WS_KV + sr);
      STAMP(1);
      lds_signal(mi + M_KD);
      if (!sel && g4 == 2) {
        const float g = rbf(__builtin_amdgcn_rcpf(1.f + __builtin_amdgcn_exp2f(-1.4426950408889634f * rbf(vs[0]))));
        ST_SC1_4(__builtin_bit_cast(unsigned, g), WS_G + (64 * qt + 16 * tq4 + l15) * 4);
      }
      float n2 = dot2(a[1], dot2(a[0], st ? dot2(r[0], 0.f) : 0.f));
      n2 += xhalf(n2);
      n2 += u2f(__builtin_amdgcn_ds_swizzle(__builtin_bit_cast(int, n2), 0x401f));
      n2 = sel ? 0.f : max16(n2);
      VM_WAIT(0);
      if (lane == 0)
        __hip_atomic_store((gu64*)(gws + WS_FKV + T * 128 + (hg & 3) * 8), ((unsigned long long)tg << 32) | __builtin_bit_cast(unsigned, n2), RLX_AGENT);
      STAMP(2);
    }
    const unsigned tag = *(LAS unsigned*)(mi + M_TAG);
    auto kv_tagged = [&](int st) LAMI {
      u32x4 r[10];
      const int o = WS_KT + ((s0 + st) & 7) * (2 * RSLOT) + lane * 16;
#pragma unroll 1
      for (unsigned spins = 0; spins < SPIN_CAP; ++spins) {
        unsigned bad = 0;
#pragma unroll
        for (int i = 0; i < 10; ++i) r[i] = LD_SC1_16(o + (i == 9 && j == 3 ? 38 : 4 * i + j) * 1024);
#pragma unroll
        for (int i = 0; i < 10; ++i) bad |= (r[i][1] ^ tag) | (r[i][3] ^ tag);
        asm volatile("" ::: "memory");
        if (ALL(bad == 0)) break;
        __builtin_amdgcn_s_sleep(POLL_SLEEP);
      }
      if (st == 0) lds_wait(mi + M_C3B, 5);
#pragma unroll
      for (int i = 0; i < 10; ++i)
        *(LAS u32x2*)(sm + st * RSLOT + (i == 9 && j == 3 ? 38 : 4 * i + j) * 512 + lane * 8) = (u32x2){r[i][0], r[i][2]};
    };
    auto kv_dma = [&](int st) LAMI {
      const char* src = ws + WS_KV + ((s0 + st) & 7) * RSLOT + j * 4992;
      const unsigned dst = sbase + st * RSLOT + j * 4992;
      GLDS4(" sc1", src, lane * 16, lane * 16, dst);
      if (lane < 56) glds_sc1(src + 4096, lane * 16, dst + 4096);
    };
    const unsigned vh3 = (lane == 63 ? 62 : lane) * 16;
    auto hload = [&](int cl, int b) LAMI {
      const char* hsrc = (const char*)(ssmk + (size_t)(72 * hg + cl) * S_) + 4 * j + 2;
      const unsigned dst = sbase + CV_H + b * CV_HB + 2 * j * CV_COPY + 64;
      GLDS4("", hsrc, lane * 16, vh3, dst);
      GLDS4("", hsrc + 2, lane * 16, vh3, dst + CV_COPY);
    };
    auto uload = [&](int cl) LAMI {
      const int tgq = ht & 7;
      const int o = cl < 64 ? (tgq >> 1) * 2048 + (cl & 1) * 1024 + ((tgq & 1) * 32 + (cl >> 1)) * 16
                            : 8192 + (cl & 1) * 512 + (tgq * 4 + ((cl - 64) >> 1)) * 16;
      return LD_SC1_16(WS_UT + ((ht >> 3) * 8 + hg) * 9216 + o);
    };
    const int ce = 64 + (qt >> 2);
    const unsigned short h8a = *(const unsigned short*)(ssmk + (size_t)(72 * hg + 2 * qt + (j >> 1)) * S_ + (lane & 7));
    const unsigned short h8e = *(const unsigned short*)(ssmk + (size_t)(72 * hg + ce) * S_ + (lane & 7));
    const float dve = (qt & 3) ? 0.f : (float)ssmd[72 * hg + ce];
    {
      lds_wait<1>(mi + POLL_AT, POLL_N);
      kv_tagged(0);
      lds_signal(mi + M_RDY);
      STAMP(3);
      kv_tagged(1);
      lds_signal(mi + M_RDY + 4, 2);
      if (j == 3) {
        float kn = 0.f;
#pragma unroll 1
        for (unsigned spins = 0; spins < SPIN_CAP; ++spins) {
          const u32x4 v0 = LD_SC1_16(WS_FKV + lane * 128), v1 = LD_SC1_16(WS_FKV + lane * 128 + 16);
          const bool ok = v0[1] == tag && v0[3] == tag && v1[1] == tag && v1[3] == tag;
          kn = __builtin_fmaxf(u2f(v0[0]), u2f(v1[0]));
          asm volatile("" ::: "memory");
          if (ALL(ok)) break;
          __builtin_amdgcn_s_sleep(POLL_SLEEP);
        }
        lds_signal(mi + M_QR);
        lds_signal(mi + M_DONE);
        kn = wave_max(kn);
        if (lane == 0) *(LAS float*)(mi + M_KN) = kn;
      }
      lds_wait<1>(mi + M_QR, 9);
      kv_dma(2);
      kv_dma(3);
      VM_WAIT(0);
      lds_signal(mi + M_RDY + 8, 2);
      lds_signal(mi + M_RDY + 12, 2);
      UNIT_LOAD(l31, 2 * j + h);
      if (j == 1) {
        const int c0 = 72 * hg + 64 + 2 * (lane & 3), t0 = 64 * qt + 4 * (lane >> 2);
        const u32x4 sw = *(const u32x4*)(shk + c0 * 4);
        const unsigned bo = (t0 * D_ + c0) * 2, bo3 = t0 ? bo - 3 * 1152 : bo;
        unsigned kk[7], vv[4];
#pragma unroll
        for (int i = 0; i < 7; ++i) kk[i] = *(const unsigned*)((const char*)bk + (i < 3 ? bo3 + i * 1152 : bo + (i - 3) * 1152));
#pragma unroll
        for (int i = 0; i < 4; ++i) vv[i] = *(const unsigned*)((const char*)bv + bo + i * 1152);
        if (!t0) kk[0] = kk[1] = kk[2] = 0u;
        float ra[4], rb[4];
#pragma unroll
        for (int i = 0; i < 4; ++i) {
          ra[i] = __builtin_fmaf(bf_lo(kk[i + 3]), bf_lo(sw[0]), __builtin_fmaf(bf_lo(kk[i + 2]), bf_hi(sw[0]),
                  __builtin_fmaf(bf_lo(kk[i + 1]), bf_lo(sw[1]), bf_lo(kk[i]) * bf_hi(sw[1])))) * bf_lo(vv[i]);
          rb[i] = __builtin_fmaf(bf_hi(kk[i + 3]), bf_lo(sw[2]), __builtin_fmaf(bf_hi(kk[i + 2]), bf_hi(sw[2]),
                  __builtin_fmaf(bf_hi(kk[i + 1]), bf_lo(sw[3]), bf_hi(kk[i]) * bf_hi(sw[3])))) * bf_hi(vv[i]);
        }
        const int uo = WS_UT + (qt * 8 + hg) * 9216 + 8192 + ((lane >> 3) * 4 + (lane & 3)) * 16 + (1 - ((lane >> 2) & 1)) * 8;
        ST_SC1_8(((u32x2){pk2(ra[3], ra[2]), pk2(ra[1], ra[0])}), uo);
        ST_SC1_8(((u32x2){pk2(rb[3], rb[2]), pk2(rb[1], rb[0])}), uo + 512);
      }
      VM_WAIT(0);
      if (j < 3) TOUCH((const char*)wo + (jj * 162 + j * 54) * 128, UMIN(lane, 53) * 128u);
      else TOUCH(ssmk + (size_t)(72 * hg + 2 * qt) * S_, lane * 128u);
      UNIT_DO();
      ST_SC1_16(o0, WS_UT + (qt * 8 + hg) * 9216 + j * 2048 + lane * 16);
      ST_SC1_16(o1, WS_UT + (qt * 8 + hg) * 9216 + j * 2048 + 1024 + lane * 16);
      {
        const u32x4 ones = {~0u, ~0u, ~0u, ~0u};
        const int ob = WS_O + 2 * qt * 36 * 1024 + (4 * hg + j) * 1024 + lane * 16;
        ST_SC1_16(ones, ob);
        ST_SC1_16(ones, ob + 36 * 1024);
        if (j == 0) ST_SC1_16(ones, WS_O + (2 * qt + h) * 36 * 1024 + 32768 + hg * 512 + l31 * 16);
        ST_SC1_16(ones, WS_Z + (72 * hg + 2 * qt) * 4096 + ht * 16);
        ST_SC1_16(ones, WS_Z + (72 * hg + 2 * qt + 1) * 4096 + ht * 16);
      }
      STAMP(4);
      VM_WAIT(0);
      if (lds_ticket(mi + M_UD) == 3u && lane == 0) __hip_atomic_store((gu32*)(gws + WS_FU) + (hg * 32 + qt) * 32, tag, RLX_AGENT);
    }
    auto cvprep = [&](int b, unsigned short h8) LAMI {
      *(LAS u32x4*)(sm + CV_U + (ht >> 7) * CV_UB + ((ht & 127) >> 2) * 80 + (ht & 3) * 16) = (u32x4){0, 0, 0, 0};
      lds_ptr hz = sm + CV_H + b * CV_HB + (lane >> 3) * CV_COPY;
      *(LAS u32x2*)(hz + (lane & 7) * 8) = (u32x2){0, 0};
      if ((lane & 7) <= (lane >> 3)) *(LAS unsigned short*)(hz + 48 + (7 - (lane >> 3) + (lane & 7)) * 2) = h8;
    };
    unsigned hs = 0;
#define HSYNC() do { lds_signal(mi + M_HS); hs += 4; lds_wait<2>(mi + M_HS, hs); } while (0)
    if (lane < 54) {
      const unsigned vo = (lane / 9) * 1152 + (lane % 9) * 16;
#pragma unroll 1
      for (int p = 24 * j; p < 24 * j + 24; p += 4)
        asm volatile("s_mov_b32 m0, %5\n\ts_nop 0\n\tglobal_load_lds_dwordx4 %0, %4\n\tglobal_load_lds_dwordx4 %1, %4 offset:864\n\t"
                     "global_load_lds_dwordx4 %2, %4 offset:1728\n\tglobal_load_lds_dwordx4 %3, %4 offset:2592\n\ts_waitcnt vmcnt(" WO_INFL ")"
                     :: "v"(vo), "v"(vo + 6048), "v"(vo + 12096), "v"(vo + 18144), "s"((const char*)wo + 144 * hg + p * 6912),
                        "s"(sbase + L_B + p * 864) : "memory");
    }
    VM_WAIT(0);
    HSYNC();
    bf16x8 wf[18], we[5];
    {
      const int cb = (32 * (j & 1) + 16 * (g4 & 1) + 4 * tp) * 2;
      lds_ptr wm = sm + L_B + (DH * (j >> 1) + 4 * h + tq) * 144 + cb;
      lds_ptr wr = sm + L_B + (DH * (8 * (j >> 1) + 4 * h + (tq >> 1)) + 16 + (tq & 1)) * 144 + cb;
      asm volatile("" : "+v"(wm), "+v"(wr));
#pragma unroll
      for (int k = 0; k < 12; ++k) wf[k] = tr_frag(wm + k * 5184, wm + k * 5184 + 1152);
      wm += 12 * 5184;
      asm volatile("" : "+v"(wm));
#pragma unroll
      for (int k = 0; k < 4; ++k) wf[12 + k] = tr_frag(wm + k * 5184, wm + k * 5184 + 1152);
#pragma unroll
      for (int k = 0; k < 2; ++k) wf[16 + k] = tr_frag(wr + k * 41472, wr + k * 41472 + 5184);
      lds_ptr em = sm + L_B + (DH * (8 * j + (g4 >> 1)) + 4 * (g4 & 1) + tq) * 144 + (64 + 4 * (tp & 1)) * 2;
      lds_ptr er = sm + L_B + (DH * (16 * (j & 1) + 4 * g4 + (tq >> 1)) + 16 + (tq & 1)) * 144 + (64 + 4 * (tp & 1)) * 2;
      asm volatile("" : "+v"(em), "+v"(er));
#pragma unroll
      for (int k = 0; k < 4; ++k) we[k] = tr_frag(em + k * 5184, em + k * 5184 + 1152);
      we[4] = tr_frag(er, er + 5184);
    }
    HSYNC();
    HSTAMP(7);
    hload(ce, 0);
    cvprep(0, h8e);
    if (j == 3) {
#pragma unroll 1
      for (unsigned spins = 0; spins < SPIN_CAP; ++spins) {
        bool ok = lane >= 40 || __hip_atomic_load((gu32*)(gws + WS_FU) + (h ? l31 * 32 + qt : hg * 32 + lane) * 32, RLX_AGENT) == tag;
        if (bid == 0) {
#pragma unroll
          for (int i = 0; i < 4; ++i) ok &= __hip_atomic_load((gu32*)(gws + WS_FU) + (64 * i + lane) * 32, RLX_AGENT) == tag;
        }
        if (ALL(ok)) break;
        __builtin_amdgcn_s_sleep(8);
      }
      if (bid == 0 && lane == 0) __hip_atomic_store((gu32*)(gws + WS_EPOCH), tag, RLX_AGENT);
      lds_signal(mi + M_UR);
    } else {
      lds_wait<4>(mi + M_UR, 1);
    }
    NSTAMP(5);
    if (j == 1) *(LAS unsigned*)(mi + M_G + lane * 4) = __builtin_amdgcn_raw_buffer_load_b32(rs, WS_G + (64 * qt + lane) * 4, 0, 16);
    *(LAS u32x4*)(sm + CV_U + 2560 + (ht >> 2) * 80 + (ht & 3) * 16) = uload(ce);
    VM_WAIT(0);
    HSYNC();
    {
      const int d0 = 8 * (qt & 3) + 2 * j;
      lds_ptr ab = sm + CV_H + (l31 & 7) * CV_COPY + (h + (l31 >> 3)) * 16 + 64 * d0;
      lds_ptr bb = sm + CV_U + 2560 + (l31 - d0 - 7) * 80 + (1 - h) * 16;
      asm volatile("" : "+v"(ab), "+v"(bb));
      f32x16 y0 = {0}, y1 = {0};
#pragma unroll
      for (int d = 0; d < 2; ++d) {
#pragma unroll
        for (int s = 0; s < 2; ++s) {
          const bf16x8 a0 = lds_frag(ab + 32 * (2 * d + 1 - s)), b0 = lds_frag(bb + 80 * (7 - d) + 32 * s);
          const bf16x8 b1 = lds_frag(bb + 80 * (39 - d) + 32 * s), a1 = lds_frag(ab + 32 * (2 * d + 65 - s));
          y0 = MFMA32(a0, b0, y0);
          y1 = MFMA32(a0, b1, y1);
          y1 = MFMA32(a1, b0, y1);
          SB0;
        }
      }
      CV_PUT(sm + CV_H + CV_HB + j * 8192 + lane * 16);
      HSYNC();
#pragma unroll
      for (int q = 0; q < 2; ++q)
        ST_SC1_16(__builtin_bit_cast(u32x4, cv_sum(sm + CV_H + CV_HB + (2 * j + q) * 1024 + lane * 16,
                                                   sm + CV_U + 2560 + (32 * (j >> 1) + l31) * 80 + (2 * (j & 1) + q) * 16 + (1 - h) * 8, dve)),
                  WS_ZP + (hg * 32 + qt) * 8192 + (2 * j + q) * 1024 + lane * 16);
    }
    HSYNC();
    NSTAMP(6);
    hload(2 * qt, 0);
    hload(2 * qt + 1, 1);
    cvprep(j >> 1, h8a);
    *(LAS u32x4*)(sm + CV_U + 2560 + (ht >> 2) * 80 + (ht & 3) * 16) = uload(2 * qt);
    *(LAS u32x4*)(sm + CV_U + CV_UB + 2560 + (ht >> 2) * 80 + (ht & 3) * 16) = uload(2 * qt + 1);
    VM_WAIT(0);
#pragma unroll
    for (int i = 0; i < 4; ++i) lds_signal(mi + M_PAIR + 4 * i);
    if (lds_ticket(mi + M_CVR) == 3u && lane == 0) __hip_atomic_store((gu32*)(gws + WS_FZ) + (hg * 32 + qt) * 32, tag, RLX_AGENT);
    lds_wait<8>(mi + M_DONE + 28, 8);
    if (j == 0) {
#pragma unroll 1
      for (unsigned spins = 0; spins < SPIN_CAP; ++spins) {
        if (ALL(h || __hip_atomic_load((gu32*)(gws + WS_FZ) + (hg * 32 + lane) * 32, RLX_AGENT) == tag)) break;
        __builtin_amdgcn_s_sleep(4);
      }
      asm volatile("" ::: "memory");
      f32x4 sa = {0}, sb = {0};
#pragma unroll
      for (int p = 0; p < 4; ++p) {
        const int zo = WS_ZP + (hg * 32 + (lane >> 3) * 4 + p) * 8192 + (qt >> 4) * 4096 + ((lane >> 2) & 1) * 2048 +
                       (2 * (qt & 15) + (lane & 1) + 32 * ((lane >> 1) & 1)) * 16;
        sa += __builtin_bit_cast(f32x4, LD_SC1_16(zo));
        sb += __builtin_bit_cast(f32x4, LD_SC1_16(zo + 1024));
      }
      zv = (u32x4){pk2(sa[0], sa[1]), pk2(sa[2], sa[3]), pk2(sb[0], sb[1]), pk2(sb[2], sb[3])};
      xv = *(const u32x4*)(xt + otok * 1152 + 144 * hg + opc * 16);
    }
    HSTAMP(8);
    __builtin_amdgcn_s_setprio(1);
#pragma unroll 1
    for (int t = 0; t < 2; ++t) {
      lds_ptr ob = sm + (36 * t + (j >> 1)) * 1024 + lane * 16;
      lds_ptr oe = sm + (36 * t + 8 * j + (g4 >> 1)) * 1024 + (l15 + 32 * (g4 & 1)) * 16;
      lds_ptr or_ = sm + (36 * t + 32 + 2 * (j & 1) + (g4 >> 1)) * 1024 + (16 * (j >> 1) + l15 + 32 * (g4 & 1)) * 16;
      lds_wait<1>(mi + (t ? M_OR : M_KVR), 4);
      if (t) NSTAMP(9);
      f32x16 a0 = {0};
      f32x4 e0 = {0}, e1 = {0};
      bf16x8 f[27];
#pragma unroll
      for (int k = 0; k < 31; ++k) {
        if (k < 18) f[k] = lds_frag(ob + k * 2048);
        else if (k < 26) f[k] = lds_frag(oe + ((k - 18) >> 2) * 256 + ((k - 18) & 3) * 2048);
        else if (k == 26) f[k] = lds_frag(or_);
        SB0;
        const int m = k - 4;
        if (m >= 0 && m < 18) a0 = MFMA32(wf[m], f[m], a0);
        else if (m >= 18 && m < 22) e0 = MFMA16(we[m - 18], f[m], e0);
        else if (m >= 22 && m < 26) e1 = MFMA16(we[m - 22], f[m], e1);
        else if (m == 26) { if (j >> 1) e1 = MFMA16(we[4], f[m], e1); else e0 = MFMA16(we[4], f[m], e0); }
        SB0;
      }
      if (t) NSTAMP(10);
      lds_wait<1>(mi + M_MM, 4);
      lds_ptr ap = sm + L_AP + (8 * (j >> 1) + 4 * t) * CV_COPY;
#pragma unroll
      for (int g = 0; g < 4; ++g)
        *(LAS f32x4*)(ap + (l31 >> 3) * CV_COPY + (l31 & 7) * 336 + (j & 1) * 128 + h * 16 + g * 32) = (f32x4){a0[4 * g], a0[4 * g + 1], a0[4 * g + 2], a0[4 * g + 3]};
      if (!h) {
        lds_ptr ae = ap + (l15 >> 3) * CV_COPY + (l15 & 7) * 336 + 256 + (j & 1) * 32 + g4 * 16;
        *(LAS f32x4*)(ae) = e0;
        *(LAS f32x4*)(ae + 2 * CV_COPY) = e1;
      }
      if (j == 0 && t == 0) { lds_wait<1>(mi + M_CE, 4); zscatter(); }
    }
    lds_signal(mi + M_APR);
  } else {
    if (w < 4) {
      const int gt = w >> 1, tt = w & 1, arow = 32 * tt + l31, xsw = (arow >> 1) & 7;
      lds_ptr xb = sm + L_X + arow * 128;
      lds_ptr wb = sm + L_B + (8 * h + tq) * 144 + (36 * gt + 20 * (g4 & 1) + 4 * tp) * 2;
      f32x16 acc = {0};
#pragma unroll 1
      for (int p = 0; p < 3; ++p) {
        if (p == 1) CSTAMP(6);
        if (p == 2) CSTAMP(8);
        lds_wait(mi + M_C1 + 4 * p, p == 1 ? 3 : 6 + p);
        if (p == 0) CSTAMP(5);
        if (p == 1) CSTAMP(7);
        if (p == 2) CSTAMP(9);
        bf16x8 fa[12], fb[12];
#pragma unroll
        for (int k = 0; k < 16; ++k) {
          if (k < 12) {
            fa[k] = tr_frag(wb + k * 2304, wb + k * 2304 + 576);
            fb[k] = lds_frag(xb + (k >> 2) * 8192 + (((2 * (k & 3) + h) ^ xsw) << 4));
          }
          SB0;
          if (k >= 4) acc = MFMA32(fa[k - 4], fb[k - 4], acc);
          SB0;
        }
        if (p < 2) lds_signal(mi + M_C3 + 4 * p);
        xb += 24576;
        wb += 27648;
      }
      STAMP(2);
      u32x4 a;
#pragma unroll
      for (int i = 0; i < 4; ++i) a[i] = pk2(acc[2 * i], acc[2 * i + 1]);
      lds_ptr qe = sm + L_Q + (4 * gt + tt) * 1024;
      *(LAS u32x4*)(qe + lane * 16) = a;
      lds_ptr qo = qe + 2048 + l31 * 16;
      *(LAS unsigned*)(qo + h * 512 + 4) = pk2(acc[8], acc[9]);
      *(LAS unsigned*)(qo + (h ? 8 : 512)) = pk2(acc[10], acc[11]);
      *(LAS unsigned*)(qo + h * 512 + 12) = pk2(acc[12], acc[13]);
      *(LAS unsigned*)(h ? sm + L_Q + 8192 + ((2 * gt + 1) * 64 + arow) * 4 : qo + 520) = pk2(acc[14], acc[15]);
    } else if (w == 5) {
      lds_wait<1>(mi + M_RDY, 4);
      const u32x4 k0 = *(LAS u32x4*)(sm + lane * 16);
      float kn = dot2(k0[3], dot2(k0[2], dot2(k0[1], dot2(k0[0], 0.f))));
      kn = wave_max(kn + xhalf(kn));
      if (lane == 0) *(LAS float*)(mi + M_KN0) = kn;
    } else if (w == 4) {
      __builtin_amdgcn_s_setprio(2);
      lds_ptr xb = sm + L_X + l15 * 128;
      lds_ptr wb = sm + L_B + (8 * g4 + tq) * 144 + (tp == 1 ? 104 : 32);
      f32x4 acc[4] = {{0}, {0}, {0}, {0}};
#pragma unroll 1
      for (int p = 0; p < 3; ++p) {
        lds_wait(mi + M_C1 + 4 * p, p == 1 ? 3 : 6 + p);
        bf16x8 fa[6], fb[24];
#pragma unroll
        for (int k = 0; k < 8; ++k) {
          if (k < 6) {
            fa[k] = tr_frag(wb + k * 4608, wb + k * 4608 + 576);
#pragma unroll
            for (int t = 0; t < 4; ++t) fb[4 * k + t] = lds_frag(xb + (k >> 1) * 8192 + t * 2048 + (((4 * (k & 1) + g4) ^ (l15 >> 1)) << 4));
          }
          SB0;
          if (k >= 2) {
#pragma unroll
            for (int t = 0; t < 4; ++t) acc[t] = MFMA16(fa[k - 2], fb[4 * (k - 2) + t], acc[t]);
          }
          SB0;
        }
        if (p < 2) lds_signal(mi + M_C3 + 4 * p);
        xb += 24576;
        wb += 27648;
      }
      STAMP(2);
      if (g4 < 2) {
#pragma unroll
        for (int t = 0; t < 4; ++t) {
          const int tok = 16 * t + l15;
          *(LAS unsigned*)(sm + L_Q + 8192 + (2 * g4 * 64 + tok) * 4) = pk2(acc[t][0], acc[t][1]);
          *(LAS unsigned*)(sm + L_Q + ((2 * g4 + 1) * 2 + (t >> 1)) * 1024 + (tok & 31) * 16) = pk2(acc[t][2], acc[t][3]);
        }
      }
    }
    lds_signal(mi + M_QD);
    const int hl = w & 3, kh = w >> 2;
    const int voff = 4096 + (l31 < 18 ? h * 288 + l31 * 16 : (l31 == 18 ? 576 : 592));
    lds_wait(mi + M_QD, 8);
    STAMP(3);
    const u32x4 q0u = *(LAS u32x4*)(sm + L_Q + (2 * hl) * 1024 + lane * 16);
    const u32x4 q1u = *(LAS u32x4*)(sm + L_Q + (2 * hl + 1) * 1024 + lane * 16);
    const unsigned qr = *(LAS unsigned*)(sm + L_Q + 8192 + (hl * 64 + lane) * 4);
    const float kn0 = *(LAS float*)(mi + M_KN0);
    lds_signal(mi + M_QR);
    float n0 = 0.f, n1 = 0.f;
#pragma unroll
    for (int i = 0; i < 4; ++i) { n0 = dot2(q0u[i], n0); n1 = dot2(q1u[i], n1); }
    const float nq = dot2(qr, (h ? n1 : n0) + xhalf(h ? n0 : n1));
    const u32x2 qrem_u = {qr, pk2(-__builtin_amdgcn_sqrtf(nq * kn0), 0.f)};
    f32x16 o0, o1;
    float m0 = 0.f, m1 = 0.f;
    STAMP(4); RSTAMP(2);
    {
      int cnt = 8, off = 0, tmp, t2, so = ((s0 + 4) & 7) * RSLOT;
      unsigned ca = sbase + L_M + M_DONE - 4, dst = sbase + w * 2496;
      const unsigned long long ksrc = (unsigned long long)ws + WS_KV + w * 2496 - (sbase + kh * KV_GROUP);
      asm volatile(
      "v_mov_b32 v107, v126\n"
      "v_mov_b32 v108, v127\n"
      "v_mov_b32 v109, v128\n"
      "v_mov_b32 v130, 1\n"
      "ds_read2st64_b64 v[110:113], v109 offset0:0 offset1:1\n"
      "ds_read_b128 v[114:117], v107 offset:0\n"
      "ds_read_b128 v[118:121], v108 offset:0\n"
      "s_mov_b32 vcc_lo, %[first]\n"
      "s_mov_b32 vcc_hi, 0\n"
      "s_setprio 1\n"
      "v_mov_b32 v56, 0\n"
      "v_mov_b32 v57, 0\n"
      "v_mov_b32 v58, 0\n"
      "v_mov_b32 v59, 0\n"
      "v_mov_b32 v60, 0\n"
      "v_mov_b32 v61, 0\n"
      "v_mov_b32 v62, 0\n"
      "v_mov_b32 v63, 0\n"
      "v_mov_b32 v122, 0\n"
      "v_mov_b32 v123, 0\n"
      "v_mov_b32 v124, 0\n"
      "v_mov_b32 v125, 0\n"
      "v_mov_b32 v64, 0\n"
      "v_mov_b32 v65, 0\n"
      "v_mov_b32 v66, 0\n"
      "v_mov_b32 v67, 0\n"
      "v_mov_b32 v68, 0\n"
      "v_mov_b32 v69, 0\n"
      "v_mov_b32 v70, 0\n"
      "v_mov_b32 v71, 0\n"
      "v_mov_b32 v72, 0\n"
      "v_mov_b32 v73, 0\n"
      "v_mov_b32 v74, 0\n"
      "v_mov_b32 v75, 0\n"
      "v_mov_b32 v76, 0\n"
      "v_mov_b32 v77, 0\n"
      "v_mov_b32 v78, 0\n"
      "v_mov_b32 v79, 0\n"
      "s_waitcnt lgkmcnt(1)\n"
      "v_mfma_f32_32x32x4_2b_bf16 v[0:31], v[110:111], v[104:105], 0 cbsz:1 abid:0\n"
      "v_mov_b32 v80, 0\n"
      "v_mov_b32 v81, 0\n"
      "v_mov_b32 v82, 0\n"
      "v_mov_b32 v83, 0\n"
      "v_mov_b32 v84, 0\n"
      "v_mov_b32 v85, 0\n"
      "v_mov_b32 v86, 0\n"
      "v_mov_b32 v87, 0\n"
      "v_mov_b32 v88, 0\n"
      "v_mov_b32 v89, 0\n"
      "v_mov_b32 v90, 0\n"
      "v_mov_b32 v91, 0\n"
      "v_mov_b32 v92, 0\n"
      "v_mov_b32 v93, 0\n"
      "v_mov_b32 v94, 0\n"
      "v_mov_b32 v95, 0\n"
      "s_nop 2\n"
      "v_mfma_f32_32x32x16_bf16 v[0:15], v[114:117], v[96:99], v[0:15]\n"
      "v_mfma_f32_32x32x16_bf16 v[16:31], v[114:117], v[100:103], v[16:31]\n"
      "s_nop 11\n"
      "1:\n"
      "s_cbranch_vccz 4f\n"
      "s_setprio 2\n"
      "4:\n"
      "v_exp_f32 v0, v0\n"
      "v_exp_f32 v1, v1\n"
      "v_smfmac_f32_32x32x32_bf16 v[80:95], v[122:125], v[56:63], v106\n"
      "ds_read_b128 v[114:117], v107 offset:1024\n"
      "ds_read_b128 v[122:125], v108 offset:608\n"
      "v_exp_f32 v2, v2\n"
      "v_exp_f32 v3, v3\n"
      "v_exp_f32 v4, v4\n"
      "v_exp_f32 v5, v5\n"
      "v_mfma_f32_32x32x4_2b_bf16 v[32:63], v[110:111], v[104:105], 0 cbsz:1 abid:1\n"
      "v_exp_f32 v6, v6\n"
      "v_exp_f32 v7, v7\n"
      "v_exp_f32 v8, v8\n"
      "v_exp_f32 v9, v9\n"
      "v_exp_f32 v10, v10\n"
      "v_exp_f32 v11, v11\n"
      "v_exp_f32 v12, v12\n"
      "s_waitcnt lgkmcnt(2)\n"
      "v_exp_f32 v13, v13\n"
      "v_smfmac_f32_32x32x32_bf16 v[64:79], v[118:121], v[0:7], v106\n"
      "v_exp_f32 v14, v14\n"
      "v_exp_f32 v15, v15\n"
      "v_exp_f32 v16, v16\n"
      "s_waitcnt lgkmcnt(0)\n"
      "v_exp_f32 v17, v17\n"
      "v_smfmac_f32_32x32x32_bf16 v[64:79], v[122:125], v[8:15], v106\n"
      "v_exp_f32 v18, v18\n"
      "v_exp_f32 v19, v19\n"
      "v_exp_f32 v20, v20\n"
      "v_exp_f32 v21, v21\n"
      "v_mfma_f32_32x32x16_bf16 v[32:47], v[114:117], v[96:99], v[32:47]\n"
      "v_exp_f32 v22, v22\n"
      "v_exp_f32 v23, v23\n"
      "v_exp_f32 v24, v24\n"
      "v_exp_f32 v25, v25\n"
      "v_smfmac_f32_32x32x32_bf16 v[80:95], v[118:121], v[16:23], v106\n"
      "v_exp_f32 v26, v26\n"
      "ds_read_b128 v[118:121], v108 offset:1216\n"
      "v_exp_f32 v27, v27\n"
      "v_exp_f32 v28, v28\n"
      "v_exp_f32 v29, v29\n"
      "v_mfma_f32_32x32x16_bf16 v[48:63], v[114:117], v[100:103], v[48:63]\n"
      "v_exp_f32 v30, v30\n"
      "v_exp_f32 v31, v31\n"
      "v_exp_f32 v32, v32\n"
      "v_exp_f32 v33, v33\n"
      "v_smfmac_f32_32x32x32_bf16 v[80:95], v[122:125], v[24:31], v106\n"
      "ds_read_b128 v[114:117], v107 offset:2048\n"
      "ds_read_b128 v[122:125], v108 offset:1824\n"
      "ds_read_b32 v131, v129\n"
      "v_exp_f32 v34, v34\n"
      "v_exp_f32 v35, v35\n"
      "v_exp_f32 v36, v36\n"
      "v_exp_f32 v37, v37\n"
      "v_mfma_f32_32x32x4_2b_bf16 v[0:31], v[112:113], v[104:105], 0 cbsz:1 abid:0\n"
      "v_exp_f32 v38, v38\n"
      "v_exp_f32 v39, v39\n"
      "v_exp_f32 v40, v40\n"
      "v_exp_f32 v41, v41\n"
      "v_exp_f32 v42, v42\n"
      "v_exp_f32 v43, v43\n"
      "v_exp_f32 v44, v44\n"
      "s_waitcnt lgkmcnt(3)\n"
      "v_exp_f32 v45, v45\n"
      "v_smfmac_f32_32x32x32_bf16 v[64:79], v[118:121], v[32:39], v106\n"
      "v_exp_f32 v46, v46\n"
      "v_exp_f32 v47, v47\n"
      "v_exp_f32 v48, v48\n"
      "s_waitcnt lgkmcnt(0)\n"
      "s_waitcnt vmcnt(0)\n"
      "s_mov_b64 exec, 1\n"
      "ds_add_u32 v129, v130 offset:76\n"
      "s_mov_b64 exec, -1\n"
      "v_exp_f32 v49, v49\n"
      "v_smfmac_f32_32x32x32_bf16 v[64:79], v[122:125], v[40:47], v106\n"
      "v_exp_f32 v50, v50\n"
      "v_exp_f32 v51, v51\n"
      "v_exp_f32 v52, v52\n"
      "v_exp_f32 v53, v53\n"
      "v_mfma_f32_32x32x16_bf16 v[0:15], v[114:117], v[96:99], v[0:15]\n"
      "v_exp_f32 v54, v54\n"
      "v_exp_f32 v55, v55\n"
      "v_exp_f32 v56, v56\n"
      "v_exp_f32 v57, v57\n"
      "v_smfmac_f32_32x32x32_bf16 v[80:95], v[118:121], v[48:55], v106\n"
      "v_exp_f32 v58, v58\n"
      "ds_read_b128 v[118:121], v108 offset:2432\n"
      "v_exp_f32 v59, v59\n"
      "v_exp_f32 v60, v60\n"
      "v_exp_f32 v61, v61\n"
      "v_mfma_f32_32x32x16_bf16 v[16:31], v[114:117], v[100:103], v[16:31]\n"
      "v_exp_f32 v62, v62\n"
      "v_exp_f32 v63, v63\n"
      "v_readfirstlane_b32 %[tmp], v131\n"
      "s_and_b32 %[t2], %[cnt], 12\n"
      "s_cmp_lg_u32 %[t2], 4\n"
      "s_cbranch_scc1 8f\n"
      "s_cmp_lt_u32 %[tmp], 8\n"
      "s_cbranch_scc1 7f\n"
      "9:\n"
      "s_mov_b32 m0, %[dst]\n"
      "s_add_u32 s96, %[slo], %[so]\n"
      "s_addc_u32 s97, %[shi], 0\n"
      "global_load_lds_dwordx4 v126, s[96:97] sc1\n"
      "global_load_lds_dwordx4 v126, s[96:97] offset:1024 sc1\n"
      "s_mov_b64 exec, 0xfffffff\n"
      "global_load_lds_dwordx4 v126, s[96:97] offset:2048 sc1\n"
      "s_mov_b64 exec, -1\n"
      "s_add_u32 %[dst], %[dst], 19968\n"
      "s_add_u32 %[so], %[so], 19968\n"
      "s_cmp_eq_u32 %[so], 159744\n"
      "s_cselect_b32 %[so], 0, %[so]\n"
      "8:\n"
      "v_exp_f32 v0, v0\n"
      "v_exp_f32 v1, v1\n"
      "v_smfmac_f32_32x32x32_bf16 v[80:95], v[122:125], v[56:63], v106\n"
      "ds_read_b128 v[114:117], v107 offset:3072\n"
      "ds_read_b128 v[122:125], v108 offset:3040\n"
      "ds_read_b32 v131, v129 offset:72\n"
      "v_exp_f32 v2, v2\n"
      "v_exp_f32 v3, v3\n"
      "v_exp_f32 v4, v4\n"
      "v_exp_f32 v5, v5\n"
      "v_mfma_f32_32x32x4_2b_bf16 v[32:63], v[112:113], v[104:105], 0 cbsz:1 abid:1\n"
      "v_exp_f32 v6, v6\n"
      "v_exp_f32 v7, v7\n"
      "v_exp_f32 v8, v8\n"
      "v_exp_f32 v9, v9\n"
      "v_exp_f32 v10, v10\n"
      "v_exp_f32 v11, v11\n"
      "v_exp_f32 v12, v12\n"
      "s_waitcnt lgkmcnt(3)\n"
      "v_exp_f32 v13, v13\n"
      "v_smfmac_f32_32x32x32_bf16 v[64:79], v[118:121], v[0:7], v106\n"
      "v_exp_f32 v14, v14\n"
      "v_exp_f32 v15, v15\n"
      "v_exp_f32 v16, v16\n"
      "s_waitcnt lgkmcnt(0)\n"
      "v_exp_f32 v17, v17\n"
      "v_smfmac_f32_32x32x32_bf16 v[64:79], v[122:125], v[8:15], v106\n"
      "v_exp_f32 v18, v18\n"
      "v_exp_f32 v19, v19\n"
      "v_exp_f32 v20, v20\n"
      "v_exp_f32 v21, v21\n"
      "v_mfma_f32_32x32x16_bf16 v[32:47], v[114:117], v[96:99], v[32:47]\n"
      "v_exp_f32 v22, v22\n"
      "v_exp_f32 v23, v23\n"
      "v_exp_f32 v24, v24\n"
      "v_exp_f32 v25, v25\n"
      "v_smfmac_f32_32x32x32_bf16 v[80:95], v[118:121], v[16:23], v106\n"
      "v_exp_f32 v26, v26\n"
      "ds_read_b128 v[118:121], v108 offset:3648\n"
      "v_exp_f32 v27, v27\n"
      "v_exp_f32 v28, v28\n"
      "v_exp_f32 v29, v29\n"
      "v_mfma_f32_32x32x16_bf16 v[48:63], v[114:117], v[100:103], v[48:63]\n"
      "v_exp_f32 v30, v30\n"
      "v_exp_f32 v31, v31\n"
      "s_cbranch_vccz 5f\n"
      "s_setprio 0\n"
      "5:\n"
      "v_readfirstlane_b32 %[tmp], v131\n"
      "s_add_u32 %[off], %[off], 19968\n"
      "s_cmp_eq_u32 %[off], 79872\n"
      "s_cselect_b32 %[off], 0, %[off]\n"
      "s_cmp_lt_i32 %[tmp], 8\n"
      "s_cbranch_scc1 3f\n"
      "2:\n"
      "v_add_u32 v109, %[off], v128\n"
      "ds_read2st64_b64 v[110:113], v109 offset0:0 offset1:1\n"
      "v_add_u32 v107, %[off], v126\n"
      "v_exp_f32 v32, v32\n"
      "v_exp_f32 v33, v33\n"
      "v_smfmac_f32_32x32x32_bf16 v[80:95], v[122:125], v[24:31], v106\n"
      "ds_read_b128 v[114:117], v107 offset:0\n"
      "ds_read_b128 v[122:125], v108 offset:4256\n"
      "v_exp_f32 v34, v34\n"
      "v_exp_f32 v35, v35\n"
      "v_exp_f32 v36, v36\n"
      "s_waitcnt lgkmcnt(2)\n"
      "v_exp_f32 v37, v37\n"
      "v_mfma_f32_32x32x4_2b_bf16 v[0:31], v[110:111], v[104:105], 0 cbsz:1 abid:0\n"
      "v_exp_f32 v38, v38\n"
      "v_exp_f32 v39, v39\n"
      "v_exp_f32 v40, v40\n"
      "v_exp_f32 v41, v41\n"
      "v_exp_f32 v42, v42\n"
      "v_exp_f32 v43, v43\n"
      "v_exp_f32 v44, v44\n"
      "v_exp_f32 v45, v45\n"
      "v_smfmac_f32_32x32x32_bf16 v[64:79], v[118:121], v[32:39], v106\n"
      "v_exp_f32 v46, v46\n"
      "v_exp_f32 v47, v47\n"
      "v_exp_f32 v48, v48\n"
      "s_waitcnt lgkmcnt(0)\n"
      "s_mov_b64 exec, 1\n"
      "ds_add_u32 v129, v130 offset:4\n"
      "s_mov_b64 exec, -1\n"
      "v_exp_f32 v49, v49\n"
      "v_smfmac_f32_32x32x32_bf16 v[64:79], v[122:125], v[40:47], v106\n"
      "v_exp_f32 v50, v50\n"
      "v_exp_f32 v51, v51\n"
      "v_exp_f32 v52, v52\n"
      "v_exp_f32 v53, v53\n"
      "v_mfma_f32_32x32x16_bf16 v[0:15], v[114:117], v[96:99], v[0:15]\n"
      "v_exp_f32 v54, v54\n"
      "v_exp_f32 v55, v55\n"
      "v_exp_f32 v56, v56\n"
      "v_exp_f32 v57, v57\n"
      "v_smfmac_f32_32x32x32_bf16 v[80:95], v[118:121], v[48:55], v106\n"
      "v_add_u32 v108, %[off], v127\n"
      "v_add_u32 v129, 4, v129\n"
      "v_exp_f32 v58, v58\n"
      "ds_read_b128 v[118:121], v108 offset:0\n"
      "v_exp_f32 v59, v59\n"
      "v_exp_f32 v60, v60\n"
      "v_exp_f32 v61, v61\n"
      "v_mfma_f32_32x32x16_bf16 v[16:31], v[114:117], v[100:103], v[16:31]\n"
      "v_exp_f32 v62, v62\n"
      "v_exp_f32 v63, v63\n"
      "s_sub_u32 %[cnt], %[cnt], 1\n"
      "s_cmp_lg_u32 %[cnt], 0\n"
      "s_cbranch_scc1 1b\n"
      "s_branch 6f\n"
      "3:\n"
      "s_sleep 1\n"
      "ds_read_b32 v131, v129 offset:72\n"
      "s_waitcnt lgkmcnt(0)\n"
      "v_readfirstlane_b32 %[tmp], v131\n"
      "s_nop 0\n"
      "s_cmp_lt_i32 %[tmp], 8\n"
      "s_cbranch_scc1 3b\n"
      "s_branch 2b\n"
      "7:\n"
      "s_sleep 1\n"
      "ds_read_b32 v131, v129\n"
      "s_waitcnt lgkmcnt(0)\n"
      "v_readfirstlane_b32 %[tmp], v131\n"
      "s_nop 0\n"
      "s_cmp_lt_u32 %[tmp], 8\n"
      "s_cbranch_scc1 7b\n"
      "s_branch 9b\n"
      "6:\n"
      "v_smfmac_f32_32x32x32_bf16 v[80:95], v[122:125], v[56:63], v106\n"
      "s_setprio 0\n"
      "s_waitcnt lgkmcnt(0)\n"
      "s_nop 15\n"
      "s_nop 7\n"
      : "=&{v[64:79]}"(o0), "=&{v[80:95]}"(o1), "+{v129}"(ca), [cnt] "+s"(cnt), [off] "+s"(off), [tmp] "=&s"(tmp),
        [t2] "=&s"(t2), [so] "+s"(so), [dst] "+s"(dst)
      : "{v[96:99]}"(q0u), "{v[100:103]}"(q1u), "{v[104:105]}"(qrem_u), "{v106}"(SPIDX),
        "{v126}"(sbase + kh * KV_GROUP + lane * 16), "{v127}"(sbase + kh * KV_GROUP + voff),
        "{v128}"(sbase + kh * KV_GROUP + 8960 + lane * 8), [first] "s"(1 - kh), [slo] "s"((unsigned)ksrc), [shi] "s"((unsigned)(ksrc >> 32))
      : "v0", "v1", "v2", "v3", "v4", "v5", "v6", "v7", "v8", "v9", "v10", "v11", "v12", "v13", "v14", "v15",
        "v16", "v17", "v18", "v19", "v20", "v21", "v22", "v23", "v24", "v25", "v26", "v27", "v28", "v29", "v30", "v31",
        "v32", "v33", "v34", "v35", "v36", "v37", "v38", "v39", "v40", "v41", "v42", "v43", "v44", "v45", "v46", "v47",
        "v48", "v49", "v50", "v51", "v52", "v53", "v54", "v55", "v56", "v57", "v58", "v59", "v60", "v61", "v62", "v63",
        "v107", "v108", "v109", "v110", "v111", "v112", "v113", "v114", "v115", "v116", "v117", "v118", "v119", "v120", "v121", "v122",
        "v123", "v124", "v125", "v130", "v131", "s96", "s97", "vcc", "scc", "memory");
    }
    xv = *(const u32x4*)(xt + otok * 1152 + 144 * hg + opc * 16);
    const float dv = (float)ssmd[72 * hg + 2 * qt + (hl >> 1)];
    if (!ALL(nq * *(LAS float*)(mi + M_KN) < 47.f * 47.f)) {
      m0 = m1 = -__builtin_inff();
      o0 = o1 = (f32x16){0};
      const bf16x8 q0 = __builtin_bit_cast(bf16x8, q0u), q1 = __builtin_bit_cast(bf16x8, q1u);
      const s16x4 qrem = __builtin_bit_cast(s16x4, (u32x2){qr, 0u});
#pragma unroll 1
      for (int t = 0; t < 32; ++t) {
        const int g = WS_KV + (t >> 2) * RSLOT + kh * KV_GROUP;
        const u32x2 kr = __builtin_bit_cast(u32x2, __builtin_amdgcn_raw_buffer_load_b64(rs, g + 8960 + (t & 3) * 256 + l31 * 8, 0, 16));
        const bf16x8 km = __builtin_bit_cast(bf16x8, LD_SC1_16(g + (t & 3) * 1024 + lane * 16));
        const bf16x8 va = __builtin_bit_cast(bf16x8, LD_SC1_16(g + voff + (t & 3) * 1216));
        const bf16x8 vb = __builtin_bit_cast(bf16x8, LD_SC1_16(g + voff + (t & 3) * 1216 + 608));
        const f32x32 z = {0};
        const f32x32 r = __builtin_amdgcn_mfma_f32_32x32x4bf16_1k(__builtin_bit_cast(s16x4, kr), qrem, z, 0, 0, 0);
        online_step(MFMA32(km, q0, lo16(r)), m0, o0, va, vb);
        online_step(MFMA32(km, q1, hi16(r)), m1, o1, va, vb);
      }
    }
    HSTAMP(9);
    lds_ptr ex = sm + L_EX + hl * 6144 + lane * 16;
    if (kh) {
      *(LAS f32x4*)(ex) = (f32x4){o0[0], o0[1], o0[2], o0[3]};
      *(LAS f32x4*)(ex + 1024) = (f32x4){o0[4], o0[5], o0[6], o0[7]};
      *(LAS f32x4*)(ex + 2048) = (f32x4){o0[8], o0[9], o0[10], m0};
      *(LAS f32x4*)(ex + 3072) = (f32x4){o1[0], o1[1], o1[2], o1[3]};
      *(LAS f32x4*)(ex + 4096) = (f32x4){o1[4], o1[5], o1[6], o1[7]};
      *(LAS f32x4*)(ex + 5120) = (f32x4){o1[8], o1[9], o1[10], m1};
      lds_signal(mi + M_PAIR + 4 * hl);
    }
    lds_wait<1>(mi + M_PAIR + 4 * hl, 5);
    STAMP(10); RSTAMP(3);
    if (!kh) {
      __builtin_amdgcn_s_setprio(3);
#pragma unroll
      for (int t = 0; t < 2; ++t) {
        const f32x16 keep = t ? o1 : o0;
        const f32x4 t0 = *(LAS f32x4*)(ex + t * 3072), t1 = *(LAS f32x4*)(ex + t * 3072 + 1024), t2 = *(LAS f32x4*)(ex + t * 3072 + 2048);
        const float mk = t ? m1 : m0, mm = __builtin_fmaxf(mk, t2[3]);
        float fa = __builtin_amdgcn_exp2f(mk - mm), fb = __builtin_amdgcn_exp2f(t2[3] - mm);
        const float l = keep[10] * fa + t2[2] * fb, lx = xhalf(l);
        const float inv = __builtin_amdgcn_rcpf(h ? lx : l);
        fa *= inv;
        fb *= inv;
        float o[10];
#pragma unroll
        for (int i = 0; i < 10; ++i) o[i] = keep[i] * fa + (i < 4 ? t0[i & 3] : (i < 8 ? t1[i & 3] : t2[i & 3])) * fb;
        u32x4 a;
#pragma unroll
        for (int i = 0; i < 4; ++i) a[i] = UMIN(pk2(o[2 * i], o[2 * i + 1]), 0xfffffffeu);
        const int ob = WS_O + (2 * qt + t) * 36 * 1024;
        ST_SC1_16(a, ob + (4 * hg + hl) * 1024 + lane * 16);
        if (!h) ST_SC1_4(UMIN(pk2(o[8], o[9]), 0xfffffffeu), ob + 32768 + (4 * hg + hl) * 128 + l31 * 4);
      }
      lds_signal(mi + M_EXD);
      __builtin_amdgcn_s_setprio(1);
      HSTAMP(5);
      __builtin_amdgcn_s_sleep(O_DELAY);
      u32x4 of[18];
#pragma unroll
      for (int i = 0; i < 18; ++i) of[i] = LD_SC1_16(WS_O + (2 * qt * 36 + (i / 9) * 36 + 9 * hl + i % 9) * 1024 + lane * 16);
      lds_wait<1>(mi + M_EXD, 4);
#pragma unroll
      for (int t = 0; t < 2; ++t) {
        unsigned mx = 0;
#pragma unroll
        for (int i = 9 * t; i < 9 * t + 9; ++i) mx = UMAX(UMAX(mx, UMAX(of[i][0], of[i][1])), UMAX(of[i][2], of[i][3]));
        if (__builtin_expect(!ALL(mx != ~0u), 0)) {
#pragma unroll 1
          for (unsigned spins = 0; spins < SPIN_CAP; ++spins) {
            asm volatile("" ::: "memory");
#pragma unroll
            for (int i = 9 * t; i < 9 * t + 9; ++i) of[i] = LD_SC1_16(WS_O + (2 * qt * 36 + t * 36 + 9 * hl + i % 9) * 1024 + lane * 16);
            mx = 0;
#pragma unroll
            for (int i = 9 * t; i < 9 * t + 9; ++i) mx = UMAX(UMAX(mx, UMAX(of[i][0], of[i][1])), UMAX(of[i][2], of[i][3]));
            if (ALL(mx != ~0u)) break;
            __builtin_amdgcn_s_sleep(2);
          }
        }
#pragma unroll
        for (int i = 0; i < 9; ++i) {
          if (i >= 5 && hl == 3) {
            lds_ptr p = sm + (36 * t + 27 + i) * 1024 + (4 * (lane & 7) + 32 * h) * 16 + ((lane >> 3) & 3) * 4;
#pragma unroll
            for (int e = 0; e < 4; ++e) *(LAS unsigned*)(p + e * 16) = of[9 * t + i][e];
          } else {
            *(LAS u32x4*)(sm + (36 * t + 9 * hl + i) * 1024 + lane * 16) = of[9 * t + i];
          }
        }
        if (t == 0) lds_signal(mi + M_KVR);
      }
      lds_signal(mi + M_OR);
      __builtin_amdgcn_s_setprio(0);
      HSTAMP(6);
    } else {
      const int b = hl >> 1, o = hl & 1;
      lds_ptr ab = sm + CV_H + b * CV_HB + (l31 & 7) * CV_COPY + (h + (l31 >> 3)) * 16 + 1024 * o;
      lds_ptr bb = sm + CV_U + b * CV_UB + 2560 + (l31 - 16 * o - 7) * 80 + (1 - h) * 16;
      f32x16 y0 = {0}, y1 = {0};
      bf16x8 fx[8], fy[8];
      __builtin_amdgcn_s_setprio(2);
      asm volatile("" : "+v"(ab), "+v"(bb));
      CV_LD(fx, 0);
#pragma unroll 1
      for (int it = 0; it < 4; ++it) {
        asm volatile("" : "+v"(ab), "+v"(bb));
        CV_STEP(fx, fy, 1);
        CV_STEP(fy, fx, 2);
        CV_STEP(fx, fy, 3);
        CV_STEP(fy, fx, 4);
        ab += 256;
        bb -= 320;
      }
      HSTAMP(5);
      lds_ptr px = sm + CV_H + b * CV_HB + h * CV_COPY + l31 * 16;
      {
        const f32x16 give = o ? y0 : y1;
#pragma unroll
        for (int g = 0; g < 4; ++g) *(LAS f32x4*)(px + o * 3200 + g * 2 * CV_COPY) = (f32x4){give[4 * g], give[4 * g + 1], give[4 * g + 2], give[4 * g + 3]};
        if (o) y0 = y1;
      }
      lds_signal(mi + M_AS + 4 * b);
      lds_signal(mi + M_MM);
      u32x2 uu[4];
#pragma unroll
      for (int g = 0; g < 4; ++g) uu[g] = *(LAS u32x2*)(sm + CV_U + b * CV_UB + 2560 + (32 * o + l31) * 80 + g * 16 + (1 - h) * 8);
#pragma unroll
      for (int g = 0; g < 4; ++g) {
        y0[4 * g] = __builtin_fmaf(bf_hi(uu[g][1]), dv, y0[4 * g]);
        y0[4 * g + 1] = __builtin_fmaf(bf_lo(uu[g][1]), dv, y0[4 * g + 1]);
        y0[4 * g + 2] = __builtin_fmaf(bf_hi(uu[g][0]), dv, y0[4 * g + 2]);
        y0[4 * g + 3] = __builtin_fmaf(bf_lo(uu[g][0]), dv, y0[4 * g + 3]);
      }
      lds_wait(mi + M_AS + 4 * b, 2);
      u32x4 z[2];
#pragma unroll
      for (int g = 0; g < 4; ++g) {
        const f32x4 t = *(LAS f32x4*)(px + (1 - o) * 3200 + g * 2 * CV_COPY);
        z[g >> 1][2 * (g & 1)] = UMIN(pk2(t[0] + y0[4 * g], t[1] + y0[4 * g + 1]), 0xfffffffeu);
        z[g >> 1][2 * (g & 1) + 1] = UMIN(pk2(t[2] + y0[4 * g + 2], t[3] + y0[4 * g + 3]), 0xfffffffeu);
      }
      const int zo = WS_Z + (72 * hg + 2 * qt + b) * 4096 + o * 2048 + lane * 16;
      ST_SC1_16(z[0], zo);
      ST_SC1_16(z[1], zo + 1024);
      lds_signal(mi + M_CE);
      __builtin_amdgcn_s_setprio(0);
      HSTAMP(6);
    }
    lds_wait<2>(mi + M_CE, 4);
    __builtin_amdgcn_s_sleep(Z_DELAY);
#pragma unroll 1
    for (unsigned spins = 0; spins < SPIN_CAP; ++spins) {
      zv = LD_SC1_16(WS_Z + (72 * hg + (tid >> 3)) * 4096 + (qt >> 4) * 2048 + ((tid >> 2) & 1) * 1024 +
                     (2 * (qt & 15) + (tid & 1) + 32 * ((tid >> 1) & 1)) * 16);
      asm volatile("" ::: "memory");
      if (ALL(UMAX(UMAX(zv[0], zv[1]), UMAX(zv[2], zv[3])) != ~0u)) break;
      __builtin_amdgcn_s_sleep(2);
    }
    HSTAMP(7);
    zscatter();
  }

  if (w < 9) {
    lds_ptr ap = sm + L_AP + (otok >> 3) * CV_COPY + (otok & 7) * 336 + opc * 32;
    lds_wait<1>(mi + M_APR, 4);
    const float gq = *(LAS float*)(mi + M_G + otok * 4), omg = rbf(1.f - gq);
    f32x4 a0 = *(LAS f32x4*)(ap) + *(LAS f32x4*)(ap + CV_HB), a1 = *(LAS f32x4*)(ap + 16) + *(LAS f32x4*)(ap + CV_HB + 16);
    if (opc == 8) {
      a0 += *(LAS f32x4*)(ap + 32) + *(LAS f32x4*)(ap + CV_HB + 32);
      a1 += *(LAS f32x4*)(ap + 48) + *(LAS f32x4*)(ap + CV_HB + 48);
    }
    a0 *= gq;
    a1 *= gq;
    f32x4 x0 = {omg * bf_lo(xv[0]), omg * bf_hi(xv[0]), omg * bf_lo(xv[1]), omg * bf_hi(xv[1])};
    f32x4 x1 = {omg * bf_lo(xv[2]), omg * bf_hi(xv[2]), omg * bf_lo(xv[3]), omg * bf_hi(xv[3])};
    lds_ptr zq = sm + L_ZT + ((otok >> 5) + ((otok >> 1) & 2) + ((otok >> 2) & 4) + 8 * (otok & 3) + 4 * (otok & 8)) * 144 + opc * 16;
    u32x4* op = (u32x4*)(out + (size_t)(64 * qt + otok) * D_ + 72 * hg + 8 * opc);
    asm volatile("" : "+v"(a0), "+v"(a1), "+v"(x0), "+v"(x1), "+v"(zq), "+v"(op));
    lds_wait(mi + M_ZS, 9);
    if (w < 8) HSTAMP(8);
    const u32x4 z = *(LAS u32x4*)zq;
    *(GAS u32x4*)op = (u32x4){pk2(__builtin_fmaf(x0[0], bf_lo(z[0]), a0[0]), __builtin_fmaf(x0[1], bf_hi(z[0]), a0[1])),
                              pk2(__builtin_fmaf(x0[2], bf_lo(z[1]), a0[2]), __builtin_fmaf(x0[3], bf_hi(z[1]), a0[3])),
                              pk2(__builtin_fmaf(x1[0], bf_lo(z[2]), a1[0]), __builtin_fmaf(x1[1], bf_hi(z[2]), a1[1])),
                              pk2(__builtin_fmaf(x1[2], bf_lo(z[3]), a1[2]), __builtin_fmaf(x1[3], bf_hi(z[3]), a1[3]))};
  }
  STAMP_FLUSH(w < 4 ? 0 : (w < 8 ? 1 : (w < 11 ? 2 : 3)));
  asm volatile("fk_end:");
}

extern "C" void kernel_launch(void* const* d_in, const int* in_sizes, int n_in, void* d_out, int out_size,
                              void* d_ws, size_t ws_size, hipStream_t stream) {
  fused_kernel<<<256, 768, 0, stream>>>((const __bf16*)d_in[0], (const __bf16*)d_in[1], (const __bf16*)d_in[2],
                                        (const __bf16*)d_in[3], (const __bf16*)d_in[4], (const __bf16*)d_in[6],
                                        (const __bf16*)d_in[7], (const __bf16*)d_in[8], (const __bf16*)d_in[9],
                                        (const __bf16*)d_in[10], (const __bf16*)d_in[11], (__bf16*)d_out, (char*)d_ws);
}
```

```cpp
#include <hip/hip_runtime.h>
#include <hip/hip_bf16.h>
#include <stdint.h>
#define WQT 0

typedef __attribute__((ext_vector_type(2))) __bf16 bf16x2;
typedef __attribute__((ext_vector_type(4))) __bf16 bf16x4;
typedef __attribute__((ext_vector_type(8))) __bf16 bf16x8;
typedef __attribute__((ext_vector_type(16))) __bf16 bf16x16;
typedef __attribute__((ext_vector_type(4))) short s16x4;
typedef __attribute__((ext_vector_type(2))) float f32x2;
typedef __attribute__((ext_vector_type(4))) float f32x4;
typedef __attribute__((ext_vector_type(16))) float f32x16;
typedef __attribute__((ext_vector_type(32))) float f32x32;
typedef __attribute__((ext_vector_type(2))) unsigned u32x2;
typedef __attribute__((ext_vector_type(4))) unsigned u32x4;
#define LAS __attribute__((address_space(3)))
#define GAS __attribute__((address_space(1)))
typedef LAS char* lds_ptr;
typedef GAS unsigned gu32;
typedef GAS unsigned long long gu64;
#ifdef __HIP_DEVICE_COMPILE__
#define NOPK __attribute__((target("no-packed-fp32-ops")))
#else
#define NOPK
#endif
#define DEVI __device__ __forceinline__ NOPK
#define LAMI __attribute__((always_inline)) NOPK
#define TIDX __builtin_amdgcn_workitem_id_x()
#define BIDX __builtin_amdgcn_workgroup_id_x()
#define ALL(p) (__builtin_amdgcn_ballot_w64(!(p)) == 0ull)
#define UMIN(a, b) __builtin_elementwise_min((unsigned)(a), (unsigned)(b))
#define UMAX(a, b) __builtin_elementwise_max((unsigned)(a), (unsigned)(b))
#define RLX_AGENT __ATOMIC_RELAXED, __HIP_MEMORY_SCOPE_AGENT
#define RLX_WG __ATOMIC_RELAXED, __HIP_MEMORY_SCOPE_WORKGROUP

constexpr int S_ = 2048, D_ = 576, DH = 18;

constexpr int KV_GROUP = 9984;
constexpr int WS_KV = 0;
constexpr int WS_EPOCH = 192 * 1024;
constexpr int WS_FKV = WS_EPOCH + 1024;
constexpr int WS_FU = WS_FKV + 8192;
constexpr int WS_FZ = WS_FU + 32768;
constexpr int WS_G = WS_FZ + 32768;
constexpr int WS_KT = 320 * 1024;
constexpr int WS_UT = 640 * 1024;
constexpr int WS_Z = WS_UT + D_ * S_ * 2;
constexpr int WS_O = WS_Z + D_ * S_ * 2;
constexpr int WS_ZP = WS_O + D_ * S_ * 2;
constexpr int WS_END = WS_ZP + 64 * 4 * 8192;

constexpr int RSLOT = 2 * KV_GROUP;
constexpr int L_X = 0;
constexpr int L_KVP = 73728;
constexpr int L_B = 80000;
constexpr int L_M = 162944;
constexpr int CV_COPY = 4160;
constexpr int CV_H = L_B, CV_HB = 8 * CV_COPY, CV_U = L_B + 2 * CV_HB, CV_UB = 7680;
constexpr int L_Q = L_B + 8192;
constexpr int M_DONE = 0, M_C1 = 32, M_C2 = 36, M_C3 = 40, M_C3B = 44, M_KVC = 48, M_KVR = 52, M_UR = 56, M_OR = 60, M_RDY = 64,
              M_QD = 192, M_UD = 104, M_HS = 108, M_APR = 112, M_CVR = 116, M_CE = 124, M_PAIR = 128, M_TAGR = 144,
              M_TAG = 148, M_KN = 152, M_EXD = 156, M_KVP = 160, M_KN0 = 164, M_KD = 168, M_QR = 172,
              M_N2 = 176, M_AS = 196, M_SE = 204, M_ZS = 208, M_MM = 212, M_G = 256;
constexpr int L_EX = 0;
constexpr int L_AP = CV_H + 512, L_ZT = CV_U;
#define CV_PRIO 3
#define CV_SLEEP 0
#define POLL_AT M_C2
#define POLL_N 3
#define BUBBLE 0
#define POLL_SLEEP 4
#define WO_INFL "2"
#define DMA_FIRST 0
#define ENTRY 0
#define H_PRIO 3
#define TCH 0
#define KV_DELAY 0
#define WO_EARLY 0
#define O_DELAY 20
#define Z_DELAY 12
constexpr unsigned SPIN_CAP = 1u << 18;

#define STAMP_DECL do {} while (0)
#define STAMP(I) do {} while (0)
#define STAMP_FLUSH(KID) do {} while (0)
#define RSTAMP(I) do {} while (0)

#define PSTAMP(I) do {} while (0)
#define NSTAMP(I) STAMP(I)
#define CSTAMP(I) do {} while (0)
#define HSTAMP(I) NSTAMP(I)

DEVI unsigned pk2(float a, float b) {
  bf16x2 v = {(__bf16)a, (__bf16)b};
  return __builtin_bit_cast(unsigned, v);
}
DEVI float u2f(unsigned u) { return __builtin_bit_cast(float, u); }
DEVI float bf_lo(unsigned u) { return __builtin_bit_cast(float, u << 16); }
DEVI float bf_hi(unsigned u) { return __builtin_bit_cast(float, u & 0xffff0000u); }
DEVI float dot2(unsigned u, float c) { const bf16x2 v = __builtin_bit_cast(bf16x2, u); return __builtin_amdgcn_fdot2_f32_bf16(v, v, c, false); }
DEVI float rbf(float a) { return (float)(__bf16)a; }
DEVI int swap23(int r) { return (r & ~12) | ((r & 4) << 1) | ((r & 8) >> 1); }
DEVI float xhalf(float v) {
  const unsigned u = __builtin_bit_cast(unsigned, v);
  const auto r = __builtin_amdgcn_permlane32_swap(u, u, false, false);
  return __builtin_bit_cast(float, (TIDX & 32) ? r[0] : r[1]);
}

template <int CTRL>
DEVI float dpp_max(float v) {
  const int u = __builtin_bit_cast(int, v);
  return __builtin_fmaxf(v, __builtin_bit_cast(float, __builtin_amdgcn_update_dpp(u, u, CTRL, 0xf, 0xf, false)));
}
DEVI float max16(float v) {
  return dpp_max<0x140>(dpp_max<0x141>(dpp_max<0x4e>(dpp_max<0xb1>(v))));
}
template <int CTRL>
DEVI float dpp_add(float v) {
  const int u = __builtin_bit_cast(int, v);
  return v + __builtin_bit_cast(float, __builtin_amdgcn_update_dpp(u, u, CTRL, 0xf, 0xf, false));
}
DEVI float sum16(float v) { return dpp_add<0x140>(dpp_add<0x141>(dpp_add<0x4e>(dpp_add<0xb1>(v)))); }
DEVI float wave_max(float v) {
  v = max16(v);
  const unsigned u = __builtin_bit_cast(unsigned, v);
  return __builtin_fmaxf(__builtin_fmaxf(u2f(__builtin_amdgcn_readlane(u, 0)), u2f(__builtin_amdgcn_readlane(u, 16))),
               __builtin_fmaxf(u2f(__builtin_amdgcn_readlane(u, 32)), u2f(__builtin_amdgcn_readlane(u, 48))));
}
DEVI void glds(const void* sbase, unsigned voff, unsigned lds_dst) {
  asm volatile("s_mov_b32 m0, %2\n\ts_nop 0\n\tglobal_load_lds_dwordx4 %0, %1" :: "v"(voff), "s"(sbase), "s"(lds_dst) : "memory");
}
DEVI void glds_sc1(const void* sbase, unsigned voff, unsigned lds_dst) {
  asm volatile("s_mov_b32 m0, %2\n\ts_nop 0\n\tglobal_load_lds_dwordx4 %0, %1 sc1" :: "v"(voff), "s"(sbase), "s"(lds_dst) : "memory");
}
#define GLDS4(SC, sbase_, v0_, v3_, dst_)                                                                                  \
  asm volatile("s_mov_b32 m0, %3\n\ts_nop 0\n\tglobal_load_lds_dwordx4 %0, %2" SC "\n\tglobal_load_lds_dwordx4 %0, %2 offset:1024" SC   \
               "\n\tglobal_load_lds_dwordx4 %0, %2 offset:2048" SC "\n\tglobal_load_lds_dwordx4 %1, %2 offset:3072" SC           \
               :: "v"(v0_), "v"(v3_), "s"(sbase_), "s"(dst_) : "memory")
#define GLDSX(src_, dst_)                                                                                                  \
  asm volatile("s_mov_b32 m0, %5\n\ts_nop 0\n\tglobal_load_lds_dwordx4 %0, %4\n\tglobal_load_lds_dwordx4 %1, %4 offset:1024\n\t"   \
               "global_load_lds_dwordx4 %2, %4 offset:2048\n\tglobal_load_lds_dwordx4 %3, %4 offset:3072"                 \
               :: "v"(vxe), "v"(vxo), "v"(vxe + 16384), "v"(vxo + 16384), "s"(src_), "s"(dst_) : "memory")
#define GLDSX2(src_, dst_)                                                                                                 \
  asm volatile("s_mov_b32 m0, %3\n\ts_nop 0\n\tglobal_load_lds_dwordx4 %0, %2\n\tglobal_load_lds_dwordx4 %1, %2 offset:1024"       \
               :: "v"(vxe), "v"(vxo), "s"(src_), "s"(dst_) : "memory")
#define GLDS3(STEP1, STEP2, v0_, v1_, v2_, src_, dst_)                                                                     \
  asm volatile("s_mov_b32 m0, %4\n\ts_nop 0\n\tglobal_load_lds_dwordx4 %0, %3\n\tglobal_load_lds_dwordx4 %1, %3 offset:" STEP1  \
               "\n\tglobal_load_lds_dwordx4 %2, %3 offset:" STEP2                                                        \
               :: "v"(v0_), "v"(v1_), "v"(v2_), "s"(src_), "s"(dst_) : "memory")
#define TOUCH(src_, v_) asm volatile("s_mov_b32 m0, %2\n\ts_nop 0\n\tglobal_load_lds_dword %0, %1 sc1" :: "v"(v_), "s"(src_), "s"(sbase + L_M + 512) : "memory")
#define VM_WAIT(N) asm volatile("s_waitcnt vmcnt(" #N ")" ::: "memory")
#define BAR() asm volatile("s_waitcnt lgkmcnt(0)\n\ts_barrier" ::: "memory")

DEVI bf16x8 tr_frag(lds_ptr p0, lds_ptr p1) {
  bf16x4 a = __builtin_amdgcn_ds_read_tr16_b64_v4bf16((LAS bf16x4*)p0);
  bf16x4 b = __builtin_amdgcn_ds_read_tr16_b64_v4bf16((LAS bf16x4*)p1);
  return __builtin_shufflevector(a, b, 0, 1, 2, 3, 4, 5, 6, 7);
}
DEVI bf16x8 lds_frag(lds_ptr p) { return *(LAS bf16x8*)p; }
DEVI void lds_signal(lds_ptr p, unsigned n = 1u) {
  asm volatile("s_waitcnt lgkmcnt(0)\n\ts_mov_b64 exec, 1\n\tds_add_u32 %0, %1\n\ts_mov_b64 exec, -1" :: "v"(p), "v"(n) : "memory");
}
DEVI unsigned lds_ticket(lds_ptr p) {
  unsigned old;
  asm volatile("s_waitcnt lgkmcnt(0)\n\ts_mov_b64 exec, 1\n\tds_add_rtn_u32 %0, %1, %2\n\ts_mov_b64 exec, -1\n\ts_waitcnt lgkmcnt(0)"
               : "=&v"(old) : "v"(p), "v"(1u) : "memory");
  return __builtin_amdgcn_readfirstlane(old);
}
template <int SLEEP = 0>
DEVI void lds_wait(lds_ptr p, unsigned n) {
  unsigned v, t;
  asm volatile("1:\n\tds_read_b32 %0, %2\n\ts_waitcnt lgkmcnt(0)\n\tv_readfirstlane_b32 %1, %0\n\ts_nop 0\n\ts_cmp_ge_u32 %1, %3\n\t"
               "s_cbranch_scc1 2f\n\ts_sleep %4\n\ts_branch 1b\n2:"
               : "=&v"(v), "=&s"(t) : "v"(p), "s"(n), "i"(SLEEP) : "scc", "memory");
}
DEVI unsigned lds_peek(lds_ptr p) {
  unsigned v;
  asm volatile("ds_read_b32 %0, %1\n\ts_waitcnt lgkmcnt(0)" : "=&v"(v) : "v"(p) : "memory");
  return __builtin_amdgcn_readfirstlane(v);
}
#define LD_SC1_16(off) __builtin_bit_cast(u32x4, __builtin_amdgcn_raw_buffer_load_b128(rs, (off), 0, 16))
#define ST_SC1_16(v, off) __builtin_amdgcn_raw_buffer_store_b128((v), rs, (off), 0, 16)
#define ST_SC1_8(v, off) __builtin_amdgcn_raw_buffer_store_b64((v), rs, (off), 0, 16)
#define ST_SC1_4(v, off) __builtin_amdgcn_raw_buffer_store_b32((v), rs, (off), 0, 16)

DEVI f32x16 lo16(f32x32 v) { return __builtin_shufflevector(v, v, 0, 1, 2, 3, 4, 5, 6, 7, 8, 9, 10, 11, 12, 13, 14, 15); }
DEVI f32x16 hi16(f32x32 v) { return __builtin_shufflevector(v, v, 16, 17, 18, 19, 20, 21, 22, 23, 24, 25, 26, 27, 28, 29, 30, 31); }
DEVI bf16x16 raw_lo(f32x16 v) { return __builtin_bit_cast(bf16x16, __builtin_shufflevector(v, v, 0, 1, 2, 3, 4, 5, 6, 7)); }
DEVI bf16x16 raw_hi(f32x16 v) { return __builtin_bit_cast(bf16x16, __builtin_shufflevector(v, v, 8, 9, 10, 11, 12, 13, 14, 15)); }
constexpr int SPIDX = (int)0xddddddddu;
#define MFMA32(a, b, c) __builtin_amdgcn_mfma_f32_32x32x16_bf16((a), (b), (c), 0, 0, 0)
#define MFMA16(a, b, c) __builtin_amdgcn_mfma_f32_16x16x32_bf16((a), (b), (c), 0, 0, 0)

DEVI void online_step(f32x16 s, float& m, f32x16& o, bf16x8 va, bf16x8 vb) {
  float t = s[0];
#pragma unroll
  for (int i = 1; i < 16; ++i) t = __builtin_fmaxf(t, s[i]);
  t = __builtin_fmaxf(t, xhalf(t));
  const float mn = __builtin_fmaxf(m, t);
  const float al = __builtin_amdgcn_exp2f(m - mn);
  m = mn;
#pragma unroll
  for (int i = 0; i < 16; ++i) { o[i] *= al; s[i] = __builtin_amdgcn_exp2f(s[i] - mn); }
  o = __builtin_amdgcn_smfmac_f32_32x32x32_bf16(va, raw_lo(s), o, SPIDX, 0, 0);
  o = __builtin_amdgcn_smfmac_f32_32x32x32_bf16(vb, raw_hi(s), o, SPIDX, 0, 0);
}

#define UNIT_LOAD(CP, TG)                                                                                        \
  const int uc0 = 72 * hg + 2 * (CP), ut0 = 64 * qt + 8 * (TG);                                                  \
  const u32x4 usw = *(const u32x4*)(shk + uc0 * 4);                                                              \
  unsigned ukr[11], uvr[8];                                                                                      \
  {                                                                                                              \
    const unsigned bo = (ut0 * D_ + uc0) * 2, bo3 = ut0 ? bo - 3 * 1152 : bo;                                    \
    _Pragma("unroll") for (int i = 0; i < 3; ++i) ukr[i] = *(const unsigned*)((const char*)bk + bo3 + i * 1152); \
    _Pragma("unroll") for (int i = 0; i < 8; ++i) {                                                              \
      ukr[3 + i] = *(const unsigned*)((const char*)bk + bo + i * 1152);                                          \
      uvr[i] = *(const unsigned*)((const char*)bv + bo + i * 1152);                                              \
    }                                                                                                            \
  }
#define UNIT_DO()                                                                                                \
  u32x4 o0, o1;                                                                                                  \
  {                                                                                                              \
    const f32x2 w3 = {bf_hi(usw[1]), bf_hi(usw[3])}, w2 = {bf_lo(usw[1]), bf_lo(usw[3])};                        \
    const f32x2 w1 = {bf_hi(usw[0]), bf_hi(usw[2])}, w0 = {bf_lo(usw[0]), bf_lo(usw[2])};                        \
    f32x2 kf[11], r[8];                                                                                          \
    _Pragma("unroll") for (int i = 0; i < 11; ++i) {                                                             \
      const unsigned kk = (i < 3 && !ut0) ? 0u : ukr[i];                                                         \
      kf[i] = (f32x2){bf_lo(kk), bf_hi(kk)};                                                                     \
    }                                                                                                            \
    _Pragma("unroll") for (int i = 0; i < 8; ++i)                                                                \
      r[i] = __builtin_elementwise_fma(kf[i + 3], w0, __builtin_elementwise_fma(kf[i + 2], w1,                   \
             __builtin_elementwise_fma(kf[i + 1], w2, kf[i] * w3))) * (f32x2){bf_lo(uvr[i]), bf_hi(uvr[i])};     \
    _Pragma("unroll") for (int i = 0; i < 4; ++i) {                                                              \
      o0[i] = pk2(r[7 - 2 * i][0], r[6 - 2 * i][0]);                                                             \
      o1[i] = pk2(r[7 - 2 * i][1], r[6 - 2 * i][1]);                                                             \
    }                                                                                                            \
  }

#define CV_LD(F, D) _Pragma("unroll") for (int s = 0; s < 2; ++s) {                                              \
    F[4 * s] = lds_frag(ab + 32 * (2 * (D) + 1 - s)); F[4 * s + 1] = lds_frag(bb + 80 * (7 - (D)) + 32 * s);     \
    F[4 * s + 2] = lds_frag(bb + 80 * (39 - (D)) + 32 * s); F[4 * s + 3] = lds_frag(ab + 32 * (2 * (D) + 65 - s)); } \
  __builtin_amdgcn_sched_barrier(0)
#define CV_MM(F) _Pragma("unroll") for (int s = 0; s < 2; ++s) {                                                 \
    y0 = MFMA32(F[4 * s], F[4 * s + 1], y0); y1 = MFMA32(F[4 * s], F[4 * s + 2], y1);                            \
    y1 = MFMA32(F[4 * s + 3], F[4 * s + 1], y1); }                                                               \
  __builtin_amdgcn_sched_barrier(0)
#define SB0 __builtin_amdgcn_sched_barrier(0)
#define CV_STEP(F, N, D) _Pragma("unroll") for (int s = 0; s < 2; ++s) {                                         \
    y0 = MFMA32(F[4 * s], F[4 * s + 1], y0); SB0;                                                                \
    N[4 * s] = lds_frag(ab + 32 * (2 * (D) + 1 - s)); N[4 * s + 1] = lds_frag(bb + 80 * (7 - (D)) + 32 * s); SB0; \
    y1 = MFMA32(F[4 * s], F[4 * s + 2], y1); SB0;                                                                \
    N[4 * s + 2] = lds_frag(bb + 80 * (39 - (D)) + 32 * s); SB0;                                                 \
    y1 = MFMA32(F[4 * s + 3], F[4 * s + 1], y1); SB0;                                                            \
    N[4 * s + 3] = lds_frag(ab + 32 * (2 * (D) + 65 - s)); SB0; }
#define CV_PUT(P) _Pragma("unroll") for (int g = 0; g < 4; ++g) {                                                \
    *(LAS f32x4*)((P) + g * 1024) = (f32x4){y0[4 * g], y0[4 * g + 1], y0[4 * g + 2], y0[4 * g + 3]};             \
    *(LAS f32x4*)((P) + 4096 + g * 1024) = (f32x4){y1[4 * g], y1[4 * g + 1], y1[4 * g + 2], y1[4 * g + 3]}; }
DEVI f32x4 cv_sum(lds_ptr p, lds_ptr u, float dv) {
  const f32x4 s = (*(LAS f32x4*)(p) + *(LAS f32x4*)(p + 8192)) + (*(LAS f32x4*)(p + 16384) + *(LAS f32x4*)(p + 24576));
  const u32x2 uu = *(LAS u32x2*)u;
  return (f32x4){__builtin_fmaf(bf_hi(uu[1]), dv, s[0]), __builtin_fmaf(bf_lo(uu[1]), dv, s[1]), __builtin_fmaf(bf_hi(uu[0]), dv, s[2]), __builtin_fmaf(bf_lo(uu[0]), dv, s[3])};
}

__global__ void __launch_bounds__(768, 3) NOPK
fused_kernel(const __bf16* __restrict__ x, const __bf16* __restrict__ wq, const __bf16* __restrict__ wk,
             const __bf16* __restrict__ wv, const __bf16* __restrict__ wo, const __bf16* __restrict__ bk,
             const __bf16* __restrict__ bv, const __bf16* __restrict__ shk, const __bf16* __restrict__ ssmk,
             const __bf16* __restrict__ ssmd, const __bf16* __restrict__ gw, __bf16* __restrict__ out, char* ws) {
  __shared__ __attribute__((aligned(16))) char smem[160 * 1024];
  STAMP_DECL; STAMP(0);
  const int tid = TIDX, lane = tid & 63;
  const int w = __builtin_amdgcn_readfirstlane(tid >> 6);
  const int bid = BIDX, jj = bid >> 3;
  const unsigned sbase = (unsigned)(size_t)smem;
  if (w == 0) {
    unsigned long long pc0;
    unsigned len;
    asm volatile("s_getpc_b64 %0\n\tfk_pc:\n\ts_mov_b32 %1, fk_end-fk_pc" : "=s"(pc0), "=s"(len));
    TOUCH(pc0, UMIN((unsigned)(lane * 8 + (jj & 7)) * 128u, len));
  }
  const int qt = (bid & 7) * 4 + (jj & 3), hg = jj >> 2;
  lds_ptr sm = (lds_ptr)smem;
  lds_ptr mi = sm + L_M;
  const int h = lane >> 5, l31 = lane & 31, l15 = lane & 15, g4 = lane >> 4;
  const int tq = (lane & 15) >> 2, tp = lane & 3;
  const auto rs = __builtin_amdgcn_make_buffer_rsrc(ws, 0, WS_END, 0x00020000);
  GAS char* gws = (GAS char*)ws;
  const int rot = (hg + qt) % 3;
  const int s0 = bid & 7;
  const int tq4 = hg >> 1, sel = hg & 1;
  const char* xt = (const char*)x + (size_t)qt * 73728;
  const char* wqs = (const char*)wq + 144 * hg;
  const unsigned vxe = (lane >> 3) * 1152 + (((lane & 7) ^ g4) << 4), vxo = 8192 + (lane >> 3) * 1152 + (((lane & 7) ^ (4 + g4)) << 4);
  const int otok = tid / 9, opc = tid - 9 * otok;

  u32x4 zv, xv;

  if (w == 4) *(LAS int*)(mi + lane * 4) = (lane == M_RDY / 4 + 2 || lane == M_RDY / 4 + 3) ? -8 : (lane == 0 ? -1 : 0);
  auto zscatter = [&]() LAMI {
    lds_ptr zp = sm + L_ZT + ((tid & 7) * 72 + (tid >> 3)) * 2;
#pragma unroll
    for (int e = 0; e < 8; ++e)
      *(LAS unsigned short*)(zp + e * 1152) = (unsigned short)((e & 1) ? zv[e >> 1] >> 16 : zv[e >> 1]);
    lds_signal(mi + M_ZS);
  };
  if (w >= 8 && w < 11) {
    const int j = w - 8;
    const int th = (j + rot) % 3;
    const unsigned stg = L_B + 2 * 27648 + j * 9216;
#pragma unroll
    for (int c = 0; c < 3; ++c) GLDSX2(xt + tq4 * 18432 + (3 * th + c) * 128, sbase + L_X + (3 * j + c) * 8192 + tq4 * 2048);
    {
      const char* wsrc = (const char*)(sel ? wv : wk) + th * 6912 - 2048;
      unsigned vo[3];
#pragma unroll
      for (int i = 0; i < 3; ++i) {
        const int f = 64 * i + lane, row = f / 3, pc = f - 3 * row;
        vo[i] = row * 36 + (pc == 2 ? 20 : 16 * pc) + 2048 - 1024 * i;
      }
#pragma unroll
      for (int p = 0; p < 3; ++p) GLDS3("1024", "2048", vo[0], vo[1], vo[2], wsrc + p * 2304, sbase + stg + p * 3072);
    }
    glds(gw + 192 * th, UMIN(lane, 23) * 16, sbase + L_KVP + j * 2048);
  }
  unsigned epoch = 0;
  if (w == 11) epoch = __hip_atomic_load((gu32*)(gws + WS_EPOCH), RLX_AGENT);
  PSTAMP(5);
  BAR();
  PSTAMP(6);
  if (w >= 5 && w < 8) {
    const int wi = w - 5;
    unsigned vw[3];
#pragma unroll
    for (int m = 0; m < 3; ++m) {
      const int f = 64 * (3 * wi + m) + lane, row = f / 9;
      vw[m] = row * 1152 + (f - 9 * row) * 16 - 1024 * m;
    }
    const char* xs = xt + (1 - (tq4 >> 1)) * 36864 + wi * 128;
    const char* xs2 = xt + (tq4 ^ 1) * 18432 + wi * 128;
    const unsigned xd = sbase + L_X + wi * 8192 + (1 - (tq4 >> 1)) * 4096, xd2 = sbase + L_X + wi * 8192 + (tq4 ^ 1) * 2048;
    const unsigned wd = sbase + L_B + wi * 3072;
    const int t1 = (rot + 1) % 3, t2 = (rot + 2) % 3;
    GLDSX(xs + rot * 384, xd);
    GLDSX2(xs2 + rot * 384, xd2);
#pragma unroll
    for (int per = 0; per < 3; ++per) GLDS3("1024", "2048", vw[0], vw[1], vw[2], wqs + (192 * rot + 64 * per) * 1152, wd + per * 9216);
    GLDSX(xs + t1 * 384, xd + 24576);
    GLDSX2(xs2 + t1 * 384, xd2 + 24576);
    GLDS3("1024", "2048", vw[0], vw[1], vw[2], wqs + 192 * t1 * 1152, wd + 27648);
    VM_WAIT(9);
    lds_signal(mi + M_C1);
#pragma unroll
    for (int per = 1; per < 3; ++per) GLDS3("1024", "2048", vw[0], vw[1], vw[2], wqs + (192 * t1 + 64 * per) * 1152, wd + 27648 + per * 9216);
    GLDSX(xs + t2 * 384, xd + 49152);
    GLDSX2(xs2 + t2 * 384, xd2 + 49152);
    lds_wait(mi + M_KVC, 3);
    GLDS3("1024", "2048", vw[0], vw[1], vw[2], wqs + 192 * t2 * 1152, wd + 55296);
    VM_WAIT(9);
    lds_signal(mi + M_C2);
#pragma unroll
    for (int per = 1; per < 3; ++per) GLDS3("1024", "2048", vw[0], vw[1], vw[2], wqs + (192 * t2 + 64 * per) * 1152, wd + 55296 + per * 9216);
    STAMP(1);
    VM_WAIT(0);
    lds_signal(mi + M_C3);
    lds_signal(mi + M_SE);
  }

  if (w >= 8) {
    const int j = w - 8, ht = tid - 512;
    __builtin_amdgcn_s_setprio(3);
    if (j < 3) {
      const unsigned stg = L_B + 2 * 27648 + j * 9216;
      lds_ptr wb = sm + stg + (8 * g4 + tq) * 48 + tp * 8;
      lds_ptr wc = sm + stg + (8 * g4 + tq) * 48 + 32 + (tp & 1) * 8;
      lds_ptr xb = sm + L_X + 3 * j * 8192 + (16 * tq4 + l15) * 128;
      VM_WAIT(0);
      STAMP(1);
      bf16x8 fa[6], fc[6], fb[6], fg[6];
#pragma unroll
      for (int k = 0; k < 6; ++k) {
        fg[k] = lds_frag(sm + L_KVP + j * 2048 + k * 64 + g4 * 16);
        fb[k] = lds_frag(xb + (k >> 1) * 8192 + (((4 * (k & 1) + g4) ^ (l15 >> 1)) << 4));
        fa[k] = tr_frag(wb + k * 1536, wb + k * 1536 + 192);
        fc[k] = tr_frag(wc + k * 1536, wc + k * 1536 + 192);
      }
      f32x4 acc = {0}, acr = {0};
      if (sel) {
#pragma unroll
        for (int k = 0; k < 6; ++k) { acc = MFMA16(fb[k], fa[k], acc); acr = MFMA16(fb[k], fc[k], acr); }
      } else {
#pragma unroll
        for (int k = 0; k < 6; ++k) { acc = MFMA16(fa[k], fb[k], acc); acr = MFMA16(l15 == 8 ? fg[k] : fc[k], fb[k], acr); }
      }
      *(LAS f32x4*)(sm + L_KVP + j * 2048 + lane * 16) = acc;
      *(LAS f32x4*)(sm + L_KVP + j * 2048 + 1024 + lane * 16) = acr;
      lds_signal(mi + M_KVC);
      lds_signal(mi + M_C1);
      STAMP(2);
      lds_wait<1>(mi + M_KD, 1);
    } else {
      const unsigned tg = __builtin_amdgcn_readfirstlane(epoch) + 1u;
      if (lane == 0) *(LAS unsigned*)(mi + M_TAG) = tg;
      const int T = 2 * qt + (tq4 >> 1), mt = tq4 & 1, i4 = T & 3, gb = (T >> 2) * KV_GROUP, rp = swap23(16 * mt + l15);
      int sa, sr;
      bool st;
      if (sel) {
        sa = gb + 4096 + i4 * 1216 + mt * 608 + (l15 + 18 * (g4 & 1)) * 16 + (g4 >> 1) * 8;
        sr = gb + 4096 + i4 * 1216 + mt * 608 + (l15 < 8 ? 10 + l15 + 18 * (g4 & 1) : 36 + (g4 & 1)) * 16 + (g4 >> 1) * 8;
        st = l15 >= 6 && l15 <= 8;
      } else {
        sa = gb + i4 * 1024 + (rp + 32 * (g4 & 1)) * 16 + (g4 >> 1) * 8;
        sr = gb + 8960 + i4 * 256 + rp * 8;
        st = g4 == 1;
      }
      lds_wait(mi + M_KVC, 3);
      lds_ptr pp = sm + L_KVP + lane * 16;
      const float ks = sel ? 1.f : 0.23570226039551584f * 1.4426950408889634f;
      const f32x4 va = (*(LAS f32x4*)(pp) + *(LAS f32x4*)(pp + 2048) + *(LAS f32x4*)(pp + 4096)) * ks;
      const f32x4 vs = *(LAS f32x4*)(pp + 1024) + *(LAS f32x4*)(pp + 3072) + *(LAS f32x4*)(pp + 5120), vr = vs * ks;
      const u32x2 a = {pk2(va[0], va[1]), pk2(va[2], va[3])};
      u32x2 r = {pk2(vr[0], vr[1]), pk2(vr[2], vr[3])};
      if (!sel) r = (u32x2){r[1], 0x3f803f80u};
      else if (l15 == 8) r[0] = r[1] = (g4 & 1) ? 0u : 0x3f803f80u;
      ST_SC1_16(((u32x4){a[0], tg, a[1], tg}), WS_KT + 2 * sa);
      if (st) ST_SC1_16(((u32x4){r[0], tg, r[1], tg}), WS_KT + 2 * sr);
      ST_SC1_8(a, WS_KV + sa);
      if (st) ST_SC1_8(r, WS_KV + sr);
      STAMP(1);
      lds_signal(mi + M_KD);
      if (!sel && g4 == 2) {
        const float g = rbf(__builtin_amdgcn_rcpf(1.f + __builtin_amdgcn_exp2f(-1.4426950408889634f * rbf(vs[0]))));
        ST_SC1_4(__builtin_bit_cast(unsigned, g), WS_G + (64 * qt + 16 * tq4 + l15) * 4);
      }
      float n2 = dot2(a[1], dot2(a[0], st ? dot2(r[0], 0.f) : 0.f));
      n2 += xhalf(n2);
      n2 += u2f(__builtin_amdgcn_ds_swizzle(__builtin_bit_cast(int, n2), 0x401f));
      n2 = sel ? 0.f : max16(n2);
      VM_WAIT(0);
      if (lane == 0)
        __hip_atomic_store((gu64*)(gws + WS_FKV + T * 128 + (hg & 3) * 8), ((unsigned long long)tg << 32) | __builtin_bit_cast(unsigned, n2), RLX_AGENT);
      STAMP(2);
    }
    const unsigned tag = *(LAS unsigned*)(mi + M_TAG);
    auto kv_tagged = [&](int st) LAMI {
      u32x4 r[10];
      const int o = WS_KT + ((s0 + st) & 7) * (2 * RSLOT) + lane * 16;
#pragma unroll 1
      for (unsigned spins = 0; spins < SPIN_CAP; ++spins) {
        unsigned bad = 0;
#pragma unroll
        for (int i = 0; i < 10; ++i) r[i] = LD_SC1_16(o + (i == 9 && j == 3 ? 38 : 4 * i + j) * 1024);
#pragma unroll
        for (int i = 0; i < 10; ++i) bad |= (r[i][1] ^ tag) | (r[i][3] ^ tag);
        asm volatile("" ::: "memory");
        if (ALL(bad == 0)) break;
        __builtin_amdgcn_s_sleep(POLL_SLEEP);
      }
      if (st == 0) {
        if (j == 0) {
          const float kn = dot2(r[0][2], dot2(r[0][0], 0.f));
          *(LAS float*)(mi + M_KN0) = wave_max(dpp_add<0xb1>(kn));
        }
        lds_wait(mi + M_C3B, 5);
      }
#pragma unroll
      for (int i = 0; i < 10; ++i)
        *(LAS u32x2*)(sm + st * RSLOT + (i == 9 && j == 3 ? 38 : 4 * i + j) * 512 + lane * 8) = (u32x2){r[i][0], r[i][2]};
    };
    auto kv_dma = [&](int st) LAMI {
      const char* src = ws + WS_KV + ((s0 + st) & 7) * RSLOT + j * 4992;
      const unsigned dst = sbase + st * RSLOT + j * 4992;
      GLDS4(" sc1", src, lane * 16, lane * 16, dst);
      if (lane < 56) glds_sc1(src + 4096, lane * 16, dst + 4096);
    };
    const unsigned vh3 = (lane == 63 ? 62 : lane) * 16;
    auto hload = [&](int cl, int b) LAMI {
      const char* hsrc = (const char*)(ssmk + (size_t)(72 * hg + cl) * S_) + 4 * j + 2;
      const unsigned dst = sbase + CV_H + b * CV_HB + 2 * j * CV_COPY + 64;
      GLDS4("", hsrc, lane * 16, vh3, dst);
      GLDS4("", hsrc + 2, lane * 16, vh3, dst + CV_COPY);
    };
    auto uload = [&](int cl) LAMI {
      const int tgq = ht & 7;
      const int o = cl < 64 ? (tgq >> 1) * 2048 + (cl & 1) * 1024 + ((tgq & 1) * 32 + (cl >> 1)) * 16
                            : 8192 + (cl & 1) * 512 + (tgq * 4 + ((cl - 64) >> 1)) * 16;
      return LD_SC1_16(WS_UT + ((ht >> 3) * 8 + hg) * 9216 + o);
    };
    const int ce = 64 + (qt >> 2);
    const unsigned short h8a = *(const unsigned short*)(ssmk + (size_t)(72 * hg + 2 * qt + (j >> 1)) * S_ + (lane & 7));
    const unsigned short h8e = *(const unsigned short*)(ssmk + (size_t)(72 * hg + ce) * S_ + (lane & 7));
    const float dve = (qt & 3) ? 0.f : (float)ssmd[72 * hg + ce];
    {
      lds_wait<1>(mi + POLL_AT, POLL_N);
      kv_tagged(0);
      lds_signal(mi + M_QD);
      STAMP(3);
      kv_tagged(1);
      lds_signal(mi + M_RDY + 4, 2);
      if (j == 3) {
        float kn = 0.f;
#pragma unroll 1
        for (unsigned spins = 0; spins < SPIN_CAP; ++spins) {
          const u32x4 v0 = LD_SC1_16(WS_FKV + lane * 128), v1 = LD_SC1_16(WS_FKV + lane * 128 + 16);
          const bool ok = v0[1] == tag && v0[3] == tag && v1[1] == tag && v1[3] == tag;
          kn = __builtin_fmaxf(u2f(v0[0]), u2f(v1[0]));
          asm volatile("" ::: "memory");
          if (ALL(ok)) break;
          __builtin_amdgcn_s_sleep(POLL_SLEEP);
        }
        lds_signal(mi + M_QR);
        lds_signal(mi + M_DONE);
        kn = wave_max(kn);
        if (lane == 0) *(LAS float*)(mi + M_KN) = kn;
      }
      lds_wait<1>(mi + M_QR, 9);
      kv_dma(2);
      kv_dma(3);
      VM_WAIT(0);
      lds_signal(mi + M_RDY + 8, 2);
      lds_signal(mi + M_RDY + 12, 2);
      UNIT_LOAD(l31, 2 * j + h);
      if (j == 1) {
        const int c0 = 72 * hg + 64 + 2 * (lane & 3), t0 = 64 * qt + 4 * (lane >> 2);
        const u32x4 sw = *(const u32x4*)(shk + c0 * 4);
        const unsigned bo = (t0 * D_ + c0) * 2, bo3 = t0 ? bo - 3 * 1152 : bo;
        unsigned kk[7], vv[4];
#pragma unroll
        for (int i = 0; i < 7; ++i) kk[i] = *(const unsigned*)((const char*)bk + (i < 3 ? bo3 + i * 1152 : bo + (i - 3) * 1152));
#pragma unroll
        for (int i = 0; i < 4; ++i) vv[i] = *(const unsigned*)((const char*)bv + bo + i * 1152);
        if (!t0) kk[0] = kk[1] = kk[2] = 0u;
        float ra[4], rb[4];
#pragma unroll
        for (int i = 0; i < 4; ++i) {
          ra[i] = __builtin_fmaf(bf_lo(kk[i + 3]), bf_lo(sw[0]), __builtin_fmaf(bf_lo(kk[i + 2]), bf_hi(sw[0]),
                  __builtin_fmaf(bf_lo(kk[i + 1]), bf_lo(sw[1]), bf_lo(kk[i]) * bf_hi(sw[1])))) * bf_lo(vv[i]);
          rb[i] = __builtin_fmaf(bf_hi(kk[i + 3]), bf_lo(sw[2]), __builtin_fmaf(bf_hi(kk[i + 2]), bf_hi(sw[2]),
                  __builtin_fmaf(bf_hi(kk[i + 1]), bf_lo(sw[3]), bf_hi(kk[i]) * bf_hi(sw[3])))) * bf_hi(vv[i]);
        }
        const int uo = WS_UT + (qt * 8 + hg) * 9216 + 8192 + ((lane >> 3) * 4 + (lane & 3)) * 16 + (1 - ((lane >> 2) & 1)) * 8;
        ST_SC1_8(((u32x2){pk2(ra[3], ra[2]), pk2(ra[1], ra[0])}), uo);
        ST_SC1_8(((u32x2){pk2(rb[3], rb[2]), pk2(rb[1], rb[0])}), uo + 512);
      }
      VM_WAIT(0);
      if (j < 3) TOUCH((const char*)wo + (jj * 162 + j * 54) * 128, UMIN(lane, 53) * 128u);
      else TOUCH(ssmk + (size_t)(72 * hg + 2 * qt) * S_, lane * 128u);
      UNIT_DO();
      ST_SC1_16(o0, WS_UT + (qt * 8 + hg) * 9216 + j * 2048 + lane * 16);
      ST_SC1_16(o1, WS_UT + (qt * 8 + hg) * 9216 + j * 2048 + 1024 + lane * 16);
      {
        const u32x4 ones = {~0u, ~0u, ~0u, ~0u};
        const int ob = WS_O + 2 * qt * 36 * 1024 + (4 * hg + j) * 1024 + lane * 16;
        ST_SC1_16(ones, ob);
        ST_SC1_16(ones, ob + 36 * 1024);
        if (j == 0) ST_SC1_16(ones, WS_O + (2 * qt + h) * 36 * 1024 + 32768 + hg * 512 + l31 * 16);
        ST_SC1_16(ones, WS_Z + (72 * hg + 2 * qt) * 4096 + ht * 16);
        ST_SC1_16(ones, WS_Z + (72 * hg + 2 * qt + 1) * 4096 + ht * 16);
      }
      STAMP(4);
      VM_WAIT(0);
      if (lds_ticket(mi + M_UD) == 3u && lane == 0) __hip_atomic_store((gu32*)(gws + WS_FU) + (hg * 32 + qt) * 32, tag, RLX_AGENT);
    }
    auto cvprep = [&](int b, unsigned short h8) LAMI {
      *(LAS u32x4*)(sm + CV_U + (ht >> 7) * CV_UB + ((ht & 127) >> 2) * 80 + (ht & 3) * 16) = (u32x4){0, 0, 0, 0};
      lds_ptr hz = sm + CV_H + b * CV_HB + (lane >> 3) * CV_COPY;
      *(LAS u32x2*)(hz + (lane & 7) * 8) = (u32x2){0, 0};
      if ((lane & 7) <= (lane >> 3)) *(LAS unsigned short*)(hz + 48 + (7 - (lane >> 3) + (lane & 7)) * 2) = h8;
    };
    unsigned hs = 0;
#define HSYNC() do { lds_signal(mi + M_HS); hs += 4; lds_wait<2>(mi + M_HS, hs); } while (0)
    if (lane < 54) {
      const unsigned vo = (lane / 9) * 1152 + (lane % 9) * 16;
#pragma unroll 1
      for (int p = 24 * j; p < 24 * j + 24; p += 4)
        asm volatile("s_mov_b32 m0, %5\n\ts_nop 0\n\tglobal_load_lds_dwordx4 %0, %4\n\tglobal_load_lds_dwordx4 %1, %4 offset:864\n\t"
                     "global_load_lds_dwordx4 %2, %4 offset:1728\n\tglobal_load_lds_dwordx4 %3, %4 offset:2592\n\ts_waitcnt vmcnt(" WO_INFL ")"
                     :: "v"(vo), "v"(vo + 6048), "v"(vo + 12096), "v"(vo + 18144), "s"((const char*)wo + 144 * hg + p * 6912),
                        "s"(sbase + L_B + p * 864) : "memory");
    }
    VM_WAIT(0);
    HSYNC();
    bf16x8 wf[18], we[5];
    {
      const int cb = (32 * (j & 1) + 16 * (g4 & 1) + 4 * tp) * 2;
      lds_ptr wm = sm + L_B + (DH * (j >> 1) + 4 * h + tq) * 144 + cb;
      lds_ptr wr = sm + L_B + (DH * (8 * (j >> 1) + 4 * h + (tq >> 1)) + 16 + (tq & 1)) * 144 + cb;
      asm volatile("" : "+v"(wm), "+v"(wr));
#pragma unroll
      for (int k = 0; k < 12; ++k) wf[k] = tr_frag(wm + k * 5184, wm + k * 5184 + 1152);
      wm += 12 * 5184;
      asm volatile("" : "+v"(wm));
#pragma unroll
      for (int k = 0; k < 4; ++k) wf[12 + k] = tr_frag(wm + k * 5184, wm + k * 5184 + 1152);
#pragma unroll
      for (int k = 0; k < 2; ++k) wf[16 + k] = tr_frag(wr + k * 41472, wr + k * 41472 + 5184);
      lds_ptr em = sm + L_B + (DH * (8 * j + (g4 >> 1)) + 4 * (g4 & 1) + tq) * 144 + (64 + 4 * (tp & 1)) * 2;
      lds_ptr er = sm + L_B + (DH * (16 * (j & 1) + 4 * g4 + (tq >> 1)) + 16 + (tq & 1)) * 144 + (64 + 4 * (tp & 1)) * 2;
      asm volatile("" : "+v"(em), "+v"(er));
#pragma unroll
      for (int k = 0; k < 4; ++k) we[k] = tr_frag(em + k * 5184, em + k * 5184 + 1152);
      we[4] = tr_frag(er, er + 5184);
    }
    HSYNC();
    HSTAMP(7);
    hload(ce, 0);
    cvprep(0, h8e);
    if (j == 3) {
#pragma unroll 1
      for (unsigned spins = 0; spins < SPIN_CAP; ++spins) {
        bool ok = lane >= 40 || __hip_atomic_load((gu32*)(gws + WS_FU) + (h ? l31 * 32 + qt : hg * 32 + lane) * 32, RLX_AGENT) == tag;
        if (bid == 0) {
#pragma unroll
          for (int i = 0; i < 4; ++i) ok &= __hip_atomic_load((gu32*)(gws + WS_FU) + (64 * i + lane) * 32, RLX_AGENT) == tag;
        }
        if (ALL(ok)) break;
        __builtin_amdgcn_s_sleep(8);
      }
      if (bid == 0 && lane == 0) __hip_atomic_store((gu32*)(gws + WS_EPOCH), tag, RLX_AGENT);
      lds_signal(mi + M_UR);
    } else {
      lds_wait<4>(mi + M_UR, 1);
    }
    NSTAMP(5);
    if (j == 1) *(LAS unsigned*)(mi + M_G + lane * 4) = __builtin_amdgcn_raw_buffer_load_b32(rs, WS_G + (64 * qt + lane) * 4, 0, 16);
    *(LAS u32x4*)(sm + CV_U + 2560 + (ht >> 2) * 80 + (ht & 3) * 16) = uload(ce);
    VM_WAIT(0);
    HSYNC();
    {
      const int d0 = 8 * (qt & 3) + 2 * j;
      lds_ptr ab = sm + CV_H + (l31 & 7) * CV_COPY + (h + (l31 >> 3)) * 16 + 64 * d0;
      lds_ptr bb = sm + CV_U + 2560 + (l31 - d0 - 7) * 80 + (1 - h) * 16;
      asm volatile("" : "+v"(ab), "+v"(bb));
      f32x16 y0 = {0}, y1 = {0};
#pragma unroll
      for (int d = 0; d < 2; ++d) {
#pragma unroll
        for (int s = 0; s < 2; ++s) {
          const bf16x8 a0 = lds_frag(ab + 32 * (2 * d + 1 - s)), b0 = lds_frag(bb + 80 * (7 - d) + 32 * s);
          const bf16x8 b1 = lds_frag(bb + 80 * (39 - d) + 32 * s), a1 = lds_frag(ab + 32 * (2 * d + 65 - s));
          y0 = MFMA32(a0, b0, y0);
          y1 = MFMA32(a0, b1, y1);
          y1 = MFMA32(a1, b0, y1);
          SB0;
        }
      }
      CV_PUT(sm + CV_H + CV_HB + j * 8192 + lane * 16);
      HSYNC();
#pragma unroll
      for (int q = 0; q < 2; ++q)
        ST_SC1_16(__builtin_bit_cast(u32x4, cv_sum(sm + CV_H + CV_HB + (2 * j + q) * 1024 + lane * 16,
                                                   sm + CV_U + 2560 + (32 * (j >> 1) + l31) * 80 + (2 * (j & 1) + q) * 16 + (1 - h) * 8, dve)),
                  WS_ZP + (hg * 32 + qt) * 8192 + (2 * j + q) * 1024 + lane * 16);
    }
    HSYNC();
    NSTAMP(6);
    hload(2 * qt, 0);
    hload(2 * qt + 1, 1);
    cvprep(j >> 1, h8a);
    *(LAS u32x4*)(sm + CV_U + 2560 + (ht >> 2) * 80 + (ht & 3) * 16) = uload(2 * qt);
    *(LAS u32x4*)(sm + CV_U + CV_UB + 2560 + (ht >> 2) * 80 + (ht & 3) * 16) = uload(2 * qt + 1);
    VM_WAIT(0);
#pragma unroll
    for (int i = 0; i < 4; ++i) lds_signal(mi + M_PAIR + 4 * i);
    if (lds_ticket(mi + M_CVR) == 3u && lane == 0) __hip_atomic_store((gu32*)(gws + WS_FZ) + (hg * 32 + qt) * 32, tag, RLX_AGENT);
    lds_wait<8>(mi + M_DONE + 28, 8);
    if (j == 0) {
#pragma unroll 1
      for (unsigned spins = 0; spins < SPIN_CAP; ++spins) {
        if (ALL(h || __hip_atomic_load((gu32*)(gws + WS_FZ) + (hg * 32 + lane) * 32, RLX_AGENT) == tag)) break;
        __builtin_amdgcn_s_sleep(4);
      }
      asm volatile("" ::: "memory");
      f32x4 sa = {0}, sb = {0};
#pragma unroll
      for (int p = 0; p < 4; ++p) {
        const int zo = WS_ZP + (hg * 32 + (lane >> 3) * 4 + p) * 8192 + (qt >> 4) * 4096 + ((lane >> 2) & 1) * 2048 +
                       (2 * (qt & 15) + (lane & 1) + 32 * ((lane >> 1) & 1)) * 16;
        sa += __builtin_bit_cast(f32x4, LD_SC1_16(zo));
        sb += __builtin_bit_cast(f32x4, LD_SC1_16(zo + 1024));
      }
      zv = (u32x4){pk2(sa[0], sa[1]), pk2(sa[2], sa[3]), pk2(sb[0], sb[1]), pk2(sb[2], sb[3])};
      xv = *(const u32x4*)(xt + otok * 1152 + 144 * hg + opc * 16);
    }
    HSTAMP(8);
    __builtin_amdgcn_s_setprio(1);
#pragma unroll 1
    for (int t = 0; t < 2; ++t) {
      lds_ptr ob = sm + (36 * t + (j >> 1)) * 1024 + lane * 16;
      lds_ptr oe = sm + (36 * t + 8 * j + (g4 >> 1)) * 1024 + (l15 + 32 * (g4 & 1)) * 16;
      lds_ptr or_ = sm + (36 * t + 32 + 2 * (j & 1) + (g4 >> 1)) * 1024 + (16 * (j >> 1) + l15 + 32 * (g4 & 1)) * 16;
      lds_wait<1>(mi + (t ? M_OR : M_KVR), 4);
      if (t) NSTAMP(9);
      f32x16 a0 = {0};
      f32x4 e0 = {0}, e1 = {0};
      bf16x8 f[27];
#pragma unroll
      for (int k = 0; k < 31; ++k) {
        if (k < 18) f[k] = lds_frag(ob + k * 2048);
        else if (k < 26) f[k] = lds_frag(oe + ((k - 18) >> 2) * 256 + ((k - 18) & 3) * 2048);
        else if (k == 26) f[k] = lds_frag(or_);
        SB0;
        const int m = k - 4;
        if (m >= 0 && m < 18) a0 = MFMA32(wf[m], f[m], a0);
        else if (m >= 18 && m < 22) e0 = MFMA16(we[m - 18], f[m], e0);
        else if (m >= 22 && m < 26) e1 = MFMA16(we[m - 22], f[m], e1);
        else if (m == 26) { if (j >> 1) e1 = MFMA16(we[4], f[m], e1); else e0 = MFMA16(we[4], f[m], e0); }
        SB0;
      }
      if (t) NSTAMP(10);
      lds_wait<1>(mi + M_MM, 4);
      lds_ptr ap = sm + L_AP + (8 * (j >> 1) + 4 * t) * CV_COPY;
#pragma unroll
      for (int g = 0; g < 4; ++g)
        *(LAS f32x4*)(ap + (l31 >> 3) * CV_COPY + (l31 & 7) * 336 + (j & 1) * 128 + h * 16 + g * 32) = (f32x4){a0[4 * g], a0[4 * g + 1], a0[4 * g + 2], a0[4 * g + 3]};
      if (!h) {
        lds_ptr ae = ap + (l15 >> 3) * CV_COPY + (l15 & 7) * 336 + 256 + (j & 1) * 32 + g4 * 16;
        *(LAS f32x4*)(ae) = e0;
        *(LAS f32x4*)(ae + 2 * CV_COPY) = e1;
      }
      if (j == 0 && t == 0) { lds_wait<1>(mi + M_CE, 4); zscatter(); }
    }
    lds_signal(mi + M_APR);
  } else {
    if (w < 4) {
      const int gt = w >> 1, tt = w & 1, arow = 32 * tt + l31, xsw = (arow >> 1) & 7;
      lds_ptr xb = sm + L_X + arow * 128;
      lds_ptr wb = sm + L_B + (8 * h + tq) * 144 + (36 * gt + 20 * (g4 & 1) + 4 * tp) * 2;
      f32x16 acc = {0};
#pragma unroll 1
      for (int p = 0; p < 3; ++p) {
        if (p == 1) CSTAMP(6);
        if (p == 2) CSTAMP(8);
        lds_wait(mi + M_C1 + 4 * p, p == 1 ? 3 : 6 + p);
        if (p == 0) CSTAMP(5);
        if (p == 1) CSTAMP(7);
        if (p == 2) CSTAMP(9);
        bf16x8 fa[12], fb[12];
#pragma unroll
        for (int k = 0; k < 16; ++k) {
          if (k < 12) {
            fa[k] = tr_frag(wb + k * 2304, wb + k * 2304 + 576);
            fb[k] = lds_frag(xb + (k >> 2) * 8192 + (((2 * (k & 3) + h) ^ xsw) << 4));
          }
          SB0;
          if (k >= 4) acc = MFMA32(fa[k - 4], fb[k - 4], acc);
          SB0;
        }
        if (p < 2) lds_signal(mi + M_C3 + 4 * p);
        xb += 24576;
        wb += 27648;
      }
      STAMP(2);
      u32x4 a;
#pragma unroll
      for (int i = 0; i < 4; ++i) a[i] = pk2(acc[2 * i], acc[2 * i + 1]);
      lds_ptr qe = sm + L_Q + (4 * gt + tt) * 1024;
      *(LAS u32x4*)(qe + lane * 16) = a;
      lds_ptr qo = qe + 2048 + l31 * 16;
      *(LAS unsigned*)(qo + h * 512 + 4) = pk2(acc[8], acc[9]);
      *(LAS unsigned*)(qo + (h ? 8 : 512)) = pk2(acc[10], acc[11]);
      *(LAS unsigned*)(qo + h * 512 + 12) = pk2(acc[12], acc[13]);
      *(LAS unsigned*)(h ? sm + L_Q + 8192 + ((2 * gt + 1) * 64 + arow) * 4 : qo + 520) = pk2(acc[14], acc[15]);
    } else if (w == 4) {
      __builtin_amdgcn_s_setprio(2);
      lds_ptr xb = sm + L_X + l15 * 128;
      lds_ptr wb = sm + L_B + (8 * g4 + tq) * 144 + (tp == 1 ? 104 : 32);
      f32x4 acc[4] = {{0}, {0}, {0}, {0}};
#pragma unroll 1
      for (int p = 0; p < 3; ++p) {
        lds_wait(mi + M_C1 + 4 * p, p == 1 ? 3 : 6 + p);
        bf16x8 fa[6], fb[24];
#pragma unroll
        for (int k = 0; k < 8; ++k) {
          if (k < 6) {
            fa[k] = tr_frag(wb + k * 4608, wb + k * 4608 + 576);
#pragma unroll
            for (int t = 0; t < 4; ++t) fb[4 * k + t] = lds_frag(xb + (k >> 1) * 8192 + t * 2048 + (((4 * (k & 1) + g4) ^ (l15 >> 1)) << 4));
          }
          SB0;
          if (k >= 2) {
#pragma unroll
            for (int t = 0; t < 4; ++t) acc[t] = MFMA16(fa[k - 2], fb[4 * (k - 2) + t], acc[t]);
          }
          SB0;
        }
        if (p < 2) lds_signal(mi + M_C3 + 4 * p);
        xb += 24576;
        wb += 27648;
      }
      STAMP(2);
      if (g4 < 2) {
#pragma unroll
        for (int t = 0; t < 4; ++t) {
          const int tok = 16 * t + l15;
          *(LAS unsigned*)(sm + L_Q + 8192 + (2 * g4 * 64 + tok) * 4) = pk2(acc[t][0], acc[t][1]);
          *(LAS unsigned*)(sm + L_Q + ((2 * g4 + 1) * 2 + (t >> 1)) * 1024 + (tok & 31) * 16) = pk2(acc[t][2], acc[t][3]);
        }
      }
    }
    lds_signal(mi + M_QD);
    __builtin_amdgcn_s_setprio(2);
    const int hl = w & 3, kh = w >> 2;
    const int voff = 4096 + (l31 < 18 ? h * 288 + l31 * 16 : (l31 == 18 ? 576 : 592));
    lds_wait(mi + M_QD, 12);
    STAMP(3);
    const u32x4 q0u = *(LAS u32x4*)(sm + L_Q + (2 * hl) * 1024 + lane * 16);
    const u32x4 q1u = *(LAS u32x4*)(sm + L_Q + (2 * hl + 1) * 1024 + lane * 16);
    const unsigned qr = *(LAS unsigned*)(sm + L_Q + 8192 + (hl * 64 + lane) * 4);
    const float kn0 = *(LAS float*)(mi + M_KN0);
    lds_signal(mi + M_QR);
    float n0 = 0.f, n1 = 0.f;
#pragma unroll
    for (int i = 0; i < 4; ++i) { n0 = dot2(q0u[i], n0); n1 = dot2(q1u[i], n1); }
    const float nq = dot2(qr, (h ? n1 : n0) + xhalf(h ? n0 : n1));
    const u32x2 qrem_u = {qr, pk2(-__builtin_amdgcn_sqrtf(nq * kn0), 0.f)};
    f32x16 o0, o1;
    float m0 = 0.f, m1 = 0.f;
    STAMP(4); RSTAMP(2);
    {
      int cnt = 8, off = 0, tmp, t2, so = ((s0 + 4) & 7) * RSLOT;
      unsigned ca = sbase + L_M + M_DONE - 4, dst = sbase + w * 2496;
      const unsigned long long ksrc = (unsigned long long)ws + WS_KV + w * 2496 - (sbase + kh * KV_GROUP);
      asm volatile(
      "v_mov_b32 v107, v126\n"
      "v_mov_b32 v108, v127\n"
      "v_mov_b32 v109, v128\n"
      "v_mov_b32 v130, 1\n"
      "ds_read2st64_b64 v[110:113], v109 offset0:0 offset1:1\n"
      "ds_read_b128 v[114:117], v107 offset:0\n"
      "ds_read_b128 v[118:121], v108 offset:0\n"
      "s_mov_b32 vcc_lo, %[first]\n"
      "s_mov_b32 vcc_hi, 0\n"
      "s_setprio 1\n"
      "v_mov_b32 v56, 0\n"
      "v_mov_b32 v57, 0\n"
      "v_mov_b32 v58, 0\n"
      "v_mov_b32 v59, 0\n"
      "v_mov_b32 v60, 0\n"
      "v_mov_b32 v61, 0\n"
      "v_mov_b32 v62, 0\n"
      "v_mov_b32 v63, 0\n"
      "v_mov_b32 v122, 0\n"
      "v_mov_b32 v123, 0\n"
      "v_mov_b32 v124, 0\n"
      "v_mov_b32 v125, 0\n"
      "v_mov_b32 v64, 0\n"
      "v_mov_b32 v65, 0\n"
      "v_mov_b32 v66, 0\n"
      "v_mov_b32 v67, 0\n"
      "v_mov_b32 v68, 0\n"
      "v_mov_b32 v69, 0\n"
      "v_mov_b32 v70, 0\n"
      "v_mov_b32 v71, 0\n"
      "v_mov_b32 v72, 0\n"
      "v_mov_b32 v73, 0\n"
      "v_mov_b32 v74, 0\n"
      "v_mov_b32 v75, 0\n"
      "v_mov_b32 v76, 0\n"
      "v_mov_b32 v77, 0\n"
      "v_mov_b32 v78, 0\n"
      "v_mov_b32 v79, 0\n"
      "s_waitcnt lgkmcnt(1)\n"
      "v_mfma_f32_32x32x4_2b_bf16 v[0:31], v[110:111], v[104:105], 0 cbsz:1 abid:0\n"
      "v_mov_b32 v80, 0\n"
      "v_mov_b32 v81, 0\n"
      "v_mov_b32 v82, 0\n"
      "v_mov_b32 v83, 0\n"
      "v_mov_b32 v84, 0\n"
      "v_mov_b32 v85, 0\n"
      "v_mov_b32 v86, 0\n"
      "v_mov_b32 v87, 0\n"
      "v_mov_b32 v88, 0\n"
      "v_mov_b32 v89, 0\n"
      "v_mov_b32 v90, 0\n"
      "v_mov_b32 v91, 0\n"
      "v_mov_b32 v92, 0\n"
      "v_mov_b32 v93, 0\n"
      "v_mov_b32 v94, 0\n"
      "v_mov_b32 v95, 0\n"
      "s_nop 2\n"
      "v_mfma_f32_32x32x16_bf16 v[0:15], v[114:117], v[96:99], v[0:15]\n"
      "v_mfma_f32_32x32x16_bf16 v[16:31], v[114:117], v[100:103], v[16:31]\n"
      "s_nop 11\n"
      "1:\n"
      "s_cbranch_vccz 4f\n"
      "s_setprio 2\n"
      "4:\n"
      "v_exp_f32 v0, v0\n"
      "v_exp_f32 v1, v1\n"
      "v_smfmac_f32_32x32x32_bf16 v[80:95], v[122:125], v[56:63], v106\n"
      "ds_read_b128 v[114:117], v107 offset:1024\n"
      "ds_read_b128 v[122:125], v108 offset:608\n"
      "v_exp_f32 v2, v2\n"
      "v_exp_f32 v3, v3\n"
      "v_exp_f32 v4, v4\n"
      "v_exp_f32 v5, v5\n"
      "v_mfma_f32_32x32x4_2b_bf16 v[32:63], v[110:111], v[104:105], 0 cbsz:1 abid:1\n"
      "v_exp_f32 v6, v6\n"
      "v_exp_f32 v7, v7\n"
      "v_exp_f32 v8, v8\n"
      "v_exp_f32 v9, v9\n"
      "v_exp_f32 v10, v10\n"
      "v_exp_f32 v11, v11\n"
      "v_exp_f32 v12, v12\n"
      "s_waitcnt lgkmcnt(2)\n"
      "v_exp_f32 v13, v13\n"
      "v_smfmac_f32_32x32x32_bf16 v[64:79], v[118:121], v[0:7], v106\n"
      "v_exp_f32 v14, v14\n"
      "v_exp_f32 v15, v15\n"
      "v_exp_f32 v16, v16\n"
      "s_waitcnt lgkmcnt(0)\n"
      "v_exp_f32 v17, v17\n"
      "v_smfmac_f32_32x32x32_bf16 v[64:79], v[122:125], v[8:15], v106\n"
      "v_exp_f32 v18, v18\n"
      "v_exp_f32 v19, v19\n"
      "v_exp_f32 v20, v20\n"
      "v_exp_f32 v21, v21\n"
      "v_mfma_f32_32x32x16_bf16 v[32:47], v[114:117], v[96:99], v[32:47]\n"
      "v_exp_f32 v22, v22\n"
      "v_exp_f32 v23, v23\n"
      "v_exp_f32 v24, v24\n"
      "v_exp_f32 v25, v25\n"
      "v_smfmac_f32_32x32x32_bf16 v[80:95], v[118:121], v[16:23], v106\n"
      "v_exp_f32 v26, v26\n"
      "ds_read_b128 v[118:121], v108 offset:1216\n"
      "v_exp_f32 v27, v27\n"
      "v_exp_f32 v28, v28\n"
      "v_exp_f32 v29, v29\n"
      "v_mfma_f32_32x32x16_bf16 v[48:63], v[114:117], v[100:103], v[48:63]\n"
      "v_exp_f32 v30, v30\n"
      "v_exp_f32 v31, v31\n"
      "v_exp_f32 v32, v32\n"
      "v_exp_f32 v33, v33\n"
      "v_smfmac_f32_32x32x32_bf16 v[80:95], v[122:125], v[24:31], v106\n"
      "ds_read_b128 v[114:117], v107 offset:2048\n"
      "ds_read_b128 v[122:125], v108 offset:1824\n"
      "ds_read_b32 v131, v129\n"
      "v_exp_f32 v34, v34\n"
      "v_exp_f32 v35, v35\n"
      "v_exp_f32 v36, v36\n"
      "v_exp_f32 v37, v37\n"
      "v_mfma_f32_32x32x4_2b_bf16 v[0:31], v[112:113], v[104:105], 0 cbsz:1 abid:0\n"
      "v_exp_f32 v38, v38\n"
      "v_exp_f32 v39, v39\n"
      "v_exp_f32 v40, v40\n"
      "v_exp_f32 v41, v41\n"
      "v_exp_f32 v42, v42\n"
      "v_exp_f32 v43, v43\n"
      "v_exp_f32 v44, v44\n"
      "s_waitcnt lgkmcnt(3)\n"
      "v_exp_f32 v45, v45\n"
      "v_smfmac_f32_32x32x32_bf16 v[64:79], v[118:121], v[32:39], v106\n"
      "v_exp_f32 v46, v46\n"
      "v_exp_f32 v47, v47\n"
      "v_exp_f32 v48, v48\n"
      "s_waitcnt lgkmcnt(0)\n"
      "s_waitcnt vmcnt(0)\n"
      "s_mov_b64 exec, 1\n"
      "ds_add_u32 v129, v130 offset:76\n"
      "s_mov_b64 exec, -1\n"
      "v_exp_f32 v49, v49\n"
      "v_smfmac_f32_32x32x32_bf16 v[64:79], v[122:125], v[40:47], v106\n"
      "v_exp_f32 v50, v50\n"
      "v_exp_f32 v51, v51\n"
      "v_exp_f32 v52, v52\n"
      "v_exp_f32 v53, v53\n"
      "v_mfma_f32_32x32x16_bf16 v[0:15], v[114:117], v[96:99], v[0:15]\n"
      "v_exp_f32 v54, v54\n"
      "v_exp_f32 v55, v55\n"
      "v_exp_f32 v56, v56\n"
      "v_exp_f32 v57, v57\n"
      "v_smfmac_f32_32x32x32_bf16 v[80:95], v[118:121], v[48:55], v106\n"
      "v_exp_f32 v58, v58\n"
      "ds_read_b128 v[118:121], v108 offset:2432\n"
      "v_exp_f32 v59, v59\n"
      "v_exp_f32 v60, v60\n"
      "v_exp_f32 v61, v61\n"
      "v_mfma_f32_32x32x16_bf16 v[16:31], v[114:117], v[100:103], v[16:31]\n"
      "v_exp_f32 v62, v62\n"
      "v_exp_f32 v63, v63\n"
      "v_readfirstlane_b32 %[tmp], v131\n"
      "s_and_b32 %[t2], %[cnt], 12\n"
      "s_cmp_lg_u32 %[t2], 4\n"
      "s_cbranch_scc1 8f\n"
      "s_cmp_lt_u32 %[tmp], 8\n"
      "s_cbranch_scc1 7f\n"
      "9:\n"
      "s_mov_b32 m0, %[dst]\n"
      "s_add_u32 s96, %[slo], %[so]\n"
      "s_addc_u32 s97, %[shi], 0\n"
      "global_load_lds_dwordx4 v126, s[96:97] sc1\n"
      "global_load_lds_dwordx4 v126, s[96:97] offset:1024 sc1\n"
      "s_mov_b64 exec, 0xfffffff\n"
      "global_load_lds_dwordx4 v126, s[96:97] offset:2048 sc1\n"
      "s_mov_b64 exec, -1\n"
      "s_add_u32 %[dst], %[dst], 19968\n"
      "s_add_u32 %[so], %[so], 19968\n"
      "s_cmp_eq_u32 %[so], 159744\n"
      "s_cselect_b32 %[so], 0, %[so]\n"
      "8:\n"
      "v_exp_f32 v0, v0\n"
      "v_exp_f32 v1, v1\n"
      "v_smfmac_f32_32x32x32_bf16 v[80:95], v[122:125], v[56:63], v106\n"
      "ds_read_b128 v[114:117], v107 offset:3072\n"
      "ds_read_b128 v[122:125], v108 offset:3040\n"
      "ds_read_b32 v131, v129 offset:72\n"
      "v_exp_f32 v2, v2\n"
      "v_exp_f32 v3, v3\n"
      "v_exp_f32 v4, v4\n"
      "v_exp_f32 v5, v5\n"
      "v_mfma_f32_32x32x4_2b_bf16 v[32:63], v[112:113], v[104:105], 0 cbsz:1 abid:1\n"
      "v_exp_f32 v6, v6\n"
      "v_exp_f32 v7, v7\n"
      "v_exp_f32 v8, v8\n"
      "v_exp_f32 v9, v9\n"
      "v_exp_f32 v10, v10\n"
      "v_exp_f32 v11, v11\n"
      "v_exp_f32 v12, v12\n"
      "s_waitcnt lgkmcnt(3)\n"
      "v_exp_f32 v13, v13\n"
      "v_smfmac_f32_32x32x32_bf16 v[64:79], v[118:121], v[0:7], v106\n"
      "v_exp_f32 v14, v14\n"
      "v_exp_f32 v15, v15\n"
      "v_exp_f32 v16, v16\n"
      "s_waitcnt lgkmcnt(0)\n"
      "v_exp_f32 v17, v17\n"
      "v_smfmac_f32_32x32x32_bf16 v[64:79], v[122:125], v[8:15], v106\n"
      "v_exp_f32 v18, v18\n"
      "v_exp_f32 v19, v19\n"
      "v_exp_f32 v20, v20\n"
      "v_exp_f32 v21, v21\n"
      "v_mfma_f32_32x32x16_bf16 v[32:47], v[114:117], v[96:99], v[32:47]\n"
      "v_exp_f32 v22, v22\n"
      "v_exp_f32 v23, v23\n"
      "v_exp_f32 v24, v24\n"
      "v_exp_f32 v25, v25\n"
      "v_smfmac_f32_32x32x32_bf16 v[80:95], v[118:121], v[16:23], v106\n"
      "v_exp_f32 v26, v26\n"
      "ds_read_b128 v[118:121], v108 offset:3648\n"
      "v_exp_f32 v27, v27\n"
      "v_exp_f32 v28, v28\n"
      "v_exp_f32 v29, v29\n"
      "v_mfma_f32_32x32x16_bf16 v[48:63], v[114:117], v[100:103], v[48:63]\n"
      "v_exp_f32 v30, v30\n"
      "v_exp_f32 v31, v31\n"
      "s_cbranch_vccz 5f\n"
      "s_setprio 0\n"
      "5:\n"
      "v_readfirstlane_b32 %[tmp], v131\n"
      "s_add_u32 %[off], %[off], 19968\n"
      "s_cmp_eq_u32 %[off], 79872\n"
      "s_cselect_b32 %[off], 0, %[off]\n"
      "s_cmp_lt_i32 %[tmp], 8\n"
      "s_cbranch_scc1 3f\n"
      "2:\n"
      "v_add_u32 v109, %[off], v128\n"
      "ds_read2st64_b64 v[110:113], v109 offset0:0 offset1:1\n"
      "v_add_u32 v107, %[off], v126\n"
      "v_exp_f32 v32, v32\n"
      "v_exp_f32 v33, v33\n"
      "v_smfmac_f32_32x32x32_bf16 v[80:95], v[122:125], v[24:31], v106\n"
      "ds_read_b128 v[114:117], v107 offset:0\n"
      "ds_read_b128 v[122:125], v108 offset:4256\n"
      "v_exp_f32 v34, v34\n"
      "v_exp_f32 v35, v35\n"
      "v_exp_f32 v36, v36\n"
      "s_waitcnt lgkmcnt(2)\n"
      "v_exp_f32 v37, v37\n"
      "v_mfma_f32_32x32x4_2b_bf16 v[0:31], v[110:111], v[104:105], 0 cbsz:1 abid:0\n"
      "v_exp_f32 v38, v38\n"
      "v_exp_f32 v39, v39\n"
      "v_exp_f32 v40, v40\n"
      "v_exp_f32 v41, v41\n"
      "v_exp_f32 v42, v42\n"
      "v_exp_f32 v43, v43\n"
      "v_exp_f32 v44, v44\n"
      "v_exp_f32 v45, v45\n"
      "v_smfmac_f32_32x32x32_bf16 v[64:79], v[118:121], v[32:39], v106\n"
      "v_exp_f32 v46, v46\n"
      "v_exp_f32 v47, v47\n"
      "v_exp_f32 v48, v48\n"
      "s_waitcnt lgkmcnt(0)\n"
      "s_mov_b64 exec, 1\n"
      "ds_add_u32 v129, v130 offset:4\n"
      "s_mov_b64 exec, -1\n"
      "v_exp_f32 v49, v49\n"
      "v_smfmac_f32_32x32x32_bf16 v[64:79], v[122:125], v[40:47], v106\n"
      "v_exp_f32 v50, v50\n"
      "v_exp_f32 v51, v51\n"
      "v_exp_f32 v52, v52\n"
      "v_exp_f32 v53, v53\n"
      "v_mfma_f32_32x32x16_bf16 v[0:15], v[114:117], v[96:99], v[0:15]\n"
      "v_exp_f32 v54, v54\n"
      "v_exp_f32 v55, v55\n"
      "v_exp_f32 v56, v56\n"
      "v_exp_f32 v57, v57\n"
      "v_smfmac_f32_32x32x32_bf16 v[80:95], v[118:121], v[48:55], v106\n"
      "v_add_u32 v108, %[off], v127\n"
      "v_add_u32 v129, 4, v129\n"
      "v_exp_f32 v58, v58\n"
      "ds_read_b128 v[118:121], v108 offset:0\n"
      "v_exp_f32 v59, v59\n"
      "v_exp_f32 v60, v60\n"
      "v_exp_f32 v61, v61\n"
      "v_mfma_f32_32x32x16_bf16 v[16:31], v[114:117], v[100:103], v[16:31]\n"
      "v_exp_f32 v62, v62\n"
      "v_exp_f32 v63, v63\n"
      "s_sub_u32 %[cnt], %[cnt], 1\n"
      "s_cmp_lg_u32 %[cnt], 0\n"
      "s_cbranch_scc1 1b\n"
      "s_branch 6f\n"
      "3:\n"
      "s_sleep 1\n"
      "ds_read_b32 v131, v129 offset:72\n"
      "s_waitcnt lgkmcnt(0)\n"
      "v_readfirstlane_b32 %[tmp], v131\n"
      "s_nop 0\n"
      "s_cmp_lt_i32 %[tmp], 8\n"
      "s_cbranch_scc1 3b\n"
      "s_branch 2b\n"
      "7:\n"
      "s_sleep 1\n"
      "ds_read_b32 v131, v129\n"
      "s_waitcnt lgkmcnt(0)\n"
      "v_readfirstlane_b32 %[tmp], v131\n"
      "s_nop 0\n"
      "s_cmp_lt_u32 %[tmp], 8\n"
      "s_cbranch_scc1 7b\n"
      "s_branch 9b\n"
      "6:\n"
      "v_smfmac_f32_32x32x32_bf16 v[80:95], v[122:125], v[56:63], v106\n"
      "s_setprio 0\n"
      "s_waitcnt lgkmcnt(0)\n"
      "s_nop 15\n"
      "s_nop 7\n"
      : "=&{v[64:79]}"(o0), "=&{v[80:95]}"(o1), "+{v129}"(ca), [cnt] "+s"(cnt), [off] "+s"(off), [tmp] "=&s"(tmp),
        [t2] "=&s"(t2), [so] "+s"(so), [dst] "+s"(dst)
      : "{v[96:99]}"(q0u), "{v[100:103]}"(q1u), "{v[104:105]}"(qrem_u), "{v106}"(SPIDX),
        "{v126}"(sbase + kh * KV_GROUP + lane * 16), "{v127}"(sbase + kh * KV_GROUP + voff),
        "{v128}"(sbase + kh * KV_GROUP + 8960 + lane * 8), [first] "s"(1 - kh), [slo] "s"((unsigned)ksrc), [shi] "s"((unsigned)(ksrc >> 32))
      : "v0", "v1", "v2", "v3", "v4", "v5", "v6", "v7", "v8", "v9", "v10", "v11", "v12", "v13", "v14", "v15",
        "v16", "v17", "v18", "v19", "v20", "v21", "v22", "v23", "v24", "v25", "v26", "v27", "v28", "v29", "v30", "v31",
        "v32", "v33", "v34", "v35", "v36", "v37", "v38", "v39", "v40", "v41", "v42", "v43", "v44", "v45", "v46", "v47",
        "v48", "v49", "v50", "v51", "v52", "v53", "v54", "v55", "v56", "v57", "v58", "v59", "v60", "v61", "v62", "v63",
        "v107", "v108", "v109", "v110", "v111", "v112", "v113", "v114", "v115", "v116", "v117", "v118", "v119", "v120", "v121", "v122",
        "v123", "v124", "v125", "v130", "v131", "s96", "s97", "vcc", "scc", "memory");
    }
    xv = *(const u32x4*)(xt + otok * 1152 + 144 * hg + opc * 16);
    const float dv = (float)ssmd[72 * hg + 2 * qt + (hl >> 1)];
    if (!ALL(nq * *(LAS float*)(mi + M_KN) < 47.f * 47.f)) {
      m0 = m1 = -__builtin_inff();
      o0 = o1 = (f32x16){0};
      const bf16x8 q0 = __builtin_bit_cast(bf16x8, q0u), q1 = __builtin_bit_cast(bf16x8, q1u);
      const s16x4 qrem = __builtin_bit_cast(s16x4, (u32x2){qr, 0u});
#pragma unroll 1
      for (int t = 0; t < 32; ++t) {
        const int g = WS_KV + (t >> 2) * RSLOT + kh * KV_GROUP;
        const u32x2 kr = __builtin_bit_cast(u32x2, __builtin_amdgcn_raw_buffer_load_b64(rs, g + 8960 + (t & 3) * 256 + l31 * 8, 0, 16));
        const bf16x8 km = __builtin_bit_cast(bf16x8, LD_SC1_16(g + (t & 3) * 1024 + lane * 16));
        const bf16x8 va = __builtin_bit_cast(bf16x8, LD_SC1_16(g + voff + (t & 3) * 1216));
        const bf16x8 vb = __builtin_bit_cast(bf16x8, LD_SC1_16(g + voff + (t & 3) * 1216 + 608));
        const f32x32 z = {0};
        const f32x32 r = __builtin_amdgcn_mfma_f32_32x32x4bf16_1k(__builtin_bit_cast(s16x4, kr), qrem, z, 0, 0, 0);
        online_step(MFMA32(km, q0, lo16(r)), m0, o0, va, vb);
        online_step(MFMA32(km, q1, hi16(r)), m1, o1, va, vb);
      }
    }
    HSTAMP(9);
    lds_ptr ex = sm + L_EX + hl * 6144 + lane * 16;
    if (kh) {
      *(LAS f32x4*)(ex) = (f32x4){o0[0], o0[1], o0[2], o0[3]};
      *(LAS f32x4*)(ex + 1024) = (f32x4){o0[4], o0[5], o0[6], o0[7]};
      *(LAS f32x4*)(ex + 2048) = (f32x4){o0[8], o0[9], o0[10], m0};
      *(LAS f32x4*)(ex + 3072) = (f32x4){o1[0], o1[1], o1[2], o1[3]};
      *(LAS f32x4*)(ex + 4096) = (f32x4){o1[4], o1[5], o1[6], o1[7]};
      *(LAS f32x4*)(ex + 5120) = (f32x4){o1[8], o1[9], o1[10], m1};
      lds_signal(mi + M_PAIR + 4 * hl);
    }
    lds_wait<1>(mi + M_PAIR + 4 * hl, 5);
    STAMP(10); RSTAMP(3);
    if (!kh) {
      __builtin_amdgcn_s_setprio(3);
#pragma unroll
      for (int t = 0; t < 2; ++t) {
        const f32x16 keep = t ? o1 : o0;
        const f32x4 t0 = *(LAS f32x4*)(ex + t * 3072), t1 = *(LAS f32x4*)(ex + t * 3072 + 1024), t2 = *(LAS f32x4*)(ex + t * 3072 + 2048);
        const float mk = t ? m1 : m0, mm = __builtin_fmaxf(mk, t2[3]);
        float fa = __builtin_amdgcn_exp2f(mk - mm), fb = __builtin_amdgcn_exp2f(t2[3] - mm);
        const float l = keep[10] * fa + t2[2] * fb, lx = xhalf(l);
        const float inv = __builtin_amdgcn_rcpf(h ? lx : l);
        fa *= inv;
        fb *= inv;
        float o[10];
#pragma unroll
        for (int i = 0; i < 10; ++i) o[i] = keep[i] * fa + (i < 4 ? t0[i & 3] : (i < 8 ? t1[i & 3] : t2[i & 3])) * fb;
        u32x4 a;
#pragma unroll
        for (int i = 0; i < 4; ++i) a[i] = UMIN(pk2(o[2 * i], o[2 * i + 1]), 0xfffffffeu);
        const int ob = WS_O + (2 * qt + t) * 36 * 1024;
        ST_SC1_16(a, ob + (4 * hg + hl) * 1024 + lane * 16);
        if (!h) ST_SC1_4(UMIN(pk2(o[8], o[9]), 0xfffffffeu), ob + 32768 + (4 * hg + hl) * 128 + l31 * 4);
      }
      lds_signal(mi + M_EXD);
      __builtin_amdgcn_s_setprio(1);
      HSTAMP(5);
      __builtin_amdgcn_s_sleep(O_DELAY);
      u32x4 of[18];
#pragma unroll
      for (int i = 0; i < 18; ++i) of[i] = LD_SC1_16(WS_O + (2 * qt * 36 + (i / 9) * 36 + 9 * hl + i % 9) * 1024 + lane * 16);
      lds_wait<1>(mi + M_EXD, 4);
#pragma unroll
      for (int t = 0; t < 2; ++t) {
        unsigned mx = 0;
#pragma unroll
        for (int i = 9 * t; i < 9 * t + 9; ++i) mx = UMAX(UMAX(mx, UMAX(of[i][0], of[i][1])), UMAX(of[i][2], of[i][3]));
        if (__builtin_expect(!ALL(mx != ~0u), 0)) {
#pragma unroll 1
          for (unsigned spins = 0; spins < SPIN_CAP; ++spins) {
            asm volatile("" ::: "memory");
#pragma unroll
            for (int i = 9 * t; i < 9 * t + 9; ++i) of[i] = LD_SC1_16(WS_O + (2 * qt * 36 + t * 36 + 9 * hl + i % 9) * 1024 + lane * 16);
            mx = 0;
#pragma unroll
            for (int i = 9 * t; i < 9 * t + 9; ++i) mx = UMAX(UMAX(mx, UMAX(of[i][0], of[i][1])), UMAX(of[i][2], of[i][3]));
            if (ALL(mx != ~0u)) break;
            __builtin_amdgcn_s_sleep(2);
          }
        }
#pragma unroll
        for (int i = 0; i < 9; ++i) {
          if (i >= 5 && hl == 3) {
            lds_ptr p = sm + (36 * t + 27 + i) * 1024 + (4 * (lane & 7) + 32 * h) * 16 + ((lane >> 3) & 3) * 4;
#pragma unroll
            for (int e = 0; e < 4; ++e) *(LAS unsigned*)(p + e * 16) = of[9 * t + i][e];
          } else {
            *(LAS u32x4*)(sm + (36 * t + 9 * hl + i) * 1024 + lane * 16) = of[9 * t + i];
          }
        }
        if (t == 0) lds_signal(mi + M_KVR);
      }
      lds_signal(mi + M_OR);
      __builtin_amdgcn_s_setprio(0);
      HSTAMP(6);
    } else {
      const int b = hl >> 1, o = hl & 1;
      lds_ptr ab = sm + CV_H + b * CV_HB + (l31 & 7) * CV_COPY + (h + (l31 >> 3)) * 16 + 1024 * o;
      lds_ptr bb = sm + CV_U + b * CV_UB + 2560 + (l31 - 16 * o - 7) * 80 + (1 - h) * 16;
      f32x16 y0 = {0}, y1 = {0};
      bf16x8 fx[8], fy[8];
      __builtin_amdgcn_s_setprio(2);
      asm volatile("" : "+v"(ab), "+v"(bb));
      CV_LD(fx, 0);
#pragma unroll 1
      for (int it = 0; it < 4; ++it) {
        asm volatile("" : "+v"(ab), "+v"(bb));
        CV_STEP(fx, fy, 1);
        CV_STEP(fy, fx, 2);
        CV_STEP(fx, fy, 3);
        CV_STEP(fy, fx, 4);
        ab += 256;
        bb -= 320;
      }
      HSTAMP(5);
      lds_ptr px = sm + CV_H + b * CV_HB + h * CV_COPY + l31 * 16;
      {
        const f32x16 give = o ? y0 : y1;
#pragma unroll
        for (int g = 0; g < 4; ++g) *(LAS f32x4*)(px + o * 3200 + g * 2 * CV_COPY) = (f32x4){give[4 * g], give[4 * g + 1], give[4 * g + 2], give[4 * g + 3]};
        if (o) y0 = y1;
      }
      lds_signal(mi + M_AS + 4 * b);
      lds_signal(mi + M_MM);
      u32x2 uu[4];
#pragma unroll
      for (int g = 0; g < 4; ++g) uu[g] = *(LAS u32x2*)(sm + CV_U + b * CV_UB + 2560 + (32 * o + l31) * 80 + g * 16 + (1 - h) * 8);
#pragma unroll
      for (int g = 0; g < 4; ++g) {
        y0[4 * g] = __builtin_fmaf(bf_hi(uu[g][1]), dv, y0[4 * g]);
        y0[4 * g + 1] = __builtin_fmaf(bf_lo(uu[g][1]), dv, y0[4 * g + 1]);
        y0[4 * g + 2] = __builtin_fmaf(bf_hi(uu[g][0]), dv, y0[4 * g + 2]);
        y0[4 * g + 3] = __builtin_fmaf(bf_lo(uu[g][0]), dv, y0[4 * g + 3]);
      }
      lds_wait(mi + M_AS + 4 * b, 2);
      u32x4 z[2];
#pragma unroll
      for (int g = 0; g < 4; ++g) {
        const f32x4 t = *(LAS f32x4*)(px + (1 - o) * 3200 + g * 2 * CV_COPY);
        z[g >> 1][2 * (g & 1)] = UMIN(pk2(t[0] + y0[4 * g], t[1] + y0[4 * g + 1]), 0xfffffffeu);
        z[g >> 1][2 * (g & 1) + 1] = UMIN(pk2(t[2] + y0[4 * g + 2], t[3] + y0[4 * g + 3]), 0xfffffffeu);
      }
      const int zo = WS_Z + (72 * hg + 2 * qt + b) * 4096 + o * 2048 + lane * 16;
      ST_SC1_16(z[0], zo);
      ST_SC1_16(z[1], zo + 1024);
      lds_signal(mi + M_CE);
      __builtin_amdgcn_s_setprio(0);
      HSTAMP(6);
    }
    lds_wait<2>(mi + M_CE, 4);
    __builtin_amdgcn_s_sleep(Z_DELAY);
#pragma unroll 1
    for (unsigned spins = 0; spins < SPIN_CAP; ++spins) {
      zv = LD_SC1_16(WS_Z + (72 * hg + (tid >> 3)) * 4096 + (qt >> 4) * 2048 + ((tid >> 2) & 1) * 1024 +
                     (2 * (qt & 15) + (tid & 1) + 32 * ((tid >> 1) & 1)) * 16);
      asm volatile("" ::: "memory");
      if (ALL(UMAX(UMAX(zv[0], zv[1]), UMAX(zv[2], zv[3])) != ~0u)) break;
      __builtin_amdgcn_s_sleep(2);
    }
    HSTAMP(7);
    zscatter();
  }

  if (w < 9) {
    lds_ptr ap = sm + L_AP + (otok >> 3) * CV_COPY + (otok & 7) * 336 + opc * 32;
    lds_wait<1>(mi + M_APR, 4);
    const float gq = *(LAS float*)(mi + M_G + otok * 4), omg = rbf(1.f - gq);
    f32x4 a0 = *(LAS f32x4*)(ap) + *(LAS f32x4*)(ap + CV_HB), a1 = *(LAS f32x4*)(ap + 16) + *(LAS f32x4*)(ap + CV_HB + 16);
    if (opc == 8) {
      a0 += *(LAS f32x4*)(ap + 32) + *(LAS f32x4*)(ap + CV_HB + 32);
      a1 += *(LAS f32x4*)(ap + 48) + *(LAS f32x4*)(ap + CV_HB + 48);
    }
    a0 *= gq;
    a1 *= gq;
    f32x4 x0 = {omg * bf_lo(xv[0]), omg * bf_hi(xv[0]), omg * bf_lo(xv[1]), omg * bf_hi(xv[1])};
    f32x4 x1 = {omg * bf_lo(xv[2]), omg * bf_hi(xv[2]), omg * bf_lo(xv[3]), omg * bf_hi(xv[3])};
    lds_ptr zq = sm + L_ZT + ((otok >> 5) + ((otok >> 1) & 2) + ((otok >> 2) & 4) + 8 * (otok & 3) + 4 * (otok & 8)) * 144 + opc * 16;
    u32x4* op = (u32x4*)(out + (size_t)(64 * qt + otok) * D_ + 72 * hg + 8 * opc);
    asm volatile("" : "+v"(a0), "+v"(a1), "+v"(x0), "+v"(x1), "+v"(zq), "+v"(op));
    lds_wait(mi + M_ZS, 9);
    if (w < 8) HSTAMP(8);
    const u32x4 z = *(LAS u32x4*)zq;
    *(GAS u32x4*)op = (u32x4){pk2(__builtin_fmaf(x0[0], bf_lo(z[0]), a0[0]), __builtin_fmaf(x0[1], bf_hi(z[0]), a0[1])),
                              pk2(__builtin_fmaf(x0[2], bf_lo(z[1]), a0[2]), __builtin_fmaf(x0[3], bf_hi(z[1]), a0[3])),
                              pk2(__builtin_fmaf(x1[0], bf_lo(z[2]), a1[0]), __builtin_fmaf(x1[1], bf_hi(z[2]), a1[1])),
                              pk2(__builtin_fmaf(x1[2], bf_lo(z[3]), a1[2]), __builtin_fmaf(x1[3], bf_hi(z[3]), a1[3]))};
  }
  STAMP_FLUSH(w < 4 ? 0 : (w < 8 ? 1 : (w < 11 ? 2 : 3)));
  asm volatile("fk_end:");
}

extern "C" void kernel_launch(void* const* d_in, const int* in_sizes, int n_in, void* d_out, int out_size,
                              void* d_ws, size_t ws_size, hipStream_t stream) {
  fused_kernel<<<256, 768, 0, stream>>>((const __bf16*)d_in[0], (const __bf16*)d_in[1], (const __bf16*)d_in[2],
                                        (const __bf16*)d_in[3], (const __bf16*)d_in[4], (const __bf16*)d_in[6],
                                        (const __bf16*)d_in[7], (const __bf16*)d_in[8], (const __bf16*)d_in[9],
                                        (const __bf16*)d_in[10], (const __bf16*)d_in[11], (__bf16*)d_out, (char*)d_ws);
}
```

```cpp
#include <hip/hip_runtime.h>
#include <hip/hip_bf16.h>
#include <stdint.h>
#define MM_PRIO 0

typedef __attribute__((ext_vector_type(2))) __bf16 bf16x2;
typedef __attribute__((ext_vector_type(4))) __bf16 bf16x4;
typedef __attribute__((ext_vector_type(8))) __bf16 bf16x8;
typedef __attribute__((ext_vector_type(16))) __bf16 bf16x16;
typedef __attribute__((ext_vector_type(4))) short s16x4;
typedef __attribute__((ext_vector_type(2))) float f32x2;
typedef __attribute__((ext_vector_type(4))) float f32x4;
typedef __attribute__((ext_vector_type(16))) float f32x16;
typedef __attribute__((ext_vector_type(32))) float f32x32;
typedef __attribute__((ext_vector_type(2))) unsigned u32x2;
typedef __attribute__((ext_vector_type(4))) unsigned u32x4;
#define LAS __attribute__((address_space(3)))
#define GAS __attribute__((address_space(1)))
typedef LAS char* lds_ptr;
typedef GAS unsigned gu32;
typedef GAS unsigned long long gu64;
#ifdef __HIP_DEVICE_COMPILE__
#define NOPK __attribute__((target("no-packed-fp32-ops")))
#else
#define NOPK
#endif
#define DEVI __device__ __forceinline__ NOPK
#define LAMI __attribute__((always_inline)) NOPK
#define TIDX __builtin_amdgcn_workitem_id_x()
#define BIDX __builtin_amdgcn_workgroup_id_x()
#define ALL(p) (__builtin_amdgcn_ballot_w64(!(p)) == 0ull)
#define UMIN(a, b) __builtin_elementwise_min((unsigned)(a), (unsigned)(b))
#define UMAX(a, b) __builtin_elementwise_max((unsigned)(a), (unsigned)(b))
#define RLX_AGENT __ATOMIC_RELAXED, __HIP_MEMORY_SCOPE_AGENT
#define RLX_WG __ATOMIC_RELAXED, __HIP_MEMORY_SCOPE_WORKGROUP

constexpr int S_ = 2048, D_ = 576, DH = 18;

constexpr int KV_GROUP = 9984;
constexpr int WS_KV = 0;
constexpr int WS_EPOCH = 192 * 1024;
constexpr int WS_FKV = WS_EPOCH + 1024;
constexpr int WS_FU = WS_FKV + 8192;
constexpr int WS_G = WS_FU + 32768;
constexpr int WS_KT = 320 * 1024;
constexpr int WS_UT = 640 * 1024;
constexpr int WS_Z = WS_UT + D_ * S_ * 2;
constexpr int WS_O = WS_Z + D_ * S_ * 2;
constexpr int WS_ZP = WS_O + D_ * S_ * 2;
constexpr int WS_END = WS_ZP + 64 * 4 * 8192;

constexpr int RSLOT = 2 * KV_GROUP;
constexpr int L_X = 0;
constexpr int L_KVP = 73728;
constexpr int L_B = 80000;
constexpr int L_M = 162944;
constexpr int CV_COPY = 4160;
constexpr int CV_H = L_B, CV_HB = 8 * CV_COPY, CV_U = L_B + 2 * CV_HB, CV_UB = 7680;
constexpr int L_Q = L_B + 8192;
constexpr int M_DONE = 0, M_C1 = 32, M_C2 = 36, M_C3 = 40, M_C3B = 44, M_KVC = 48, M_KVR = 52, M_UR = 56, M_OR = 60, M_RDY = 64,
              M_QD = 192, M_UD = 104, M_HS = 108, M_APR = 112, M_CVR = 116, M_CE = 124, M_PAIR = 128, M_TAGR = 144,
              M_TAG = 148, M_KN = 152, M_EXD = 156, M_KVP = 160, M_KN0 = 164, M_KD = 168, M_QR = 172,
              M_N2 = 176, M_AS = 196, M_SE = 204, M_ZS = 208, M_MM = 212, M_EQ = 216, M_G = 256;
constexpr int E_H = 24576, E_COPY = 1344, E_U = 35328, E_P = 43008;
constexpr int L_EX = 0;
constexpr int L_AP = CV_H + 512, L_ZT = CV_U;
#define CV_PRIO 3
#define CV_SLEEP 0
#define POLL_AT M_C2
#define POLL_N 3
#define BUBBLE 0
#define POLL_SLEEP 4
#define WO_INFL "2"
#define DMA_FIRST 0
#define ENTRY 0
#define H_PRIO 3
#define TCH 0
#define KV_DELAY 0
#define WO_EARLY 0
#define WQT 0
#define SH_C 0.f
#define SH_A 1.f
#define EP_DELAY 10
#define O_DELAY 20
#define Z_DELAY 12
constexpr unsigned SPIN_CAP = 1u << 18;

#define STAMP_DECL do {} while (0)
#define STAMP(I) do {} while (0)
#define STAMP_FLUSH(KID) do {} while (0)
#define RSTAMP(I) do {} while (0)

#define PSTAMP(I) do {} while (0)
#define NSTAMP(I) STAMP(I)
#define CSTAMP(I) do {} while (0)
#define HSTAMP(I) NSTAMP(I)

DEVI unsigned pk2(float a, float b) {
  bf16x2 v = {(__bf16)a, (__bf16)b};
  return __builtin_bit_cast(unsigned, v);
}
DEVI float u2f(unsigned u) { return __builtin_bit_cast(float, u); }
DEVI float bf_lo(unsigned u) { return __builtin_bit_cast(float, u << 16); }
DEVI float bf_hi(unsigned u) { return __builtin_bit_cast(float, u & 0xffff0000u); }
DEVI float dot2(unsigned u, float c) { const bf16x2 v = __builtin_bit_cast(bf16x2, u); return __builtin_amdgcn_fdot2_f32_bf16(v, v, c, false); }
DEVI float rbf(float a) { return (float)(__bf16)a; }
DEVI int swap23(int r) { return (r & ~12) | ((r & 4) << 1) | ((r & 8) >> 1); }
DEVI float xhalf(float v) {
  const unsigned u = __builtin_bit_cast(unsigned, v);
  const auto r = __builtin_amdgcn_permlane32_swap(u, u, false, false);
  return __builtin_bit_cast(float, (TIDX & 32) ? r[0] : r[1]);
}

template <int CTRL>
DEVI float dpp_max(float v) {
  const int u = __builtin_bit_cast(int, v);
  return __builtin_fmaxf(v, __builtin_bit_cast(float, __builtin_amdgcn_update_dpp(u, u, CTRL, 0xf, 0xf, false)));
}
DEVI float max16(float v) {
  return dpp_max<0x140>(dpp_max<0x141>(dpp_max<0x4e>(dpp_max<0xb1>(v))));
}
template <int CTRL>
DEVI float dpp_add(float v) {
  const int u = __builtin_bit_cast(int, v);
  return v + __builtin_bit_cast(float, __builtin_amdgcn_update_dpp(u, u, CTRL, 0xf, 0xf, false));
}
DEVI float sum16(float v) { return dpp_add<0x140>(dpp_add<0x141>(dpp_add<0x4e>(dpp_add<0xb1>(v)))); }
DEVI float wave_max(float v) {
  v = max16(v);
  const unsigned u = __builtin_bit_cast(unsigned, v);
  return __builtin_fmaxf(__builtin_fmaxf(u2f(__builtin_amdgcn_readlane(u, 0)), u2f(__builtin_amdgcn_readlane(u, 16))),
               __builtin_fmaxf(u2f(__builtin_amdgcn_readlane(u, 32)), u2f(__builtin_amdgcn_readlane(u, 48))));
}
DEVI void glds(const void* sbase, unsigned voff, unsigned lds_dst) {
  asm volatile("s_mov_b32 m0, %2\n\ts_nop 0\n\tglobal_load_lds_dwordx4 %0, %1" :: "v"(voff), "s"(sbase), "s"(lds_dst) : "memory");
}
DEVI void glds_sc1(const void* sbase, unsigned voff, unsigned lds_dst) {
  asm volatile("s_mov_b32 m0, %2\n\ts_nop 0\n\tglobal_load_lds_dwordx4 %0, %1 sc1" :: "v"(voff), "s"(sbase), "s"(lds_dst) : "memory");
}
#define GLDS4(SC, sbase_, v0_, v3_, dst_)                                                                                  \
  asm volatile("s_mov_b32 m0, %3\n\ts_nop 0\n\tglobal_load_lds_dwordx4 %0, %2" SC "\n\tglobal_load_lds_dwordx4 %0, %2 offset:1024" SC   \
               "\n\tglobal_load_lds_dwordx4 %0, %2 offset:2048" SC "\n\tglobal_load_lds_dwordx4 %1, %2 offset:3072" SC           \
               :: "v"(v0_), "v"(v3_), "s"(sbase_), "s"(dst_) : "memory")
#define GLDSX(src_, dst_)                                                                                                  \
  asm volatile("s_mov_b32 m0, %5\n\ts_nop 0\n\tglobal_load_lds_dwordx4 %0, %4\n\tglobal_load_lds_dwordx4 %1, %4 offset:1024\n\t"   \
               "global_load_lds_dwordx4 %2, %4 offset:2048\n\tglobal_load_lds_dwordx4 %3, %4 offset:3072"                 \
               :: "v"(vxe), "v"(vxo), "v"(vxe + 16384), "v"(vxo + 16384), "s"(src_), "s"(dst_) : "memory")
#define GLDSX2(src_, dst_)                                                                                                 \
  asm volatile("s_mov_b32 m0, %3\n\ts_nop 0\n\tglobal_load_lds_dwordx4 %0, %2\n\tglobal_load_lds_dwordx4 %1, %2 offset:1024"       \
               :: "v"(vxe), "v"(vxo), "s"(src_), "s"(dst_) : "memory")
#define GLDS3(STEP1, STEP2, v0_, v1_, v2_, src_, dst_)                                                                     \
  asm volatile("s_mov_b32 m0, %4\n\ts_nop 0\n\tglobal_load_lds_dwordx4 %0, %3\n\tglobal_load_lds_dwordx4 %1, %3 offset:" STEP1  \
               "\n\tglobal_load_lds_dwordx4 %2, %3 offset:" STEP2                                                        \
               :: "v"(v0_), "v"(v1_), "v"(v2_), "s"(src_), "s"(dst_) : "memory")
#define TOUCH(src_, v_) asm volatile("s_mov_b32 m0, %2\n\ts_nop 0\n\tglobal_load_lds_dword %0, %1 sc1" :: "v"(v_), "s"(src_), "s"(sbase + L_M + 512) : "memory")
#define VM_WAIT(N) asm volatile("s_waitcnt vmcnt(" #N ")" ::: "memory")
#define BAR() asm volatile("s_waitcnt lgkmcnt(0)\n\ts_barrier" ::: "memory")

DEVI bf16x8 tr_frag(lds_ptr p0, lds_ptr p1) {
  bf16x4 a = __builtin_amdgcn_ds_read_tr16_b64_v4bf16((LAS bf16x4*)p0);
  bf16x4 b = __builtin_amdgcn_ds_read_tr16_b64_v4bf16((LAS bf16x4*)p1);
  return __builtin_shufflevector(a, b, 0, 1, 2, 3, 4, 5, 6, 7);
}
DEVI bf16x8 lds_frag(lds_ptr p) { return *(LAS bf16x8*)p; }
DEVI void lds_signal(lds_ptr p, unsigned n = 1u) {
  asm volatile("s_waitcnt lgkmcnt(0)\n\ts_mov_b64 exec, 1\n\tds_add_u32 %0, %1\n\ts_mov_b64 exec, -1" :: "v"(p), "v"(n) : "memory");
}
DEVI unsigned lds_ticket(lds_ptr p) {
  unsigned old;
  asm volatile("s_waitcnt lgkmcnt(0)\n\ts_mov_b64 exec, 1\n\tds_add_rtn_u32 %0, %1, %2\n\ts_mov_b64 exec, -1\n\ts_waitcnt lgkmcnt(0)"
               : "=&v"(old) : "v"(p), "v"(1u) : "memory");
  return __builtin_amdgcn_readfirstlane(old);
}
template <int SLEEP = 0>
DEVI void lds_wait(lds_ptr p, unsigned n) {
  unsigned v, t;
  asm volatile("1:\n\tds_read_b32 %0, %2\n\ts_waitcnt lgkmcnt(0)\n\tv_readfirstlane_b32 %1, %0\n\ts_nop 0\n\ts_cmp_ge_u32 %1, %3\n\t"
               "s_cbranch_scc1 2f\n\ts_sleep %4\n\ts_branch 1b\n2:"
               : "=&v"(v), "=&s"(t) : "v"(p), "s"(n), "i"(SLEEP) : "scc", "memory");
}
DEVI unsigned lds_peek(lds_ptr p) {
  unsigned v;
  asm volatile("ds_read_b32 %0, %1\n\ts_waitcnt lgkmcnt(0)" : "=&v"(v) : "v"(p) : "memory");
  return __builtin_amdgcn_readfirstlane(v);
}
#define LD_SC1_16(off) __builtin_bit_cast(u32x4, __builtin_amdgcn_raw_buffer_load_b128(rs, (off), 0, 16))
#define ST_SC1_16(v, off) __builtin_amdgcn_raw_buffer_store_b128((v), rs, (off), 0, 16)
#define ST_SC1_8(v, off) __builtin_amdgcn_raw_buffer_store_b64((v), rs, (off), 0, 16)
#define ST_SC1_4(v, off) __builtin_amdgcn_raw_buffer_store_b32((v), rs, (off), 0, 16)

DEVI f32x16 lo16(f32x32 v) { return __builtin_shufflevector(v, v, 0, 1, 2, 3, 4, 5, 6, 7, 8, 9, 10, 11, 12, 13, 14, 15); }
DEVI f32x16 hi16(f32x32 v) { return __builtin_shufflevector(v, v, 16, 17, 18, 19, 20, 21, 22, 23, 24, 25, 26, 27, 28, 29, 30, 31); }
DEVI bf16x16 raw_lo(f32x16 v) { return __builtin_bit_cast(bf16x16, __builtin_shufflevector(v, v, 0, 1, 2, 3, 4, 5, 6, 7)); }
DEVI bf16x16 raw_hi(f32x16 v) { return __builtin_bit_cast(bf16x16, __builtin_shufflevector(v, v, 8, 9, 10, 11, 12, 13, 14, 15)); }
constexpr int SPIDX = (int)0xddddddddu;
#define MFMA32(a, b, c) __builtin_amdgcn_mfma_f32_32x32x16_bf16((a), (b), (c), 0, 0, 0)
#define MFMA16(a, b, c) __builtin_amdgcn_mfma_f32_16x16x32_bf16((a), (b), (c), 0, 0, 0)

DEVI void online_step(f32x16 s, float& m, f32x16& o, bf16x8 va, bf16x8 vb) {
  float t = s[0];
#pragma unroll
  for (int i = 1; i < 16; ++i) t = __builtin_fmaxf(t, s[i]);
  t = __builtin_fmaxf(t, xhalf(t));
  const float mn = __builtin_fmaxf(m, t);
  const float al = __builtin_amdgcn_exp2f(m - mn);
  m = mn;
#pragma unroll
  for (int i = 0; i < 16; ++i) { o[i] *= al; s[i] = __builtin_amdgcn_exp2f(s[i] - mn); }
  o = __builtin_amdgcn_smfmac_f32_32x32x32_bf16(va, raw_lo(s), o, SPIDX, 0, 0);
  o = __builtin_amdgcn_smfmac_f32_32x32x32_bf16(vb, raw_hi(s), o, SPIDX, 0, 0);
}

#define UNIT_LOAD(CP, TG)                                                                                        \
  const int uc0 = 72 * hg + 2 * (CP), ut0 = 64 * qt + 8 * (TG);                                                  \
  const u32x4 usw = *(const u32x4*)(shk + uc0 * 4);                                                              \
  unsigned ukr[11], uvr[8];                                                                                      \
  {                                                                                                              \
    const unsigned bo = (ut0 * D_ + uc0) * 2, bo3 = ut0 ? bo - 3 * 1152 : bo;                                    \
    _Pragma("unroll") for (int i = 0; i < 3; ++i) ukr[i] = *(const unsigned*)((const char*)bk + bo3 + i * 1152); \
    _Pragma("unroll") for (int i = 0; i < 8; ++i) {                                                              \
      ukr[3 + i] = *(const unsigned*)((const char*)bk + bo + i * 1152);                                          \
      uvr[i] = *(const unsigned*)((const char*)bv + bo + i * 1152);                                              \
    }                                                                                                            \
  }
#define UNIT_DO()                                                                                                \
  u32x4 o0, o1;                                                                                                  \
  {                                                                                                              \
    const f32x2 w3 = {bf_hi(usw[1]), bf_hi(usw[3])}, w2 = {bf_lo(usw[1]), bf_lo(usw[3])};                        \
    const f32x2 w1 = {bf_hi(usw[0]), bf_hi(usw[2])}, w0 = {bf_lo(usw[0]), bf_lo(usw[2])};                        \
    f32x2 kf[11], r[8];                                                                                          \
    _Pragma("unroll") for (int i = 0; i < 11; ++i) {                                                             \
      const unsigned kk = (i < 3 && !ut0) ? 0u : ukr[i];                                                         \
      kf[i] = (f32x2){bf_lo(kk), bf_hi(kk)};                                                                     \
    }                                                                                                            \
    _Pragma("unroll") for (int i = 0; i < 8; ++i)                                                                \
      r[i] = __builtin_elementwise_fma(kf[i + 3], w0, __builtin_elementwise_fma(kf[i + 2], w1,                   \
             __builtin_elementwise_fma(kf[i + 1], w2, kf[i] * w3))) * (f32x2){bf_lo(uvr[i]), bf_hi(uvr[i])};     \
    _Pragma("unroll") for (int i = 0; i < 4; ++i) {                                                              \
      o0[i] = pk2(r[7 - 2 * i][0], r[6 - 2 * i][0]);                                                             \
      o1[i] = pk2(r[7 - 2 * i][1], r[6 - 2 * i][1]);                                                             \
    }                                                                                                            \
  }

#define CV_LD(F, D) _Pragma("unroll") for (int s = 0; s < 2; ++s) {                                              \
    F[4 * s] = lds_frag(ab + 32 * (2 * (D) + 1 - s)); F[4 * s + 1] = lds_frag(bb + 80 * (7 - (D)) + 32 * s);     \
    F[4 * s + 2] = lds_frag(bb + 80 * (39 - (D)) + 32 * s); F[4 * s + 3] = lds_frag(ab + 32 * (2 * (D) + 65 - s)); } \
  __builtin_amdgcn_sched_barrier(0)
#define CV_MM(F) _Pragma("unroll") for (int s = 0; s < 2; ++s) {                                                 \
    y0 = MFMA32(F[4 * s], F[4 * s + 1], y0); y1 = MFMA32(F[4 * s], F[4 * s + 2], y1);                            \
    y1 = MFMA32(F[4 * s + 3], F[4 * s + 1], y1); }                                                               \
  __builtin_amdgcn_sched_barrier(0)
#define SB0 __builtin_amdgcn_sched_barrier(0)
#define CV_STEP(F, N, D) _Pragma("unroll") for (int s = 0; s < 2; ++s) {                                         \
    y0 = MFMA32(F[4 * s], F[4 * s + 1], y0); SB0;                                                                \
    N[4 * s] = lds_frag(ab + 32 * (2 * (D) + 1 - s)); N[4 * s + 1] = lds_frag(bb + 80 * (7 - (D)) + 32 * s); SB0; \
    y1 = MFMA32(F[4 * s], F[4 * s + 2], y1); SB0;                                                                \
    N[4 * s + 2] = lds_frag(bb + 80 * (39 - (D)) + 32 * s); SB0;                                                 \
    y1 = MFMA32(F[4 * s + 3], F[4 * s + 1], y1); SB0;                                                            \
    N[4 * s + 3] = lds_frag(ab + 32 * (2 * (D) + 65 - s)); SB0; }
#define CV_PUT(P) _Pragma("unroll") for (int g = 0; g < 4; ++g) {                                                \
    *(LAS f32x4*)((P) + g * 1024) = (f32x4){y0[4 * g], y0[4 * g + 1], y0[4 * g + 2], y0[4 * g + 3]};             \
    *(LAS f32x4*)((P) + 4096 + g * 1024) = (f32x4){y1[4 * g], y1[4 * g + 1], y1[4 * g + 2], y1[4 * g + 3]}; }
DEVI f32x4 cv_sum(lds_ptr p, lds_ptr u, float dv) {
  const f32x4 s = (*(LAS f32x4*)(p) + *(LAS f32x4*)(p + 8192)) + (*(LAS f32x4*)(p + 16384) + *(LAS f32x4*)(p + 24576));
  const u32x2 uu = *(LAS u32x2*)u;
  return (f32x4){__builtin_fmaf(bf_hi(uu[1]), dv, s[0]), __builtin_fmaf(bf_lo(uu[1]), dv, s[1]), __builtin_fmaf(bf_hi(uu[0]), dv, s[2]), __builtin_fmaf(bf_lo(uu[0]), dv, s[3])};
}

__global__ void __launch_bounds__(768, 3) NOPK
fused_kernel(const __bf16* __restrict__ x, const __bf16* __restrict__ wq, const __bf16* __restrict__ wk,
             const __bf16* __restrict__ wv, const __bf16* __restrict__ wo, const __bf16* __restrict__ bk,
             const __bf16* __restrict__ bv, const __bf16* __restrict__ shk, const __bf16* __restrict__ ssmk,
             const __bf16* __restrict__ ssmd, const __bf16* __restrict__ gw, __bf16* __restrict__ out, char* ws) {
  __shared__ __attribute__((aligned(16))) char smem[160 * 1024];
  STAMP_DECL; STAMP(0);
  const int tid = TIDX, lane = tid & 63;
  const int w = __builtin_amdgcn_readfirstlane(tid >> 6);
  const int bid = BIDX, jj = bid >> 3;
  const unsigned sbase = (unsigned)(size_t)smem;
  if (w == 0) {
    unsigned long long pc0;
    unsigned len;
    asm volatile("s_getpc_b64 %0\n\tfk_pc:\n\ts_mov_b32 %1, fk_end-fk_pc" : "=s"(pc0), "=s"(len));
    TOUCH(pc0, UMIN((unsigned)(lane * 8 + (jj & 7)) * 128u, len));
  }
  const int qt = (bid & 7) * 4 + (jj & 3), hg = jj >> 2;
  lds_ptr sm = (lds_ptr)smem;
  lds_ptr mi = sm + L_M;
  const int h = lane >> 5, l31 = lane & 31, l15 = lane & 15, g4 = lane >> 4;
  const int tq = (lane & 15) >> 2, tp = lane & 3;
  const auto rs = __builtin_amdgcn_make_buffer_rsrc(ws, 0, WS_END, 0x00020000);
  GAS char* gws = (GAS char*)ws;
  const int rot = (hg + qt) % 3;
  const int s0 = bid & 7;
  const int tq4 = hg >> 1, sel = hg & 1;
  const char* xt = (const char*)x + (size_t)qt * 73728;
  const char* wqs = (const char*)wq + 144 * hg;
  const unsigned vxe = (lane >> 3) * 1152 + (((lane & 7) ^ g4) << 4), vxo = 8192 + (lane >> 3) * 1152 + (((lane & 7) ^ (4 + g4)) << 4);
  const int otok = tid / 9, opc = tid - 9 * otok;

  u32x4 zv, xv;

  if (w == 4) *(LAS int*)(mi + lane * 4) = (lane == M_RDY / 4 + 2 || lane == M_RDY / 4 + 3) ? -8 : (lane == 0 ? -1 : 0);
  auto zscatter = [&](int n) LAMI {
    lds_ptr zp = sm + L_ZT + ((n & 7) * 72 + (n >> 3)) * 2;
#pragma unroll
    for (int e = 0; e < 8; ++e)
      *(LAS unsigned short*)(zp + e * 1152) = (unsigned short)((e & 1) ? zv[e >> 1] >> 16 : zv[e >> 1]);
    lds_signal(mi + M_ZS);
  };
  if (w >= 8 && w < 11) {
    const int j = w - 8;
    const int th = (j + rot) % 3;
    const unsigned stg = L_B + 2 * 27648 + j * 9216;
#pragma unroll
    for (int c = 0; c < 3; ++c) GLDSX2(xt + tq4 * 18432 + (3 * th + c) * 128, sbase + L_X + (3 * j + c) * 8192 + tq4 * 2048);
    {
      const char* wsrc = (const char*)(sel ? wv : wk) + th * 6912 - 2048;
      unsigned vo[3];
#pragma unroll
      for (int i = 0; i < 3; ++i) {
        const int f = 64 * i + lane, row = f / 3, pc = f - 3 * row;
        vo[i] = row * 36 + (pc == 2 ? 20 : 16 * pc) + 2048 - 1024 * i;
      }
#pragma unroll
      for (int p = 0; p < 3; ++p) GLDS3("1024", "2048", vo[0], vo[1], vo[2], wsrc + p * 2304, sbase + stg + p * 3072);
    }
    glds(gw + 192 * th, UMIN(lane, 23) * 16, sbase + L_KVP + j * 2048);
  }
  unsigned epoch = 0;
  if (w == 11) epoch = __hip_atomic_load((gu32*)(gws + WS_EPOCH), RLX_AGENT);
  PSTAMP(5);
  BAR();
  PSTAMP(6);
  if (w >= 5 && w < 8) {
    const int wi = w - 5;
    unsigned vw[3];
#pragma unroll
    for (int m = 0; m < 3; ++m) {
      const int f = 64 * (3 * wi + m) + lane, row = f / 9;
      vw[m] = row * 1152 + (f - 9 * row) * 16 - 1024 * m;
    }
    const char* xs = xt + (1 - (tq4 >> 1)) * 36864 + wi * 128;
    const char* xs2 = xt + (tq4 ^ 1) * 18432 + wi * 128;
    const unsigned xd = sbase + L_X + wi * 8192 + (1 - (tq4 >> 1)) * 4096, xd2 = sbase + L_X + wi * 8192 + (tq4 ^ 1) * 2048;
    const unsigned wd = sbase + L_B + wi * 3072;
    const int t1 = (rot + 1) % 3, t2 = (rot + 2) % 3;
    GLDSX(xs + rot * 384, xd);
    GLDSX2(xs2 + rot * 384, xd2);
#pragma unroll
    for (int per = 0; per < 3; ++per) GLDS3("1024", "2048", vw[0], vw[1], vw[2], wqs + (192 * rot + 64 * per) * 1152, wd + per * 9216);
    GLDSX(xs + t1 * 384, xd + 24576);
    GLDSX2(xs2 + t1 * 384, xd2 + 24576);
    GLDS3("1024", "2048", vw[0], vw[1], vw[2], wqs + 192 * t1 * 1152, wd + 27648);
    VM_WAIT(9);
    lds_signal(mi + M_C1);
#pragma unroll
    for (int per = 1; per < 3; ++per) GLDS3("1024", "2048", vw[0], vw[1], vw[2], wqs + (192 * t1 + 64 * per) * 1152, wd + 27648 + per * 9216);
    GLDSX(xs + t2 * 384, xd + 49152);
    GLDSX2(xs2 + t2 * 384, xd2 + 49152);
    lds_wait(mi + M_KVC, 3);
    GLDS3("1024", "2048", vw[0], vw[1], vw[2], wqs + 192 * t2 * 1152, wd + 55296);
    VM_WAIT(9);
    lds_signal(mi + M_C2);
#pragma unroll
    for (int per = 1; per < 3; ++per) GLDS3("1024", "2048", vw[0], vw[1], vw[2], wqs + (192 * t2 + 64 * per) * 1152, wd + 55296 + per * 9216);
    STAMP(1);
    VM_WAIT(0);
    lds_signal(mi + M_C3);
    lds_signal(mi + M_SE);
  }

  if (w >= 8) {
    const int j = w - 8, ht = tid - 512;
    __builtin_amdgcn_s_setprio(3);
    if (j < 3) {
      const unsigned stg = L_B + 2 * 27648 + j * 9216;
      lds_ptr wb = sm + stg + (8 * g4 + tq) * 48 + tp * 8;
      lds_ptr wc = sm + stg + (8 * g4 + tq) * 48 + 32 + (tp & 1) * 8;
      lds_ptr xb = sm + L_X + 3 * j * 8192 + (16 * tq4 + l15) * 128;
      VM_WAIT(0);
      STAMP(1);
      bf16x8 fa[6], fc[6], fb[6], fg[6];
#pragma unroll
      for (int k = 0; k < 6; ++k) {
        fg[k] = lds_frag(sm + L_KVP + j * 2048 + k * 64 + g4 * 16);
        fb[k] = lds_frag(xb + (k >> 1) * 8192 + (((4 * (k & 1) + g4) ^ (l15 >> 1)) << 4));
        fa[k] = tr_frag(wb + k * 1536, wb + k * 1536 + 192);
        fc[k] = tr_frag(wc + k * 1536, wc + k * 1536 + 192);
      }
      f32x4 acc = {0}, acr = {0};
      if (sel) {
#pragma unroll
        for (int k = 0; k < 6; ++k) { acc = MFMA16(fb[k], fa[k], acc); acr = MFMA16(fb[k], fc[k], acr); }
      } else {
#pragma unroll
        for (int k = 0; k < 6; ++k) { acc = MFMA16(fa[k], fb[k], acc); acr = MFMA16(l15 == 8 ? fg[k] : fc[k], fb[k], acr); }
      }
      *(LAS f32x4*)(sm + L_KVP + j * 2048 + lane * 16) = acc;
      *(LAS f32x4*)(sm + L_KVP + j * 2048 + 1024 + lane * 16) = acr;
      lds_signal(mi + M_KVC);
      lds_signal(mi + M_C1);
      STAMP(2);
      lds_wait<1>(mi + M_KD, 1);
    } else {
      const unsigned tg = __builtin_amdgcn_readfirstlane(epoch) + 1u;
      if (lane == 0) *(LAS unsigned*)(mi + M_TAG) = tg;
      const int T = 2 * qt + (tq4 >> 1), mt = tq4 & 1, i4 = T & 3, gb = (T >> 2) * KV_GROUP, rp = swap23(16 * mt + l15);
      int sa, sr;
      bool st;
      if (sel) {
        sa = gb + 4096 + i4 * 1216 + mt * 608 + (l15 + 18 * (g4 & 1)) * 16 + (g4 >> 1) * 8;
        sr = gb + 4096 + i4 * 1216 + mt * 608 + (l15 < 8 ? 10 + l15 + 18 * (g4 & 1) : 36 + (g4 & 1)) * 16 + (g4 >> 1) * 8;
        st = l15 >= 6 && l15 <= 8;
      } else {
        sa = gb + i4 * 1024 + (rp + 32 * (g4 & 1)) * 16 + (g4 >> 1) * 8;
        sr = gb + 8960 + i4 * 256 + rp * 8;
        st = g4 == 1;
      }
      lds_wait(mi + M_KVC, 3);
      lds_ptr pp = sm + L_KVP + lane * 16;
      const float ks = sel ? 1.f : 0.23570226039551584f * 1.4426950408889634f;
      const f32x4 va = (*(LAS f32x4*)(pp) + *(LAS f32x4*)(pp + 2048) + *(LAS f32x4*)(pp + 4096)) * ks;
      const f32x4 vs = *(LAS f32x4*)(pp + 1024) + *(LAS f32x4*)(pp + 3072) + *(LAS f32x4*)(pp + 5120), vr = vs * ks;
      const u32x2 a = {pk2(va[0], va[1]), pk2(va[2], va[3])};
      u32x2 r = {pk2(vr[0], vr[1]), pk2(vr[2], vr[3])};
      if (!sel) r = (u32x2){r[1], 0x3f803f80u};
      else if (l15 == 8) r[0] = r[1] = (g4 & 1) ? 0u : 0x3f803f80u;
      ST_SC1_16(((u32x4){a[0], tg, a[1], tg}), WS_KT + 2 * sa);
      if (st) ST_SC1_16(((u32x4){r[0], tg, r[1], tg}), WS_KT + 2 * sr);
      ST_SC1_8(a, WS_KV + sa);
      if (st) ST_SC1_8(r, WS_KV + sr);
      STAMP(1);
      lds_signal(mi + M_KD);
      if (!sel && g4 == 2) {
        const float g = rbf(__builtin_amdgcn_rcpf(1.f + __builtin_amdgcn_exp2f(-1.4426950408889634f * rbf(vs[0]))));
        ST_SC1_4(__builtin_bit_cast(unsigned, g), WS_G + (64 * qt + 16 * tq4 + l15) * 4);
      }
      float n2 = dot2(a[1], dot2(a[0], st ? dot2(r[0], 0.f) : 0.f));
      n2 += xhalf(n2);
      n2 += u2f(__builtin_amdgcn_ds_swizzle(__builtin_bit_cast(int, n2), 0x401f));
      n2 = sel ? 0.f : max16(n2);
      VM_WAIT(0);
      if (lane == 0)
        __hip_atomic_store((gu64*)(gws + WS_FKV + T * 128 + (hg & 3) * 8), ((unsigned long long)tg << 32) | __builtin_bit_cast(unsigned, n2), RLX_AGENT);
      STAMP(2);
    }
    const unsigned tag = *(LAS unsigned*)(mi + M_TAG);
    auto kv_tagged = [&](int st) LAMI {
      u32x4 r[10];
      const int o = WS_KT + ((s0 + st) & 7) * (2 * RSLOT) + lane * 16;
#pragma unroll 1
      for (unsigned spins = 0; spins < SPIN_CAP; ++spins) {
        unsigned bad = 0;
#pragma unroll
        for (int i = 0; i < 10; ++i) r[i] = LD_SC1_16(o + (i == 9 && j == 3 ? 38 : 4 * i + j) * 1024);
#pragma unroll
        for (int i = 0; i < 10; ++i) bad |= (r[i][1] ^ tag) | (r[i][3] ^ tag);
        asm volatile("" ::: "memory");
        if (ALL(bad == 0)) break;
        __builtin_amdgcn_s_sleep(POLL_SLEEP);
      }
      if (st == 0) {
        if (j == 0) {
          const float kn = dot2(r[0][2], dot2(r[0][0], 0.f));
          *(LAS float*)(mi + M_KN0) = wave_max(dpp_add<0xb1>(kn));
        }
        lds_wait(mi + M_C3B, 5);
      }
#pragma unroll
      for (int i = 0; i < 10; ++i)
        *(LAS u32x2*)(sm + st * RSLOT + (i == 9 && j == 3 ? 38 : 4 * i + j) * 512 + lane * 8) = (u32x2){r[i][0], r[i][2]};
    };
    auto kv_dma = [&](int st) LAMI {
      const char* src = ws + WS_KV + ((s0 + st) & 7) * RSLOT + j * 4992;
      const unsigned dst = sbase + st * RSLOT + j * 4992;
      GLDS4(" sc1", src, lane * 16, lane * 16, dst);
      if (lane < 56) glds_sc1(src + 4096, lane * 16, dst + 4096);
    };
    const unsigned vh3 = (lane == 63 ? 62 : lane) * 16;
    auto hload = [&](int cl, int b) LAMI {
      const char* hsrc = (const char*)(ssmk + (size_t)(72 * hg + cl) * S_) + 4 * j + 2;
      const unsigned dst = sbase + CV_H + b * CV_HB + 2 * j * CV_COPY + 64;
      GLDS4("", hsrc, lane * 16, vh3, dst);
      GLDS4("", hsrc + 2, lane * 16, vh3, dst + CV_COPY);
    };
    auto uload = [&](int cl) LAMI {
      const int tgq = ht & 7;
      const int o = cl < 64 ? (tgq >> 1) * 2048 + (cl & 1) * 1024 + ((tgq & 1) * 32 + (cl >> 1)) * 16
                            : 8192 + (cl & 1) * 512 + (tgq * 4 + ((cl - 64) >> 1)) * 16;
      return LD_SC1_16(WS_UT + ((ht >> 3) * 8 + hg) * 9216 + o);
    };
    const int ce = 64 + (qt >> 2);
    const unsigned short h8a = *(const unsigned short*)(ssmk + (size_t)(72 * hg + 2 * qt + (j >> 1)) * S_ + (lane & 7));
    const unsigned short h8e = *(const unsigned short*)(ssmk + (size_t)(72 * hg + ce) * S_ + (lane & 7));
    const float dve = (qt & 3) ? 0.f : (float)ssmd[72 * hg + ce];
    {
      lds_wait<1>(mi + POLL_AT, POLL_N);
      kv_tagged(0);
      lds_signal(mi + M_QD);
      STAMP(3);
      kv_tagged(1);
      lds_signal(mi + M_RDY + 4, 2);
      if (j == 3) {
        float kn = 0.f;
#pragma unroll 1
        for (unsigned spins = 0; spins < SPIN_CAP; ++spins) {
          const u32x4 v0 = LD_SC1_16(WS_FKV + lane * 128), v1 = LD_SC1_16(WS_FKV + lane * 128 + 16);
          const bool ok = v0[1] == tag && v0[3] == tag && v1[1] == tag && v1[3] == tag;
          kn = __builtin_fmaxf(u2f(v0[0]), u2f(v1[0]));
          asm volatile("" ::: "memory");
          if (ALL(ok)) break;
          __builtin_amdgcn_s_sleep(POLL_SLEEP);
        }
        lds_signal(mi + M_QR);
        lds_signal(mi + M_DONE);
        kn = wave_max(kn);
        if (lane == 0) *(LAS float*)(mi + M_KN) = kn;
      }
      lds_wait<1>(mi + M_QR, 9);
      kv_dma(2);
      kv_dma(3);
      VM_WAIT(0);
      lds_signal(mi + M_RDY + 8, 2);
      lds_signal(mi + M_RDY + 12, 2);
      __builtin_amdgcn_s_setprio(H_PRIO);
      UNIT_LOAD(l31, 2 * j + h);
      if (j == 1) {
        const int c0 = 72 * hg + 64 + 2 * (lane & 3), t0 = 64 * qt + 4 * (lane >> 2);
        const u32x4 sw = *(const u32x4*)(shk + c0 * 4);
        const unsigned bo = (t0 * D_ + c0) * 2, bo3 = t0 ? bo - 3 * 1152 : bo;
        unsigned kk[7], vv[4];
#pragma unroll
        for (int i = 0; i < 7; ++i) kk[i] = *(const unsigned*)((const char*)bk + (i < 3 ? bo3 + i * 1152 : bo + (i - 3) * 1152));
#pragma unroll
        for (int i = 0; i < 4; ++i) vv[i] = *(const unsigned*)((const char*)bv + bo + i * 1152);
        if (!t0) kk[0] = kk[1] = kk[2] = 0u;
        float ra[4], rb[4];
#pragma unroll
        for (int i = 0; i < 4; ++i) {
          ra[i] = __builtin_fmaf(bf_lo(kk[i + 3]), bf_lo(sw[0]), __builtin_fmaf(bf_lo(kk[i + 2]), bf_hi(sw[0]),
                  __builtin_fmaf(bf_lo(kk[i + 1]), bf_lo(sw[1]), bf_lo(kk[i]) * bf_hi(sw[1])))) * bf_lo(vv[i]);
          rb[i] = __builtin_fmaf(bf_hi(kk[i + 3]), bf_lo(sw[2]), __builtin_fmaf(bf_hi(kk[i + 2]), bf_hi(sw[2]),
                  __builtin_fmaf(bf_hi(kk[i + 1]), bf_lo(sw[3]), bf_hi(kk[i]) * bf_hi(sw[3])))) * bf_hi(vv[i]);
        }
        const int uo = WS_UT + (qt * 8 + hg) * 9216 + 8192 + ((lane >> 3) * 4 + (lane & 3)) * 16 + (1 - ((lane >> 2) & 1)) * 8;
        ST_SC1_8(((u32x2){pk2(ra[3], ra[2]), pk2(ra[1], ra[0])}), uo);
        ST_SC1_8(((u32x2){pk2(rb[3], rb[2]), pk2(rb[1], rb[0])}), uo + 512);
      }
      VM_WAIT(0);
      if (j < 3) TOUCH((const char*)wo + (jj * 162 + j * 54) * 128, UMIN(lane, 53) * 128u);
      else { TOUCH(ssmk + (size_t)(72 * hg + 2 * qt) * S_, lane * 128u); TOUCH(ssmk + (size_t)(72 * hg + ce) * S_, l31 * 128u); }
      UNIT_DO();
      ST_SC1_16(o0, WS_UT + (qt * 8 + hg) * 9216 + j * 2048 + lane * 16);
      ST_SC1_16(o1, WS_UT + (qt * 8 + hg) * 9216 + j * 2048 + 1024 + lane * 16);
      {
        const u32x4 ones = {~0u, ~0u, ~0u, ~0u};
        const int ob = WS_O + 2 * qt * 36 * 1024 + (4 * hg + j) * 1024 + lane * 16;
        ST_SC1_16(ones, ob);
        ST_SC1_16(ones, ob + 36 * 1024);
        if (j == 0) ST_SC1_16(ones, WS_O + (2 * qt + h) * 36 * 1024 + 32768 + hg * 512 + l31 * 16);
        ST_SC1_16(ones, WS_Z + (72 * hg + 2 * qt) * 4096 + ht * 16);
        ST_SC1_16(ones, WS_Z + (72 * hg + 2 * qt + 1) * 4096 + ht * 16);
        ST_SC1_16(ones, WS_ZP + (hg * 32 + qt) * 8192 + ht * 16);
        ST_SC1_16(ones, WS_ZP + (hg * 32 + qt) * 8192 + 4096 + ht * 16);
      }
      STAMP(4);
      VM_WAIT(0);
      if (lds_ticket(mi + M_UD) == 3u && lane == 0) __hip_atomic_store((gu32*)(gws + WS_FU) + (hg * 32 + qt) * 32, tag, RLX_AGENT);
    }
    auto cvprep = [&](int b, unsigned short h8) LAMI {
      *(LAS u32x4*)(sm + CV_U + (ht >> 7) * CV_UB + ((ht & 127) >> 2) * 80 + (ht & 3) * 16) = (u32x4){0, 0, 0, 0};
      lds_ptr hz = sm + CV_H + b * CV_HB + (lane >> 3) * CV_COPY;
      *(LAS u32x2*)(hz + (lane & 7) * 8) = (u32x2){0, 0};
      if ((lane & 7) <= (lane >> 3)) *(LAS unsigned short*)(hz + 48 + (7 - (lane >> 3) + (lane & 7)) * 2) = h8;
    };
    unsigned hs = 0;
#define HSYNC() do { lds_signal(mi + M_HS); hs += 4; lds_wait<2>(mi + M_HS, hs); } while (0)
    if (lane < 54) {
      const unsigned vo = (lane / 9) * 1152 + (lane % 9) * 16;
#pragma unroll 1
      for (int p = 24 * j; p < 24 * j + 24; p += 4)
        asm volatile("s_mov_b32 m0, %5\n\ts_nop 0\n\tglobal_load_lds_dwordx4 %0, %4\n\tglobal_load_lds_dwordx4 %1, %4 offset:864\n\t"
                     "global_load_lds_dwordx4 %2, %4 offset:1728\n\tglobal_load_lds_dwordx4 %3, %4 offset:2592\n\ts_waitcnt vmcnt(" WO_INFL ")"
                     :: "v"(vo), "v"(vo + 6048), "v"(vo + 12096), "v"(vo + 18144), "s"((const char*)wo + 144 * hg + p * 6912),
                        "s"(sbase + L_B + p * 864) : "memory");
    }
    VM_WAIT(0);
    HSYNC();
    bf16x8 wf[18], we[5];
    {
      const int cb = (32 * (j & 1) + 16 * (g4 & 1) + 4 * tp) * 2;
      lds_ptr wm = sm + L_B + (DH * (j >> 1) + 4 * h + tq) * 144 + cb;
      lds_ptr wr = sm + L_B + (DH * (8 * (j >> 1) + 4 * h + (tq >> 1)) + 16 + (tq & 1)) * 144 + cb;
      asm volatile("" : "+v"(wm), "+v"(wr));
#pragma unroll
      for (int k = 0; k < 12; ++k) wf[k] = tr_frag(wm + k * 5184, wm + k * 5184 + 1152);
      wm += 12 * 5184;
      asm volatile("" : "+v"(wm));
#pragma unroll
      for (int k = 0; k < 4; ++k) wf[12 + k] = tr_frag(wm + k * 5184, wm + k * 5184 + 1152);
#pragma unroll
      for (int k = 0; k < 2; ++k) wf[16 + k] = tr_frag(wr + k * 41472, wr + k * 41472 + 5184);
      lds_ptr em = sm + L_B + (DH * (8 * j + (g4 >> 1)) + 4 * (g4 & 1) + tq) * 144 + (64 + 4 * (tp & 1)) * 2;
      lds_ptr er = sm + L_B + (DH * (16 * (j & 1) + 4 * g4 + (tq >> 1)) + 16 + (tq & 1)) * 144 + (64 + 4 * (tp & 1)) * 2;
      asm volatile("" : "+v"(em), "+v"(er));
#pragma unroll
      for (int k = 0; k < 4; ++k) we[k] = tr_frag(em + k * 5184, em + k * 5184 + 1152);
      we[4] = tr_frag(er, er + 5184);
    }
    HSYNC();
    HSTAMP(7);
    cvprep(j >> 1, h8a);
    if (j == 3) {
#pragma unroll 1
      for (unsigned spins = 0; spins < SPIN_CAP; ++spins) {
        bool ok = lane >= 40 || __hip_atomic_load((gu32*)(gws + WS_FU) + (h ? l31 * 32 + qt : hg * 32 + lane) * 32, RLX_AGENT) == tag;
        if (bid == 0) {
#pragma unroll
          for (int i = 0; i < 4; ++i) ok &= __hip_atomic_load((gu32*)(gws + WS_FU) + (64 * i + lane) * 32, RLX_AGENT) == tag;
        }
        if (ALL(ok)) break;
        __builtin_amdgcn_s_sleep(8);
      }
      if (bid == 0 && lane == 0) __hip_atomic_store((gu32*)(gws + WS_EPOCH), tag, RLX_AGENT);
      lds_signal(mi + M_UR);
    } else {
      lds_wait<4>(mi + M_UR, 1);
    }
    NSTAMP(5);
    __builtin_amdgcn_s_setprio(3);
    lds_wait<8>(mi + M_DONE + 16, 8);
    hload(2 * qt, 0);
    hload(2 * qt + 1, 1);
    if (j == 1) *(LAS unsigned*)(mi + M_G + lane * 4) = __builtin_amdgcn_raw_buffer_load_b32(rs, WS_G + (64 * qt + lane) * 4, 0, 16);
    *(LAS u32x4*)(sm + CV_U + 2560 + (ht >> 2) * 80 + (ht & 3) * 16) = uload(2 * qt);
    *(LAS u32x4*)(sm + CV_U + CV_UB + 2560 + (ht >> 2) * 80 + (ht & 3) * 16) = uload(2 * qt + 1);
    const u32x4 ue = uload(ce);
    if (j == 0) xv = *(const u32x4*)(xt + otok * 1152 + 144 * hg + opc * 16);
    VM_WAIT(0);
#pragma unroll
    for (int i = 0; i < 4; ++i) lds_signal(mi + M_PAIR + 4 * i);
    NSTAMP(6);
    {
      const int p = qt & 3;
      lds_wait<8>(mi + M_DONE + 20, 8);
      if (lane < 35) {
        const char* hsrc = (const char*)(ssmk + (size_t)(72 * hg + ce) * S_) + 4 * j + 2 + 512 * p - 64;
        const unsigned dst = sbase + E_H + 2 * j * E_COPY;
        glds(hsrc + 2048, lane * 16, dst + 640);
        glds(hsrc + 2050, lane * 16, dst + E_COPY + 640);
        if (p || lane >= 4) {
          glds(hsrc, lane * 16, dst);
          glds(hsrc + 2, lane * 16, dst + E_COPY);
        }
      }
      if (!p && j == 0) {
        lds_ptr hz = sm + E_H + (lane >> 3) * E_COPY;
        *(LAS u32x2*)(hz + (lane & 7) * 8) = (u32x2){0, 0};
        if ((lane & 7) <= (lane >> 3)) *(LAS unsigned short*)(hz + 48 + (7 - (lane >> 3) + (lane & 7)) * 2) = h8e;
      }
      if (ht < 160) *(LAS u32x4*)(sm + E_U + ht * 16) = (u32x4){0, 0, 0, 0};
      lds_ptr ab = sm + E_H + (l31 & 7) * E_COPY + (h + (l31 >> 3)) * 16 + 128 * j;
      lds_ptr bb = sm + E_U + 2560 + (l31 - 8 * p - 2 * j - 1) * 80 + (1 - h) * 16;
      asm volatile("" : "+v"(ab), "+v"(bb));
      lds_wait<8>(mi + M_DONE + 24, 8);
      *(LAS u32x4*)(sm + E_U + 2560 + (ht >> 2) * 80 + (ht & 3) * 16) = ue;
      VM_WAIT(0);
      lds_signal(mi + M_DONE + 28);
      lds_wait<2>(mi + M_DONE + 28, 12);
      f32x16 y0 = {0}, y1 = {0};
#pragma unroll
      for (int d = 0; d < 2; ++d) {
#pragma unroll
        for (int s = 0; s < 2; ++s) {
          const bf16x8 a0 = lds_frag(ab + 32 * (2 * d + 1 - s)), b0 = lds_frag(bb + 80 * (1 - d) + 32 * s);
          const bf16x8 b1 = lds_frag(bb + 80 * (33 - d) + 32 * s), a1 = lds_frag(ab + 640 + 32 * (2 * d + 1 - s));
          y0 = MFMA32(a0, b0, y0);
          y1 = MFMA32(a0, b1, y1);
          y1 = MFMA32(a1, b0, y1);
          SB0;
        }
      }
      __builtin_amdgcn_s_setprio(1);
      __builtin_amdgcn_s_sleep(EP_DELAY);
      CV_PUT(sm + E_P + j * 8192 + lane * 16);
      HSYNC();
#pragma unroll
      for (int q = 0; q < 2; ++q) {
        u32x4 t = __builtin_bit_cast(u32x4, cv_sum(sm + E_P + (2 * j + q) * 1024 + lane * 16,
                                                   sm + E_U + 2560 + (32 * (j >> 1) + l31) * 80 + (2 * (j & 1) + q) * 16 + (1 - h) * 8, dve));
#pragma unroll
        for (int e = 0; e < 4; ++e) t[e] = UMIN(t[e], 0xfffffffeu);
        ST_SC1_16(t, WS_ZP + (hg * 32 + qt) * 8192 + (2 * j + q) * 1024 + lane * 16);
      }
      lds_signal(mi + M_EXD);
    }
    HSTAMP(8);
    __builtin_amdgcn_s_setprio(1);
#pragma unroll 1
    for (int t = 0; t < 2; ++t) {
      lds_ptr ob = sm + (36 * t + (j >> 1)) * 1024 + lane * 16;
      lds_ptr oe = sm + (36 * t + 8 * j + (g4 >> 1)) * 1024 + (l15 + 32 * (g4 & 1)) * 16;
      lds_ptr or_ = sm + (36 * t + 32 + 2 * (j & 1) + (g4 >> 1)) * 1024 + (16 * (j >> 1) + l15 + 32 * (g4 & 1)) * 16;
      lds_wait<1>(mi + (t ? M_OR : M_KVR), 4);
      if (t) NSTAMP(9);
      f32x16 a0 = {0};
      f32x4 e0 = {0}, e1 = {0};
      bf16x8 f[27];
#pragma unroll
      for (int k = 0; k < 31; ++k) {
        if (k < 18) f[k] = lds_frag(ob + k * 2048);
        else if (k < 26) f[k] = lds_frag(oe + ((k - 18) >> 2) * 256 + ((k - 18) & 3) * 2048);
        else if (k == 26) f[k] = lds_frag(or_);
        SB0;
        const int m = k - 4;
        if (m >= 0 && m < 18) a0 = MFMA32(wf[m], f[m], a0);
        else if (m >= 18 && m < 22) e0 = MFMA16(we[m - 18], f[m], e0);
        else if (m >= 22 && m < 26) e1 = MFMA16(we[m - 22], f[m], e1);
        else if (m == 26) { if (j >> 1) e1 = MFMA16(we[4], f[m], e1); else e0 = MFMA16(we[4], f[m], e0); }
        SB0;
      }
      if (t) NSTAMP(10);
      lds_wait<1>(mi + M_MM, 4);
      lds_ptr ap = sm + L_AP + (8 * (j >> 1) + 4 * t) * CV_COPY;
#pragma unroll
      for (int g = 0; g < 4; ++g)
        *(LAS f32x4*)(ap + (l31 >> 3) * CV_COPY + (l31 & 7) * 336 + (j & 1) * 128 + h * 16 + g * 32) = (f32x4){a0[4 * g], a0[4 * g + 1], a0[4 * g + 2], a0[4 * g + 3]};
      if (!h) {
        lds_ptr ae = ap + (l15 >> 3) * CV_COPY + (l15 & 7) * 336 + 256 + (j & 1) * 32 + g4 * 16;
        *(LAS f32x4*)(ae) = e0;
        *(LAS f32x4*)(ae + 2 * CV_COPY) = e1;
      }
    }
    lds_signal(mi + M_APR);
  } else {
    if (w < 4) {
      const int gt = w >> 1, tt = w & 1, arow = 32 * tt + l31, xsw = (arow >> 1) & 7;
      lds_ptr xb = sm + L_X + arow * 128;
      lds_ptr wb = sm + L_B + (8 * h + tq) * 144 + (36 * gt + 20 * (g4 & 1) + 4 * tp) * 2;
      f32x16 acc = {0};
#pragma unroll 1
      for (int p = 0; p < 3; ++p) {
        if (p == 1) CSTAMP(6);
        if (p == 2) CSTAMP(8);
        lds_wait(mi + M_C1 + 4 * p, p == 1 ? 3 : 6 + p);
        if (p == 0) CSTAMP(5);
        if (p == 1) CSTAMP(7);
        if (p == 2) CSTAMP(9);
        bf16x8 fa[12], fb[12];
#pragma unroll
        for (int k = 0; k < 16; ++k) {
          if (k < 12) {
            fa[k] = tr_frag(wb + k * 2304, wb + k * 2304 + 576);
            fb[k] = lds_frag(xb + (k >> 2) * 8192 + (((2 * (k & 3) + h) ^ xsw) << 4));
          }
          SB0;
          if (k >= 4) acc = MFMA32(fa[k - 4], fb[k - 4], acc);
          SB0;
        }
        if (p < 2) lds_signal(mi + M_C3 + 4 * p);
        xb += 24576;
        wb += 27648;
      }
      STAMP(2);
      u32x4 a;
#pragma unroll
      for (int i = 0; i < 4; ++i) a[i] = pk2(acc[2 * i], acc[2 * i + 1]);
      lds_ptr qe = sm + L_Q + (4 * gt + tt) * 1024;
      *(LAS u32x4*)(qe + lane * 16) = a;
      lds_ptr qo = qe + 2048 + l31 * 16;
      *(LAS unsigned*)(qo + h * 512 + 4) = pk2(acc[8], acc[9]);
      *(LAS unsigned*)(qo + (h ? 8 : 512)) = pk2(acc[10], acc[11]);
      *(LAS unsigned*)(qo + h * 512 + 12) = pk2(acc[12], acc[13]);
      *(LAS unsigned*)(h ? sm + L_Q + 8192 + ((2 * gt + 1) * 64 + arow) * 4 : qo + 520) = pk2(acc[14], acc[15]);
    } else if (w == 4) {
      __builtin_amdgcn_s_setprio(2);
      lds_ptr xb = sm + L_X + l15 * 128;
      lds_ptr wb = sm + L_B + (8 * g4 + tq) * 144 + (tp == 1 ? 104 : 32);
      f32x4 acc[4] = {{0}, {0}, {0}, {0}};
#pragma unroll 1
      for (int p = 0; p < 3; ++p) {
        lds_wait(mi + M_C1 + 4 * p, p == 1 ? 3 : 6 + p);
        bf16x8 fa[6], fb[24];
#pragma unroll
        for (int k = 0; k < 8; ++k) {
          if (k < 6) {
            fa[k] = tr_frag(wb + k * 4608, wb + k * 4608 + 576);
#pragma unroll
            for (int t = 0; t < 4; ++t) fb[4 * k + t] = lds_frag(xb + (k >> 1) * 8192 + t * 2048 + (((4 * (k & 1) + g4) ^ (l15 >> 1)) << 4));
          }
          SB0;
          if (k >= 2) {
#pragma unroll
            for (int t = 0; t < 4; ++t) acc[t] = MFMA16(fa[k - 2], fb[4 * (k - 2) + t], acc[t]);
          }
          SB0;
        }
        if (p < 2) lds_signal(mi + M_C3 + 4 * p);
        xb += 24576;
        wb += 27648;
      }
      STAMP(2);
      if (g4 < 2) {
#pragma unroll
        for (int t = 0; t < 4; ++t) {
          const int tok = 16 * t + l15;
          *(LAS unsigned*)(sm + L_Q + 8192 + (2 * g4 * 64 + tok) * 4) = pk2(acc[t][0], acc[t][1]);
          *(LAS unsigned*)(sm + L_Q + ((2 * g4 + 1) * 2 + (t >> 1)) * 1024 + (tok & 31) * 16) = pk2(acc[t][2], acc[t][3]);
        }
      }
    }
    lds_signal(mi + M_QD);
    __builtin_amdgcn_s_setprio(2);
    const int hl = w & 3, kh = w >> 2;
    const int voff = 4096 + (l31 < 18 ? h * 288 + l31 * 16 : (l31 == 18 ? 576 : 592));
    lds_wait(mi + M_QD, 12);
    STAMP(3);
    const u32x4 q0u = *(LAS u32x4*)(sm + L_Q + (2 * hl) * 1024 + lane * 16);
    const u32x4 q1u = *(LAS u32x4*)(sm + L_Q + (2 * hl + 1) * 1024 + lane * 16);
    const unsigned qr = *(LAS unsigned*)(sm + L_Q + 8192 + (hl * 64 + lane) * 4);
    const float kn0 = *(LAS float*)(mi + M_KN0);
    lds_signal(mi + M_QR);
    float n0 = 0.f, n1 = 0.f;
#pragma unroll
    for (int i = 0; i < 4; ++i) { n0 = dot2(q0u[i], n0); n1 = dot2(q1u[i], n1); }
    const float nq = dot2(qr, (h ? n1 : n0) + xhalf(h ? n0 : n1));
    const u32x2 qrem_u = {qr, pk2(-SH_A * __builtin_amdgcn_sqrtf(nq * kn0), -SH_C)};
    f32x16 o0, o1;
    float m0 = 0.f, m1 = 0.f;
    STAMP(4); RSTAMP(2);
    {
      int cnt = 8, off = 0, tmp, t2, so = ((s0 + 4) & 7) * RSLOT;
      unsigned ca = sbase + L_M + M_DONE - 4, dst = sbase + hl * 4992 + 2048;
      const unsigned long long ksrc = (unsigned long long)ws + WS_KV + hl * 4992 + 2048 - (sbase + kh * KV_GROUP);
      asm volatile(
      "v_mov_b32 v107, v126\n"
      "v_mov_b32 v108, v127\n"
      "v_mov_b32 v109, v128\n"
      "v_mov_b32 v130, 1\n"
      "ds_read2st64_b64 v[110:113], v109 offset0:0 offset1:1\n"
      "ds_read_b128 v[114:117], v107 offset:0\n"
      "ds_read_b128 v[118:121], v108 offset:0\n"
      "s_mov_b32 vcc_lo, %[first]\n"
      "s_mov_b32 vcc_hi, 0\n"
      "s_setprio 1\n"
      "v_mov_b32 v56, 0\n"
      "v_mov_b32 v57, 0\n"
      "v_mov_b32 v58, 0\n"
      "v_mov_b32 v59, 0\n"
      "v_mov_b32 v60, 0\n"
      "v_mov_b32 v61, 0\n"
      "v_mov_b32 v62, 0\n"
      "v_mov_b32 v63, 0\n"
      "v_mov_b32 v122, 0\n"
      "v_mov_b32 v123, 0\n"
      "v_mov_b32 v124, 0\n"
      "v_mov_b32 v125, 0\n"
      "v_mov_b32 v64, 0\n"
      "v_mov_b32 v65, 0\n"
      "v_mov_b32 v66, 0\n"
      "v_mov_b32 v67, 0\n"
      "v_mov_b32 v68, 0\n"
      "v_mov_b32 v69, 0\n"
      "v_mov_b32 v70, 0\n"
      "v_mov_b32 v71, 0\n"
      "v_mov_b32 v72, 0\n"
      "v_mov_b32 v73, 0\n"
      "v_mov_b32 v74, 0\n"
      "v_mov_b32 v75, 0\n"
      "v_mov_b32 v76, 0\n"
      "v_mov_b32 v77, 0\n"
      "v_mov_b32 v78, 0\n"
      "v_mov_b32 v79, 0\n"
      "s_waitcnt lgkmcnt(1)\n"
      "v_mfma_f32_32x32x4_2b_bf16 v[0:31], v[110:111], v[104:105], 0 cbsz:1 abid:0\n"
      "v_mov_b32 v80, 0\n"
      "v_mov_b32 v81, 0\n"
      "v_mov_b32 v82, 0\n"
      "v_mov_b32 v83, 0\n"
      "v_mov_b32 v84, 0\n"
      "v_mov_b32 v85, 0\n"
      "v_mov_b32 v86, 0\n"
      "v_mov_b32 v87, 0\n"
      "v_mov_b32 v88, 0\n"
      "v_mov_b32 v89, 0\n"
      "v_mov_b32 v90, 0\n"
      "v_mov_b32 v91, 0\n"
      "v_mov_b32 v92, 0\n"
      "v_mov_b32 v93, 0\n"
      "v_mov_b32 v94, 0\n"
      "v_mov_b32 v95, 0\n"
      "s_nop 2\n"
      "v_mfma_f32_32x32x16_bf16 v[0:15], v[114:117], v[96:99], v[0:15]\n"
      "v_mfma_f32_32x32x16_bf16 v[16:31], v[114:117], v[100:103], v[16:31]\n"
      "s_nop 11\n"
      "1:\n"
      "s_cbranch_vccz 4f\n"
      "s_setprio 2\n"
      "4:\n"
      "v_exp_f32 v0, v0\n"
      "v_exp_f32 v1, v1\n"
      "v_smfmac_f32_32x32x32_bf16 v[80:95], v[122:125], v[56:63], v106\n"
      "ds_read_b128 v[114:117], v107 offset:1024\n"
      "ds_read_b128 v[122:125], v108 offset:608\n"
      "v_exp_f32 v2, v2\n"
      "v_exp_f32 v3, v3\n"
      "v_exp_f32 v4, v4\n"
      "v_exp_f32 v5, v5\n"
      "v_mfma_f32_32x32x4_2b_bf16 v[32:63], v[110:111], v[104:105], 0 cbsz:1 abid:1\n"
      "v_exp_f32 v6, v6\n"
      "v_exp_f32 v7, v7\n"
      "v_exp_f32 v8, v8\n"
      "v_exp_f32 v9, v9\n"
      "v_exp_f32 v10, v10\n"
      "v_exp_f32 v11, v11\n"
      "v_exp_f32 v12, v12\n"
      "s_waitcnt lgkmcnt(2)\n"
      "v_exp_f32 v13, v13\n"
      "v_smfmac_f32_32x32x32_bf16 v[64:79], v[118:121], v[0:7], v106\n"
      "v_exp_f32 v14, v14\n"
      "v_exp_f32 v15, v15\n"
      "v_exp_f32 v16, v16\n"
      "s_waitcnt lgkmcnt(0)\n"
      "v_exp_f32 v17, v17\n"
      "v_smfmac_f32_32x32x32_bf16 v[64:79], v[122:125], v[8:15], v106\n"
      "v_exp_f32 v18, v18\n"
      "v_exp_f32 v19, v19\n"
      "v_exp_f32 v20, v20\n"
      "v_exp_f32 v21, v21\n"
      "v_mfma_f32_32x32x16_bf16 v[32:47], v[114:117], v[96:99], v[32:47]\n"
      "v_exp_f32 v22, v22\n"
      "v_exp_f32 v23, v23\n"
      "v_exp_f32 v24, v24\n"
      "v_exp_f32 v25, v25\n"
      "v_smfmac_f32_32x32x32_bf16 v[80:95], v[118:121], v[16:23], v106\n"
      "v_exp_f32 v26, v26\n"
      "ds_read_b128 v[118:121], v108 offset:1216\n"
      "v_exp_f32 v27, v27\n"
      "v_exp_f32 v28, v28\n"
      "v_exp_f32 v29, v29\n"
      "v_mfma_f32_32x32x16_bf16 v[48:63], v[114:117], v[100:103], v[48:63]\n"
      "v_exp_f32 v30, v30\n"
      "v_exp_f32 v31, v31\n"
      "v_exp_f32 v32, v32\n"
      "v_exp_f32 v33, v33\n"
      "v_smfmac_f32_32x32x32_bf16 v[80:95], v[122:125], v[24:31], v106\n"
      "ds_read_b128 v[114:117], v107 offset:2048\n"
      "ds_read_b128 v[122:125], v108 offset:1824\n"
      "ds_read_b32 v131, v129\n"
      "v_exp_f32 v34, v34\n"
      "v_exp_f32 v35, v35\n"
      "v_exp_f32 v36, v36\n"
      "v_exp_f32 v37, v37\n"
      "v_mfma_f32_32x32x4_2b_bf16 v[0:31], v[112:113], v[104:105], 0 cbsz:1 abid:0\n"
      "v_exp_f32 v38, v38\n"
      "v_exp_f32 v39, v39\n"
      "v_exp_f32 v40, v40\n"
      "v_exp_f32 v41, v41\n"
      "v_exp_f32 v42, v42\n"
      "v_exp_f32 v43, v43\n"
      "v_exp_f32 v44, v44\n"
      "s_waitcnt lgkmcnt(3)\n"
      "v_exp_f32 v45, v45\n"
      "v_smfmac_f32_32x32x32_bf16 v[64:79], v[118:121], v[32:39], v106\n"
      "v_exp_f32 v46, v46\n"
      "v_exp_f32 v47, v47\n"
      "v_exp_f32 v48, v48\n"
      "s_waitcnt lgkmcnt(0)\n"
      "s_waitcnt vmcnt(0)\n"
      "s_mov_b64 exec, 1\n"
      "ds_add_u32 v129, v130 offset:76\n"
      "s_mov_b64 exec, -1\n"
      "v_exp_f32 v49, v49\n"
      "v_smfmac_f32_32x32x32_bf16 v[64:79], v[122:125], v[40:47], v106\n"
      "v_exp_f32 v50, v50\n"
      "v_exp_f32 v51, v51\n"
      "v_exp_f32 v52, v52\n"
      "v_exp_f32 v53, v53\n"
      "v_mfma_f32_32x32x16_bf16 v[0:15], v[114:117], v[96:99], v[0:15]\n"
      "v_exp_f32 v54, v54\n"
      "v_exp_f32 v55, v55\n"
      "v_exp_f32 v56, v56\n"
      "v_exp_f32 v57, v57\n"
      "v_smfmac_f32_32x32x32_bf16 v[80:95], v[118:121], v[48:55], v106\n"
      "v_exp_f32 v58, v58\n"
      "ds_read_b128 v[118:121], v108 offset:2432\n"
      "v_exp_f32 v59, v59\n"
      "v_exp_f32 v60, v60\n"
      "v_exp_f32 v61, v61\n"
      "v_mfma_f32_32x32x16_bf16 v[16:31], v[114:117], v[100:103], v[16:31]\n"
      "v_exp_f32 v62, v62\n"
      "v_exp_f32 v63, v63\n"
      "s_and_b32 %[t2], %[cnt], 12\n"
      "s_or_b32 %[t2], %[t2], vcc_lo\n"
      "s_cmp_lg_u32 %[t2], 5\n"
      "s_cbranch_scc1 8f\n"
      "v_readfirstlane_b32 %[tmp], v131\n"
      "s_nop 0\n"
      "s_cmp_lt_u32 %[tmp], 8\n"
      "s_cbranch_scc1 7f\n"
      "9:\n"
      "s_mov_b32 m0, %[dst]\n"
      "s_add_u32 s96, %[slo], %[so]\n"
      "s_addc_u32 s97, %[shi], 0\n"
      "global_load_lds_dwordx4 v126, s[96:97] offset:-2048 sc1\n"
      "global_load_lds_dwordx4 v126, s[96:97] offset:-1024 sc1\n"
      "global_load_lds_dwordx4 v126, s[96:97] sc1\n"
      "global_load_lds_dwordx4 v126, s[96:97] offset:1024 sc1\n"
      "s_mov_b32 exec_hi, 0xffffff\n"
      "global_load_lds_dwordx4 v126, s[96:97] offset:2048 sc1\n"
      "s_mov_b32 exec_hi, -1\n"
      "s_add_u32 %[dst], %[dst], 19968\n"
      "s_add_u32 %[so], %[so], 19968\n"
      "s_cmp_eq_u32 %[so], 159744\n"
      "s_cselect_b32 %[so], 0, %[so]\n"
      "8:\n"
      "v_exp_f32 v0, v0\n"
      "v_exp_f32 v1, v1\n"
      "v_smfmac_f32_32x32x32_bf16 v[80:95], v[122:125], v[56:63], v106\n"
      "ds_read_b128 v[114:117], v107 offset:3072\n"
      "ds_read_b128 v[122:125], v108 offset:3040\n"
      "ds_read_b32 v131, v129 offset:72\n"
      "v_exp_f32 v2, v2\n"
      "v_exp_f32 v3, v3\n"
      "v_exp_f32 v4, v4\n"
      "v_exp_f32 v5, v5\n"
      "v_mfma_f32_32x32x4_2b_bf16 v[32:63], v[112:113], v[104:105], 0 cbsz:1 abid:1\n"
      "v_exp_f32 v6, v6\n"
      "v_exp_f32 v7, v7\n"
      "v_exp_f32 v8, v8\n"
      "v_exp_f32 v9, v9\n"
      "v_exp_f32 v10, v10\n"
      "v_exp_f32 v11, v11\n"
      "v_exp_f32 v12, v12\n"
      "s_waitcnt lgkmcnt(3)\n"
      "v_exp_f32 v13, v13\n"
      "v_smfmac_f32_32x32x32_bf16 v[64:79], v[118:121], v[0:7], v106\n"
      "v_exp_f32 v14, v14\n"
      "v_exp_f32 v15, v15\n"
      "v_exp_f32 v16, v16\n"
      "s_waitcnt lgkmcnt(0)\n"
      "v_exp_f32 v17, v17\n"
      "v_smfmac_f32_32x32x32_bf16 v[64:79], v[122:125], v[8:15], v106\n"
      "v_exp_f32 v18, v18\n"
      "v_exp_f32 v19, v19\n"
      "v_exp_f32 v20, v20\n"
      "v_exp_f32 v21, v21\n"
      "v_mfma_f32_32x32x16_bf16 v[32:47], v[114:117], v[96:99], v[32:47]\n"
      "v_exp_f32 v22, v22\n"
      "v_exp_f32 v23, v23\n"
      "v_exp_f32 v24, v24\n"
      "v_exp_f32 v25, v25\n"
      "v_smfmac_f32_32x32x32_bf16 v[80:95], v[118:121], v[16:23], v106\n"
      "v_exp_f32 v26, v26\n"
      "ds_read_b128 v[118:121], v108 offset:3648\n"
      "v_exp_f32 v27, v27\n"
      "v_exp_f32 v28, v28\n"
      "v_exp_f32 v29, v29\n"
      "v_mfma_f32_32x32x16_bf16 v[48:63], v[114:117], v[100:103], v[48:63]\n"
      "v_exp_f32 v30, v30\n"
      "v_exp_f32 v31, v31\n"
      "s_cbranch_vccz 5f\n"
      "s_setprio 0\n"
      "5:\n"
      "v_readfirstlane_b32 %[tmp], v131\n"
      "s_add_u32 %[off], %[off], 19968\n"
      "s_cmp_eq_u32 %[off], 79872\n"
      "s_cselect_b32 %[off], 0, %[off]\n"
      "s_cmp_lt_i32 %[tmp], 8\n"
      "s_cbranch_scc1 3f\n"
      "2:\n"
      "v_add_u32 v109, %[off], v128\n"
      "ds_read2st64_b64 v[110:113], v109 offset0:0 offset1:1\n"
      "v_add_u32 v107, %[off], v126\n"
      "v_exp_f32 v32, v32\n"
      "v_exp_f32 v33, v33\n"
      "v_smfmac_f32_32x32x32_bf16 v[80:95], v[122:125], v[24:31], v106\n"
      "ds_read_b128 v[114:117], v107 offset:0\n"
      "ds_read_b128 v[122:125], v108 offset:4256\n"
      "v_exp_f32 v34, v34\n"
      "v_exp_f32 v35, v35\n"
      "v_exp_f32 v36, v36\n"
      "s_waitcnt lgkmcnt(2)\n"
      "v_exp_f32 v37, v37\n"
      "v_mfma_f32_32x32x4_2b_bf16 v[0:31], v[110:111], v[104:105], 0 cbsz:1 abid:0\n"
      "v_exp_f32 v38, v38\n"
      "v_exp_f32 v39, v39\n"
      "v_exp_f32 v40, v40\n"
      "v_exp_f32 v41, v41\n"
      "v_exp_f32 v42, v42\n"
      "v_exp_f32 v43, v43\n"
      "v_exp_f32 v44, v44\n"
      "v_exp_f32 v45, v45\n"
      "v_smfmac_f32_32x32x32_bf16 v[64:79], v[118:121], v[32:39], v106\n"
      "v_exp_f32 v46, v46\n"
      "v_exp_f32 v47, v47\n"
      "v_exp_f32 v48, v48\n"
      "s_waitcnt lgkmcnt(0)\n"
      "s_mov_b64 exec, 1\n"
      "ds_add_u32 v129, v130 offset:4\n"
      "s_mov_b64 exec, -1\n"
      "v_exp_f32 v49, v49\n"
      "v_smfmac_f32_32x32x32_bf16 v[64:79], v[122:125], v[40:47], v106\n"
      "v_exp_f32 v50, v50\n"
      "v_exp_f32 v51, v51\n"
      "v_exp_f32 v52, v52\n"
      "v_exp_f32 v53, v53\n"
      "v_mfma_f32_32x32x16_bf16 v[0:15], v[114:117], v[96:99], v[0:15]\n"
      "v_exp_f32 v54, v54\n"
      "v_exp_f32 v55, v55\n"
      "v_exp_f32 v56, v56\n"
      "v_exp_f32 v57, v57\n"
      "v_smfmac_f32_32x32x32_bf16 v[80:95], v[118:121], v[48:55], v106\n"
      "v_add_u32 v108, %[off], v127\n"
      "v_add_u32 v129, 4, v129\n"
      "v_exp_f32 v58, v58\n"
      "ds_read_b128 v[118:121], v108 offset:0\n"
      "v_exp_f32 v59, v59\n"
      "v_exp_f32 v60, v60\n"
      "v_exp_f32 v61, v61\n"
      "v_mfma_f32_32x32x16_bf16 v[16:31], v[114:117], v[100:103], v[16:31]\n"
      "v_exp_f32 v62, v62\n"
      "v_exp_f32 v63, v63\n"
      "s_sub_u32 %[cnt], %[cnt], 1\n"
      "s_cmp_lg_u32 %[cnt], 0\n"
      "s_cbranch_scc1 1b\n"
      "s_branch 6f\n"
      "3:\n"
      "s_sleep 1\n"
      "ds_read_b32 v131, v129 offset:72\n"
      "s_waitcnt lgkmcnt(0)\n"
      "v_readfirstlane_b32 %[tmp], v131\n"
      "s_nop 0\n"
      "s_cmp_lt_i32 %[tmp], 8\n"
      "s_cbranch_scc1 3b\n"
      "s_branch 2b\n"
      "7:\n"
      "s_sleep 1\n"
      "ds_read_b32 v131, v129\n"
      "s_waitcnt lgkmcnt(0)\n"
      "v_readfirstlane_b32 %[tmp], v131\n"
      "s_nop 0\n"
      "s_cmp_lt_u32 %[tmp], 8\n"
      "s_cbranch_scc1 7b\n"
      "s_branch 9b\n"
      "6:\n"
      "v_smfmac_f32_32x32x32_bf16 v[80:95], v[122:125], v[56:63], v106\n"
      "s_setprio 0\n"
      "s_waitcnt lgkmcnt(0)\n"
      "s_nop 15\n"
      "s_nop 7\n"
      : "=&{v[64:79]}"(o0), "=&{v[80:95]}"(o1), "+{v129}"(ca), [cnt] "+s"(cnt), [off] "+s"(off), [tmp] "=&s"(tmp),
        [t2] "=&s"(t2), [so] "+s"(so), [dst] "+s"(dst)
      : "{v[96:99]}"(q0u), "{v[100:103]}"(q1u), "{v[104:105]}"(qrem_u), "{v106}"(SPIDX),
        "{v126}"(sbase + kh * KV_GROUP + lane * 16), "{v127}"(sbase + kh * KV_GROUP + voff),
        "{v128}"(sbase + kh * KV_GROUP + 8960 + lane * 8), [first] "s"(1 - kh), [slo] "s"((unsigned)ksrc), [shi] "s"((unsigned)(ksrc >> 32))
      : "v0", "v1", "v2", "v3", "v4", "v5", "v6", "v7", "v8", "v9", "v10", "v11", "v12", "v13", "v14", "v15",
        "v16", "v17", "v18", "v19", "v20", "v21", "v22", "v23", "v24", "v25", "v26", "v27", "v28", "v29", "v30", "v31",
        "v32", "v33", "v34", "v35", "v36", "v37", "v38", "v39", "v40", "v41", "v42", "v43", "v44", "v45", "v46", "v47",
        "v48", "v49", "v50", "v51", "v52", "v53", "v54", "v55", "v56", "v57", "v58", "v59", "v60", "v61", "v62", "v63",
        "v107", "v108", "v109", "v110", "v111", "v112", "v113", "v114", "v115", "v116", "v117", "v118", "v119", "v120", "v121", "v122",
        "v123", "v124", "v125", "v130", "v131", "s96", "s97", "vcc", "scc", "memory");
    }
    xv = *(const u32x4*)(xt + otok * 1152 + 144 * hg + opc * 16);
    const float dv = (float)ssmd[72 * hg + 2 * qt + (hl >> 1)];
    if (!ALL(nq * *(LAS float*)(mi + M_KN) < 47.f * 47.f)) {
      m0 = m1 = -__builtin_inff();
      o0 = o1 = (f32x16){0};
      const bf16x8 q0 = __builtin_bit_cast(bf16x8, q0u), q1 = __builtin_bit_cast(bf16x8, q1u);
      const s16x4 qrem = __builtin_bit_cast(s16x4, (u32x2){qr, 0u});
#pragma unroll 1
      for (int t = 0; t < 32; ++t) {
        const int g = WS_KV + (t >> 2) * RSLOT + kh * KV_GROUP;
        const u32x2 kr = __builtin_bit_cast(u32x2, __builtin_amdgcn_raw_buffer_load_b64(rs, g + 8960 + (t & 3) * 256 + l31 * 8, 0, 16));
        const bf16x8 km = __builtin_bit_cast(bf16x8, LD_SC1_16(g + (t & 3) * 1024 + lane * 16));
        const bf16x8 va = __builtin_bit_cast(bf16x8, LD_SC1_16(g + voff + (t & 3) * 1216));
        const bf16x8 vb = __builtin_bit_cast(bf16x8, LD_SC1_16(g + voff + (t & 3) * 1216 + 608));
        const f32x32 z = {0};
        const f32x32 r = __builtin_amdgcn_mfma_f32_32x32x4bf16_1k(__builtin_bit_cast(s16x4, kr), qrem, z, 0, 0, 0);
        online_step(MFMA32(km, q0, lo16(r)), m0, o0, va, vb);
        online_step(MFMA32(km, q1, hi16(r)), m1, o1, va, vb);
      }
    }
    HSTAMP(9);
    lds_ptr ex = sm + L_EX + hl * 6144 + lane * 16;
    if (kh) {
      *(LAS f32x4*)(ex) = (f32x4){o0[0], o0[1], o0[2], o0[3]};
      *(LAS f32x4*)(ex + 1024) = (f32x4){o0[4], o0[5], o0[6], o0[7]};
      *(LAS f32x4*)(ex + 2048) = (f32x4){o0[8], o0[9], o0[10], m0};
      *(LAS f32x4*)(ex + 3072) = (f32x4){o1[0], o1[1], o1[2], o1[3]};
      *(LAS f32x4*)(ex + 4096) = (f32x4){o1[4], o1[5], o1[6], o1[7]};
      *(LAS f32x4*)(ex + 5120) = (f32x4){o1[8], o1[9], o1[10], m1};
      lds_signal(mi + M_PAIR + 4 * hl);
    }
    lds_wait<1>(mi + M_PAIR + 4 * hl, 5);
    STAMP(10); RSTAMP(3);
    if (!kh) {
      __builtin_amdgcn_s_setprio(3);
#pragma unroll
      for (int t = 0; t < 2; ++t) {
        const f32x16 keep = t ? o1 : o0;
        const f32x4 t0 = *(LAS f32x4*)(ex + t * 3072), t1 = *(LAS f32x4*)(ex + t * 3072 + 1024), t2 = *(LAS f32x4*)(ex + t * 3072 + 2048);
        const float mk = t ? m1 : m0, mm = __builtin_fmaxf(mk, t2[3]);
        float fa = __builtin_amdgcn_exp2f(mk - mm), fb = __builtin_amdgcn_exp2f(t2[3] - mm);
        const float l = keep[10] * fa + t2[2] * fb, lx = xhalf(l);
        const float inv = __builtin_amdgcn_rcpf(h ? lx : l);
        fa *= inv;
        fb *= inv;
        float o[10];
#pragma unroll
        for (int i = 0; i < 10; ++i) o[i] = keep[i] * fa + (i < 4 ? t0[i & 3] : (i < 8 ? t1[i & 3] : t2[i & 3])) * fb;
        u32x4 a;
#pragma unroll
        for (int i = 0; i < 4; ++i) a[i] = UMIN(pk2(o[2 * i], o[2 * i + 1]), 0xfffffffeu);
        const int ob = WS_O + (2 * qt + t) * 36 * 1024;
        ST_SC1_16(a, ob + (4 * hg + hl) * 1024 + lane * 16);
        if (!h) ST_SC1_4(UMIN(pk2(o[8], o[9]), 0xfffffffeu), ob + 32768 + (4 * hg + hl) * 128 + l31 * 4);
      }
      lds_signal(mi + M_EXD);
      __builtin_amdgcn_s_setprio(1);
      HSTAMP(5);
      __builtin_amdgcn_s_sleep(O_DELAY);
      u32x4 of[18];
#pragma unroll
      for (int i = 0; i < 18; ++i) of[i] = LD_SC1_16(WS_O + (2 * qt * 36 + (i / 9) * 36 + 9 * hl + i % 9) * 1024 + lane * 16);
      lds_wait<1>(mi + M_EXD, 8);
#pragma unroll
      for (int t = 0; t < 2; ++t) {
        unsigned mx = 0;
#pragma unroll
        for (int i = 9 * t; i < 9 * t + 9; ++i) mx = UMAX(UMAX(mx, UMAX(of[i][0], of[i][1])), UMAX(of[i][2], of[i][3]));
        if (__builtin_expect(!ALL(mx != ~0u), 0)) {
#pragma unroll 1
          for (unsigned spins = 0; spins < SPIN_CAP; ++spins) {
            asm volatile("" ::: "memory");
#pragma unroll
            for (int i = 9 * t; i < 9 * t + 9; ++i) of[i] = LD_SC1_16(WS_O + (2 * qt * 36 + t * 36 + 9 * hl + i % 9) * 1024 + lane * 16);
            mx = 0;
#pragma unroll
            for (int i = 9 * t; i < 9 * t + 9; ++i) mx = UMAX(UMAX(mx, UMAX(of[i][0], of[i][1])), UMAX(of[i][2], of[i][3]));
            if (ALL(mx != ~0u)) break;
            __builtin_amdgcn_s_sleep(2);
          }
        }
#pragma unroll
        for (int i = 0; i < 9; ++i) {
          if (i >= 5 && hl == 3) {
            lds_ptr p = sm + (36 * t + 27 + i) * 1024 + (4 * (lane & 7) + 32 * h) * 16 + ((lane >> 3) & 3) * 4;
#pragma unroll
            for (int e = 0; e < 4; ++e) *(LAS unsigned*)(p + e * 16) = of[9 * t + i][e];
          } else {
            *(LAS u32x4*)(sm + (36 * t + 9 * hl + i) * 1024 + lane * 16) = of[9 * t + i];
          }
        }
        if (t == 0) lds_signal(mi + M_KVR);
      }
      lds_signal(mi + M_OR);
      __builtin_amdgcn_s_setprio(0);
      HSTAMP(6);
    } else {
      const int b = hl >> 1, o = hl & 1;
      lds_ptr ab = sm + CV_H + b * CV_HB + (l31 & 7) * CV_COPY + (h + (l31 >> 3)) * 16 + 1024 * o;
      lds_ptr bb = sm + CV_U + b * CV_UB + 2560 + (l31 - 16 * o - 7) * 80 + (1 - h) * 16;
      f32x16 y0 = {0}, y1 = {0};
      bf16x8 fx[8], fy[8];
      __builtin_amdgcn_s_setprio(MM_PRIO);
      asm volatile("" : "+v"(ab), "+v"(bb));
      CV_LD(fx, 0);
#pragma unroll 1
      for (int it = 0; it < 4; ++it) {
        asm volatile("" : "+v"(ab), "+v"(bb));
        CV_STEP(fx, fy, 1);
        CV_STEP(fy, fx, 2);
        CV_STEP(fx, fy, 3);
        CV_STEP(fy, fx, 4);
        __builtin_amdgcn_s_setprio(2);
        ab += 256;
        bb -= 320;
      }
      HSTAMP(5);
      lds_ptr px = sm + CV_H + b * CV_HB + h * CV_COPY + l31 * 16;
      {
        const f32x16 give = o ? y0 : y1;
#pragma unroll
        for (int g = 0; g < 4; ++g) *(LAS f32x4*)(px + o * 3200 + g * 2 * CV_COPY) = (f32x4){give[4 * g], give[4 * g + 1], give[4 * g + 2], give[4 * g + 3]};
        if (o) y0 = y1;
      }
      lds_signal(mi + M_AS + 4 * b);
      lds_signal(mi + M_MM);
      u32x2 uu[4];
#pragma unroll
      for (int g = 0; g < 4; ++g) uu[g] = *(LAS u32x2*)(sm + CV_U + b * CV_UB + 2560 + (32 * o + l31) * 80 + g * 16 + (1 - h) * 8);
#pragma unroll
      for (int g = 0; g < 4; ++g) {
        y0[4 * g] = __builtin_fmaf(bf_hi(uu[g][1]), dv, y0[4 * g]);
        y0[4 * g + 1] = __builtin_fmaf(bf_lo(uu[g][1]), dv, y0[4 * g + 1]);
        y0[4 * g + 2] = __builtin_fmaf(bf_hi(uu[g][0]), dv, y0[4 * g + 2]);
        y0[4 * g + 3] = __builtin_fmaf(bf_lo(uu[g][0]), dv, y0[4 * g + 3]);
      }
      lds_wait(mi + M_AS + 4 * b, 2);
      u32x4 z[2];
#pragma unroll
      for (int g = 0; g < 4; ++g) {
        const f32x4 t = *(LAS f32x4*)(px + (1 - o) * 3200 + g * 2 * CV_COPY);
        z[g >> 1][2 * (g & 1)] = UMIN(pk2(t[0] + y0[4 * g], t[1] + y0[4 * g + 1]), 0xfffffffeu);
        z[g >> 1][2 * (g & 1) + 1] = UMIN(pk2(t[2] + y0[4 * g + 2], t[3] + y0[4 * g + 3]), 0xfffffffeu);
      }
      const int zo = WS_Z + (72 * hg + 2 * qt + b) * 4096 + o * 2048 + lane * 16;
      ST_SC1_16(z[0], zo);
      ST_SC1_16(z[1], zo + 1024);
      lds_signal(mi + M_CE);
      __builtin_amdgcn_s_setprio(0);
      HSTAMP(6);
    }
    if (w == 0) {
      u32x4 zp[8];
#pragma unroll 1
      for (unsigned spins = 0; spins < SPIN_CAP; ++spins) {
        unsigned mx = 0;
#pragma unroll
        for (int i = 0; i < 8; ++i)
          zp[i] = LD_SC1_16(WS_ZP + (hg * 32 + (lane >> 3) * 4 + (i >> 1)) * 8192 + (qt >> 4) * 4096 + ((lane >> 2) & 1) * 2048 + (i & 1) * 1024 +
                            (2 * (qt & 15) + (lane & 1) + 32 * ((lane >> 1) & 1)) * 16);
#pragma unroll
        for (int i = 0; i < 8; ++i) mx = UMAX(UMAX(mx, UMAX(zp[i][0], zp[i][1])), UMAX(zp[i][2], zp[i][3]));
        asm volatile("" ::: "memory");
        if (ALL(mx != ~0u)) break;
        __builtin_amdgcn_s_sleep(2);
      }
#define ZF(I) __builtin_bit_cast(f32x4, zp[I])
      const f32x4 sa = (ZF(0) + ZF(2)) + (ZF(4) + ZF(6)), sb = (ZF(1) + ZF(3)) + (ZF(5) + ZF(7));
      zv = (u32x4){pk2(sa[0], sa[1]), pk2(sa[2], sa[3]), pk2(sb[0], sb[1]), pk2(sb[2], sb[3])};
    }
    lds_wait<2>(mi + M_CE, 4);
    if (w == 0) zscatter(512 + lane);
    else __builtin_amdgcn_s_sleep(Z_DELAY);
#pragma unroll 1
    for (unsigned spins = 0; spins < SPIN_CAP; ++spins) {
      zv = LD_SC1_16(WS_Z + (72 * hg + (tid >> 3)) * 4096 + (qt >> 4) * 2048 + ((tid >> 2) & 1) * 1024 +
                     (2 * (qt & 15) + (tid & 1) + 32 * ((tid >> 1) & 1)) * 16);
      asm volatile("" ::: "memory");
      if (ALL(UMAX(UMAX(zv[0], zv[1]), UMAX(zv[2], zv[3])) != ~0u)) break;
      __builtin_amdgcn_s_sleep(2);
    }
    HSTAMP(7);
    zscatter(tid);
  }

  if (w < 9) {
    lds_ptr ap = sm + L_AP + (otok >> 3) * CV_COPY + (otok & 7) * 336 + opc * 32;
    lds_wait<1>(mi + M_APR, 4);
    const float gq = *(LAS float*)(mi + M_G + otok * 4), omg = rbf(1.f - gq);
    f32x4 a0 = *(LAS f32x4*)(ap) + *(LAS f32x4*)(ap + CV_HB), a1 = *(LAS f32x4*)(ap + 16) + *(LAS f32x4*)(ap + CV_HB + 16);
    if (opc == 8) {
      a0 += *(LAS f32x4*)(ap + 32) + *(LAS f32x4*)(ap + CV_HB + 32);
      a1 += *(LAS f32x4*)(ap + 48) + *(LAS f32x4*)(ap + CV_HB + 48);
    }
    a0 *= gq;
    a1 *= gq;
    f32x4 x0 = {omg * bf_lo(xv[0]), omg * bf_hi(xv[0]), omg * bf_lo(xv[1]), omg * bf_hi(xv[1])};
    f32x4 x1 = {omg * bf_lo(xv[2]), omg * bf_hi(xv[2]), omg * bf_lo(xv[3]), omg * bf_hi(xv[3])};
    lds_ptr zq = sm + L_ZT + ((otok >> 5) + ((otok >> 1) & 2) + ((otok >> 2) & 4) + 8 * (otok & 3) + 4 * (otok & 8)) * 144 + opc * 16;
    u32x4* op = (u32x4*)(out + (size_t)(64 * qt + otok) * D_ + 72 * hg + 8 * opc);
    asm volatile("" : "+v"(a0), "+v"(a1), "+v"(x0), "+v"(x1), "+v"(zq), "+v"(op));
    lds_wait(mi + M_ZS, 9);
    if (w < 8) HSTAMP(8);
    const u32x4 z = *(LAS u32x4*)zq;
    *(GAS u32x4*)op = (u32x4){pk2(__builtin_fmaf(x0[0], bf_lo(z[0]), a0[0]), __builtin_fmaf(x0[1], bf_hi(z[0]), a0[1])),
                              pk2(__builtin_fmaf(x0[2], bf_lo(z[1]), a0[2]), __builtin_fmaf(x0[3], bf_hi(z[1]), a0[3])),
                              pk2(__builtin_fmaf(x1[0], bf_lo(z[2]), a1[0]), __builtin_fmaf(x1[1], bf_hi(z[2]), a1[1])),
                              pk2(__builtin_fmaf(x1[2], bf_lo(z[3]), a1[2]), __builtin_fmaf(x1[3], bf_hi(z[3]), a1[3]))};
  }
  STAMP_FLUSH(w < 4 ? 0 : (w < 8 ? 1 : (w < 11 ? 2 : 3)));
  asm volatile("fk_end:");
}

extern "C" void kernel_launch(void* const* d_in, const int* in_sizes, int n_in, void* d_out, int out_size,
                              void* d_ws, size_t ws_size, hipStream_t stream) {
  fused_kernel<<<256, 768, 0, stream>>>((const __bf16*)d_in[0], (const __bf16*)d_in[1], (const __bf16*)d_in[2],
                                        (const __bf16*)d_in[3], (const __bf16*)d_in[4], (const __bf16*)d_in[6],
                                        (const __bf16*)d_in[7], (const __bf16*)d_in[8], (const __bf16*)d_in[9],
                                        (const __bf16*)d_in[10], (const __bf16*)d_in[11], (__bf16*)d_out, (char*)d_ws);
}
```

```cpp
#include <hip/hip_runtime.h>
#include <hip/hip_bf16.h>
#include <stdint.h>

typedef __attribute__((ext_vector_type(2))) __bf16 bf16x2;
typedef __attribute__((ext_vector_type(4))) __bf16 bf16x4;
typedef __attribute__((ext_vector_type(8))) __bf16 bf16x8;
typedef __attribute__((ext_vector_type(16))) __bf16 bf16x16;
typedef __attribute__((ext_vector_type(4))) short s16x4;
typedef __attribute__((ext_vector_type(2))) float f32x2;
typedef __attribute__((ext_vector_type(4))) float f32x4;
typedef __attribute__((ext_vector_type(16))) float f32x16;
typedef __attribute__((ext_vector_type(32))) float f32x32;
typedef __attribute__((ext_vector_type(2))) unsigned u32x2;
typedef __attribute__((ext_vector_type(4))) unsigned u32x4;
#define LAS __attribute__((address_space(3)))
#define GAS __attribute__((address_space(1)))
typedef LAS char* lds_ptr;
typedef GAS unsigned gu32;
typedef GAS unsigned long long gu64;
#ifdef __HIP_DEVICE_COMPILE__
#define NOPK __attribute__((target("no-packed-fp32-ops")))
#else
#define NOPK
#endif
#define DEVI __device__ __forceinline__ NOPK
#define LAMI __attribute__((always_inline)) NOPK
#define TIDX __builtin_amdgcn_workitem_id_x()
#define BIDX __builtin_amdgcn_workgroup_id_x()
#define ALL(p) (__builtin_amdgcn_ballot_w64(!(p)) == 0ull)
#define UMIN(a, b) __builtin_elementwise_min((unsigned)(a), (unsigned)(b))
#define UMAX(a, b) __builtin_elementwise_max((unsigned)(a), (unsigned)(b))
#define RLX_AGENT __ATOMIC_RELAXED, __HIP_MEMORY_SCOPE_AGENT
#define RLX_WG __ATOMIC_RELAXED, __HIP_MEMORY_SCOPE_WORKGROUP

constexpr int S_ = 2048, D_ = 576, DH = 18;

constexpr int KV_GROUP = 9984;
constexpr int WS_KV = 0;
constexpr int WS_EPOCH = 192 * 1024;
constexpr int WS_FKV = WS_EPOCH + 1024;
constexpr int WS_FU = WS_FKV + 8192;
constexpr int WS_G = WS_FU + 32768;
constexpr int WS_KT = 320 * 1024;
constexpr int WS_UT = 640 * 1024;
constexpr int WS_Z = WS_UT + D_ * S_ * 2;
constexpr int WS_O = WS_Z + D_ * S_ * 2;
constexpr int WS_ZP = WS_O + D_ * S_ * 2;
constexpr int WS_END = WS_ZP + 64 * 4 * 8192;

constexpr int RSLOT = 2 * KV_GROUP;
constexpr int L_X = 0;
constexpr int L_KVP = 73728;
constexpr int L_B = 80000;
constexpr int L_M = 162944;
constexpr int CV_COPY = 4160;
constexpr int CV_H = L_B, CV_HB = 8 * CV_COPY, CV_U = L_B + 2 * CV_HB, CV_UB = 7680;
constexpr int L_Q = L_B + 8192;
constexpr int M_DONE = 0, M_C1 = 32, M_C2 = 36, M_C3 = 40, M_C3B = 44, M_KVC = 48, M_KVR = 52, M_UR = 56, M_OR = 60, M_RDY = 64,
              M_QD = 192, M_UD = 104, M_HS = 108, M_APR = 112, M_CVR = 116, M_CE = 124, M_PAIR = 128, M_TAGR = 144,
              M_TAG = 148, M_KN = 152, M_EXD = 156, M_KVP = 160, M_KN0 = 164, M_KD = 168, M_QR = 172,
              M_N2 = 176, M_AS = 196, M_SE = 204, M_ZS = 208, M_MM = 212, M_DV = 216, M_APR1 = 224, M_G = 256;
constexpr int E_H = 24576, E_COPY = 1344, E_U = 35328, E_P = 43008;
constexpr int L_EX = 0;
constexpr int L_AP = CV_H + 512, L_ZT = CV_U;
#define CV_PRIO 3
#define CV_SLEEP 0
#define POLL_AT M_C2
#define POLL_N 3
#define BUBBLE 0
#define POLL_SLEEP 4
#define WO_INFL "2"
#define DMA_FIRST 0
#define ENTRY 0
#define H_PRIO 3
#define TCH 2
#define KV_DELAY 0
#define WO_EARLY 0
#define WQT 0
#define SH_C 0.f
#define SH_A 1.f
#define MM_PRIO 0
#define MM_UP 0
#define HT_PRIO 1
#define EP_DELAY 10
#define G_PRIO 1
#define OUT_POL " sc1"
#define O_DELAY 20
#define Z_DELAY 12
constexpr unsigned SPIN_CAP = 1u << 18;

#define STAMP_DECL do {} while (0)
#define STAMP(I) do {} while (0)
#define STAMP_FLUSH(KID) do {} while (0)
#define RSTAMP(I) do {} while (0)

#define PSTAMP(I) do {} while (0)
#define NSTAMP(I) STAMP(I)
#define CSTAMP(I) do {} while (0)
#define HSTAMP(I) NSTAMP(I)

DEVI unsigned pk2(float a, float b) {
  bf16x2 v = {(__bf16)a, (__bf16)b};
  return __builtin_bit_cast(unsigned, v);
}
DEVI float u2f(unsigned u) { return __builtin_bit_cast(float, u); }
DEVI float bf_lo(unsigned u) { return __builtin_bit_cast(float, u << 16); }
DEVI float bf_hi(unsigned u) { return __builtin_bit_cast(float, u & 0xffff0000u); }
DEVI float dot2(unsigned u, float c) { const bf16x2 v = __builtin_bit_cast(bf16x2, u); return __builtin_amdgcn_fdot2_f32_bf16(v, v, c, false); }
DEVI float rbf(float a) { return (float)(__bf16)a; }
DEVI int swap23(int r) { return (r & ~12) | ((r & 4) << 1) | ((r & 8) >> 1); }
DEVI float xhalf(float v) {
  const unsigned u = __builtin_bit_cast(unsigned, v);
  const auto r = __builtin_amdgcn_permlane32_swap(u, u, false, false);
  return __builtin_bit_cast(float, (TIDX & 32) ? r[0] : r[1]);
}

template <int CTRL>
DEVI float dpp_max(float v) {
  const int u = __builtin_bit_cast(int, v);
  return __builtin_fmaxf(v, __builtin_bit_cast(float, __builtin_amdgcn_update_dpp(u, u, CTRL, 0xf, 0xf, false)));
}
DEVI float max16(float v) {
  return dpp_max<0x140>(dpp_max<0x141>(dpp_max<0x4e>(dpp_max<0xb1>(v))));
}
template <int CTRL>
DEVI float dpp_add(float v) {
  const int u = __builtin_bit_cast(int, v);
  return v + __builtin_bit_cast(float, __builtin_amdgcn_update_dpp(u, u, CTRL, 0xf, 0xf, false));
}
DEVI float sum16(float v) { return dpp_add<0x140>(dpp_add<0x141>(dpp_add<0x4e>(dpp_add<0xb1>(v)))); }
DEVI float wave_max(float v) {
  v = max16(v);
  const unsigned u = __builtin_bit_cast(unsigned, v);
  return __builtin_fmaxf(__builtin_fmaxf(u2f(__builtin_amdgcn_readlane(u, 0)), u2f(__builtin_amdgcn_readlane(u, 16))),
               __builtin_fmaxf(u2f(__builtin_amdgcn_readlane(u, 32)), u2f(__builtin_amdgcn_readlane(u, 48))));
}
DEVI void glds(const void* sbase, unsigned voff, unsigned lds_dst) {
  asm volatile("s_mov_b32 m0, %2\n\ts_nop 0\n\tglobal_load_lds_dwordx4 %0, %1" :: "v"(voff), "s"(sbase), "s"(lds_dst) : "memory");
}
DEVI void glds_sc1(const void* sbase, unsigned voff, unsigned lds_dst) {
  asm volatile("s_mov_b32 m0, %2\n\ts_nop 0\n\tglobal_load_lds_dwordx4 %0, %1 sc1" :: "v"(voff), "s"(sbase), "s"(lds_dst) : "memory");
}
#define GLDS4(SC, sbase_, v0_, v3_, dst_)                                                                                  \
  asm volatile("s_mov_b32 m0, %3\n\ts_nop 0\n\tglobal_load_lds_dwordx4 %0, %2" SC "\n\tglobal_load_lds_dwordx4 %0, %2 offset:1024" SC   \
               "\n\tglobal_load_lds_dwordx4 %0, %2 offset:2048" SC "\n\tglobal_load_lds_dwordx4 %1, %2 offset:3072" SC           \
               :: "v"(v0_), "v"(v3_), "s"(sbase_), "s"(dst_) : "memory")
#define GLDSX(src_, dst_)                                                                                                  \
  asm volatile("s_mov_b32 m0, %5\n\ts_nop 0\n\tglobal_load_lds_dwordx4 %0, %4\n\tglobal_load_lds_dwordx4 %1, %4 offset:1024\n\t"   \
               "global_load_lds_dwordx4 %2, %4 offset:2048\n\tglobal_load_lds_dwordx4 %3, %4 offset:3072"                 \
               :: "v"(vxe), "v"(vxo), "v"(vxe + 16384), "v"(vxo + 16384), "s"(src_), "s"(dst_) : "memory")
#define GLDSX2(src_, dst_)                                                                                                 \
  asm volatile("s_mov_b32 m0, %3\n\ts_nop 0\n\tglobal_load_lds_dwordx4 %0, %2\n\tglobal_load_lds_dwordx4 %1, %2 offset:1024"       \
               :: "v"(vxe), "v"(vxo), "s"(src_), "s"(dst_) : "memory")
#define GLDS3(STEP1, STEP2, v0_, v1_, v2_, src_, dst_)                                                                     \
  asm volatile("s_mov_b32 m0, %4\n\ts_nop 0\n\tglobal_load_lds_dwordx4 %0, %3\n\tglobal_load_lds_dwordx4 %1, %3 offset:" STEP1  \
               "\n\tglobal_load_lds_dwordx4 %2, %3 offset:" STEP2                                                        \
               :: "v"(v0_), "v"(v1_), "v"(v2_), "s"(src_), "s"(dst_) : "memory")
#define TOUCH(src_, v_) asm volatile("s_mov_b32 m0, %2\n\ts_nop 0\n\tglobal_load_lds_dword %0, %1 sc1" :: "v"(v_), "s"(src_), "s"(sbase + L_M + 512) : "memory")
#define VM_WAIT(N) asm volatile("s_waitcnt vmcnt(" #N ")" ::: "memory")
#define BAR() asm volatile("s_waitcnt lgkmcnt(0)\n\ts_barrier" ::: "memory")

DEVI bf16x8 tr_frag(lds_ptr p0, lds_ptr p1) {
  bf16x4 a = __builtin_amdgcn_ds_read_tr16_b64_v4bf16((LAS bf16x4*)p0);
  bf16x4 b = __builtin_amdgcn_ds_read_tr16_b64_v4bf16((LAS bf16x4*)p1);
  return __builtin_shufflevector(a, b, 0, 1, 2, 3, 4, 5, 6, 7);
}
DEVI bf16x8 lds_frag(lds_ptr p) { return *(LAS bf16x8*)p; }
DEVI void lds_signal(lds_ptr p, unsigned n = 1u) {
  asm volatile("s_waitcnt lgkmcnt(0)\n\ts_mov_b64 exec, 1\n\tds_add_u32 %0, %1\n\ts_mov_b64 exec, -1" :: "v"(p), "v"(n) : "memory");
}
DEVI unsigned lds_ticket(lds_ptr p) {
  unsigned old;
  asm volatile("s_waitcnt lgkmcnt(0)\n\ts_mov_b64 exec, 1\n\tds_add_rtn_u32 %0, %1, %2\n\ts_mov_b64 exec, -1\n\ts_waitcnt lgkmcnt(0)"
               : "=&v"(old) : "v"(p), "v"(1u) : "memory");
  return __builtin_amdgcn_readfirstlane(old);
}
template <int SLEEP = 0>
DEVI void lds_wait(lds_ptr p, unsigned n) {
  unsigned v, t;
  asm volatile("1:\n\tds_read_b32 %0, %2\n\ts_waitcnt lgkmcnt(0)\n\tv_readfirstlane_b32 %1, %0\n\ts_nop 0\n\ts_cmp_ge_u32 %1, %3\n\t"
               "s_cbranch_scc1 2f\n\ts_sleep %4\n\ts_branch 1b\n2:"
               : "=&v"(v), "=&s"(t) : "v"(p), "s"(n), "i"(SLEEP) : "scc", "memory");
}
DEVI unsigned lds_peek(lds_ptr p) {
  unsigned v;
  asm volatile("ds_read_b32 %0, %1\n\ts_waitcnt lgkmcnt(0)" : "=&v"(v) : "v"(p) : "memory");
  return __builtin_amdgcn_readfirstlane(v);
}
#define LD_SC1_16(off) __builtin_bit_cast(u32x4, __builtin_amdgcn_raw_buffer_load_b128(rs, (off), 0, 16))
#define ST_SC1_16(v, off) __builtin_amdgcn_raw_buffer_store_b128((v), rs, (off), 0, 16)
#define ST_SC1_8(v, off) __builtin_amdgcn_raw_buffer_store_b64((v), rs, (off), 0, 16)
#define ST_SC1_4(v, off) __builtin_amdgcn_raw_buffer_store_b32((v), rs, (off), 0, 16)

DEVI f32x16 lo16(f32x32 v) { return __builtin_shufflevector(v, v, 0, 1, 2, 3, 4, 5, 6, 7, 8, 9, 10, 11, 12, 13, 14, 15); }
DEVI f32x16 hi16(f32x32 v) { return __builtin_shufflevector(v, v, 16, 17, 18, 19, 20, 21, 22, 23, 24, 25, 26, 27, 28, 29, 30, 31); }
DEVI bf16x16 raw_lo(f32x16 v) { return __builtin_bit_cast(bf16x16, __builtin_shufflevector(v, v, 0, 1, 2, 3, 4, 5, 6, 7)); }
DEVI bf16x16 raw_hi(f32x16 v) { return __builtin_bit_cast(bf16x16, __builtin_shufflevector(v, v, 8, 9, 10, 11, 12, 13, 14, 15)); }
constexpr int SPIDX = (int)0xddddddddu;
#define MFMA32(a, b, c) __builtin_amdgcn_mfma_f32_32x32x16_bf16((a), (b), (c), 0, 0, 0)
#define MFMA16(a, b, c) __builtin_amdgcn_mfma_f32_16x16x32_bf16((a), (b), (c), 0, 0, 0)

DEVI void online_step(f32x16 s, float& m, f32x16& o, bf16x8 va, bf16x8 vb) {
  float t = s[0];
#pragma unroll
  for (int i = 1; i < 16; ++i) t = __builtin_fmaxf(t, s[i]);
  t = __builtin_fmaxf(t, xhalf(t));
  const float mn = __builtin_fmaxf(m, t);
  const float al = __builtin_amdgcn_exp2f(m - mn);
  m = mn;
#pragma unroll
  for (int i = 0; i < 16; ++i) { o[i] *= al; s[i] = __builtin_amdgcn_exp2f(s[i] - mn); }
  o = __builtin_amdgcn_smfmac_f32_32x32x32_bf16(va, raw_lo(s), o, SPIDX, 0, 0);
  o = __builtin_amdgcn_smfmac_f32_32x32x32_bf16(vb, raw_hi(s), o, SPIDX, 0, 0);
}

#define UNIT_LOAD(CP, TG)                                                                                        \
  const int uc0 = 72 * hg + 2 * (CP), ut0 = 64 * qt + 8 * (TG);                                                  \
  const u32x4 usw = *(const u32x4*)(shk + uc0 * 4);                                                              \
  unsigned ukr[11], uvr[8];                                                                                      \
  {                                                                                                              \
    const unsigned bo = (ut0 * D_ + uc0) * 2, bo3 = ut0 ? bo - 3 * 1152 : bo;                                    \
    _Pragma("unroll") for (int i = 0; i < 3; ++i) ukr[i] = *(const unsigned*)((const char*)bk + bo3 + i * 1152); \
    _Pragma("unroll") for (int i = 0; i < 8; ++i) {                                                              \
      ukr[3 + i] = *(const unsigned*)((const char*)bk + bo + i * 1152);                                          \
      uvr[i] = *(const unsigned*)((const char*)bv + bo + i * 1152);                                              \
    }                                                                                                            \
  }
#define UNIT_DO()                                                                                                \
  u32x4 o0, o1;                                                                                                  \
  {                                                                                                              \
    const f32x2 w3 = {bf_hi(usw[1]), bf_hi(usw[3])}, w2 = {bf_lo(usw[1]), bf_lo(usw[3])};                        \
    const f32x2 w1 = {bf_hi(usw[0]), bf_hi(usw[2])}, w0 = {bf_lo(usw[0]), bf_lo(usw[2])};                        \
    f32x2 kf[11], r[8];                                                                                          \
    _Pragma("unroll") for (int i = 0; i < 11; ++i) {                                                             \
      const unsigned kk = (i < 3 && !ut0) ? 0u : ukr[i];                                                         \
      kf[i] = (f32x2){bf_lo(kk), bf_hi(kk)};                                                                     \
    }                                                                                                            \
    _Pragma("unroll") for (int i = 0; i < 8; ++i)                                                                \
      r[i] = __builtin_elementwise_fma(kf[i + 3], w0, __builtin_elementwise_fma(kf[i + 2], w1,                   \
             __builtin_elementwise_fma(kf[i + 1], w2, kf[i] * w3))) * (f32x2){bf_lo(uvr[i]), bf_hi(uvr[i])};     \
    _Pragma("unroll") for (int i = 0; i < 4; ++i) {                                                              \
      o0[i] = pk2(r[7 - 2 * i][0], r[6 - 2 * i][0]);                                                             \
      o1[i] = pk2(r[7 - 2 * i][1], r[6 - 2 * i][1]);                                                             \
    }                                                                                                            \
  }

#define CV_LD(F, D) _Pragma("unroll") for (int s = 0; s < 2; ++s) {                                              \
    F[4 * s] = lds_frag(ab + 32 * (2 * (D) + 1 - s)); F[4 * s + 1] = lds_frag(bb + 80 * (7 - (D)) + 32 * s);     \
    F[4 * s + 2] = lds_frag(bb + 80 * (39 - (D)) + 32 * s); F[4 * s + 3] = lds_frag(ab + 32 * (2 * (D) + 65 - s)); } \
  __builtin_amdgcn_sched_barrier(0)
#define CV_MM(F) _Pragma("unroll") for (int s = 0; s < 2; ++s) {                                                 \
    y0 = MFMA32(F[4 * s], F[4 * s + 1], y0); y1 = MFMA32(F[4 * s], F[4 * s + 2], y1);                            \
    y1 = MFMA32(F[4 * s + 3], F[4 * s + 1], y1); }                                                               \
  __builtin_amdgcn_sched_barrier(0)
#define SB0 __builtin_amdgcn_sched_barrier(0)
#define CV_STEP(F, N, D) _Pragma("unroll") for (int s = 0; s < 2; ++s) {                                         \
    y0 = MFMA32(F[4 * s], F[4 * s + 1], y0); SB0;                                                                \
    N[4 * s] = lds_frag(ab + 32 * (2 * (D) + 1 - s)); N[4 * s + 1] = lds_frag(bb + 80 * (7 - (D)) + 32 * s); SB0; \
    y1 = MFMA32(F[4 * s], F[4 * s + 2], y1); SB0;                                                                \
    N[4 * s + 2] = lds_frag(bb + 80 * (39 - (D)) + 32 * s); SB0;                                                 \
    y1 = MFMA32(F[4 * s + 3], F[4 * s + 1], y1); SB0;                                                            \
    N[4 * s + 3] = lds_frag(ab + 32 * (2 * (D) + 65 - s)); SB0; }
#define CV_PUT(P) _Pragma("unroll") for (int g = 0; g < 4; ++g) {                                                \
    *(LAS f32x4*)((P) + g * 1024) = (f32x4){y0[4 * g], y0[4 * g + 1], y0[4 * g + 2], y0[4 * g + 3]};             \
    *(LAS f32x4*)((P) + 4096 + g * 1024) = (f32x4){y1[4 * g], y1[4 * g + 1], y1[4 * g + 2], y1[4 * g + 3]}; }
DEVI f32x4 cv_sum(lds_ptr p, lds_ptr u, float dv) {
  const f32x4 s = (*(LAS f32x4*)(p) + *(LAS f32x4*)(p + 8192)) + (*(LAS f32x4*)(p + 16384) + *(LAS f32x4*)(p + 24576));
  const u32x2 uu = *(LAS u32x2*)u;
  return (f32x4){__builtin_fmaf(bf_hi(uu[1]), dv, s[0]), __builtin_fmaf(bf_lo(uu[1]), dv, s[1]), __builtin_fmaf(bf_hi(uu[0]), dv, s[2]), __builtin_fmaf(bf_lo(uu[0]), dv, s[3])};
}

__global__ void __launch_bounds__(768, 3) NOPK
fused_kernel(const __bf16* __restrict__ x, const __bf16* __restrict__ wq, const __bf16* __restrict__ wk,
             const __bf16* __restrict__ wv, const __bf16* __restrict__ wo, const __bf16* __restrict__ bk,
             const __bf16* __restrict__ bv, const __bf16* __restrict__ shk, const __bf16* __restrict__ ssmk,
             const __bf16* __restrict__ ssmd, const __bf16* __restrict__ gw, __bf16* __restrict__ out, char* ws) {
  __shared__ __attribute__((aligned(16))) char smem[160 * 1024];
  STAMP_DECL; STAMP(0);
  const int tid = TIDX, lane = tid & 63;
  const int w = __builtin_amdgcn_readfirstlane(tid >> 6);
  const int bid = BIDX, jj = bid >> 3;
  const unsigned sbase = (unsigned)(size_t)smem;
  if (w == 0) {
    unsigned long long pc0;
    unsigned len;
    asm volatile("s_getpc_b64 %0\n\tfk_pc:\n\ts_mov_b32 %1, fk_end-fk_pc" : "=s"(pc0), "=s"(len));
    TOUCH(pc0, UMIN((unsigned)(lane * 8 + (jj & 7)) * 128u, len));
  }
  const int qt = (bid & 7) * 4 + (jj & 3), hg = jj >> 2;
  lds_ptr sm = (lds_ptr)smem;
  lds_ptr mi = sm + L_M;
  const int h = lane >> 5, l31 = lane & 31, l15 = lane & 15, g4 = lane >> 4;
  const int tq = (lane & 15) >> 2, tp = lane & 3;
  const auto rs = __builtin_amdgcn_make_buffer_rsrc(ws, 0, WS_END, 0x00020000);
  GAS char* gws = (GAS char*)ws;
  const int rot = (hg + qt) % 3;
  const int s0 = bid & 7;
  const int tq4 = hg >> 1, sel = hg & 1;
  const char* xt = (const char*)x + (size_t)qt * 73728;
  const char* wqs = (const char*)wq + 144 * hg;
  const unsigned vxe = (lane >> 3) * 1152 + (((lane & 7) ^ g4) << 4), vxo = 8192 + (lane >> 3) * 1152 + (((lane & 7) ^ (4 + g4)) << 4);
  const int otok = tid / 9, opc = tid - 9 * otok;

  u32x4 zv, xv;

  if (w == 4) *(LAS int*)(mi + lane * 4) = (lane == M_RDY / 4 + 2 || lane == M_RDY / 4 + 3) ? -8 : (lane == 0 ? -1 : 0);
  auto zscatter = [&](int n) LAMI {
    lds_ptr zp = sm + L_ZT + ((n & 7) * 72 + (n >> 3)) * 2;
#pragma unroll
    for (int e = 0; e < 8; ++e)
      *(LAS unsigned short*)(zp + e * 1152) = (unsigned short)((e & 1) ? zv[e >> 1] >> 16 : zv[e >> 1]);
    lds_signal(mi + M_ZS);
  };
  if (w >= 8 && w < 11) {
    const int j = w - 8;
    const int th = (j + rot) % 3;
    const unsigned stg = L_B + 2 * 27648 + j * 9216;
#pragma unroll
    for (int c = 0; c < 3; ++c) GLDSX2(xt + tq4 * 18432 + (3 * th + c) * 128, sbase + L_X + (3 * j + c) * 8192 + tq4 * 2048);
    {
      const char* wsrc = (const char*)(sel ? wv : wk) + th * 6912 - 2048;
      unsigned vo[3];
#pragma unroll
      for (int i = 0; i < 3; ++i) {
        const int f = 64 * i + lane, row = f / 3, pc = f - 3 * row;
        vo[i] = row * 36 + (pc == 2 ? 20 : 16 * pc) + 2048 - 1024 * i;
      }
#pragma unroll
      for (int p = 0; p < 3; ++p) GLDS3("1024", "2048", vo[0], vo[1], vo[2], wsrc + p * 2304, sbase + stg + p * 3072);
    }
    glds(gw + 192 * th, UMIN(lane, 23) * 16, sbase + L_KVP + j * 2048);
  }
  unsigned epoch = 0;
  if (w == 11) epoch = __hip_atomic_load((gu32*)(gws + WS_EPOCH), RLX_AGENT);
  PSTAMP(5);
  BAR();
  PSTAMP(6);
  if (w >= 5 && w < 8) {
    const int wi = w - 5;
    unsigned vw[3];
#pragma unroll
    for (int m = 0; m < 3; ++m) {
      const int f = 64 * (3 * wi + m) + lane, row = f / 9;
      vw[m] = row * 1152 + (f - 9 * row) * 16 - 1024 * m;
    }
    const char* xs = xt + (1 - (tq4 >> 1)) * 36864 + wi * 128;
    const char* xs2 = xt + (tq4 ^ 1) * 18432 + wi * 128;
    const unsigned xd = sbase + L_X + wi * 8192 + (1 - (tq4 >> 1)) * 4096, xd2 = sbase + L_X + wi * 8192 + (tq4 ^ 1) * 2048;
    const unsigned wd = sbase + L_B + wi * 3072;
    const int t1 = (rot + 1) % 3, t2 = (rot + 2) % 3;
    GLDSX(xs + rot * 384, xd);
    GLDSX2(xs2 + rot * 384, xd2);
#pragma unroll
    for (int per = 0; per < 3; ++per) GLDS3("1024", "2048", vw[0], vw[1], vw[2], wqs + (192 * rot + 64 * per) * 1152, wd + per * 9216);
    GLDSX(xs + t1 * 384, xd + 24576);
    GLDSX2(xs2 + t1 * 384, xd2 + 24576);
    GLDS3("1024", "2048", vw[0], vw[1], vw[2], wqs + 192 * t1 * 1152, wd + 27648);
    VM_WAIT(9);
    lds_signal(mi + M_C1);
#pragma unroll
    for (int per = 1; per < 3; ++per) GLDS3("1024", "2048", vw[0], vw[1], vw[2], wqs + (192 * t1 + 64 * per) * 1152, wd + 27648 + per * 9216);
    GLDSX(xs + t2 * 384, xd + 49152);
    GLDSX2(xs2 + t2 * 384, xd2 + 49152);
    lds_wait(mi + M_KVC, 3);
    GLDS3("1024", "2048", vw[0], vw[1], vw[2], wqs + 192 * t2 * 1152, wd + 55296);
    VM_WAIT(9);
    lds_signal(mi + M_C2);
#pragma unroll
    for (int per = 1; per < 3; ++per) GLDS3("1024", "2048", vw[0], vw[1], vw[2], wqs + (192 * t2 + 64 * per) * 1152, wd + 55296 + per * 9216);
    STAMP(1);
    VM_WAIT(0);
    lds_signal(mi + M_C3);
    lds_signal(mi + M_SE);
  }

  if (w >= 8) {
    const int j = w - 8, ht = tid - 512;
    __builtin_amdgcn_s_setprio(3);
    if (j < 3) {
      const unsigned stg = L_B + 2 * 27648 + j * 9216;
      lds_ptr wb = sm + stg + (8 * g4 + tq) * 48 + tp * 8;
      lds_ptr wc = sm + stg + (8 * g4 + tq) * 48 + 32 + (tp & 1) * 8;
      lds_ptr xb = sm + L_X + 3 * j * 8192 + (16 * tq4 + l15) * 128;
      VM_WAIT(0);
      STAMP(1);
      bf16x8 fa[6], fc[6], fb[6], fg[6];
#pragma unroll
      for (int k = 0; k < 6; ++k) {
        fg[k] = lds_frag(sm + L_KVP + j * 2048 + k * 64 + g4 * 16);
        fb[k] = lds_frag(xb + (k >> 1) * 8192 + (((4 * (k & 1) + g4) ^ (l15 >> 1)) << 4));
        fa[k] = tr_frag(wb + k * 1536, wb + k * 1536 + 192);
        fc[k] = tr_frag(wc + k * 1536, wc + k * 1536 + 192);
      }
      f32x4 acc = {0}, acr = {0};
      if (sel) {
#pragma unroll
        for (int k = 0; k < 6; ++k) { acc = MFMA16(fb[k], fa[k], acc); acr = MFMA16(fb[k], fc[k], acr); }
      } else {
#pragma unroll
        for (int k = 0; k < 6; ++k) { acc = MFMA16(fa[k], fb[k], acc); acr = MFMA16(l15 == 8 ? fg[k] : fc[k], fb[k], acr); }
      }
      *(LAS f32x4*)(sm + L_KVP + j * 2048 + lane * 16) = acc;
      *(LAS f32x4*)(sm + L_KVP + j * 2048 + 1024 + lane * 16) = acr;
      lds_signal(mi + M_KVC);
      lds_signal(mi + M_C1);
      STAMP(2);
      lds_wait<1>(mi + M_KD, 1);
    } else {
      const unsigned tg = __builtin_amdgcn_readfirstlane(epoch) + 1u;
      if (lane == 0) *(LAS unsigned*)(mi + M_TAG) = tg;
      const int T = 2 * qt + (tq4 >> 1), mt = tq4 & 1, i4 = T & 3, gb = (T >> 2) * KV_GROUP, rp = swap23(16 * mt + l15);
      int sa, sr;
      bool st;
      if (sel) {
        sa = gb + 4096 + i4 * 1216 + mt * 608 + (l15 + 18 * (g4 & 1)) * 16 + (g4 >> 1) * 8;
        sr = gb + 4096 + i4 * 1216 + mt * 608 + (l15 < 8 ? 10 + l15 + 18 * (g4 & 1) : 36 + (g4 & 1)) * 16 + (g4 >> 1) * 8;
        st = l15 >= 6 && l15 <= 8;
      } else {
        sa = gb + i4 * 1024 + (rp + 32 * (g4 & 1)) * 16 + (g4 >> 1) * 8;
        sr = gb + 8960 + i4 * 256 + rp * 8;
        st = g4 == 1;
      }
      lds_wait(mi + M_KVC, 3);
      lds_ptr pp = sm + L_KVP + lane * 16;
      const float ks = sel ? 1.f : 0.23570226039551584f * 1.4426950408889634f;
      const f32x4 va = (*(LAS f32x4*)(pp) + *(LAS f32x4*)(pp + 2048) + *(LAS f32x4*)(pp + 4096)) * ks;
      const f32x4 vs = *(LAS f32x4*)(pp + 1024) + *(LAS f32x4*)(pp + 3072) + *(LAS f32x4*)(pp + 5120), vr = vs * ks;
      const u32x2 a = {pk2(va[0], va[1]), pk2(va[2], va[3])};
      u32x2 r = {pk2(vr[0], vr[1]), pk2(vr[2], vr[3])};
      if (!sel) r = (u32x2){r[1], 0x3f803f80u};
      else if (l15 == 8) r[0] = r[1] = (g4 & 1) ? 0u : 0x3f803f80u;
      ST_SC1_16(((u32x4){a[0], tg, a[1], tg}), WS_KT + 2 * sa);
      if (st) ST_SC1_16(((u32x4){r[0], tg, r[1], tg}), WS_KT + 2 * sr);
      ST_SC1_8(a, WS_KV + sa);
      if (st) ST_SC1_8(r, WS_KV + sr);
      STAMP(1);
      lds_signal(mi + M_KD);
      if (!sel && g4 == 2) {
        const float g = rbf(__builtin_amdgcn_rcpf(1.f + __builtin_amdgcn_exp2f(-1.4426950408889634f * rbf(vs[0]))));
        ST_SC1_4(__builtin_bit_cast(unsigned, g), WS_G + (64 * qt + 16 * tq4 + l15) * 4);
      }
      float n2 = dot2(a[1], dot2(a[0], st ? dot2(r[0], 0.f) : 0.f));
      n2 += xhalf(n2);
      n2 += u2f(__builtin_amdgcn_ds_swizzle(__builtin_bit_cast(int, n2), 0x401f));
      n2 = sel ? 0.f : max16(n2);
      VM_WAIT(0);
      if (lane == 0)
        __hip_atomic_store((gu64*)(gws + WS_FKV + T * 128 + (hg & 3) * 8), ((unsigned long long)tg << 32) | __builtin_bit_cast(unsigned, n2), RLX_AGENT);
      STAMP(2);
    }
    const unsigned tag = *(LAS unsigned*)(mi + M_TAG);
    auto kv_tagged = [&](int st) LAMI {
      u32x4 r[10];
      const int o = WS_KT + ((s0 + st) & 7) * (2 * RSLOT) + lane * 16;
#pragma unroll 1
      for (unsigned spins = 0; spins < SPIN_CAP; ++spins) {
        unsigned bad = 0;
#pragma unroll
        for (int i = 0; i < 10; ++i) r[i] = LD_SC1_16(o + (i == 9 && j == 3 ? 38 : 4 * i + j) * 1024);
#pragma unroll
        for (int i = 0; i < 10; ++i) bad |= (r[i][1] ^ tag) | (r[i][3] ^ tag);
        asm volatile("" ::: "memory");
        if (ALL(bad == 0)) break;
        __builtin_amdgcn_s_sleep(POLL_SLEEP);
      }
      if (st == 0) {
        if (j == 0) {
          const float kn = dot2(r[0][2], dot2(r[0][0], 0.f));
          *(LAS float*)(mi + M_KN0) = wave_max(dpp_add<0xb1>(kn));
        }
        lds_wait(mi + M_C3B, 5);
      }
#pragma unroll
      for (int i = 0; i < 10; ++i)
        *(LAS u32x2*)(sm + st * RSLOT + (i == 9 && j == 3 ? 38 : 4 * i + j) * 512 + lane * 8) = (u32x2){r[i][0], r[i][2]};
    };
    auto kv_dma = [&](int st) LAMI {
      const char* src = ws + WS_KV + ((s0 + st) & 7) * RSLOT + j * 4992;
      const unsigned dst = sbase + st * RSLOT + j * 4992;
      GLDS4(" sc1", src, lane * 16, lane * 16, dst);
      if (lane < 56) glds_sc1(src + 4096, lane * 16, dst + 4096);
    };
    const unsigned vh3 = (lane == 63 ? 62 : lane) * 16;
    auto hload = [&](int cl, int b) LAMI {
      const char* hsrc = (const char*)(ssmk + (size_t)(72 * hg + cl) * S_) + 4 * j + 2;
      const unsigned dst = sbase + CV_H + b * CV_HB + 2 * j * CV_COPY + 64;
      GLDS4("", hsrc, lane * 16, vh3, dst);
      GLDS4("", hsrc + 2, lane * 16, vh3, dst + CV_COPY);
    };
    auto uload = [&](int cl) LAMI {
      const int tgq = ht & 7;
      const int o = cl < 64 ? (tgq >> 1) * 2048 + (cl & 1) * 1024 + ((tgq & 1) * 32 + (cl >> 1)) * 16
                            : 8192 + (cl & 1) * 512 + (tgq * 4 + ((cl - 64) >> 1)) * 16;
      return LD_SC1_16(WS_UT + ((ht >> 3) * 8 + hg) * 9216 + o);
    };
    const int ce = 64 + (qt >> 2);
    const unsigned short h8a = *(const unsigned short*)(ssmk + (size_t)(72 * hg + 2 * qt + (j >> 1)) * S_ + (lane & 7));
    const unsigned short h8e = *(const unsigned short*)(ssmk + (size_t)(72 * hg + ce) * S_ + (lane & 7));
    const float dve = (qt & 3) ? 0.f : (float)ssmd[72 * hg + ce];
    {
      lds_wait<1>(mi + POLL_AT, POLL_N);
      kv_tagged(0);
      lds_signal(mi + M_QD);
      STAMP(3);
      kv_tagged(1);
      lds_signal(mi + M_RDY + 4, 2);
      if (j == 3) {
        float kn = 0.f;
#pragma unroll 1
        for (unsigned spins = 0; spins < SPIN_CAP; ++spins) {
          const u32x4 v0 = LD_SC1_16(WS_FKV + lane * 128), v1 = LD_SC1_16(WS_FKV + lane * 128 + 16);
          const bool ok = v0[1] == tag && v0[3] == tag && v1[1] == tag && v1[3] == tag;
          kn = __builtin_fmaxf(u2f(v0[0]), u2f(v1[0]));
          asm volatile("" ::: "memory");
          if (ALL(ok)) break;
          __builtin_amdgcn_s_sleep(POLL_SLEEP);
        }
        lds_signal(mi + M_QR);
        lds_signal(mi + M_DONE);
        kn = wave_max(kn);
        if (lane == 0) *(LAS float*)(mi + M_KN) = kn;
      }
      lds_wait<1>(mi + M_QR, 9);
      kv_dma(2);
      kv_dma(3);
      VM_WAIT(0);
      lds_signal(mi + M_RDY + 8, 2);
      lds_signal(mi + M_RDY + 12, 2);
      __builtin_amdgcn_s_setprio(H_PRIO);
      UNIT_LOAD(l31, 2 * j + h);
      if (j == 1) {
        const int c0 = 72 * hg + 64 + 2 * (lane & 3), t0 = 64 * qt + 4 * (lane >> 2);
        const u32x4 sw = *(const u32x4*)(shk + c0 * 4);
        const unsigned bo = (t0 * D_ + c0) * 2, bo3 = t0 ? bo - 3 * 1152 : bo;
        unsigned kk[7], vv[4];
#pragma unroll
        for (int i = 0; i < 7; ++i) kk[i] = *(const unsigned*)((const char*)bk + (i < 3 ? bo3 + i * 1152 : bo + (i - 3) * 1152));
#pragma unroll
        for (int i = 0; i < 4; ++i) vv[i] = *(const unsigned*)((const char*)bv + bo + i * 1152);
        if (!t0) kk[0] = kk[1] = kk[2] = 0u;
        float ra[4], rb[4];
#pragma unroll
        for (int i = 0; i < 4; ++i) {
          ra[i] = __builtin_fmaf(bf_lo(kk[i + 3]), bf_lo(sw[0]), __builtin_fmaf(bf_lo(kk[i + 2]), bf_hi(sw[0]),
                  __builtin_fmaf(bf_lo(kk[i + 1]), bf_lo(sw[1]), bf_lo(kk[i]) * bf_hi(sw[1])))) * bf_lo(vv[i]);
          rb[i] = __builtin_fmaf(bf_hi(kk[i + 3]), bf_lo(sw[2]), __builtin_fmaf(bf_hi(kk[i + 2]), bf_hi(sw[2]),
                  __builtin_fmaf(bf_hi(kk[i + 1]), bf_lo(sw[3]), bf_hi(kk[i]) * bf_hi(sw[3])))) * bf_hi(vv[i]);
        }
        const int uo = WS_UT + (qt * 8 + hg) * 9216 + 8192 + ((lane >> 3) * 4 + (lane & 3)) * 16 + (1 - ((lane >> 2) & 1)) * 8;
        ST_SC1_8(((u32x2){pk2(ra[3], ra[2]), pk2(ra[1], ra[0])}), uo);
        ST_SC1_8(((u32x2){pk2(rb[3], rb[2]), pk2(rb[1], rb[0])}), uo + 512);
      }
      VM_WAIT(0);
      UNIT_DO();
      ST_SC1_16(o0, WS_UT + (qt * 8 + hg) * 9216 + j * 2048 + lane * 16);
      ST_SC1_16(o1, WS_UT + (qt * 8 + hg) * 9216 + j * 2048 + 1024 + lane * 16);
      {
        const u32x4 ones = {~0u, ~0u, ~0u, ~0u};
        const int ob = WS_O + 2 * qt * 36 * 1024 + (4 * hg + j) * 1024 + lane * 16;
        ST_SC1_16(ones, ob);
        ST_SC1_16(ones, ob + 36 * 1024);
        if (j == 0) ST_SC1_16(ones, WS_O + (2 * qt + h) * 36 * 1024 + 32768 + hg * 512 + l31 * 16);
        ST_SC1_16(ones, WS_Z + (72 * hg + 2 * qt) * 4096 + ht * 16);
        ST_SC1_16(ones, WS_Z + (72 * hg + 2 * qt + 1) * 4096 + ht * 16);
        ST_SC1_16(ones, WS_ZP + (hg * 32 + qt) * 8192 + ht * 16);
        ST_SC1_16(ones, WS_ZP + (hg * 32 + qt) * 8192 + 4096 + ht * 16);
      }
      STAMP(4);
      VM_WAIT(0);
      if (lds_ticket(mi + M_UD) == 3u && lane == 0) __hip_atomic_store((gu32*)(gws + WS_FU) + (hg * 32 + qt) * 32, tag, RLX_AGENT);
    }
    auto cvprep = [&](int b, unsigned short h8) LAMI {
      *(LAS u32x4*)(sm + CV_U + (ht >> 7) * CV_UB + ((ht & 127) >> 2) * 80 + (ht & 3) * 16) = (u32x4){0, 0, 0, 0};
      lds_ptr hz = sm + CV_H + b * CV_HB + (lane >> 3) * CV_COPY;
      *(LAS u32x2*)(hz + (lane & 7) * 8) = (u32x2){0, 0};
      if ((lane & 7) <= (lane >> 3)) *(LAS unsigned short*)(hz + 48 + (7 - (lane >> 3) + (lane & 7)) * 2) = h8;
    };
    unsigned hs = 0;
#define HSYNC() do { lds_signal(mi + M_HS); hs += 4; lds_wait<2>(mi + M_HS, hs); } while (0)
    if (lane < 54) {
      const unsigned vo = (lane / 9) * 1152 + (lane % 9) * 16;
#pragma unroll 1
      for (int p = 24 * j; p < 24 * j + 24; p += 4)
        asm volatile("s_mov_b32 m0, %5\n\ts_nop 0\n\tglobal_load_lds_dwordx4 %0, %4\n\tglobal_load_lds_dwordx4 %1, %4 offset:864\n\t"
                     "global_load_lds_dwordx4 %2, %4 offset:1728\n\tglobal_load_lds_dwordx4 %3, %4 offset:2592\n\ts_waitcnt vmcnt(" WO_INFL ")"
                     :: "v"(vo), "v"(vo + 6048), "v"(vo + 12096), "v"(vo + 18144), "s"((const char*)wo + 144 * hg + p * 6912),
                        "s"(sbase + L_B + p * 864) : "memory");
    }
    VM_WAIT(0);
    HSYNC();
    bf16x8 wf[18], we[5];
    {
      const int cb = (32 * (j & 1) + 16 * (g4 & 1) + 4 * tp) * 2;
      lds_ptr wm = sm + L_B + (DH * (j >> 1) + 4 * h + tq) * 144 + cb;
      lds_ptr wr = sm + L_B + (DH * (8 * (j >> 1) + 4 * h + (tq >> 1)) + 16 + (tq & 1)) * 144 + cb;
      asm volatile("" : "+v"(wm), "+v"(wr));
#pragma unroll
      for (int k = 0; k < 12; ++k) wf[k] = tr_frag(wm + k * 5184, wm + k * 5184 + 1152);
      wm += 12 * 5184;
      asm volatile("" : "+v"(wm));
#pragma unroll
      for (int k = 0; k < 4; ++k) wf[12 + k] = tr_frag(wm + k * 5184, wm + k * 5184 + 1152);
#pragma unroll
      for (int k = 0; k < 2; ++k) wf[16 + k] = tr_frag(wr + k * 41472, wr + k * 41472 + 5184);
      lds_ptr em = sm + L_B + (DH * (8 * j + (g4 >> 1)) + 4 * (g4 & 1) + tq) * 144 + (64 + 4 * (tp & 1)) * 2;
      lds_ptr er = sm + L_B + (DH * (16 * (j & 1) + 4 * g4 + (tq >> 1)) + 16 + (tq & 1)) * 144 + (64 + 4 * (tp & 1)) * 2;
      asm volatile("" : "+v"(em), "+v"(er));
#pragma unroll
      for (int k = 0; k < 4; ++k) we[k] = tr_frag(em + k * 5184, em + k * 5184 + 1152);
      we[4] = tr_frag(er, er + 5184);
    }
    HSYNC();
    HSTAMP(7);
    cvprep(j >> 1, h8a);
    if (j == 3) {
#pragma unroll 1
      for (unsigned spins = 0; spins < SPIN_CAP; ++spins) {
        bool ok = lane >= 40 || __hip_atomic_load((gu32*)(gws + WS_FU) + (h ? l31 * 32 + qt : hg * 32 + lane) * 32, RLX_AGENT) == tag;
        if (bid == 0) {
#pragma unroll
          for (int i = 0; i < 4; ++i) ok &= __hip_atomic_load((gu32*)(gws + WS_FU) + (64 * i + lane) * 32, RLX_AGENT) == tag;
        }
        if (ALL(ok)) break;
        __builtin_amdgcn_s_sleep(8);
      }
      if (bid == 0 && lane == 0) __hip_atomic_store((gu32*)(gws + WS_EPOCH), tag, RLX_AGENT);
      lds_signal(mi + M_UR);
    } else {
      lds_wait<4>(mi + M_UR, 1);
    }
    NSTAMP(5);
    __builtin_amdgcn_s_setprio(3);
    lds_wait<8>(mi + M_DONE + 16, 8);
    hload(2 * qt, 0);
    hload(2 * qt + 1, 1);
    if (j == 1) *(LAS unsigned*)(mi + M_G + lane * 4) = __builtin_amdgcn_raw_buffer_load_b32(rs, WS_G + (64 * qt + lane) * 4, 0, 16);
    *(LAS u32x4*)(sm + CV_U + 2560 + (ht >> 2) * 80 + (ht & 3) * 16) = uload(2 * qt);
    *(LAS u32x4*)(sm + CV_U + CV_UB + 2560 + (ht >> 2) * 80 + (ht & 3) * 16) = uload(2 * qt + 1);
    const u32x4 ue = uload(ce);
    if (j == 0) xv = *(const u32x4*)(xt + otok * 1152 + 144 * hg + opc * 16);
    if (j == 2 && lane < 2) *(LAS float*)(mi + M_DV + lane * 4) = (float)ssmd[72 * hg + 2 * qt + lane];
    VM_WAIT(0);
#pragma unroll
    for (int i = 0; i < 4; ++i) lds_signal(mi + M_PAIR + 4 * i);
    NSTAMP(6);
    {
      const int p = qt & 3;
      lds_wait<8>(mi + M_DONE + 20, 8);
      if (lane < 35) {
        const char* hsrc = (const char*)(ssmk + (size_t)(72 * hg + ce) * S_) + 4 * j + 2 + 512 * p - 64;
        const unsigned dst = sbase + E_H + 2 * j * E_COPY;
        glds(hsrc + 2048, lane * 16, dst + 640);
        glds(hsrc + 2050, lane * 16, dst + E_COPY + 640);
        if (p || lane >= 4) {
          glds(hsrc, lane * 16, dst);
          glds(hsrc + 2, lane * 16, dst + E_COPY);
        }
      }
      if (!p && j == 0) {
        lds_ptr hz = sm + E_H + (lane >> 3) * E_COPY;
        *(LAS u32x2*)(hz + (lane & 7) * 8) = (u32x2){0, 0};
        if ((lane & 7) <= (lane >> 3)) *(LAS unsigned short*)(hz + 48 + (7 - (lane >> 3) + (lane & 7)) * 2) = h8e;
      }
      if (ht < 160) *(LAS u32x4*)(sm + E_U + ht * 16) = (u32x4){0, 0, 0, 0};
      lds_ptr ab = sm + E_H + (l31 & 7) * E_COPY + (h + (l31 >> 3)) * 16 + 128 * j;
      lds_ptr bb = sm + E_U + 2560 + (l31 - 8 * p - 2 * j - 1) * 80 + (1 - h) * 16;
      asm volatile("" : "+v"(ab), "+v"(bb));
      lds_wait<8>(mi + M_DONE + 24, 8);
      *(LAS u32x4*)(sm + E_U + 2560 + (ht >> 2) * 80 + (ht & 3) * 16) = ue;
      VM_WAIT(0);
      lds_signal(mi + M_DONE + 28);
      lds_wait<2>(mi + M_DONE + 28, 12);
      f32x16 y0 = {0}, y1 = {0};
#pragma unroll
      for (int d = 0; d < 2; ++d) {
#pragma unroll
        for (int s = 0; s < 2; ++s) {
          const bf16x8 a0 = lds_frag(ab + 32 * (2 * d + 1 - s)), b0 = lds_frag(bb + 80 * (1 - d) + 32 * s);
          const bf16x8 b1 = lds_frag(bb + 80 * (33 - d) + 32 * s), a1 = lds_frag(ab + 640 + 32 * (2 * d + 1 - s));
          y0 = MFMA32(a0, b0, y0);
          y1 = MFMA32(a0, b1, y1);
          y1 = MFMA32(a1, b0, y1);
          SB0;
        }
      }
      __builtin_amdgcn_s_setprio(HT_PRIO);
      __builtin_amdgcn_s_sleep(EP_DELAY);
      CV_PUT(sm + E_P + j * 8192 + lane * 16);
      HSYNC();
#pragma unroll
      for (int q = 0; q < 2; ++q) {
        u32x4 t = __builtin_bit_cast(u32x4, cv_sum(sm + E_P + (2 * j + q) * 1024 + lane * 16,
                                                   sm + E_U + 2560 + (32 * (j >> 1) + l31) * 80 + (2 * (j & 1) + q) * 16 + (1 - h) * 8, dve));
#pragma unroll
        for (int e = 0; e < 4; ++e) t[e] = UMIN(t[e], 0xfffffffeu);
        ST_SC1_16(t, WS_ZP + (hg * 32 + qt) * 8192 + (2 * j + q) * 1024 + lane * 16);
      }
      lds_signal(mi + M_EXD);
    }
    HSTAMP(8);
    __builtin_amdgcn_s_setprio(HT_PRIO);
#pragma unroll 1
    for (int t = 0; t < 2; ++t) {
      lds_ptr ob = sm + (36 * t + (j >> 1)) * 1024 + lane * 16;
      lds_ptr oe = sm + (36 * t + 8 * j + (g4 >> 1)) * 1024 + (l15 + 32 * (g4 & 1)) * 16;
      lds_ptr or_ = sm + (36 * t + 32 + 2 * (j & 1) + (g4 >> 1)) * 1024 + (16 * (j >> 1) + l15 + 32 * (g4 & 1)) * 16;
      lds_wait<1>(mi + (t ? M_OR : M_KVR), 4);
      if (t) NSTAMP(9);
      f32x16 a0 = {0};
      f32x4 e0 = {0}, e1 = {0};
      bf16x8 f[27];
#pragma unroll
      for (int k = 0; k < 31; ++k) {
        if (k < 18) f[k] = lds_frag(ob + k * 2048);
        else if (k < 26) f[k] = lds_frag(oe + ((k - 18) >> 2) * 256 + ((k - 18) & 3) * 2048);
        else if (k == 26) f[k] = lds_frag(or_);
        SB0;
        const int m = k - 4;
        if (m >= 0 && m < 18) a0 = MFMA32(wf[m], f[m], a0);
        else if (m >= 18 && m < 22) e0 = MFMA16(we[m - 18], f[m], e0);
        else if (m >= 22 && m < 26) e1 = MFMA16(we[m - 22], f[m], e1);
        else if (m == 26) { if (j >> 1) e1 = MFMA16(we[4], f[m], e1); else e0 = MFMA16(we[4], f[m], e0); }
        SB0;
      }
      if (t) NSTAMP(10);
      lds_wait<1>(mi + M_MM, 4);
      lds_ptr ap = sm + L_AP + (8 * (j >> 1) + 4 * t) * CV_COPY;
#pragma unroll
      for (int g = 0; g < 4; ++g)
        *(LAS f32x4*)(ap + (l31 >> 3) * CV_COPY + (l31 & 7) * 336 + (j & 1) * 128 + h * 16 + g * 32) = (f32x4){a0[4 * g], a0[4 * g + 1], a0[4 * g + 2], a0[4 * g + 3]};
      if (!h) {
        lds_ptr ae = ap + (l15 >> 3) * CV_COPY + (l15 & 7) * 336 + 256 + (j & 1) * 32 + g4 * 16;
        *(LAS f32x4*)(ae) = e0;
        *(LAS f32x4*)(ae + 2 * CV_COPY) = e1;
      }
      lds_signal(mi + (t ? M_APR1 : M_APR));
    }
  } else {
    if (w < 4) {
      const int gt = w >> 1, tt = w & 1, arow = 32 * tt + l31, xsw = (arow >> 1) & 7;
      lds_ptr xb = sm + L_X + arow * 128;
      lds_ptr wb = sm + L_B + (8 * h + tq) * 144 + (36 * gt + 20 * (g4 & 1) + 4 * tp) * 2;
      f32x16 acc = {0};
#pragma unroll 1
      for (int p = 0; p < 3; ++p) {
        if (p == 1) CSTAMP(6);
        if (p == 2) CSTAMP(8);
        lds_wait(mi + M_C1 + 4 * p, p == 1 ? 3 : 6 + p);
        if (p == 0) CSTAMP(5);
        if (p == 1) CSTAMP(7);
        if (p == 2) CSTAMP(9);
        bf16x8 fa[12], fb[12];
#pragma unroll
        for (int k = 0; k < 16; ++k) {
          if (k < 12) {
            fa[k] = tr_frag(wb + k * 2304, wb + k * 2304 + 576);
            fb[k] = lds_frag(xb + (k >> 2) * 8192 + (((2 * (k & 3) + h) ^ xsw) << 4));
          }
          SB0;
          if (k >= 4) acc = MFMA32(fa[k - 4], fb[k - 4], acc);
          SB0;
        }
        if (p < 2) lds_signal(mi + M_C3 + 4 * p);
        xb += 24576;
        wb += 27648;
      }
      STAMP(2);
      u32x4 a;
#pragma unroll
      for (int i = 0; i < 4; ++i) a[i] = pk2(acc[2 * i], acc[2 * i + 1]);
      lds_ptr qe = sm + L_Q + (4 * gt + tt) * 1024;
      *(LAS u32x4*)(qe + lane * 16) = a;
      lds_ptr qo = qe + 2048 + l31 * 16;
      *(LAS unsigned*)(qo + h * 512 + 4) = pk2(acc[8], acc[9]);
      *(LAS unsigned*)(qo + (h ? 8 : 512)) = pk2(acc[10], acc[11]);
      *(LAS unsigned*)(qo + h * 512 + 12) = pk2(acc[12], acc[13]);
      *(LAS unsigned*)(h ? sm + L_Q + 8192 + ((2 * gt + 1) * 64 + arow) * 4 : qo + 520) = pk2(acc[14], acc[15]);
    } else if (w == 4) {
      __builtin_amdgcn_s_setprio(2);
      lds_ptr xb = sm + L_X + l15 * 128;
      lds_ptr wb = sm + L_B + (8 * g4 + tq) * 144 + (tp == 1 ? 104 : 32);
      f32x4 acc[4] = {{0}, {0}, {0}, {0}};
#pragma unroll 1
      for (int p = 0; p < 3; ++p) {
        lds_wait(mi + M_C1 + 4 * p, p == 1 ? 3 : 6 + p);
        bf16x8 fa[6], fb[24];
#pragma unroll
        for (int k = 0; k < 8; ++k) {
          if (k < 6) {
            fa[k] = tr_frag(wb + k * 4608, wb + k * 4608 + 576);
#pragma unroll
            for (int t = 0; t < 4; ++t) fb[4 * k + t] = lds_frag(xb + (k >> 1) * 8192 + t * 2048 + (((4 * (k & 1) + g4) ^ (l15 >> 1)) << 4));
          }
          SB0;
          if (k >= 2) {
#pragma unroll
            for (int t = 0; t < 4; ++t) acc[t] = MFMA16(fa[k - 2], fb[4 * (k - 2) + t], acc[t]);
          }
          SB0;
        }
        if (p < 2) lds_signal(mi + M_C3 + 4 * p);
        xb += 24576;
        wb += 27648;
      }
      STAMP(2);
      if (g4 < 2) {
#pragma unroll
        for (int t = 0; t < 4; ++t) {
          const int tok = 16 * t + l15;
          *(LAS unsigned*)(sm + L_Q + 8192 + (2 * g4 * 64 + tok) * 4) = pk2(acc[t][0], acc[t][1]);
          *(LAS unsigned*)(sm + L_Q + ((2 * g4 + 1) * 2 + (t >> 1)) * 1024 + (tok & 31) * 16) = pk2(acc[t][2], acc[t][3]);
        }
      }
    }
    lds_signal(mi + M_QD);
    __builtin_amdgcn_s_setprio(2);
    const int hl = w & 3, kh = w >> 2;
    const int voff = 4096 + (l31 < 18 ? h * 288 + l31 * 16 : (l31 == 18 ? 576 : 592));
    lds_wait(mi + M_QD, 12);
    STAMP(3);
    const u32x4 q0u = *(LAS u32x4*)(sm + L_Q + (2 * hl) * 1024 + lane * 16);
    const u32x4 q1u = *(LAS u32x4*)(sm + L_Q + (2 * hl + 1) * 1024 + lane * 16);
    const unsigned qr = *(LAS unsigned*)(sm + L_Q + 8192 + (hl * 64 + lane) * 4);
    const float kn0 = *(LAS float*)(mi + M_KN0);
    lds_signal(mi + M_QR);
    float n0 = 0.f, n1 = 0.f;
#pragma unroll
    for (int i = 0; i < 4; ++i) { n0 = dot2(q0u[i], n0); n1 = dot2(q1u[i], n1); }
    const float nq = dot2(qr, (h ? n1 : n0) + xhalf(h ? n0 : n1));
    const u32x2 qrem_u = {qr, pk2(-SH_A * __builtin_amdgcn_sqrtf(nq * kn0), -SH_C)};
    f32x16 o0, o1;
    float m0 = 0.f, m1 = 0.f;
    STAMP(4); RSTAMP(2);
    {
      int cnt = 8, off = 0, tmp, t2, so = ((s0 + 4) & 7) * RSLOT;
      unsigned ca = sbase + L_M + M_DONE - 4, dst = sbase + hl * 4992 + 2048;
      const unsigned long long ksrc = (unsigned long long)ws + WS_KV + hl * 4992 + 2048 - (sbase + kh * KV_GROUP);
      asm volatile(
      "v_mov_b32 v107, v126\n"
      "v_mov_b32 v108, v127\n"
      "v_mov_b32 v109, v128\n"
      "v_mov_b32 v130, 1\n"
      "ds_read2st64_b64 v[110:113], v109 offset0:0 offset1:1\n"
      "ds_read_b128 v[114:117], v107 offset:0\n"
      "ds_read_b128 v[118:121], v108 offset:0\n"
      "s_mov_b32 vcc_lo, %[first]\n"
      "s_mov_b32 vcc_hi, 0\n"
      "s_setprio 1\n"
      "v_mov_b32 v56, 0\n"
      "v_mov_b32 v57, 0\n"
      "v_mov_b32 v58, 0\n"
      "v_mov_b32 v59, 0\n"
      "v_mov_b32 v60, 0\n"
      "v_mov_b32 v61, 0\n"
      "v_mov_b32 v62, 0\n"
      "v_mov_b32 v63, 0\n"
      "v_mov_b32 v122, 0\n"
      "v_mov_b32 v123, 0\n"
      "v_mov_b32 v124, 0\n"
      "v_mov_b32 v125, 0\n"
      "v_mov_b32 v64, 0\n"
      "v_mov_b32 v65, 0\n"
      "v_mov_b32 v66, 0\n"
      "v_mov_b32 v67, 0\n"
      "v_mov_b32 v68, 0\n"
      "v_mov_b32 v69, 0\n"
      "v_mov_b32 v70, 0\n"
      "v_mov_b32 v71, 0\n"
      "v_mov_b32 v72, 0\n"
      "v_mov_b32 v73, 0\n"
      "v_mov_b32 v74, 0\n"
      "v_mov_b32 v75, 0\n"
      "v_mov_b32 v76, 0\n"
      "v_mov_b32 v77, 0\n"
      "v_mov_b32 v78, 0\n"
      "v_mov_b32 v79, 0\n"
      "s_waitcnt lgkmcnt(1)\n"
      "v_mfma_f32_32x32x4_2b_bf16 v[0:31], v[110:111], v[104:105], 0 cbsz:1 abid:0\n"
      "v_mov_b32 v80, 0\n"
      "v_mov_b32 v81, 0\n"
      "v_mov_b32 v82, 0\n"
      "v_mov_b32 v83, 0\n"
      "v_mov_b32 v84, 0\n"
      "v_mov_b32 v85, 0\n"
      "v_mov_b32 v86, 0\n"
      "v_mov_b32 v87, 0\n"
      "v_mov_b32 v88, 0\n"
      "v_mov_b32 v89, 0\n"
      "v_mov_b32 v90, 0\n"
      "v_mov_b32 v91, 0\n"
      "v_mov_b32 v92, 0\n"
      "v_mov_b32 v93, 0\n"
      "v_mov_b32 v94, 0\n"
      "v_mov_b32 v95, 0\n"
      "s_nop 2\n"
      "v_mfma_f32_32x32x16_bf16 v[0:15], v[114:117], v[96:99], v[0:15]\n"
      "v_mfma_f32_32x32x16_bf16 v[16:31], v[114:117], v[100:103], v[16:31]\n"
      "s_nop 11\n"
      "1:\n"
      "s_cbranch_vccz 4f\n"
      "s_setprio 2\n"
      "4:\n"
      "v_exp_f32 v0, v0\n"
      "v_exp_f32 v1, v1\n"
      "v_smfmac_f32_32x32x32_bf16 v[80:95], v[122:125], v[56:63], v106\n"
      "ds_read_b128 v[114:117], v107 offset:1024\n"
      "ds_read_b128 v[122:125], v108 offset:608\n"
      "v_exp_f32 v2, v2\n"
      "v_exp_f32 v3, v3\n"
      "v_exp_f32 v4, v4\n"
      "v_exp_f32 v5, v5\n"
      "v_mfma_f32_32x32x4_2b_bf16 v[32:63], v[110:111], v[104:105], 0 cbsz:1 abid:1\n"
      "v_exp_f32 v6, v6\n"
      "v_exp_f32 v7, v7\n"
      "v_exp_f32 v8, v8\n"
      "v_exp_f32 v9, v9\n"
      "v_exp_f32 v10, v10\n"
      "v_exp_f32 v11, v11\n"
      "v_exp_f32 v12, v12\n"
      "s_waitcnt lgkmcnt(2)\n"
      "v_exp_f32 v13, v13\n"
      "v_smfmac_f32_32x32x32_bf16 v[64:79], v[118:121], v[0:7], v106\n"
      "v_exp_f32 v14, v14\n"
      "v_exp_f32 v15, v15\n"
      "v_exp_f32 v16, v16\n"
      "s_waitcnt lgkmcnt(0)\n"
      "v_exp_f32 v17, v17\n"
      "v_smfmac_f32_32x32x32_bf16 v[64:79], v[122:125], v[8:15], v106\n"
      "v_exp_f32 v18, v18\n"
      "v_exp_f32 v19, v19\n"
      "v_exp_f32 v20, v20\n"
      "v_exp_f32 v21, v21\n"
      "v_mfma_f32_32x32x16_bf16 v[32:47], v[114:117], v[96:99], v[32:47]\n"
      "v_exp_f32 v22, v22\n"
      "v_exp_f32 v23, v23\n"
      "v_exp_f32 v24, v24\n"
      "v_exp_f32 v25, v25\n"
      "v_smfmac_f32_32x32x32_bf16 v[80:95], v[118:121], v[16:23], v106\n"
      "v_exp_f32 v26, v26\n"
      "ds_read_b128 v[118:121], v108 offset:1216\n"
      "v_exp_f32 v27, v27\n"
      "v_exp_f32 v28, v28\n"
      "v_exp_f32 v29, v29\n"
      "v_mfma_f32_32x32x16_bf16 v[48:63], v[114:117], v[100:103], v[48:63]\n"
      "v_exp_f32 v30, v30\n"
      "v_exp_f32 v31, v31\n"
      "v_exp_f32 v32, v32\n"
      "v_exp_f32 v33, v33\n"
      "v_smfmac_f32_32x32x32_bf16 v[80:95], v[122:125], v[24:31], v106\n"
      "ds_read_b128 v[114:117], v107 offset:2048\n"
      "ds_read_b128 v[122:125], v108 offset:1824\n"
      "ds_read_b32 v131, v129\n"
      "v_exp_f32 v34, v34\n"
      "v_exp_f32 v35, v35\n"
      "v_exp_f32 v36, v36\n"
      "v_exp_f32 v37, v37\n"
      "v_mfma_f32_32x32x4_2b_bf16 v[0:31], v[112:113], v[104:105], 0 cbsz:1 abid:0\n"
      "v_exp_f32 v38, v38\n"
      "v_exp_f32 v39, v39\n"
      "v_exp_f32 v40, v40\n"
      "v_exp_f32 v41, v41\n"
      "v_exp_f32 v42, v42\n"
      "v_exp_f32 v43, v43\n"
      "v_exp_f32 v44, v44\n"
      "s_waitcnt lgkmcnt(3)\n"
      "v_exp_f32 v45, v45\n"
      "v_smfmac_f32_32x32x32_bf16 v[64:79], v[118:121], v[32:39], v106\n"
      "v_exp_f32 v46, v46\n"
      "v_exp_f32 v47, v47\n"
      "v_exp_f32 v48, v48\n"
      "s_waitcnt lgkmcnt(0)\n"
      "s_waitcnt vmcnt(0)\n"
      "s_mov_b64 exec, 1\n"
      "ds_add_u32 v129, v130 offset:76\n"
      "s_mov_b64 exec, -1\n"
      "v_exp_f32 v49, v49\n"
      "v_smfmac_f32_32x32x32_bf16 v[64:79], v[122:125], v[40:47], v106\n"
      "v_exp_f32 v50, v50\n"
      "v_exp_f32 v51, v51\n"
      "v_exp_f32 v52, v52\n"
      "v_exp_f32 v53, v53\n"
      "v_mfma_f32_32x32x16_bf16 v[0:15], v[114:117], v[96:99], v[0:15]\n"
      "v_exp_f32 v54, v54\n"
      "v_exp_f32 v55, v55\n"
      "v_exp_f32 v56, v56\n"
      "v_exp_f32 v57, v57\n"
      "v_smfmac_f32_32x32x32_bf16 v[80:95], v[118:121], v[48:55], v106\n"
      "v_exp_f32 v58, v58\n"
      "ds_read_b128 v[118:121], v108 offset:2432\n"
      "v_exp_f32 v59, v59\n"
      "v_exp_f32 v60, v60\n"
      "v_exp_f32 v61, v61\n"
      "v_mfma_f32_32x32x16_bf16 v[16:31], v[114:117], v[100:103], v[16:31]\n"
      "v_exp_f32 v62, v62\n"
      "v_exp_f32 v63, v63\n"
      "s_and_b32 %[t2], %[cnt], 12\n"
      "s_or_b32 %[t2], %[t2], vcc_lo\n"
      "s_cmp_lg_u32 %[t2], 5\n"
      "s_cbranch_scc1 8f\n"
      "v_readfirstlane_b32 %[tmp], v131\n"
      "s_nop 0\n"
      "s_cmp_lt_u32 %[tmp], 8\n"
      "s_cbranch_scc1 7f\n"
      "9:\n"
      "s_mov_b32 m0, %[dst]\n"
      "s_add_u32 s96, %[slo], %[so]\n"
      "s_addc_u32 s97, %[shi], 0\n"
      "global_load_lds_dwordx4 v126, s[96:97] offset:-2048 sc1\n"
      "global_load_lds_dwordx4 v126, s[96:97] offset:-1024 sc1\n"
      "global_load_lds_dwordx4 v126, s[96:97] sc1\n"
      "global_load_lds_dwordx4 v126, s[96:97] offset:1024 sc1\n"
      "s_mov_b32 exec_hi, 0xffffff\n"
      "global_load_lds_dwordx4 v126, s[96:97] offset:2048 sc1\n"
      "s_mov_b32 exec_hi, -1\n"
      "s_add_u32 %[dst], %[dst], 19968\n"
      "s_add_u32 %[so], %[so], 19968\n"
      "s_cmp_eq_u32 %[so], 159744\n"
      "s_cselect_b32 %[so], 0, %[so]\n"
      "8:\n"
      "v_exp_f32 v0, v0\n"
      "v_exp_f32 v1, v1\n"
      "v_smfmac_f32_32x32x32_bf16 v[80:95], v[122:125], v[56:63], v106\n"
      "ds_read_b128 v[114:117], v107 offset:3072\n"
      "ds_read_b128 v[122:125], v108 offset:3040\n"
      "ds_read_b32 v131, v129 offset:72\n"
      "v_exp_f32 v2, v2\n"
      "v_exp_f32 v3, v3\n"
      "v_exp_f32 v4, v4\n"
      "v_exp_f32 v5, v5\n"
      "v_mfma_f32_32x32x4_2b_bf16 v[32:63], v[112:113], v[104:105], 0 cbsz:1 abid:1\n"
      "v_exp_f32 v6, v6\n"
      "v_exp_f32 v7, v7\n"
      "v_exp_f32 v8, v8\n"
      "v_exp_f32 v9, v9\n"
      "v_exp_f32 v10, v10\n"
      "v_exp_f32 v11, v11\n"
      "v_exp_f32 v12, v12\n"
      "s_waitcnt lgkmcnt(3)\n"
      "v_exp_f32 v13, v13\n"
      "v_smfmac_f32_32x32x32_bf16 v[64:79], v[118:121], v[0:7], v106\n"
      "v_exp_f32 v14, v14\n"
      "v_exp_f32 v15, v15\n"
      "v_exp_f32 v16, v16\n"
      "s_waitcnt lgkmcnt(0)\n"
      "v_exp_f32 v17, v17\n"
      "v_smfmac_f32_32x32x32_bf16 v[64:79], v[122:125], v[8:15], v106\n"
      "v_exp_f32 v18, v18\n"
      "v_exp_f32 v19, v19\n"
      "v_exp_f32 v20, v20\n"
      "v_exp_f32 v21, v21\n"
      "v_mfma_f32_32x32x16_bf16 v[32:47], v[114:117], v[96:99], v[32:47]\n"
      "v_exp_f32 v22, v22\n"
      "v_exp_f32 v23, v23\n"
      "v_exp_f32 v24, v24\n"
      "v_exp_f32 v25, v25\n"
      "v_smfmac_f32_32x32x32_bf16 v[80:95], v[118:121], v[16:23], v106\n"
      "v_exp_f32 v26, v26\n"
      "ds_read_b128 v[118:121], v108 offset:3648\n"
      "v_exp_f32 v27, v27\n"
      "v_exp_f32 v28, v28\n"
      "v_exp_f32 v29, v29\n"
      "v_mfma_f32_32x32x16_bf16 v[48:63], v[114:117], v[100:103], v[48:63]\n"
      "v_exp_f32 v30, v30\n"
      "v_exp_f32 v31, v31\n"
      "s_cbranch_vccz 5f\n"
      "s_setprio 0\n"
      "5:\n"
      "v_readfirstlane_b32 %[tmp], v131\n"
      "s_add_u32 %[off], %[off], 19968\n"
      "s_cmp_eq_u32 %[off], 79872\n"
      "s_cselect_b32 %[off], 0, %[off]\n"
      "s_cmp_lt_i32 %[tmp], 8\n"
      "s_cbranch_scc1 3f\n"
      "2:\n"
      "v_add_u32 v109, %[off], v128\n"
      "ds_read2st64_b64 v[110:113], v109 offset0:0 offset1:1\n"
      "v_add_u32 v107, %[off], v126\n"
      "v_exp_f32 v32, v32\n"
      "v_exp_f32 v33, v33\n"
      "v_smfmac_f32_32x32x32_bf16 v[80:95], v[122:125], v[24:31], v106\n"
      "ds_read_b128 v[114:117], v107 offset:0\n"
      "ds_read_b128 v[122:125], v108 offset:4256\n"
      "v_exp_f32 v34, v34\n"
      "v_exp_f32 v35, v35\n"
      "v_exp_f32 v36, v36\n"
      "s_waitcnt lgkmcnt(2)\n"
      "v_exp_f32 v37, v37\n"
      "v_mfma_f32_32x32x4_2b_bf16 v[0:31], v[110:111], v[104:105], 0 cbsz:1 abid:0\n"
      "v_exp_f32 v38, v38\n"
      "v_exp_f32 v39, v39\n"
      "v_exp_f32 v40, v40\n"
      "v_exp_f32 v41, v41\n"
      "v_exp_f32 v42, v42\n"
      "v_exp_f32 v43, v43\n"
      "v_exp_f32 v44, v44\n"
      "v_exp_f32 v45, v45\n"
      "v_smfmac_f32_32x32x32_bf16 v[64:79], v[118:121], v[32:39], v106\n"
      "v_exp_f32 v46, v46\n"
      "v_exp_f32 v47, v47\n"
      "v_exp_f32 v48, v48\n"
      "s_waitcnt lgkmcnt(0)\n"
      "s_mov_b64 exec, 1\n"
      "ds_add_u32 v129, v130 offset:4\n"
      "s_mov_b64 exec, -1\n"
      "v_exp_f32 v49, v49\n"
      "v_smfmac_f32_32x32x32_bf16 v[64:79], v[122:125], v[40:47], v106\n"
      "v_exp_f32 v50, v50\n"
      "v_exp_f32 v51, v51\n"
      "v_exp_f32 v52, v52\n"
      "v_exp_f32 v53, v53\n"
      "v_mfma_f32_32x32x16_bf16 v[0:15], v[114:117], v[96:99], v[0:15]\n"
      "v_exp_f32 v54, v54\n"
      "v_exp_f32 v55, v55\n"
      "v_exp_f32 v56, v56\n"
      "v_exp_f32 v57, v57\n"
      "v_smfmac_f32_32x32x32_bf16 v[80:95], v[118:121], v[48:55], v106\n"
      "v_add_u32 v108, %[off], v127\n"
      "v_add_u32 v129, 4, v129\n"
      "v_exp_f32 v58, v58\n"
      "ds_read_b128 v[118:121], v108 offset:0\n"
      "v_exp_f32 v59, v59\n"
      "v_exp_f32 v60, v60\n"
      "v_exp_f32 v61, v61\n"
      "v_mfma_f32_32x32x16_bf16 v[16:31], v[114:117], v[100:103], v[16:31]\n"
      "v_exp_f32 v62, v62\n"
      "v_exp_f32 v63, v63\n"
      "s_sub_u32 %[cnt], %[cnt], 1\n"
      "s_cmp_lg_u32 %[cnt], 0\n"
      "s_cbranch_scc1 1b\n"
      "s_branch 6f\n"
      "3:\n"
      "s_sleep 1\n"
      "ds_read_b32 v131, v129 offset:72\n"
      "s_waitcnt lgkmcnt(0)\n"
      "v_readfirstlane_b32 %[tmp], v131\n"
      "s_nop 0\n"
      "s_cmp_lt_i32 %[tmp], 8\n"
      "s_cbranch_scc1 3b\n"
      "s_branch 2b\n"
      "7:\n"
      "s_sleep 1\n"
      "ds_read_b32 v131, v129\n"
      "s_waitcnt lgkmcnt(0)\n"
      "v_readfirstlane_b32 %[tmp], v131\n"
      "s_nop 0\n"
      "s_cmp_lt_u32 %[tmp], 8\n"
      "s_cbranch_scc1 7b\n"
      "s_branch 9b\n"
      "6:\n"
      "v_smfmac_f32_32x32x32_bf16 v[80:95], v[122:125], v[56:63], v106\n"
      "s_setprio 0\n"
      "s_waitcnt lgkmcnt(0)\n"
      "s_nop 15\n"
      "s_nop 7\n"
      : "=&{v[64:79]}"(o0), "=&{v[80:95]}"(o1), "+{v129}"(ca), [cnt] "+s"(cnt), [off] "+s"(off), [tmp] "=&s"(tmp),
        [t2] "=&s"(t2), [so] "+s"(so), [dst] "+s"(dst)
      : "{v[96:99]}"(q0u), "{v[100:103]}"(q1u), "{v[104:105]}"(qrem_u), "{v106}"(SPIDX),
        "{v126}"(sbase + kh * KV_GROUP + lane * 16), "{v127}"(sbase + kh * KV_GROUP + voff),
        "{v128}"(sbase + kh * KV_GROUP + 8960 + lane * 8), [first] "s"(1 - kh), [slo] "s"((unsigned)ksrc), [shi] "s"((unsigned)(ksrc >> 32))
      : "v0", "v1", "v2", "v3", "v4", "v5", "v6", "v7", "v8", "v9", "v10", "v11", "v12", "v13", "v14", "v15",
        "v16", "v17", "v18", "v19", "v20", "v21", "v22", "v23", "v24", "v25", "v26", "v27", "v28", "v29", "v30", "v31",
        "v32", "v33", "v34", "v35", "v36", "v37", "v38", "v39", "v40", "v41", "v42", "v43", "v44", "v45", "v46", "v47",
        "v48", "v49", "v50", "v51", "v52", "v53", "v54", "v55", "v56", "v57", "v58", "v59", "v60", "v61", "v62", "v63",
        "v107", "v108", "v109", "v110", "v111", "v112", "v113", "v114", "v115", "v116", "v117", "v118", "v119", "v120", "v121", "v122",
        "v123", "v124", "v125", "v130", "v131", "s96", "s97", "vcc", "scc", "memory");
    }
    xv = *(const u32x4*)(xt + otok * 1152 + 144 * hg + opc * 16);
    if (!ALL(nq * *(LAS float*)(mi + M_KN) < 47.f * 47.f)) {
      m0 = m1 = -__builtin_inff();
      o0 = o1 = (f32x16){0};
      const bf16x8 q0 = __builtin_bit_cast(bf16x8, q0u), q1 = __builtin_bit_cast(bf16x8, q1u);
      const s16x4 qrem = __builtin_bit_cast(s16x4, (u32x2){qr, 0u});
#pragma unroll 1
      for (int t = 0; t < 32; ++t) {
        const int g = WS_KV + (t >> 2) * RSLOT + kh * KV_GROUP;
        const u32x2 kr = __builtin_bit_cast(u32x2, __builtin_amdgcn_raw_buffer_load_b64(rs, g + 8960 + (t & 3) * 256 + l31 * 8, 0, 16));
        const bf16x8 km = __builtin_bit_cast(bf16x8, LD_SC1_16(g + (t & 3) * 1024 + lane * 16));
        const bf16x8 va = __builtin_bit_cast(bf16x8, LD_SC1_16(g + voff + (t & 3) * 1216));
        const bf16x8 vb = __builtin_bit_cast(bf16x8, LD_SC1_16(g + voff + (t & 3) * 1216 + 608));
        const f32x32 z = {0};
        const f32x32 r = __builtin_amdgcn_mfma_f32_32x32x4bf16_1k(__builtin_bit_cast(s16x4, kr), qrem, z, 0, 0, 0);
        online_step(MFMA32(km, q0, lo16(r)), m0, o0, va, vb);
        online_step(MFMA32(km, q1, hi16(r)), m1, o1, va, vb);
      }
    }
    HSTAMP(9);
    lds_ptr ex = sm + L_EX + hl * 6144 + lane * 16;
    if (kh) {
      *(LAS f32x4*)(ex) = (f32x4){o0[0], o0[1], o0[2], o0[3]};
      *(LAS f32x4*)(ex + 1024) = (f32x4){o0[4], o0[5], o0[6], o0[7]};
      *(LAS f32x4*)(ex + 2048) = (f32x4){o0[8], o0[9], o0[10], m0};
      *(LAS f32x4*)(ex + 3072) = (f32x4){o1[0], o1[1], o1[2], o1[3]};
      *(LAS f32x4*)(ex + 4096) = (f32x4){o1[4], o1[5], o1[6], o1[7]};
      *(LAS f32x4*)(ex + 5120) = (f32x4){o1[8], o1[9], o1[10], m1};
      lds_signal(mi + M_PAIR + 4 * hl);
    }
    lds_wait<1>(mi + M_PAIR + 4 * hl, 5);
    STAMP(10); RSTAMP(3);
    if (!kh) {
      __builtin_amdgcn_s_setprio(3);
#pragma unroll
      for (int t = 0; t < 2; ++t) {
        const f32x16 keep = t ? o1 : o0;
        const f32x4 t0 = *(LAS f32x4*)(ex + t * 3072), t1 = *(LAS f32x4*)(ex + t * 3072 + 1024), t2 = *(LAS f32x4*)(ex + t * 3072 + 2048);
        const float mk = t ? m1 : m0, mm = __builtin_fmaxf(mk, t2[3]);
        float fa = __builtin_amdgcn_exp2f(mk - mm), fb = __builtin_amdgcn_exp2f(t2[3] - mm);
        const float l = keep[10] * fa + t2[2] * fb, lx = xhalf(l);
        const float inv = __builtin_amdgcn_rcpf(h ? lx : l);
        fa *= inv;
        fb *= inv;
        float o[10];
#pragma unroll
        for (int i = 0; i < 10; ++i) o[i] = keep[i] * fa + (i < 4 ? t0[i & 3] : (i < 8 ? t1[i & 3] : t2[i & 3])) * fb;
        u32x4 a;
#pragma unroll
        for (int i = 0; i < 4; ++i) a[i] = UMIN(pk2(o[2 * i], o[2 * i + 1]), 0xfffffffeu);
        const int ob = WS_O + (2 * qt + t) * 36 * 1024;
        ST_SC1_16(a, ob + (4 * hg + hl) * 1024 + lane * 16);
        if (!h) ST_SC1_4(UMIN(pk2(o[8], o[9]), 0xfffffffeu), ob + 32768 + (4 * hg + hl) * 128 + l31 * 4);
      }
      lds_signal(mi + M_EXD);
      __builtin_amdgcn_s_setprio(G_PRIO);
      HSTAMP(5);
      __builtin_amdgcn_s_sleep(O_DELAY);
      u32x4 of[18];
#pragma unroll
      for (int i = 0; i < 18; ++i) of[i] = LD_SC1_16(WS_O + (2 * qt * 36 + (i / 9) * 36 + 9 * hl + i % 9) * 1024 + lane * 16);
      lds_wait<1>(mi + M_EXD, 8);
#pragma unroll
      for (int t = 0; t < 2; ++t) {
        unsigned mx = 0;
#pragma unroll
        for (int i = 9 * t; i < 9 * t + 9; ++i) mx = UMAX(UMAX(mx, UMAX(of[i][0], of[i][1])), UMAX(of[i][2], of[i][3]));
        if (__builtin_expect(!ALL(mx != ~0u), 0)) {
#pragma unroll 1
          for (unsigned spins = 0; spins < SPIN_CAP; ++spins) {
            asm volatile("" ::: "memory");
#pragma unroll
            for (int i = 9 * t; i < 9 * t + 9; ++i) of[i] = LD_SC1_16(WS_O + (2 * qt * 36 + t * 36 + 9 * hl + i % 9) * 1024 + lane * 16);
            mx = 0;
#pragma unroll
            for (int i = 9 * t; i < 9 * t + 9; ++i) mx = UMAX(UMAX(mx, UMAX(of[i][0], of[i][1])), UMAX(of[i][2], of[i][3]));
            if (ALL(mx != ~0u)) break;
            __builtin_amdgcn_s_sleep(2);
          }
        }
#pragma unroll
        for (int i = 0; i < 9; ++i) {
          if (i >= 5 && hl == 3) {
            lds_ptr p = sm + (36 * t + 27 + i) * 1024 + (4 * (lane & 7) + 32 * h) * 16 + ((lane >> 3) & 3) * 4;
#pragma unroll
            for (int e = 0; e < 4; ++e) *(LAS unsigned*)(p + e * 16) = of[9 * t + i][e];
          } else {
            *(LAS u32x4*)(sm + (36 * t + 9 * hl + i) * 1024 + lane * 16) = of[9 * t + i];
          }
        }
        if (t == 0) { lds_signal(mi + M_KVR); NSTAMP(1); }
      }
      lds_signal(mi + M_OR);
      __builtin_amdgcn_s_setprio(0);
      HSTAMP(6);
    } else {
      const int b = hl >> 1, o = hl & 1;
      lds_ptr ab = sm + CV_H + b * CV_HB + (l31 & 7) * CV_COPY + (h + (l31 >> 3)) * 16 + 1024 * o;
      lds_ptr bb = sm + CV_U + b * CV_UB + 2560 + (l31 - 16 * o - 7) * 80 + (1 - h) * 16;
      f32x16 y0, y1;
      bf16x8 fx[8], fy[8];
      __builtin_amdgcn_s_setprio(MM_PRIO);
      asm volatile("" : "+v"(ab), "+v"(bb));
      const float dv = *(LAS float*)(mi + M_DV + 4 * b), d0 = o ? 0.f : dv, d1 = o ? dv : 0.f;
      u32x2 uu[4];
#pragma unroll
      for (int g = 0; g < 4; ++g) uu[g] = *(LAS u32x2*)(sm + CV_U + b * CV_UB + 2560 + (32 * o + l31) * 80 + g * 16 + (1 - h) * 8);
      CV_LD(fx, 0);
#pragma unroll
      for (int g = 0; g < 4; ++g) {
        const float e[4] = {bf_hi(uu[g][1]), bf_lo(uu[g][1]), bf_hi(uu[g][0]), bf_lo(uu[g][0])};
#pragma unroll
        for (int i = 0; i < 4; ++i) { y0[4 * g + i] = o ? 0.f : e[i] * d0; y1[4 * g + i] = o ? e[i] * d1 : 0.f; }
      }
#pragma unroll 1
      for (int it = 0; it < 4; ++it) {
        asm volatile("" : "+v"(ab), "+v"(bb));
        CV_STEP(fx, fy, 1);
        CV_STEP(fy, fx, 2);
        CV_STEP(fx, fy, 3);
        CV_STEP(fy, fx, 4);
        if (it >= MM_UP) __builtin_amdgcn_s_setprio(2);
        ab += 256;
        bb -= 320;
      }
      HSTAMP(5);
      lds_ptr px = sm + CV_H + b * CV_HB + h * CV_COPY + l31 * 16;
      {
        const f32x16 give = o ? y0 : y1;
#pragma unroll
        for (int g = 0; g < 4; ++g) *(LAS f32x4*)(px + o * 3200 + g * 2 * CV_COPY) = (f32x4){give[4 * g], give[4 * g + 1], give[4 * g + 2], give[4 * g + 3]};
        if (o) y0 = y1;
      }
      lds_signal(mi + M_AS + 4 * b);
      lds_signal(mi + M_MM);
      lds_wait(mi + M_AS + 4 * b, 2);
      u32x4 z[2];
#pragma unroll
      for (int g = 0; g < 4; ++g) {
        const f32x4 t = *(LAS f32x4*)(px + (1 - o) * 3200 + g * 2 * CV_COPY);
        z[g >> 1][2 * (g & 1)] = UMIN(pk2(t[0] + y0[4 * g], t[1] + y0[4 * g + 1]), 0xfffffffeu);
        z[g >> 1][2 * (g & 1) + 1] = UMIN(pk2(t[2] + y0[4 * g + 2], t[3] + y0[4 * g + 3]), 0xfffffffeu);
      }
      const int zo = WS_Z + (72 * hg + 2 * qt + b) * 4096 + o * 2048 + lane * 16;
      ST_SC1_16(z[0], zo);
      ST_SC1_16(z[1], zo + 1024);
      lds_signal(mi + M_CE);
      __builtin_amdgcn_s_setprio(0);
      HSTAMP(6);
    }
    if (w == 0) {
      u32x4 zp[8];
#pragma unroll 1
      for (unsigned spins = 0; spins < SPIN_CAP; ++spins) {
        unsigned mx = 0;
#pragma unroll
        for (int i = 0; i < 8; ++i)
          zp[i] = LD_SC1_16(WS_ZP + (hg * 32 + (lane >> 3) * 4 + (i >> 1)) * 8192 + (qt >> 4) * 4096 + ((lane >> 2) & 1) * 2048 + (i & 1) * 1024 +
                            (2 * (qt & 15) + (lane & 1) + 32 * ((lane >> 1) & 1)) * 16);
#pragma unroll
        for (int i = 0; i < 8; ++i) mx = UMAX(UMAX(mx, UMAX(zp[i][0], zp[i][1])), UMAX(zp[i][2], zp[i][3]));
        asm volatile("" ::: "memory");
        if (ALL(mx != ~0u)) break;
        __builtin_amdgcn_s_sleep(2);
      }
#define ZF(I) __builtin_bit_cast(f32x4, zp[I])
      const f32x4 sa = (ZF(0) + ZF(2)) + (ZF(4) + ZF(6)), sb = (ZF(1) + ZF(3)) + (ZF(5) + ZF(7));
      zv = (u32x4){pk2(sa[0], sa[1]), pk2(sa[2], sa[3]), pk2(sb[0], sb[1]), pk2(sb[2], sb[3])};
    }
    lds_wait<2>(mi + M_CE, 4);
    if (w == 0) zscatter(512 + lane);
    else __builtin_amdgcn_s_sleep(Z_DELAY);
#pragma unroll 1
    for (unsigned spins = 0; spins < SPIN_CAP; ++spins) {
      zv = LD_SC1_16(WS_Z + (72 * hg + (tid >> 3)) * 4096 + (qt >> 4) * 2048 + ((tid >> 2) & 1) * 1024 +
                     (2 * (qt & 15) + (tid & 1) + 32 * ((tid >> 1) & 1)) * 16);
      asm volatile("" ::: "memory");
      if (ALL(UMAX(UMAX(zv[0], zv[1]), UMAX(zv[2], zv[3])) != ~0u)) break;
      __builtin_amdgcn_s_sleep(2);
    }
    HSTAMP(7);
    zscatter(tid);
  }

  if (w < 9) {
    lds_ptr ap = sm + L_AP + (otok >> 3) * CV_COPY + (otok & 7) * 336 + opc * 32;
    lds_wait<1>(mi + (w < 4 ? M_APR : M_APR1), 4);
    const float gq = *(LAS float*)(mi + M_G + otok * 4), omg = rbf(1.f - gq);
    f32x4 a0 = *(LAS f32x4*)(ap) + *(LAS f32x4*)(ap + CV_HB), a1 = *(LAS f32x4*)(ap + 16) + *(LAS f32x4*)(ap + CV_HB + 16);
    if (opc == 8) {
      a0 += *(LAS f32x4*)(ap + 32) + *(LAS f32x4*)(ap + CV_HB + 32);
      a1 += *(LAS f32x4*)(ap + 48) + *(LAS f32x4*)(ap + CV_HB + 48);
    }
    a0 *= gq;
    a1 *= gq;
    f32x4 x0 = {omg * bf_lo(xv[0]), omg * bf_hi(xv[0]), omg * bf_lo(xv[1]), omg * bf_hi(xv[1])};
    f32x4 x1 = {omg * bf_lo(xv[2]), omg * bf_hi(xv[2]), omg * bf_lo(xv[3]), omg * bf_hi(xv[3])};
    lds_ptr zq = sm + L_ZT + ((otok >> 5) + ((otok >> 1) & 2) + ((otok >> 2) & 4) + 8 * (otok & 3) + 4 * (otok & 8)) * 144 + opc * 16;
    u32x4* op = (u32x4*)(out + (size_t)(64 * qt + otok) * D_ + 72 * hg + 8 * opc);
    asm volatile("" : "+v"(a0), "+v"(a1), "+v"(x0), "+v"(x1), "+v"(zq), "+v"(op));
    lds_wait(mi + M_ZS, 9);
    if (w < 8) HSTAMP(8);
    const u32x4 z = *(LAS u32x4*)zq;
    const u32x4 o = {pk2(__builtin_fmaf(x0[0], bf_lo(z[0]), a0[0]), __builtin_fmaf(x0[1], bf_hi(z[0]), a0[1])),
                     pk2(__builtin_fmaf(x0[2], bf_lo(z[1]), a0[2]), __builtin_fmaf(x0[3], bf_hi(z[1]), a0[3])),
                     pk2(__builtin_fmaf(x1[0], bf_lo(z[2]), a1[0]), __builtin_fmaf(x1[1], bf_hi(z[2]), a1[1])),
                     pk2(__builtin_fmaf(x1[2], bf_lo(z[3]), a1[2]), __builtin_fmaf(x1[3], bf_hi(z[3]), a1[3]))};
    asm volatile("global_store_dwordx4 %0, %1, off" OUT_POL "\n\ts_nop 1" :: "v"(op), "v"(o) : "memory");
  }
  STAMP_FLUSH(w < 4 ? 0 : (w < 8 ? 1 : (w < 11 ? 2 : 3)));
  asm volatile("fk_end:");
}

extern "C" void kernel_launch(void* const* d_in, const int* in_sizes, int n_in, void* d_out, int out_size,
                              void* d_ws, size_t ws_size, hipStream_t stream) {
  fused_kernel<<<256, 768, 0, stream>>>((const __bf16*)d_in[0], (const __bf16*)d_in[1], (const __bf16*)d_in[2],
                                        (const __bf16*)d_in[3], (const __bf16*)d_in[4], (const __bf16*)d_in[6],
                                        (const __bf16*)d_in[7], (const __bf16*)d_in[8], (const __bf16*)d_in[9],
                                        (const __bf16*)d_in[10], (const __bf16*)d_in[11], (__bf16*)d_out, (char*)d_ws);
}
```

```cpp
#include <hip/hip_runtime.h>
#include <hip/hip_bf16.h>
#include <stdint.h>

typedef __attribute__((ext_vector_type(2))) __bf16 bf16x2;
typedef __attribute__((ext_vector_type(4))) __bf16 bf16x4;
typedef __attribute__((ext_vector_type(8))) __bf16 bf16x8;
typedef __attribute__((ext_vector_type(16))) __bf16 bf16x16;
typedef __attribute__((ext_vector_type(4))) short s16x4;
typedef __attribute__((ext_vector_type(2))) float f32x2;
typedef __attribute__((ext_vector_type(4))) float f32x4;
typedef __attribute__((ext_vector_type(16))) float f32x16;
typedef __attribute__((ext_vector_type(32))) float f32x32;
typedef __attribute__((ext_vector_type(2))) unsigned u32x2;
typedef __attribute__((ext_vector_type(4))) unsigned u32x4;
#define LAS __attribute__((address_space(3)))
#define GAS __attribute__((address_space(1)))
typedef LAS char* lds_ptr;
typedef GAS unsigned gu32;
typedef GAS unsigned long long gu64;
#ifdef __HIP_DEVICE_COMPILE__
#define NOPK __attribute__((target("no-packed-fp32-ops")))
#else
#define NOPK
#endif
#define DEVI __device__ __forceinline__ NOPK
#define LAMI __attribute__((always_inline)) NOPK
#define TIDX __builtin_amdgcn_workitem_id_x()
#define BIDX __builtin_amdgcn_workgroup_id_x()
#define ALL(p) (__builtin_amdgcn_ballot_w64(!(p)) == 0ull)
#define UMIN(a, b) __builtin_elementwise_min((unsigned)(a), (unsigned)(b))
#define UMAX(a, b) __builtin_elementwise_max((unsigned)(a), (unsigned)(b))
#define RLX_AGENT __ATOMIC_RELAXED, __HIP_MEMORY_SCOPE_AGENT

constexpr int S_ = 2048, D_ = 576, DH = 18;

constexpr int KV_GROUP = 9984;
constexpr int WS_KV = 0;
constexpr int WS_EPOCH = 192 * 1024;
constexpr int WS_FKV = WS_EPOCH + 1024;
constexpr int WS_FU = WS_FKV + 8192;
constexpr int WS_G = WS_FU + 32768;
constexpr int WS_KT = 320 * 1024;
constexpr int WS_UT = 640 * 1024;
constexpr int WS_Z = WS_UT + D_ * S_ * 2;
constexpr int WS_O = WS_Z + D_ * S_ * 2;
constexpr int WS_ZP = WS_O + D_ * S_ * 2;
constexpr int WS_END = WS_ZP + 64 * 4 * 8192;

constexpr int RSLOT = 2 * KV_GROUP;
constexpr int L_X = 0;
constexpr int L_KVP = 73728;
constexpr int L_B = 80000;
constexpr int L_M = 162944;
constexpr int CV_COPY = 4160;
constexpr int CV_H = L_B, CV_HB = 8 * CV_COPY, CV_U = L_B + 2 * CV_HB, CV_UB = 7680;
constexpr int L_Q = L_B + 8192;
constexpr int M_DONE = 0,
              M_C1 = 32, M_C2 = 36, M_C3 = 40,
              M_C3B = 44, M_KVC = 48, M_KVR = 52, M_UR = 56, M_OR = 60,
              M_RDY = 64,
              M_UD = 104, M_HS = 108, M_CE = 124, M_PAIR = 128, M_TAG = 148, M_KN = 152, M_EXD = 156, M_KN0 = 164,
              M_KD = 168, M_QR = 172, M_QD = 192, M_AS = 196, M_SE = 204, M_ZS = 208, M_DV = 216,
              M_G = 256;
constexpr int E_H = 24576, E_COPY = 1344, E_U = 35328, E_P = 43008;
constexpr int L_EX = 0;
constexpr int L_AP = CV_H + 512, L_ZT = CV_U;
constexpr unsigned SPIN_CAP = 1u << 18;

DEVI unsigned pk2(float a, float b) {
  bf16x2 v = {(__bf16)a, (__bf16)b};
  return __builtin_bit_cast(unsigned, v);
}
DEVI float u2f(unsigned u) { return __builtin_bit_cast(float, u); }
DEVI float bf_lo(unsigned u) { return __builtin_bit_cast(float, u << 16); }
DEVI float bf_hi(unsigned u) { return __builtin_bit_cast(float, u & 0xffff0000u); }
DEVI float dot2(unsigned u, float c) { const bf16x2 v = __builtin_bit_cast(bf16x2, u); return __builtin_amdgcn_fdot2_f32_bf16(v, v, c, false); }
DEVI float rbf(float a) { return (float)(__bf16)a; }
DEVI int swap23(int r) { return (r & ~12) | ((r & 4) << 1) | ((r & 8) >> 1); }
DEVI float xhalf(float v) {
  const unsigned u = __builtin_bit_cast(unsigned, v);
  const auto r = __builtin_amdgcn_permlane32_swap(u, u, false, false);
  return __builtin_bit_cast(float, (TIDX & 32) ? r[0] : r[1]);
}

template <int CTRL>
DEVI float dpp_max(float v) {
  const int u = __builtin_bit_cast(int, v);
  return __builtin_fmaxf(v, __builtin_bit_cast(float, __builtin_amdgcn_update_dpp(u, u, CTRL, 0xf, 0xf, false)));
}
DEVI float max16(float v) {
  return dpp_max<0x140>(dpp_max<0x141>(dpp_max<0x4e>(dpp_max<0xb1>(v))));
}
template <int CTRL>
DEVI float dpp_add(float v) {
  const int u = __builtin_bit_cast(int, v);
  return v + __builtin_bit_cast(float, __builtin_amdgcn_update_dpp(u, u, CTRL, 0xf, 0xf, false));
}
DEVI float wave_max(float v) {
  v = max16(v);
  const unsigned u = __builtin_bit_cast(unsigned, v);
  return __builtin_fmaxf(__builtin_fmaxf(u2f(__builtin_amdgcn_readlane(u, 0)), u2f(__builtin_amdgcn_readlane(u, 16))),
               __builtin_fmaxf(u2f(__builtin_amdgcn_readlane(u, 32)), u2f(__builtin_amdgcn_readlane(u, 48))));
}
DEVI void glds(const void* sbase, unsigned voff, unsigned lds_dst) {
  asm volatile("s_mov_b32 m0, %2\n\ts_nop 0\n\tglobal_load_lds_dwordx4 %0, %1" :: "v"(voff), "s"(sbase), "s"(lds_dst) : "memory");
}
DEVI void glds_sc1(const void* sbase, unsigned voff, unsigned lds_dst) {
  asm volatile("s_mov_b32 m0, %2\n\ts_nop 0\n\tglobal_load_lds_dwordx4 %0, %1 sc1" :: "v"(voff), "s"(sbase), "s"(lds_dst) : "memory");
}
#define GLDS4(SC, sbase_, v0_, v3_, dst_)                                                                                  \
  asm volatile("s_mov_b32 m0, %3\n\ts_nop 0\n\tglobal_load_lds_dwordx4 %0, %2" SC "\n\tglobal_load_lds_dwordx4 %0, %2 offset:1024" SC   \
               "\n\tglobal_load_lds_dwordx4 %0, %2 offset:2048" SC "\n\tglobal_load_lds_dwordx4 %1, %2 offset:3072" SC           \
               :: "v"(v0_), "v"(v3_), "s"(sbase_), "s"(dst_) : "memory")
#define GLDSX(src_, dst_)                                                                                                  \
  asm volatile("s_mov_b32 m0, %5\n\ts_nop 0\n\tglobal_load_lds_dwordx4 %0, %4\n\tglobal_load_lds_dwordx4 %1, %4 offset:1024\n\t"   \
               "global_load_lds_dwordx4 %2, %4 offset:2048\n\tglobal_load_lds_dwordx4 %3, %4 offset:3072"                 \
               :: "v"(vxe), "v"(vxo), "v"(vxe + 16384), "v"(vxo + 16384), "s"(src_), "s"(dst_) : "memory")
#define GLDSX2(src_, dst_)                                                                                                 \
  asm volatile("s_mov_b32 m0, %3\n\ts_nop 0\n\tglobal_load_lds_dwordx4 %0, %2\n\tglobal_load_lds_dwordx4 %1, %2 offset:1024"       \
               :: "v"(vxe), "v"(vxo), "s"(src_), "s"(dst_) : "memory")
#define GLDS3(STEP1, STEP2, v0_, v1_, v2_, src_, dst_)                                                                     \
  asm volatile("s_mov_b32 m0, %4\n\ts_nop 0\n\tglobal_load_lds_dwordx4 %0, %3\n\tglobal_load_lds_dwordx4 %1, %3 offset:" STEP1  \
               "\n\tglobal_load_lds_dwordx4 %2, %3 offset:" STEP2                                                        \
               :: "v"(v0_), "v"(v1_), "v"(v2_), "s"(src_), "s"(dst_) : "memory")
#define TOUCH(src_, v_) asm volatile("s_mov_b32 m0, %2\n\ts_nop 0\n\tglobal_load_lds_dword %0, %1 sc1" :: "v"(v_), "s"(src_), "s"(sbase + L_M + 512) : "memory")
#define VM_WAIT(N) asm volatile("s_waitcnt vmcnt(" #N ")" ::: "memory")
#define BAR() asm volatile("s_waitcnt lgkmcnt(0)\n\ts_barrier" ::: "memory")

DEVI bf16x8 tr_frag(lds_ptr p0, lds_ptr p1) {
  bf16x4 a = __builtin_amdgcn_ds_read_tr16_b64_v4bf16((LAS bf16x4*)p0);
  bf16x4 b = __builtin_amdgcn_ds_read_tr16_b64_v4bf16((LAS bf16x4*)p1);
  return __builtin_shufflevector(a, b, 0, 1, 2, 3, 4, 5, 6, 7);
}
DEVI bf16x8 lds_frag(lds_ptr p) { return *(LAS bf16x8*)p; }
DEVI void lds_signal(lds_ptr p, unsigned n = 1u) {
  asm volatile("s_mov_b64 exec, 1\n\tds_add_u32 %0, %1\n\ts_mov_b64 exec, -1" :: "v"(p), "v"(n) : "memory");
}
DEVI unsigned lds_ticket(lds_ptr p) {
  unsigned old;
  asm volatile("s_mov_b64 exec, 1\n\tds_add_rtn_u32 %0, %1, %2\n\ts_mov_b64 exec, -1\n\ts_waitcnt lgkmcnt(0)"
               : "=&v"(old) : "v"(p), "v"(1u) : "memory");
  return __builtin_amdgcn_readfirstlane(old);
}
template <int SLEEP = 0>
DEVI void lds_wait(lds_ptr p, unsigned n) {
  unsigned v, t;
  asm volatile("1:\n\tds_read_b32 %0, %2\n\ts_waitcnt lgkmcnt(0)\n\tv_readfirstlane_b32 %1, %0\n\ts_nop 0\n\ts_cmp_ge_u32 %1, %3\n\t"
               "s_cbranch_scc1 2f\n\ts_sleep %4\n\ts_branch 1b\n2:"
               : "=&v"(v), "=&s"(t) : "v"(p), "s"(n), "i"(SLEEP) : "scc", "memory");
}
#define LD_SC1_16(off) __builtin_bit_cast(u32x4, __builtin_amdgcn_raw_buffer_load_b128(rs, (off), 0, 16))
#define ST_SC1_16(v, off) __builtin_amdgcn_raw_buffer_store_b128((v), rs, (off), 0, 16)
#define ST_SC1_8(v, off) __builtin_amdgcn_raw_buffer_store_b64((v), rs, (off), 0, 16)
#define ST_SC1_4(v, off) __builtin_amdgcn_raw_buffer_store_b32((v), rs, (off), 0, 16)

DEVI f32x16 lo16(f32x32 v) { return __builtin_shufflevector(v, v, 0, 1, 2, 3, 4, 5, 6, 7, 8, 9, 10, 11, 12, 13, 14, 15); }
DEVI f32x16 hi16(f32x32 v) { return __builtin_shufflevector(v, v, 16, 17, 18, 19, 20, 21, 22, 23, 24, 25, 26, 27, 28, 29, 30, 31); }
DEVI bf16x16 raw_lo(f32x16 v) { return __builtin_bit_cast(bf16x16, __builtin_shufflevector(v, v, 0, 1, 2, 3, 4, 5, 6, 7)); }
DEVI bf16x16 raw_hi(f32x16 v) { return __builtin_bit_cast(bf16x16, __builtin_shufflevector(v, v, 8, 9, 10, 11, 12, 13, 14, 15)); }
constexpr int SPIDX = (int)0xddddddddu;
#define MFMA32(a, b, c) __builtin_amdgcn_mfma_f32_32x32x16_bf16((a), (b), (c), 0, 0, 0)
#define MFMA16(a, b, c) __builtin_amdgcn_mfma_f32_16x16x32_bf16((a), (b), (c), 0, 0, 0)

DEVI void online_step(f32x16 s, float& m, f32x16& o, bf16x8 va, bf16x8 vb) {
  float t = s[0];
#pragma unroll
  for (int i = 1; i < 16; ++i) t = __builtin_fmaxf(t, s[i]);
  t = __builtin_fmaxf(t, xhalf(t));
  const float mn = __builtin_fmaxf(m, t);
  const float al = __builtin_amdgcn_exp2f(m - mn);
  m = mn;
#pragma unroll
  for (int i = 0; i < 16; ++i) { o[i] *= al; s[i] = __builtin_amdgcn_exp2f(s[i] - mn); }
  o = __builtin_amdgcn_smfmac_f32_32x32x32_bf16(va, raw_lo(s), o, SPIDX, 0, 0);
  o = __builtin_amdgcn_smfmac_f32_32x32x32_bf16(vb, raw_hi(s), o, SPIDX, 0, 0);
}

#define UNIT_LOAD(CP, TG)                                                                                        \
  const int uc0 = 72 * hg + 2 * (CP), ut0 = 64 * qt + 8 * (TG);                                                  \
  const u32x4 usw = *(const u32x4*)(shk + uc0 * 4);                                                              \
  unsigned ukr[11], uvr[8];                                                                                      \
  {                                                                                                              \
    const unsigned bo = (ut0 * D_ + uc0) * 2, bo3 = ut0 ? bo - 3 * 1152 : bo;                                    \
    _Pragma("unroll") for (int i = 0; i < 3; ++i) ukr[i] = *(const unsigned*)((const char*)bk + bo3 + i * 1152); \
    _Pragma("unroll") for (int i = 0; i < 8; ++i) {                                                              \
      ukr[3 + i] = *(const unsigned*)((const char*)bk + bo + i * 1152);                                          \
      uvr[i] = *(const unsigned*)((const char*)bv + bo + i * 1152);                                              \
    }                                                                                                            \
  }
#define UNIT_DO()                                                                                                \
  u32x4 o0, o1;                                                                                                  \
  {                                                                                                              \
    const f32x2 w3 = {bf_hi(usw[1]), bf_hi(usw[3])}, w2 = {bf_lo(usw[1]), bf_lo(usw[3])};                        \
    const f32x2 w1 = {bf_hi(usw[0]), bf_hi(usw[2])}, w0 = {bf_lo(usw[0]), bf_lo(usw[2])};                        \
    f32x2 kf[11], r[8];                                                                                          \
    _Pragma("unroll") for (int i = 0; i < 11; ++i) {                                                             \
      const unsigned kk = (i < 3 && !ut0) ? 0u : ukr[i];                                                         \
      kf[i] = (f32x2){bf_lo(kk), bf_hi(kk)};                                                                     \
    }                                                                                                            \
    _Pragma("unroll") for (int i = 0; i < 8; ++i)                                                                \
      r[i] = __builtin_elementwise_fma(kf[i + 3], w0, __builtin_elementwise_fma(kf[i + 2], w1,                   \
             __builtin_elementwise_fma(kf[i + 1], w2, kf[i] * w3))) * (f32x2){bf_lo(uvr[i]), bf_hi(uvr[i])};     \
    _Pragma("unroll") for (int i = 0; i < 4; ++i) {                                                              \
      o0[i] = pk2(r[7 - 2 * i][0], r[6 - 2 * i][0]);                                                             \
      o1[i] = pk2(r[7 - 2 * i][1], r[6 - 2 * i][1]);                                                             \
    }                                                                                                            \
  }

#define CV_LD(F, D) _Pragma("unroll") for (int s = 0; s < 2; ++s) {                                              \
    F[4 * s] = lds_frag(ab + 32 * (2 * (D) + 1 - s)); F[4 * s + 1] = lds_frag(bb + 80 * (7 - (D)) + 32 * s);     \
    F[4 * s + 2] = lds_frag(bb + 80 * (39 - (D)) + 32 * s); F[4 * s + 3] = lds_frag(ab + 32 * (2 * (D) + 65 - s)); } \
  __builtin_amdgcn_sched_barrier(0)
#define SB0 __builtin_amdgcn_sched_barrier(0)
#define CV_STEP(F, N, D) _Pragma("unroll") for (int s = 0; s < 2; ++s) {                                         \
    y0 = MFMA32(F[4 * s], F[4 * s + 1], y0); SB0;                                                                \
    N[4 * s] = lds_frag(ab + 32 * (2 * (D) + 1 - s)); N[4 * s + 1] = lds_frag(bb + 80 * (7 - (D)) + 32 * s); SB0; \
    y1 = MFMA32(F[4 * s], F[4 * s + 2], y1); SB0;                                                                \
    N[4 * s + 2] = lds_frag(bb + 80 * (39 - (D)) + 32 * s); SB0;                                                 \
    y1 = MFMA32(F[4 * s + 3], F[4 * s + 1], y1); SB0;                                                            \
    N[4 * s + 3] = lds_frag(ab + 32 * (2 * (D) + 65 - s)); SB0; }
#define CV_PUT(P) _Pragma("unroll") for (int g = 0; g < 4; ++g) {                                                \
    *(LAS f32x4*)((P) + g * 1024) = (f32x4){y0[4 * g], y0[4 * g + 1], y0[4 * g + 2], y0[4 * g + 3]};             \
    *(LAS f32x4*)((P) + 4096 + g * 1024) = (f32x4){y1[4 * g], y1[4 * g + 1], y1[4 * g + 2], y1[4 * g + 3]}; }
DEVI f32x4 cv_sum(lds_ptr p, lds_ptr u, float dv) {
  const f32x4 s = (*(LAS f32x4*)(p) + *(LAS f32x4*)(p + 8192)) + (*(LAS f32x4*)(p + 16384) + *(LAS f32x4*)(p + 24576));
  const u32x2 uu = *(LAS u32x2*)u;
  return (f32x4){__builtin_fmaf(bf_hi(uu[1]), dv, s[0]), __builtin_fmaf(bf_lo(uu[1]), dv, s[1]), __builtin_fmaf(bf_hi(uu[0]), dv, s[2]), __builtin_fmaf(bf_lo(uu[0]), dv, s[3])};
}

__global__ void __launch_bounds__(768, 3) NOPK
fused_kernel(const __bf16* __restrict__ x, const __bf16* __restrict__ wq, const __bf16* __restrict__ wk,
             const __bf16* __restrict__ wv, const __bf16* __restrict__ wo, const __bf16* __restrict__ bk,
             const __bf16* __restrict__ bv, const __bf16* __restrict__ shk, const __bf16* __restrict__ ssmk,
             const __bf16* __restrict__ ssmd, const __bf16* __restrict__ gw, __bf16* __restrict__ out, char* ws) {
  __shared__ __attribute__((aligned(16))) char smem[160 * 1024];
  const int tid = TIDX, lane = tid & 63;
  const int w = __builtin_amdgcn_readfirstlane(tid >> 6);
  const int bid = BIDX, jj = bid >> 3;
  const unsigned sbase = (unsigned)(size_t)smem;
  if (w == 0) {
    unsigned long long pc0;
    unsigned len;
    asm volatile("s_getpc_b64 %0\n\tfk_pc:\n\ts_mov_b32 %1, fk_end-fk_pc" : "=s"(pc0), "=s"(len));
    TOUCH(pc0, UMIN((unsigned)(lane * 8 + (jj & 7)) * 128u, len));
  }
  const int qt = (bid & 7) * 4 + (jj & 3), hg = jj >> 2;
  lds_ptr sm = (lds_ptr)smem;
  lds_ptr mi = sm + L_M;
  const int h = lane >> 5, l31 = lane & 31, l15 = lane & 15, g4 = lane >> 4;
  const int tq = (lane & 15) >> 2, tp = lane & 3;
  const auto rs = __builtin_amdgcn_make_buffer_rsrc(ws, 0, WS_END, 0x00020000);
  GAS char* gws = (GAS char*)ws;
  const int rot = (hg + qt) % 3;
  const int s0 = bid & 7;
  const int tq4 = hg >> 1, sel = hg & 1;
  const char* xt = (const char*)x + (size_t)qt * 73728;
  const char* wqs = (const char*)wq + 144 * hg;
  const unsigned vxe = (lane >> 3) * 1152 + (((lane & 7) ^ g4) << 4), vxo = 8192 + (lane >> 3) * 1152 + (((lane & 7) ^ (4 + g4)) << 4);
  const int otok = tid / 9, opc = tid - 9 * otok;

  u32x4 zv, xv;

  if (w == 4) *(LAS int*)(mi + lane * 4) = (lane == M_RDY / 4 + 2 || lane == M_RDY / 4 + 3) ? -8 : (lane == 0 ? -1 : 0);
  auto zscatter = [&](int n) LAMI {
    lds_ptr zp = sm + L_ZT + ((n & 7) * 72 + (n >> 3)) * 2;
#pragma unroll
    for (int e = 0; e < 8; ++e)
      *(LAS unsigned short*)(zp + e * 1152) = (unsigned short)((e & 1) ? zv[e >> 1] >> 16 : zv[e >> 1]);
    lds_signal(mi + M_ZS);
  };
  if (w >= 8 && w < 11) {
    const int j = w - 8;
    const int th = (j + rot) % 3;
    const unsigned stg = L_B + 2 * 27648 + j * 9216;
#pragma unroll
    for (int c = 0; c < 3; ++c) GLDSX2(xt + tq4 * 18432 + (3 * th + c) * 128, sbase + L_X + (3 * j + c) * 8192 + tq4 * 2048);
    {
      const char* wsrc = (const char*)(sel ? wv : wk) + th * 6912 - 2048;
      unsigned vo[3];
#pragma unroll
      for (int i = 0; i < 3; ++i) {
        const int f = 64 * i + lane, row = f / 3, pc = f - 3 * row;
        vo[i] = row * 36 + (pc == 2 ? 20 : 16 * pc) + 2048 - 1024 * i;
      }
#pragma unroll
      for (int p = 0; p < 3; ++p) GLDS3("1024", "2048", vo[0], vo[1], vo[2], wsrc + p * 2304, sbase + stg + p * 3072);
    }
    glds(gw + 192 * th, UMIN(lane, 23) * 16, sbase + L_KVP + j * 2048);
  }
  unsigned epoch = 0;
  if (w == 11) epoch = __hip_atomic_load((gu32*)(gws + WS_EPOCH), RLX_AGENT);
  BAR();
  if (w >= 5 && w < 8) {
    const int wi = w - 5;
    unsigned vw[3];
#pragma unroll
    for (int m = 0; m < 3; ++m) {
      const int f = 64 * (3 * wi + m) + lane, row = f / 9;
      vw[m] = row * 1152 + (f - 9 * row) * 16 - 1024 * m;
    }
    const char* xs = xt + (1 - (tq4 >> 1)) * 36864 + wi * 128;
    const char* xs2 = xt + (tq4 ^ 1) * 18432 + wi * 128;
    const unsigned xd = sbase + L_X + wi * 8192 + (1 - (tq4 >> 1)) * 4096, xd2 = sbase + L_X + wi * 8192 + (tq4 ^ 1) * 2048;
    const unsigned wd = sbase + L_B + wi * 3072;
    const int t1 = (rot + 1) % 3, t2 = (rot + 2) % 3;
    GLDSX(xs + rot * 384, xd);
    GLDSX2(xs2 + rot * 384, xd2);
#pragma unroll
    for (int per = 0; per < 3; ++per) GLDS3("1024", "2048", vw[0], vw[1], vw[2], wqs + (192 * rot + 64 * per) * 1152, wd + per * 9216);
    GLDSX(xs + t1 * 384, xd + 24576);
    GLDSX2(xs2 + t1 * 384, xd2 + 24576);
#pragma unroll
    for (int per = 0; per < 2; ++per) GLDS3("1024", "2048", vw[0], vw[1], vw[2], wqs + (192 * t1 + 64 * per) * 1152, wd + 27648 + per * 9216);
    VM_WAIT(12);
    lds_signal(mi + M_C1);
#pragma unroll
    for (int per = 2; per < 3; ++per) GLDS3("1024", "2048", vw[0], vw[1], vw[2], wqs + (192 * t1 + 64 * per) * 1152, wd + 27648 + per * 9216);
    GLDSX(xs + t2 * 384, xd + 49152);
    GLDSX2(xs2 + t2 * 384, xd2 + 49152);
    lds_wait(mi + M_KVC, 3);
#pragma unroll
    for (int per = 0; per < 2; ++per) GLDS3("1024", "2048", vw[0], vw[1], vw[2], wqs + (192 * t2 + 64 * per) * 1152, wd + 55296 + per * 9216);
    VM_WAIT(12);
    lds_signal(mi + M_C2);
#pragma unroll
    for (int per = 2; per < 3; ++per) GLDS3("1024", "2048", vw[0], vw[1], vw[2], wqs + (192 * t2 + 64 * per) * 1152, wd + 55296 + per * 9216);
    lds_signal(mi + M_SE);
    VM_WAIT(0);
    lds_signal(mi + M_C3);
  }

  if (w >= 8) {
    const int j = w - 8, ht = tid - 512;
    __builtin_amdgcn_s_setprio(3);
    if (j < 3) {
      const unsigned stg = L_B + 2 * 27648 + j * 9216;
      lds_ptr wb = sm + stg + (8 * g4 + tq) * 48 + tp * 8;
      lds_ptr wc = sm + stg + (8 * g4 + tq) * 48 + 32 + (tp & 1) * 8;
      lds_ptr xb = sm + L_X + 3 * j * 8192 + (16 * tq4 + l15) * 128;
      VM_WAIT(0);
      bf16x8 fa[6], fc[6], fb[6], fg[6];
#pragma unroll
      for (int k = 0; k < 6; ++k) {
        fg[k] = lds_frag(sm + L_KVP + j * 2048 + k * 64 + g4 * 16);
        fb[k] = lds_frag(xb + (k >> 1) * 8192 + (((4 * (k & 1) + g4) ^ (l15 >> 1)) << 4));
        fa[k] = tr_frag(wb + k * 1536, wb + k * 1536 + 192);
        fc[k] = tr_frag(wc + k * 1536, wc + k * 1536 + 192);
      }
      f32x4 acc = {0}, acr = {0};
      if (sel) {
#pragma unroll
        for (int k = 0; k < 6; ++k) { acc = MFMA16(fb[k], fa[k], acc); acr = MFMA16(fb[k], fc[k], acr); }
      } else {
#pragma unroll
        for (int k = 0; k < 6; ++k) { acc = MFMA16(fa[k], fb[k], acc); acr = MFMA16(l15 == 8 ? fg[k] : fc[k], fb[k], acr); }
      }
      *(LAS f32x4*)(sm + L_KVP + j * 2048 + lane * 16) = acc;
      *(LAS f32x4*)(sm + L_KVP + j * 2048 + 1024 + lane * 16) = acr;
      lds_signal(mi + M_KVC);
      lds_signal(mi + M_C1);
      lds_wait<1>(mi + M_KD, 1);
    } else {
      const unsigned tg = __builtin_amdgcn_readfirstlane(epoch) + 1u;
      if (lane == 0) *(LAS unsigned*)(mi + M_TAG) = tg;
      const int T = 2 * qt + (tq4 >> 1), mt = tq4 & 1, i4 = T & 3, gb = (T >> 2) * KV_GROUP, rp = swap23(16 * mt + l15);
      int sa, sr;
      bool st;
      if (sel) {
        sa = gb + 4096 + i4 * 1216 + mt * 608 + (l15 + 18 * (g4 & 1)) * 16 + (g4 >> 1) * 8;
        sr = gb + 4096 + i4 * 1216 + mt * 608 + (l15 < 8 ? 10 + l15 + 18 * (g4 & 1) : 36 + (g4 & 1)) * 16 + (g4 >> 1) * 8;
        st = l15 >= 6 && l15 <= 8;
      } else {
        sa = gb + i4 * 1024 + (rp + 32 * (g4 & 1)) * 16 + (g4 >> 1) * 8;
        sr = gb + 8960 + i4 * 256 + rp * 8;
        st = g4 == 1;
      }
      lds_wait(mi + M_KVC, 3);
      lds_ptr pp = sm + L_KVP + lane * 16;
      const float ks = sel ? 1.f : 0.23570226039551584f * 1.4426950408889634f;
      const f32x4 va = (*(LAS f32x4*)(pp) + *(LAS f32x4*)(pp + 2048) + *(LAS f32x4*)(pp + 4096)) * ks;
      const f32x4 vs = *(LAS f32x4*)(pp + 1024) + *(LAS f32x4*)(pp + 3072) + *(LAS f32x4*)(pp + 5120), vr = vs * ks;
      const u32x2 a = {pk2(va[0], va[1]), pk2(va[2], va[3])};
      u32x2 r = {pk2(vr[0], vr[1]), pk2(vr[2], vr[3])};
      if (!sel) r = (u32x2){r[1], 0x3f803f80u};
      else if (l15 == 8) r[0] = r[1] = (g4 & 1) ? 0u : 0x3f803f80u;
      ST_SC1_16(((u32x4){a[0], tg, a[1], tg}), WS_KT + 2 * sa);
      if (st) ST_SC1_16(((u32x4){r[0], tg, r[1], tg}), WS_KT + 2 * sr);
      ST_SC1_8(a, WS_KV + sa);
      if (st) ST_SC1_8(r, WS_KV + sr);
      lds_signal(mi + M_KD);
      if (!sel && g4 == 2) {
        const float g = rbf(__builtin_amdgcn_rcpf(1.f + __builtin_amdgcn_exp2f(-1.4426950408889634f * rbf(vs[0]))));
        ST_SC1_4(__builtin_bit_cast(unsigned, g), WS_G + (64 * qt + 16 * tq4 + l15) * 4);
      }
      float n2 = dot2(a[1], dot2(a[0], st ? dot2(r[0], 0.f) : 0.f));
      n2 += xhalf(n2);
      n2 += u2f(__builtin_amdgcn_ds_swizzle(__builtin_bit_cast(int, n2), 0x401f));
      n2 = sel ? 0.f : max16(n2);
      VM_WAIT(0);
      if (lane == 0)
        __hip_atomic_store((gu64*)(gws + WS_FKV + T * 128 + (hg & 3) * 8), ((unsigned long long)tg << 32) | __builtin_bit_cast(unsigned, n2), RLX_AGENT);
    }
    const unsigned tag = *(LAS unsigned*)(mi + M_TAG);
    auto kv_tagged = [&](int st) LAMI {
      u32x4 r[10];
      const int o = WS_KT + ((s0 + st) & 7) * (2 * RSLOT) + lane * 16;
#pragma unroll 1
      for (unsigned spins = 0; spins < SPIN_CAP; ++spins) {
        unsigned bad = 0;
#pragma unroll
        for (int i = 0; i < 10; ++i) r[i] = LD_SC1_16(o + (i == 9 && j == 3 ? 38 : 4 * i + j) * 1024);
#pragma unroll
        for (int i = 0; i < 10; ++i) bad |= (r[i][1] ^ tag) | (r[i][3] ^ tag);
        asm volatile("" ::: "memory");
        if (ALL(bad == 0)) break;
        __builtin_amdgcn_s_sleep(4);
      }
      if (st == 0) {
        if (j == 0) {
          const float kn = dot2(r[0][2], dot2(r[0][0], 0.f));
          *(LAS float*)(mi + M_KN0) = wave_max(dpp_add<0xb1>(kn));
        }
        lds_wait(mi + M_C3B, 5);
      }
#pragma unroll
      for (int i = 0; i < 10; ++i)
        *(LAS u32x2*)(sm + st * RSLOT + (i == 9 && j == 3 ? 38 : 4 * i + j) * 512 + lane * 8) = (u32x2){r[i][0], r[i][2]};
    };
    auto kv_dma = [&](int st) LAMI {
      const char* src = ws + WS_KV + ((s0 + st) & 7) * RSLOT + j * 4992;
      const unsigned dst = sbase + st * RSLOT + j * 4992;
      GLDS4(" sc1", src, lane * 16, lane * 16, dst);
      if (lane < 56) glds_sc1(src + 4096, lane * 16, dst + 4096);
    };
    const unsigned vh3 = (lane == 63 ? 62 : lane) * 16;
    auto hload = [&](int cl, int b) LAMI {
      const char* hsrc = (const char*)(ssmk + (size_t)(72 * hg + cl) * S_) + 4 * j + 2;
      const unsigned dst = sbase + CV_H + b * CV_HB + 2 * j * CV_COPY + 64;
      GLDS4("", hsrc, lane * 16, vh3, dst);
      GLDS4("", hsrc + 2, lane * 16, vh3, dst + CV_COPY);
    };
    auto uload = [&](int cl) LAMI {
      const int tgq = ht & 7;
      const int o = cl < 64 ? (tgq >> 1) * 2048 + (cl & 1) * 1024 + ((tgq & 1) * 32 + (cl >> 1)) * 16
                            : 8192 + (cl & 1) * 512 + (tgq * 4 + ((cl - 64) >> 1)) * 16;
      return LD_SC1_16(WS_UT + ((ht >> 3) * 8 + hg) * 9216 + o);
    };
    const int ce = 64 + (qt >> 2);
    {
      lds_wait<1>(mi + M_SE, 3);
      kv_tagged(0);
      lds_signal(mi + M_QD);
      kv_tagged(1);
      lds_signal(mi + M_RDY + 4, 2);
      if (j == 3) {
        float kn = 0.f;
#pragma unroll 1
        for (unsigned spins = 0; spins < SPIN_CAP; ++spins) {
          const u32x4 v0 = LD_SC1_16(WS_FKV + lane * 128), v1 = LD_SC1_16(WS_FKV + lane * 128 + 16);
          const bool ok = v0[1] == tag && v0[3] == tag && v1[1] == tag && v1[3] == tag;
          kn = __builtin_fmaxf(u2f(v0[0]), u2f(v1[0]));
          asm volatile("" ::: "memory");
          if (ALL(ok)) break;
          __builtin_amdgcn_s_sleep(4);
        }
        lds_signal(mi + M_QR);
        lds_signal(mi + M_DONE);
        kn = wave_max(kn);
        if (lane == 0) {
          *(LAS float*)(mi + M_KN) = kn;
          if (bid == 0) __hip_atomic_store((gu32*)(gws + WS_EPOCH), tag, RLX_AGENT);
        }
      }
      lds_wait<1>(mi + M_QR, 9);
      kv_dma(2);
      kv_dma(3);
      VM_WAIT(0);
      lds_signal(mi + M_RDY + 8, 2);
      lds_signal(mi + M_RDY + 12, 2);
      UNIT_LOAD(l31, 2 * j + h);
      if (j == 1) {
        const int c0 = 72 * hg + 64 + 2 * (lane & 3), t0 = 64 * qt + 4 * (lane >> 2);
        const u32x4 sw = *(const u32x4*)(shk + c0 * 4);
        const unsigned bo = (t0 * D_ + c0) * 2, bo3 = t0 ? bo - 3 * 1152 : bo;
        unsigned kk[7], vv[4];
#pragma unroll
        for (int i = 0; i < 7; ++i) kk[i] = *(const unsigned*)((const char*)bk + (i < 3 ? bo3 + i * 1152 : bo + (i - 3) * 1152));
#pragma unroll
        for (int i = 0; i < 4; ++i) vv[i] = *(const unsigned*)((const char*)bv + bo + i * 1152);
        if (!t0) kk[0] = kk[1] = kk[2] = 0u;
        float ra[4], rb[4];
#pragma unroll
        for (int i = 0; i < 4; ++i) {
          ra[i] = __builtin_fmaf(bf_lo(kk[i + 3]), bf_lo(sw[0]), __builtin_fmaf(bf_lo(kk[i + 2]), bf_hi(sw[0]),
                  __builtin_fmaf(bf_lo(kk[i + 1]), bf_lo(sw[1]), bf_lo(kk[i]) * bf_hi(sw[1])))) * bf_lo(vv[i]);
          rb[i] = __builtin_fmaf(bf_hi(kk[i + 3]), bf_lo(sw[2]), __builtin_fmaf(bf_hi(kk[i + 2]), bf_hi(sw[2]),
                  __builtin_fmaf(bf_hi(kk[i + 1]), bf_lo(sw[3]), bf_hi(kk[i]) * bf_hi(sw[3])))) * bf_hi(vv[i]);
        }
        const int uo = WS_UT + (qt * 8 + hg) * 9216 + 8192 + ((lane >> 3) * 4 + (lane & 3)) * 16 + (1 - ((lane >> 2) & 1)) * 8;
        ST_SC1_8(((u32x2){pk2(ra[3], ra[2]), pk2(ra[1], ra[0])}), uo);
        ST_SC1_8(((u32x2){pk2(rb[3], rb[2]), pk2(rb[1], rb[0])}), uo + 512);
      }
      UNIT_DO();
      ST_SC1_16(o0, WS_UT + (qt * 8 + hg) * 9216 + j * 2048 + lane * 16);
      ST_SC1_16(o1, WS_UT + (qt * 8 + hg) * 9216 + j * 2048 + 1024 + lane * 16);
      {
        const u32x4 ones = {~0u, ~0u, ~0u, ~0u};
        const int ob = WS_O + 2 * qt * 36 * 1024 + (4 * hg + j) * 1024 + lane * 16;
        ST_SC1_16(ones, ob);
        ST_SC1_16(ones, ob + 36 * 1024);
        if (j == 0) ST_SC1_16(ones, WS_O + (2 * qt + h) * 36 * 1024 + 32768 + hg * 512 + l31 * 16);
        ST_SC1_16(ones, WS_Z + (72 * hg + 2 * qt) * 4096 + ht * 16);
        ST_SC1_16(ones, WS_Z + (72 * hg + 2 * qt + 1) * 4096 + ht * 16);
        ST_SC1_16(ones, WS_ZP + (hg * 32 + qt) * 8192 + ht * 16);
        ST_SC1_16(ones, WS_ZP + (hg * 32 + qt) * 8192 + 4096 + ht * 16);
      }
      VM_WAIT(0);
      if (lds_ticket(mi + M_UD) == 3u && lane == 0) __hip_atomic_store((gu32*)(gws + WS_FU) + (hg * 32 + qt) * 32, tag, RLX_AGENT);
    }
    const __bf16 *sk2 = ssmk, *sd2 = ssmd;
    asm volatile("" : "+s"(sk2), "+s"(sd2));
    const unsigned short h8a = *(const GAS unsigned short*)(sk2 + (size_t)(72 * hg + 2 * qt + (j >> 1)) * S_ + (lane & 7));
    const unsigned short h8e = *(const GAS unsigned short*)(sk2 + (size_t)(72 * hg + ce) * S_ + (lane & 7));
    const float dve = (qt & 3) ? 0.f : (float)*(const GAS __bf16*)(sd2 + 72 * hg + ce);
    auto cvprep = [&](int b, unsigned short h8) LAMI {
      *(LAS u32x4*)(sm + CV_U + (ht >> 7) * CV_UB + ((ht & 127) >> 2) * 80 + (ht & 3) * 16) = (u32x4){0, 0, 0, 0};
      lds_ptr hz = sm + CV_H + b * CV_HB + (lane >> 3) * CV_COPY;
      *(LAS u32x2*)(hz + (lane & 7) * 8) = (u32x2){0, 0};
      if ((lane & 7) <= (lane >> 3)) *(LAS unsigned short*)(hz + 48 + (7 - (lane >> 3) + (lane & 7)) * 2) = h8;
    };
    unsigned hs = 0;
#define HSYNC() do { lds_signal(mi + M_HS); hs += 4; lds_wait<2>(mi + M_HS, hs); } while (0)
    if (lane < 54) {
      const unsigned vo = (lane / 9) * 1152 + (lane % 9) * 16;
#pragma unroll 1
      for (int i = 0; i < 6; ++i) {
        const int p = 24 * j + 4 * ((i + qt + 3 * hg) % 6);
        asm volatile("s_mov_b32 m0, %5\n\ts_nop 0\n\tglobal_load_lds_dwordx4 %0, %4\n\tglobal_load_lds_dwordx4 %1, %4 offset:864\n\t"
                     "global_load_lds_dwordx4 %2, %4 offset:1728\n\tglobal_load_lds_dwordx4 %3, %4 offset:2592\n\ts_waitcnt vmcnt(2)"
                     :: "v"(vo), "v"(vo + 6048), "v"(vo + 12096), "v"(vo + 18144), "s"((const char*)wo + 144 * hg + p * 6912),
                        "s"(sbase + L_B + p * 864) : "memory");
      }
    }
    if (j == 2) TOUCH(ssmk + (size_t)(72 * hg + 2 * qt) * S_, lane * 128u);
    VM_WAIT(0);
    HSYNC();
    bf16x8 wf[18], we[5];
    {
      const int cb = (32 * (j & 1) + 16 * (g4 & 1) + 4 * tp) * 2;
      lds_ptr wm = sm + L_B + (DH * (j >> 1) + 4 * h + tq) * 144 + cb;
      lds_ptr wr = sm + L_B + (DH * (8 * (j >> 1) + 4 * h + (tq >> 1)) + 16 + (tq & 1)) * 144 + cb;
      asm volatile("" : "+v"(wm), "+v"(wr));
#pragma unroll
      for (int k = 0; k < 12; ++k) wf[k] = tr_frag(wm + k * 5184, wm + k * 5184 + 1152);
      wm += 12 * 5184;
      asm volatile("" : "+v"(wm));
#pragma unroll
      for (int k = 0; k < 4; ++k) wf[12 + k] = tr_frag(wm + k * 5184, wm + k * 5184 + 1152);
#pragma unroll
      for (int k = 0; k < 2; ++k) wf[16 + k] = tr_frag(wr + k * 41472, wr + k * 41472 + 5184);
      lds_ptr em = sm + L_B + (DH * (8 * j + (g4 >> 1)) + 4 * (g4 & 1) + tq) * 144 + (64 + 4 * (tp & 1)) * 2;
      lds_ptr er = sm + L_B + (DH * (16 * (j & 1) + 4 * g4 + (tq >> 1)) + 16 + (tq & 1)) * 144 + (64 + 4 * (tp & 1)) * 2;
      asm volatile("" : "+v"(em), "+v"(er));
#pragma unroll
      for (int k = 0; k < 4; ++k) we[k] = tr_frag(em + k * 5184, em + k * 5184 + 1152);
      we[4] = tr_frag(er, er + 5184);
    }
    HSYNC();
    cvprep(j >> 1, h8a);
    if (j == 3) {
#pragma unroll 1
      for (unsigned spins = 0; spins < SPIN_CAP; ++spins) {
        if (ALL(lane >= 40 || __hip_atomic_load((gu32*)(gws + WS_FU) + (h ? l31 * 32 + qt : hg * 32 + lane) * 32, RLX_AGENT) == tag)) break;
        __builtin_amdgcn_s_sleep(8);
      }
      lds_signal(mi + M_UR);
    } else {
      lds_wait<4>(mi + M_UR, 1);
    }
    lds_wait<8>(mi + M_DONE + 16, 8);
    hload(2 * qt, 0);
    hload(2 * qt + 1, 1);
    if (j == 1) *(LAS unsigned*)(mi + M_G + lane * 4) = __builtin_amdgcn_raw_buffer_load_b32(rs, WS_G + (64 * qt + lane) * 4, 0, 16);
    *(LAS u32x4*)(sm + CV_U + 2560 + (ht >> 2) * 80 + (ht & 3) * 16) = uload(2 * qt);
    *(LAS u32x4*)(sm + CV_U + CV_UB + 2560 + (ht >> 2) * 80 + (ht & 3) * 16) = uload(2 * qt + 1);
    const u32x4 ue = uload(ce);
    if (j == 0) xv = *(const u32x4*)(xt + otok * 1152 + 144 * hg + opc * 16);
    if (j == 2 && lane < 2) *(LAS float*)(mi + M_DV + lane * 4) = (float)ssmd[72 * hg + 2 * qt + lane];
    VM_WAIT(0);
#pragma unroll
    for (int i = 0; i < 4; ++i) lds_signal(mi + M_PAIR + 4 * i);
    {
      const int p = qt & 3;
      lds_wait<8>(mi + M_DONE + 20, 8);
      if (lane < 35) {
        const char* hsrc = (const char*)(ssmk + (size_t)(72 * hg + ce) * S_) + 4 * j + 2 + 512 * p - 64;
        const unsigned dst = sbase + E_H + 2 * j * E_COPY;
        glds(hsrc + 2048, lane * 16, dst + 640);
        glds(hsrc + 2050, lane * 16, dst + E_COPY + 640);
        if (p || lane >= 4) {
          glds(hsrc, lane * 16, dst);
          glds(hsrc + 2, lane * 16, dst + E_COPY);
        }
      }
      if (!p && j == 0) {
        lds_ptr hz = sm + E_H + (lane >> 3) * E_COPY;
        *(LAS u32x2*)(hz + (lane & 7) * 8) = (u32x2){0, 0};
        if ((lane & 7) <= (lane >> 3)) *(LAS unsigned short*)(hz + 48 + (7 - (lane >> 3) + (lane & 7)) * 2) = h8e;
      }
      if (ht < 160) *(LAS u32x4*)(sm + E_U + ht * 16) = (u32x4){0, 0, 0, 0};
      lds_ptr ab = sm + E_H + (l31 & 7) * E_COPY + (h + (l31 >> 3)) * 16 + 128 * j;
      lds_ptr bb = sm + E_U + 2560 + (l31 - 8 * p - 2 * j - 1) * 80 + (1 - h) * 16;
      asm volatile("" : "+v"(ab), "+v"(bb));
      lds_wait<8>(mi + M_DONE + 24, 8);
      *(LAS u32x4*)(sm + E_U + 2560 + (ht >> 2) * 80 + (ht & 3) * 16) = ue;
      VM_WAIT(0);
      lds_signal(mi + M_DONE + 28);
      lds_wait<2>(mi + M_DONE + 28, 12);
      f32x16 y0 = {0}, y1 = {0};
#pragma unroll
      for (int d = 0; d < 2; ++d) {
#pragma unroll
        for (int s = 0; s < 2; ++s) {
          const bf16x8 a0 = lds_frag(ab + 32 * (2 * d + 1 - s)), b0 = lds_frag(bb + 80 * (1 - d) + 32 * s);
          const bf16x8 b1 = lds_frag(bb + 80 * (33 - d) + 32 * s), a1 = lds_frag(ab + 640 + 32 * (2 * d + 1 - s));
          y0 = MFMA32(a0, b0, y0);
          y1 = MFMA32(a0, b1, y1);
          y1 = MFMA32(a1, b0, y1);
          SB0;
        }
      }
      __builtin_amdgcn_s_setprio(1);
      __builtin_amdgcn_s_sleep(10);
      CV_PUT(sm + E_P + j * 8192 + lane * 16);
      HSYNC();
#pragma unroll
      for (int q = 0; q < 2; ++q) {
        u32x4 t = __builtin_bit_cast(u32x4, cv_sum(sm + E_P + (2 * j + q) * 1024 + lane * 16,
                                                   sm + E_U + 2560 + (32 * (j >> 1) + l31) * 80 + (2 * (j & 1) + q) * 16 + (1 - h) * 8, dve));
#pragma unroll
        for (int e = 0; e < 4; ++e) t[e] = UMIN(t[e], 0xfffffffeu);
        ST_SC1_16(t, WS_ZP + (hg * 32 + qt) * 8192 + (2 * j + q) * 1024 + lane * 16);
      }
      lds_signal(mi + M_EXD);
    }
    __builtin_amdgcn_s_setprio(1);
    {
      lds_ptr ob = sm + (j >> 1) * 1024 + lane * 16;
      lds_ptr oe = sm + (8 * j + (g4 >> 1)) * 1024 + (l15 + 32 * (g4 & 1)) * 16;
      lds_ptr or_ = sm + (32 + 2 * (j & 1) + (g4 >> 1)) * 1024 + (16 * (j >> 1) + l15 + 32 * (g4 & 1)) * 16;
      lds_ptr ap = sm + L_AP + (8 * (j >> 1) + (l31 >> 3)) * CV_COPY + (l31 & 7) * 336 + (j & 1) * 128 + h * 16;
      lds_ptr ae = sm + L_AP + (8 * (j >> 1) + 2 * h + (l15 >> 3)) * CV_COPY + (l15 & 7) * 336 + 256 + (j & 1) * 32 + (g4 & 1) * 16;
      asm volatile("" : "+v"(ob), "+v"(oe), "+v"(or_), "+v"(ap), "+v"(ae));
      f32x16 a[2] = {{0}, {0}};
      f32x4 e0[2] = {{0}, {0}}, e1[2] = {{0}, {0}};
      bf16x8 f[54];
      lds_wait<1>(mi + M_KVR, 8);
#pragma unroll
      for (int k = 0; k < 59; ++k) {
        if (k == 27) { lds_wait(mi + M_OR, 4); ob += 36864; }
        const int t = k / 27, c = k % 27;
        if (k < 54) {
          if (c < 8) f[k] = lds_frag(oe + t * 36864 + (c >> 2) * 256 + (c & 3) * 2048);
          else if (c == 8) f[k] = lds_frag(or_ + t * 36864);
          else f[k] = lds_frag(ob + (c - 9) * 2048);
        }
        SB0;
        if (k >= 5) {
          const int m = k - 5, u = m / 27, d = m % 27;
          if (d < 4) e0[u] = MFMA16(we[d], f[m], e0[u]);
          else if (d < 8) e1[u] = MFMA16(we[d - 4], f[m], e1[u]);
          else if (d == 8) { if (j >> 1) e1[u] = MFMA16(we[4], f[m], e1[u]); else e0[u] = MFMA16(we[4], f[m], e0[u]); }
          else a[u] = MFMA32(wf[d - 9], f[m], a[u]);
          if (d == 12) *(LAS f32x4*)(ae + u * 4 * CV_COPY) = h ? e1[u] : e0[u];
          if (m == 35 || m == 53) {
            const int v = m == 53;
#pragma unroll
            for (int g = 0; g < 4; ++g)
              *(LAS f32x4*)(ap + v * 4 * CV_COPY + g * 32) = (f32x4){a[v][4 * g], a[v][4 * g + 1], a[v][4 * g + 2], a[v][4 * g + 3]};
            if (v) lds_signal(mi + M_ZS);
          }
        }
        SB0;
      }
    }
  } else {
    if (w < 4) {
      const int gt = w >> 1, tt = w & 1, arow = 32 * tt + l31, xsw = (arow >> 1) & 7;
      lds_ptr xb = sm + L_X + arow * 128;
      lds_ptr wb = sm + L_B + (8 * h + tq) * 144 + (36 * gt + 20 * (g4 & 1) + 4 * tp) * 2;
      lds_ptr xq[4];
#pragma unroll
      for (int i = 0; i < 4; ++i) xq[i] = xb + (((2 * i + h) ^ xsw) << 4);
      f32x16 acc = {0};
      bf16x8 fa[36], fb[36];
#pragma unroll
      for (int k = 0; k < 40; ++k) {
        if (k == 12 || k == 24) lds_signal(mi + M_C3 + (k == 12 ? 0 : 4));
        if (k % 12 == 0 && k < 36) lds_wait(mi + M_C1 + k / 3, k == 12 ? 3 : 6 + k / 12);
        if (k == 24) {
          wb += 55296;
#pragma unroll
          for (int i = 0; i < 4; ++i) xq[i] += 49152;
        }
        if (k < 36) {
          const int kk = k < 24 ? k : k - 24;
          fa[k] = tr_frag(wb + kk * 2304, wb + kk * 2304 + 576);
          fb[k] = lds_frag(xq[k & 3] + (kk >> 2) * 8192);
        }
        SB0;
        if (k >= 4) acc = MFMA32(fa[k - 4], fb[k - 4], acc);
        SB0;
      }
      u32x4 a;
#pragma unroll
      for (int i = 0; i < 4; ++i) a[i] = pk2(acc[2 * i], acc[2 * i + 1]);
      lds_ptr qe = sm + L_Q + (4 * gt + tt) * 1024;
      *(LAS u32x4*)(qe + lane * 16) = a;
      lds_ptr qo = qe + 2048 + l31 * 16;
      *(LAS unsigned*)(qo + h * 512 + 4) = pk2(acc[8], acc[9]);
      *(LAS unsigned*)(qo + (h ? 8 : 512)) = pk2(acc[10], acc[11]);
      *(LAS unsigned*)(qo + h * 512 + 12) = pk2(acc[12], acc[13]);
      *(LAS unsigned*)(h ? sm + L_Q + 8192 + ((2 * gt + 1) * 64 + arow) * 4 : qo + 520) = pk2(acc[14], acc[15]);
    } else if (w == 4) {
      __builtin_amdgcn_s_setprio(2);
      lds_ptr xb = sm + L_X + l15 * 128;
      lds_ptr wb = sm + L_B + (8 * g4 + tq) * 144 + (tp == 1 ? 104 : 32);
      lds_ptr xe = xb + ((g4 ^ (l15 >> 1)) << 4), xo = xb + (((4 + g4) ^ (l15 >> 1)) << 4);
      f32x4 acc[4] = {{0}, {0}, {0}, {0}};
      bf16x8 fa[18], fb[72];
#pragma unroll
      for (int k = 0; k < 20; ++k) {
        if (k == 6 || k == 12) lds_signal(mi + M_C3 + (k == 6 ? 0 : 4));
        if (k % 6 == 0 && k < 18) lds_wait(mi + M_C1 + 4 * (k / 6), k == 6 ? 3 : 6 + k / 6);
        if (k == 12) { wb += 55296; xe += 49152; xo += 49152; }
        if (k < 18) {
          const int kk = k < 12 ? k : k - 12;
          fa[k] = tr_frag(wb + kk * 4608, wb + kk * 4608 + 576);
#pragma unroll
          for (int t = 0; t < 4; ++t) fb[4 * k + t] = lds_frag(((k & 1) ? xo : xe) + (kk >> 1) * 8192 + t * 2048);
        }
        SB0;
        if (k >= 2) {
#pragma unroll
          for (int t = 0; t < 4; ++t) acc[t] = MFMA16(fa[k - 2], fb[4 * (k - 2) + t], acc[t]);
        }
        SB0;
      }
      if (g4 < 2) {
#pragma unroll
        for (int t = 0; t < 4; ++t) {
          const int tok = 16 * t + l15;
          *(LAS unsigned*)(sm + L_Q + 8192 + (2 * g4 * 64 + tok) * 4) = pk2(acc[t][0], acc[t][1]);
          *(LAS unsigned*)(sm + L_Q + ((2 * g4 + 1) * 2 + (t >> 1)) * 1024 + (tok & 31) * 16) = pk2(acc[t][2], acc[t][3]);
        }
      }
    }
    lds_signal(mi + M_QD);
    __builtin_amdgcn_s_setprio(2);
    const int hl = w & 3, kh = w >> 2;
    const int voff = 4096 + (l31 < 18 ? h * 288 + l31 * 16 : (l31 == 18 ? 576 : 592));
    lds_wait(mi + M_QD, 12);
    const u32x4 q0u = *(LAS u32x4*)(sm + L_Q + (2 * hl) * 1024 + lane * 16);
    const u32x4 q1u = *(LAS u32x4*)(sm + L_Q + (2 * hl + 1) * 1024 + lane * 16);
    const unsigned qr = *(LAS unsigned*)(sm + L_Q + 8192 + (hl * 64 + lane) * 4);
    const float kn0 = *(LAS float*)(mi + M_KN0);
    lds_signal(mi + M_QR);
    float n0 = 0.f, n1 = 0.f;
#pragma unroll
    for (int i = 0; i < 4; ++i) { n0 = dot2(q0u[i], n0); n1 = dot2(q1u[i], n1); }
    const float nq = dot2(qr, (h ? n1 : n0) + xhalf(h ? n0 : n1));
    const u32x2 qrem_u = {qr, pk2(-__builtin_amdgcn_sqrtf(nq * kn0), 0.f)};
    f32x16 o0, o1;
    float m0 = 0.f, m1 = 0.f;
    {
      int cnt = 8, off = 0, tmp, t2, so = ((s0 + 4) & 7) * RSLOT;
      unsigned ca = sbase + L_M + M_DONE - 4, dst = sbase + hl * 4992 + 2048;
      const unsigned long long ksrc = (unsigned long long)ws + WS_KV + hl * 4992 + 2048 - (sbase + kh * KV_GROUP);
      asm volatile(
      "v_mov_b32 v107, v126\n"
      "v_mov_b32 v108, v127\n"
      "v_mov_b32 v109, v128\n"
      "v_mov_b32 v130, 1\n"
      "ds_read2st64_b64 v[110:113], v109 offset0:0 offset1:1\n"
      "ds_read_b128 v[114:117], v107 offset:0\n"
      "ds_read_b128 v[118:121], v108 offset:0\n"
      "s_mov_b32 vcc_lo, %[first]\n"
      "s_mov_b32 vcc_hi, 0\n"
      "s_setprio 1\n"
      "v_mov_b32 v56, 0\n"
      "v_mov_b32 v57, 0\n"
      "v_mov_b32 v58, 0\n"
      "v_mov_b32 v59, 0\n"
      "v_mov_b32 v60, 0\n"
      "v_mov_b32 v61, 0\n"
      "v_mov_b32 v62, 0\n"
      "v_mov_b32 v63, 0\n"
      "v_mov_b32 v122, 0\n"
      "v_mov_b32 v123, 0\n"
      "v_mov_b32 v124, 0\n"
      "v_mov_b32 v125, 0\n"
      "v_mov_b32 v64, 0\n"
      "v_mov_b32 v65, 0\n"
      "v_mov_b32 v66, 0\n"
      "v_mov_b32 v67, 0\n"
      "v_mov_b32 v68, 0\n"
      "v_mov_b32 v69, 0\n"
      "v_mov_b32 v70, 0\n"
      "v_mov_b32 v71, 0\n"
      "v_mov_b32 v72, 0\n"
      "v_mov_b32 v73, 0\n"
      "v_mov_b32 v74, 0\n"
      "v_mov_b32 v75, 0\n"
      "v_mov_b32 v76, 0\n"
      "v_mov_b32 v77, 0\n"
      "v_mov_b32 v78, 0\n"
      "v_mov_b32 v79, 0\n"
      "s_waitcnt lgkmcnt(1)\n"
      "v_mfma_f32_32x32x4_2b_bf16 v[0:31], v[110:111], v[104:105], 0 cbsz:1 abid:0\n"
      "v_mov_b32 v80, 0\n"
      "v_mov_b32 v81, 0\n"
      "v_mov_b32 v82, 0\n"
      "v_mov_b32 v83, 0\n"
      "v_mov_b32 v84, 0\n"
      "v_mov_b32 v85, 0\n"
      "v_mov_b32 v86, 0\n"
      "v_mov_b32 v87, 0\n"
      "v_mov_b32 v88, 0\n"
      "v_mov_b32 v89, 0\n"
      "v_mov_b32 v90, 0\n"
      "v_mov_b32 v91, 0\n"
      "v_mov_b32 v92, 0\n"
      "v_mov_b32 v93, 0\n"
      "v_mov_b32 v94, 0\n"
      "v_mov_b32 v95, 0\n"
      "s_nop 2\n"
      "v_mfma_f32_32x32x16_bf16 v[0:15], v[114:117], v[96:99], v[0:15]\n"
      "v_mfma_f32_32x32x16_bf16 v[16:31], v[114:117], v[100:103], v[16:31]\n"
      "s_nop 11\n"
      "1:\n"
      "s_cbranch_vccz 10f\n"
      "s_setprio 2\n"
      "10:\n"
      "v_exp_f32 v0, v0\n"
      "v_exp_f32 v1, v1\n"
      "v_smfmac_f32_32x32x32_bf16 v[80:95], v[122:125], v[56:63], v106\n"
      "ds_read_b128 v[114:117], v107 offset:1024\n"
      "ds_read_b128 v[122:125], v108 offset:608\n"
      "v_exp_f32 v2, v2\n"
      "v_exp_f32 v3, v3\n"
      "v_exp_f32 v4, v4\n"
      "v_exp_f32 v5, v5\n"
      "v_mfma_f32_32x32x4_2b_bf16 v[32:63], v[110:111], v[104:105], 0 cbsz:1 abid:1\n"
      "v_exp_f32 v6, v6\n"
      "v_exp_f32 v7, v7\n"
      "v_exp_f32 v8, v8\n"
      "v_exp_f32 v9, v9\n"
      "v_exp_f32 v10, v10\n"
      "v_exp_f32 v11, v11\n"
      "v_exp_f32 v12, v12\n"
      "s_waitcnt lgkmcnt(2)\n"
      "v_exp_f32 v13, v13\n"
      "v_smfmac_f32_32x32x32_bf16 v[64:79], v[118:121], v[0:7], v106\n"
      "v_exp_f32 v14, v14\n"
      "v_exp_f32 v15, v15\n"
      "v_exp_f32 v16, v16\n"
      "s_waitcnt lgkmcnt(0)\n"
      "v_exp_f32 v17, v17\n"
      "v_smfmac_f32_32x32x32_bf16 v[64:79], v[122:125], v[8:15], v106\n"
      "v_exp_f32 v18, v18\n"
      "v_exp_f32 v19, v19\n"
      "v_exp_f32 v20, v20\n"
      "v_exp_f32 v21, v21\n"
      "v_mfma_f32_32x32x16_bf16 v[32:47], v[114:117], v[96:99], v[32:47]\n"
      "v_exp_f32 v22, v22\n"
      "v_exp_f32 v23, v23\n"
      "v_exp_f32 v24, v24\n"
      "v_exp_f32 v25, v25\n"
      "v_smfmac_f32_32x32x32_bf16 v[80:95], v[118:121], v[16:23], v106\n"
      "v_exp_f32 v26, v26\n"
      "ds_read_b128 v[118:121], v108 offset:1216\n"
      "v_exp_f32 v27, v27\n"
      "v_exp_f32 v28, v28\n"
      "v_exp_f32 v29, v29\n"
      "v_mfma_f32_32x32x16_bf16 v[48:63], v[114:117], v[100:103], v[48:63]\n"
      "v_exp_f32 v30, v30\n"
      "v_exp_f32 v31, v31\n"
      "v_exp_f32 v32, v32\n"
      "v_exp_f32 v33, v33\n"
      "v_smfmac_f32_32x32x32_bf16 v[80:95], v[122:125], v[24:31], v106\n"
      "ds_read_b128 v[114:117], v107 offset:2048\n"
      "ds_read_b128 v[122:125], v108 offset:1824\n"
      "ds_read_b32 v131, v129\n"
      "v_exp_f32 v34, v34\n"
      "v_exp_f32 v35, v35\n"
      "v_exp_f32 v36, v36\n"
      "v_exp_f32 v37, v37\n"
      "v_mfma_f32_32x32x4_2b_bf16 v[0:31], v[112:113], v[104:105], 0 cbsz:1 abid:0\n"
      "v_exp_f32 v38, v38\n"
      "v_exp_f32 v39, v39\n"
      "v_exp_f32 v40, v40\n"
      "v_exp_f32 v41, v41\n"
      "v_exp_f32 v42, v42\n"
      "v_exp_f32 v43, v43\n"
      "v_exp_f32 v44, v44\n"
      "s_waitcnt lgkmcnt(3)\n"
      "v_exp_f32 v45, v45\n"
      "v_smfmac_f32_32x32x32_bf16 v[64:79], v[118:121], v[32:39], v106\n"
      "v_exp_f32 v46, v46\n"
      "v_exp_f32 v47, v47\n"
      "v_exp_f32 v48, v48\n"
      "s_waitcnt lgkmcnt(0)\n"
      "s_waitcnt vmcnt(0)\n"
      "s_mov_b64 exec, 1\n"
      "ds_add_u32 v129, v130 offset:76\n"
      "s_mov_b64 exec, -1\n"
      "v_exp_f32 v49, v49\n"
      "v_smfmac_f32_32x32x32_bf16 v[64:79], v[122:125], v[40:47], v106\n"
      "v_exp_f32 v50, v50\n"
      "v_exp_f32 v51, v51\n"
      "v_exp_f32 v52, v52\n"
      "v_exp_f32 v53, v53\n"
      "v_mfma_f32_32x32x16_bf16 v[0:15], v[114:117], v[96:99], v[0:15]\n"
      "v_exp_f32 v54, v54\n"
      "v_exp_f32 v55, v55\n"
      "v_exp_f32 v56, v56\n"
      "v_exp_f32 v57, v57\n"
      "v_smfmac_f32_32x32x32_bf16 v[80:95], v[118:121], v[48:55], v106\n"
      "v_exp_f32 v58, v58\n"
      "ds_read_b128 v[118:121], v108 offset:2432\n"
      "v_exp_f32 v59, v59\n"
      "v_exp_f32 v60, v60\n"
      "v_exp_f32 v61, v61\n"
      "v_mfma_f32_32x32x16_bf16 v[16:31], v[114:117], v[100:103], v[16:31]\n"
      "v_exp_f32 v62, v62\n"
      "v_exp_f32 v63, v63\n"
      "s_and_b32 %[t2], %[cnt], 12\n"
      "s_or_b32 %[t2], %[t2], vcc_lo\n"
      "s_cmp_lg_u32 %[t2], 5\n"
      "s_cbranch_scc1 8f\n"
      "v_readfirstlane_b32 %[tmp], v131\n"
      "s_nop 0\n"
      "s_cmp_lt_u32 %[tmp], 8\n"
      "s_cbranch_scc1 7f\n"
      "9:\n"
      "s_mov_b32 m0, %[dst]\n"
      "s_add_u32 s96, %[slo], %[so]\n"
      "s_addc_u32 s97, %[shi], 0\n"
      "global_load_lds_dwordx4 v126, s[96:97] offset:-2048 sc1\n"
      "global_load_lds_dwordx4 v126, s[96:97] offset:-1024 sc1\n"
      "global_load_lds_dwordx4 v126, s[96:97] sc1\n"
      "global_load_lds_dwordx4 v126, s[96:97] offset:1024 sc1\n"
      "s_mov_b32 exec_hi, 0xffffff\n"
      "global_load_lds_dwordx4 v126, s[96:97] offset:2048 sc1\n"
      "s_mov_b32 exec_hi, -1\n"
      "s_add_u32 %[dst], %[dst], 19968\n"
      "s_add_u32 %[so], %[so], 19968\n"
      "s_cmp_eq_u32 %[so], 159744\n"
      "s_cselect_b32 %[so], 0, %[so]\n"
      "8:\n"
      "v_exp_f32 v0, v0\n"
      "v_exp_f32 v1, v1\n"
      "v_smfmac_f32_32x32x32_bf16 v[80:95], v[122:125], v[56:63], v106\n"
      "ds_read_b128 v[114:117], v107 offset:3072\n"
      "ds_read_b128 v[122:125], v108 offset:3040\n"
      "ds_read_b32 v131, v129 offset:72\n"
      "v_exp_f32 v2, v2\n"
      "v_exp_f32 v3, v3\n"
      "v_exp_f32 v4, v4\n"
      "v_exp_f32 v5, v5\n"
      "v_mfma_f32_32x32x4_2b_bf16 v[32:63], v[112:113], v[104:105], 0 cbsz:1 abid:1\n"
      "v_exp_f32 v6, v6\n"
      "v_exp_f32 v7, v7\n"
      "v_exp_f32 v8, v8\n"
      "v_exp_f32 v9, v9\n"
      "v_exp_f32 v10, v10\n"
      "v_exp_f32 v11, v11\n"
      "v_exp_f32 v12, v12\n"
      "s_waitcnt lgkmcnt(3)\n"
      "v_exp_f32 v13, v13\n"
      "v_smfmac_f32_32x32x32_bf16 v[64:79], v[118:121], v[0:7], v106\n"
      "v_exp_f32 v14, v14\n"
      "v_exp_f32 v15, v15\n"
      "v_exp_f32 v16, v16\n"
      "s_waitcnt lgkmcnt(0)\n"
      "v_exp_f32 v17, v17\n"
      "v_smfmac_f32_32x32x32_bf16 v[64:79], v[122:125], v[8:15], v106\n"
      "v_exp_f32 v18, v18\n"
      "v_exp_f32 v19, v19\n"
      "v_exp_f32 v20, v20\n"
      "v_exp_f32 v21, v21\n"
      "v_mfma_f32_32x32x16_bf16 v[32:47], v[114:117], v[96:99], v[32:47]\n"
      "v_exp_f32 v22, v22\n"
      "v_exp_f32 v23, v23\n"
      "v_exp_f32 v24, v24\n"
      "v_exp_f32 v25, v25\n"
      "v_smfmac_f32_32x32x32_bf16 v[80:95], v[118:121], v[16:23], v106\n"
      "s_cbranch_vccz 11f\n"
      "s_setprio 0\n"
      "11:\n"
      "v_exp_f32 v26, v26\n"
      "ds_read_b128 v[118:121], v108 offset:3648\n"
      "v_exp_f32 v27, v27\n"
      "v_exp_f32 v28, v28\n"
      "v_exp_f32 v29, v29\n"
      "v_mfma_f32_32x32x16_bf16 v[48:63], v[114:117], v[100:103], v[48:63]\n"
      "v_exp_f32 v30, v30\n"
      "v_exp_f32 v31, v31\n"
      "v_readfirstlane_b32 %[tmp], v131\n"
      "s_add_u32 %[off], %[off], 19968\n"
      "s_cmp_eq_u32 %[off], 79872\n"
      "s_cselect_b32 %[off], 0, %[off]\n"
      "s_cmp_lt_i32 %[tmp], 8\n"
      "s_cbranch_scc1 3f\n"
      "2:\n"
      "v_add_u32 v109, %[off], v128\n"
      "ds_read2st64_b64 v[110:113], v109 offset0:0 offset1:1\n"
      "v_add_u32 v107, %[off], v126\n"
      "v_exp_f32 v32, v32\n"
      "v_exp_f32 v33, v33\n"
      "v_smfmac_f32_32x32x32_bf16 v[80:95], v[122:125], v[24:31], v106\n"
      "ds_read_b128 v[114:117], v107 offset:0\n"
      "ds_read_b128 v[122:125], v108 offset:4256\n"
      "v_exp_f32 v34, v34\n"
      "v_exp_f32 v35, v35\n"
      "v_exp_f32 v36, v36\n"
      "s_waitcnt lgkmcnt(2)\n"
      "v_exp_f32 v37, v37\n"
      "v_mfma_f32_32x32x4_2b_bf16 v[0:31], v[110:111], v[104:105], 0 cbsz:1 abid:0\n"
      "v_exp_f32 v38, v38\n"
      "v_exp_f32 v39, v39\n"
      "v_exp_f32 v40, v40\n"
      "v_exp_f32 v41, v41\n"
      "v_exp_f32 v42, v42\n"
      "v_exp_f32 v43, v43\n"
      "v_exp_f32 v44, v44\n"
      "v_exp_f32 v45, v45\n"
      "v_smfmac_f32_32x32x32_bf16 v[64:79], v[118:121], v[32:39], v106\n"
      "v_exp_f32 v46, v46\n"
      "v_exp_f32 v47, v47\n"
      "v_exp_f32 v48, v48\n"
      "s_waitcnt lgkmcnt(0)\n"
      "s_mov_b64 exec, 1\n"
      "ds_add_u32 v129, v130 offset:4\n"
      "s_mov_b64 exec, -1\n"
      "v_exp_f32 v49, v49\n"
      "v_smfmac_f32_32x32x32_bf16 v[64:79], v[122:125], v[40:47], v106\n"
      "v_exp_f32 v50, v50\n"
      "v_exp_f32 v51, v51\n"
      "v_exp_f32 v52, v52\n"
      "v_exp_f32 v53, v53\n"
      "v_mfma_f32_32x32x16_bf16 v[0:15], v[114:117], v[96:99], v[0:15]\n"
      "v_exp_f32 v54, v54\n"
      "v_exp_f32 v55, v55\n"
      "v_exp_f32 v56, v56\n"
      "v_exp_f32 v57, v57\n"
      "v_smfmac_f32_32x32x32_bf16 v[80:95], v[118:121], v[48:55], v106\n"
      "v_add_u32 v108, %[off], v127\n"
      "v_add_u32 v129, 4, v129\n"
      "v_exp_f32 v58, v58\n"
      "ds_read_b128 v[118:121], v108 offset:0\n"
      "v_exp_f32 v59, v59\n"
      "v_exp_f32 v60, v60\n"
      "v_exp_f32 v61, v61\n"
      "v_mfma_f32_32x32x16_bf16 v[16:31], v[114:117], v[100:103], v[16:31]\n"
      "v_exp_f32 v62, v62\n"
      "v_exp_f32 v63, v63\n"
      "s_sub_u32 %[cnt], %[cnt], 1\n"
      "s_cmp_lg_u32 %[cnt], 0\n"
      "s_cbranch_scc1 1b\n"
      "s_branch 6f\n"
      "3:\n"
      "s_sleep 1\n"
      "ds_read_b32 v131, v129 offset:72\n"
      "s_waitcnt lgkmcnt(0)\n"
      "v_readfirstlane_b32 %[tmp], v131\n"
      "s_nop 0\n"
      "s_cmp_lt_i32 %[tmp], 8\n"
      "s_cbranch_scc1 3b\n"
      "s_branch 2b\n"
      "7:\n"
      "s_sleep 1\n"
      "ds_read_b32 v131, v129\n"
      "s_waitcnt lgkmcnt(0)\n"
      "v_readfirstlane_b32 %[tmp], v131\n"
      "s_nop 0\n"
      "s_cmp_lt_u32 %[tmp], 8\n"
      "s_cbranch_scc1 7b\n"
      "s_branch 9b\n"
      "6:\n"
      "v_smfmac_f32_32x32x32_bf16 v[80:95], v[122:125], v[56:63], v106\n"
      "s_setprio 0\n"
      "s_waitcnt lgkmcnt(0)\n"
      "s_nop 15\n"
      "s_nop 7\n"
      : "=&{v[64:79]}"(o0), "=&{v[80:95]}"(o1), "+{v129}"(ca), [cnt] "+s"(cnt), [off] "+s"(off), [tmp] "=&s"(tmp),
        [t2] "=&s"(t2), [so] "+s"(so), [dst] "+s"(dst)
      : "{v[96:99]}"(q0u), "{v[100:103]}"(q1u), "{v[104:105]}"(qrem_u), "{v106}"(SPIDX),
        "{v126}"(sbase + kh * KV_GROUP + lane * 16), "{v127}"(sbase + kh * KV_GROUP + voff),
        "{v128}"(sbase + kh * KV_GROUP + 8960 + lane * 8), [first] "s"(1 - kh), [slo] "s"((unsigned)ksrc), [shi] "s"((unsigned)(ksrc >> 32))
      : "v0", "v1", "v2", "v3", "v4", "v5", "v6", "v7", "v8", "v9", "v10", "v11", "v12", "v13", "v14", "v15",
        "v16", "v17", "v18", "v19", "v20", "v21", "v22", "v23", "v24", "v25", "v26", "v27", "v28", "v29", "v30", "v31",
        "v32", "v33", "v34", "v35", "v36", "v37", "v38", "v39", "v40", "v41", "v42", "v43", "v44", "v45", "v46", "v47",
        "v48", "v49", "v50", "v51", "v52", "v53", "v54", "v55", "v56", "v57", "v58", "v59", "v60", "v61", "v62", "v63",
        "v107", "v108", "v109", "v110", "v111", "v112", "v113", "v114", "v115", "v116", "v117", "v118", "v119", "v120", "v121", "v122",
        "v123", "v124", "v125", "v130", "v131", "s96", "s97", "vcc", "scc", "memory");
    }
    xv = *(const u32x4*)(xt + otok * 1152 + 144 * hg + opc * 16);
    if (!ALL(nq * *(LAS float*)(mi + M_KN) < 31.f * 31.f)) {
      m0 = m1 = -__builtin_inff();
      o0 = o1 = (f32x16){0};
      const bf16x8 q0 = __builtin_bit_cast(bf16x8, q0u), q1 = __builtin_bit_cast(bf16x8, q1u);
      const s16x4 qrem = __builtin_bit_cast(s16x4, (u32x2){qr, 0u});
#pragma unroll 1
      for (int t = 0; t < 32; ++t) {
        const int g = WS_KV + (t >> 2) * RSLOT + kh * KV_GROUP;
        const u32x2 kr = __builtin_bit_cast(u32x2, __builtin_amdgcn_raw_buffer_load_b64(rs, g + 8960 + (t & 3) * 256 + l31 * 8, 0, 16));
        const bf16x8 km = __builtin_bit_cast(bf16x8, LD_SC1_16(g + (t & 3) * 1024 + lane * 16));
        const bf16x8 va = __builtin_bit_cast(bf16x8, LD_SC1_16(g + voff + (t & 3) * 1216));
        const bf16x8 vb = __builtin_bit_cast(bf16x8, LD_SC1_16(g + voff + (t & 3) * 1216 + 608));
        const f32x32 z = {0};
        const f32x32 r = __builtin_amdgcn_mfma_f32_32x32x4bf16_1k(__builtin_bit_cast(s16x4, kr), qrem, z, 0, 0, 0);
        online_step(MFMA32(km, q0, lo16(r)), m0, o0, va, vb);
        online_step(MFMA32(km, q1, hi16(r)), m1, o1, va, vb);
      }
    }
    lds_ptr ex = sm + L_EX + hl * 6144 + lane * 16;
    const int zl = (2 * (qt & 15) + (lane & 1) + 32 * ((lane >> 1) & 1)) * 16;
    const int zpo = WS_ZP + (hg * 32 + 8 * hl + (lane >> 3)) * 8192 + (qt >> 4) * 4096 + ((lane >> 2) & 1) * 2048 + zl;
    u32x4 zp[2];
    if (kh) {
      *(LAS f32x4*)(ex) = (f32x4){o0[0], o0[1], o0[2], o0[3]};
      *(LAS f32x4*)(ex + 1024) = (f32x4){o0[4], o0[5], o0[6], o0[7]};
      *(LAS f32x4*)(ex + 2048) = (f32x4){o0[8], o0[9], o0[10], m0};
      *(LAS f32x4*)(ex + 3072) = (f32x4){o1[0], o1[1], o1[2], o1[3]};
      *(LAS f32x4*)(ex + 4096) = (f32x4){o1[4], o1[5], o1[6], o1[7]};
      *(LAS f32x4*)(ex + 5120) = (f32x4){o1[8], o1[9], o1[10], m1};
      lds_signal(mi + M_PAIR + 4 * hl);
    }
    lds_wait<1>(mi + M_PAIR + 4 * hl, 5);
    if (!kh) {
      __builtin_amdgcn_s_setprio(3);
#pragma unroll
      for (int t = 0; t < 2; ++t) {
        const f32x16 keep = t ? o1 : o0;
        const f32x4 t0 = *(LAS f32x4*)(ex + t * 3072), t1 = *(LAS f32x4*)(ex + t * 3072 + 1024), t2 = *(LAS f32x4*)(ex + t * 3072 + 2048);
        const float mk = t ? m1 : m0, mm = __builtin_fmaxf(mk, t2[3]);
        float fa = __builtin_amdgcn_exp2f(mk - mm), fb = __builtin_amdgcn_exp2f(t2[3] - mm);
        const float l = keep[10] * fa + t2[2] * fb, lx = xhalf(l);
        const float inv = __builtin_amdgcn_rcpf(h ? lx : l);
        fa *= inv;
        fb *= inv;
        float o[10];
#pragma unroll
        for (int i = 0; i < 10; ++i) o[i] = keep[i] * fa + (i < 4 ? t0[i & 3] : (i < 8 ? t1[i & 3] : t2[i & 3])) * fb;
        u32x4 a;
#pragma unroll
        for (int i = 0; i < 4; ++i) a[i] = UMIN(pk2(o[2 * i], o[2 * i + 1]), 0xfffffffeu);
        const int ob = WS_O + (2 * qt + t) * 36 * 1024;
        ST_SC1_16(a, ob + (4 * hg + hl) * 1024 + lane * 16);
        if (!h) ST_SC1_4(UMIN(pk2(o[8], o[9]), 0xfffffffeu), ob + 32768 + (4 * hg + hl) * 128 + l31 * 4);
      }
      lds_signal(mi + M_EXD);
      __builtin_amdgcn_s_setprio(1);
      __builtin_amdgcn_s_sleep(20);
      u32x4 of[18];
#pragma unroll
      for (int i = 0; i < 18; ++i) of[i] = LD_SC1_16(WS_O + (2 * qt * 36 + (i / 9) * 36 + (i % 9 < 8 ? 8 * hl + i % 9 : 32 + hl)) * 1024 + lane * 16);
      lds_wait<1>(mi + M_EXD, 8);
#pragma unroll
      for (int t = 0; t < 2; ++t) {
        unsigned mx = 0;
#pragma unroll
        for (int i = 9 * t; i < 9 * t + 9; ++i) mx = UMAX(UMAX(mx, UMAX(of[i][0], of[i][1])), UMAX(of[i][2], of[i][3]));
        if (__builtin_expect(!ALL(mx != ~0u), 0)) {
#pragma unroll 1
          for (unsigned spins = 0; spins < SPIN_CAP; ++spins) {
            asm volatile("" ::: "memory");
#pragma unroll
            for (int i = 9 * t; i < 9 * t + 9; ++i) of[i] = LD_SC1_16(WS_O + (2 * qt * 36 + t * 36 + (i % 9 < 8 ? 8 * hl + i % 9 : 32 + hl)) * 1024 + lane * 16);
            mx = 0;
#pragma unroll
            for (int i = 9 * t; i < 9 * t + 9; ++i) mx = UMAX(UMAX(mx, UMAX(of[i][0], of[i][1])), UMAX(of[i][2], of[i][3]));
            if (ALL(mx != ~0u)) break;
            __builtin_amdgcn_s_sleep(2);
          }
        }
#pragma unroll
        for (int i = 0; i < 9; ++i) {
          if (i == 8) {
            lds_ptr p = sm + (36 * t + 32 + hl) * 1024 + (4 * (lane & 7) + 32 * h) * 16 + ((lane >> 3) & 3) * 4;
#pragma unroll
            for (int e = 0; e < 4; ++e) *(LAS unsigned*)(p + e * 16) = of[9 * t + i][e];
          } else {
            *(LAS u32x4*)(sm + (36 * t + 8 * hl + i) * 1024 + lane * 16) = of[9 * t + i];
          }
        }
        if (t == 0) lds_signal(mi + M_KVR);
        if (t == 0) { zp[0] = LD_SC1_16(zpo); zp[1] = LD_SC1_16(zpo + 1024); }
      }
      lds_signal(mi + M_OR);
      __builtin_amdgcn_s_setprio(0);
    } else {
      const int b = hl >> 1, o = hl & 1;
      lds_ptr ab = sm + CV_H + b * CV_HB + (l31 & 7) * CV_COPY + (h + (l31 >> 3)) * 16 + 1024 * o;
      lds_ptr bb = sm + CV_U + b * CV_UB + 2560 + (l31 - 16 * o - 7) * 80 + (1 - h) * 16;
      f32x16 y0, y1;
      bf16x8 fx[8], fy[8];
      __builtin_amdgcn_s_setprio(0);
      asm volatile("" : "+v"(ab), "+v"(bb));
      const float dv = *(LAS float*)(mi + M_DV + 4 * b), d0 = o ? 0.f : dv, d1 = o ? dv : 0.f;
      u32x2 uu[4];
#pragma unroll
      for (int g = 0; g < 4; ++g) uu[g] = *(LAS u32x2*)(sm + CV_U + b * CV_UB + 2560 + (32 * o + l31) * 80 + g * 16 + (1 - h) * 8);
      CV_LD(fx, 0);
#pragma unroll
      for (int g = 0; g < 4; ++g) {
        const float e[4] = {bf_hi(uu[g][1]), bf_lo(uu[g][1]), bf_hi(uu[g][0]), bf_lo(uu[g][0])};
#pragma unroll
        for (int i = 0; i < 4; ++i) { y0[4 * g + i] = o ? 0.f : e[i] * d0; y1[4 * g + i] = o ? e[i] * d1 : 0.f; }
      }
#pragma unroll 1
      for (int it = 0; it < 4; ++it) {
        asm volatile("" : "+v"(ab), "+v"(bb));
        CV_STEP(fx, fy, 1);
        CV_STEP(fy, fx, 2);
        CV_STEP(fx, fy, 3);
        CV_STEP(fy, fx, 4);
        __builtin_amdgcn_s_setprio(2);
        ab += 256;
        bb -= 320;
      }
      lds_ptr px = sm + CV_H + b * CV_HB + h * CV_COPY + l31 * 16;
      {
        const f32x16 give = o ? y0 : y1;
#pragma unroll
        for (int g = 0; g < 4; ++g) *(LAS f32x4*)(px + o * 3200 + g * 2 * CV_COPY) = (f32x4){give[4 * g], give[4 * g + 1], give[4 * g + 2], give[4 * g + 3]};
        if (o) y0 = y1;
      }
      lds_signal(mi + M_AS + 4 * b);
      lds_signal(mi + M_KVR);
      lds_wait(mi + M_AS + 4 * b, 2);
      u32x4 z[2];
#pragma unroll
      for (int g = 0; g < 4; ++g) {
        const f32x4 t = *(LAS f32x4*)(px + (1 - o) * 3200 + g * 2 * CV_COPY);
        z[g >> 1][2 * (g & 1)] = UMIN(pk2(t[0] + y0[4 * g], t[1] + y0[4 * g + 1]), 0xfffffffeu);
        z[g >> 1][2 * (g & 1) + 1] = UMIN(pk2(t[2] + y0[4 * g + 2], t[3] + y0[4 * g + 3]), 0xfffffffeu);
      }
      const int zs = WS_Z + (72 * hg + 2 * qt + b) * 4096 + o * 2048 + lane * 16;
      ST_SC1_16(z[0], zs);
      ST_SC1_16(z[1], zs + 1024);
      lds_signal(mi + M_CE);
      __builtin_amdgcn_s_setprio(0);
    }
    lds_wait<2>(mi + M_CE, 4);
    if (kh) __builtin_amdgcn_s_sleep(12); else __builtin_amdgcn_s_sleep(2);
    const int zo = WS_Z + (72 * hg + (tid >> 3)) * 4096 + (qt >> 4) * 2048 + ((tid >> 2) & 1) * 1024 + zl;
    if (!kh) {
#pragma unroll 1
      for (unsigned spins = 0; spins < SPIN_CAP; ++spins) {
        if (ALL(UMAX(UMAX(UMAX(zp[0][0], zp[0][1]), UMAX(zp[0][2], zp[0][3])), UMAX(UMAX(zp[1][0], zp[1][1]), UMAX(zp[1][2], zp[1][3]))) != ~0u)) break;
        __builtin_amdgcn_s_sleep(2);
        asm volatile("" ::: "memory");
        zp[0] = LD_SC1_16(zpo);
        zp[1] = LD_SC1_16(zpo + 1024);
      }
      float e[8];
#pragma unroll
      for (int i = 0; i < 8; ++i) {
        e[i] = dpp_add<0x128>(u2f(zp[i >> 2][i & 3]));
        e[i] += u2f(__builtin_amdgcn_ds_swizzle(__builtin_bit_cast(int, e[i]), 0x401f));
      }
      zv = (u32x4){pk2(e[0], e[1]), pk2(e[2], e[3]), pk2(e[4], e[5]), pk2(e[6], e[7])};
      zscatter(512 + 16 * hl + 8 * h + (lane & 7));
    }
    u32x4 zn = LD_SC1_16(zo);
#pragma unroll 1
    for (unsigned spins = 0; spins < SPIN_CAP; ++spins) {
      if (ALL(UMAX(UMAX(zn[0], zn[1]), UMAX(zn[2], zn[3])) != ~0u)) break;
      __builtin_amdgcn_s_sleep(2);
      asm volatile("" ::: "memory");
      zn = LD_SC1_16(zo);
    }
    zv = zn;
    zscatter(tid);
  }

  if (w < 9) {
    lds_ptr ap = sm + L_AP + (otok >> 3) * CV_COPY + (otok & 7) * 336 + opc * 32;
    float gq = *(LAS float*)(mi + M_G + otok * 4);
    const float omg = rbf(1.f - gq);
    lds_ptr aq = ap + (opc == 8 ? 32 : 0);
    f32x4 x0 = {omg * bf_lo(xv[0]), omg * bf_hi(xv[0]), omg * bf_lo(xv[1]), omg * bf_hi(xv[1])};
    f32x4 x1 = {omg * bf_lo(xv[2]), omg * bf_hi(xv[2]), omg * bf_lo(xv[3]), omg * bf_hi(xv[3])};
    lds_ptr zq = sm + L_ZT + ((otok >> 5) + ((otok >> 1) & 2) + ((otok >> 2) & 4) + 8 * (otok & 3) + 4 * (otok & 8)) * 144 + opc * 16;
    u32x4* op = (u32x4*)(out + (size_t)(64 * qt + otok) * D_ + 72 * hg + 8 * opc);
    asm volatile("" : "+v"(gq), "+v"(ap), "+v"(aq), "+v"(x0), "+v"(x1), "+v"(zq), "+v"(op));
    lds_wait(mi + M_ZS, 16);
    const u32x4 z = *(LAS u32x4*)zq;
    f32x4 a0 = *(LAS f32x4*)(ap) + *(LAS f32x4*)(ap + CV_HB), a1 = *(LAS f32x4*)(ap + 16) + *(LAS f32x4*)(ap + CV_HB + 16);
    const f32x4 b0 = *(LAS f32x4*)(aq) + *(LAS f32x4*)(aq + CV_HB), b1 = *(LAS f32x4*)(aq + 16) + *(LAS f32x4*)(aq + CV_HB + 16);
    if (opc == 8) { a0 += b0; a1 += b1; }
    const u32x4 o = {pk2(__builtin_fmaf(x0[0], bf_lo(z[0]), gq * a0[0]), __builtin_fmaf(x0[1], bf_hi(z[0]), gq * a0[1])),
                     pk2(__builtin_fmaf(x0[2], bf_lo(z[1]), gq * a0[2]), __builtin_fmaf(x0[3], bf_hi(z[1]), gq * a0[3])),
                     pk2(__builtin_fmaf(x1[0], bf_lo(z[2]), gq * a1[0]), __builtin_fmaf(x1[1], bf_hi(z[2]), gq * a1[1])),
                     pk2(__builtin_fmaf(x1[2], bf_lo(z[3]), gq * a1[2]), __builtin_fmaf(x1[3], bf_hi(z[3]), gq * a1[3]))};
    asm volatile("global_store_dwordx4 %0, %1, off sc1\n\ts_nop 1" :: "v"(op), "v"(o) : "memory");
  }
  asm volatile("fk_end:");
}

extern "C" void kernel_launch(void* const* d_in, const int* in_sizes, int n_in, void* d_out, int out_size,
                              void* d_ws, size_t ws_size, hipStream_t stream) {
  fused_kernel<<<256, 768, 0, stream>>>((const __bf16*)d_in[0], (const __bf16*)d_in[1], (const __bf16*)d_in[2],
                                        (const __bf16*)d_in[3], (const __bf16*)d_in[4], (const __bf16*)d_in[6],
                                        (const __bf16*)d_in[7], (const __bf16*)d_in[8], (const __bf16*)d_in[9],
                                        (const __bf16*)d_in[10], (const __bf16*)d_in[11], (__bf16*)d_out, (char*)d_ws);
}
```
